```python
import torch

def _softmax(x, axis=-1):
    return torch.softmax(x, dim=axis)
def _gelu(x):
    return torch.nn.functional.gelu(x, approximate="tanh")
def _silu(x):
    return torch.nn.functional.silu(x)
def _sigmoid(x):
    return torch.sigmoid(x)
def _layernorm(x, axis=-1, eps=1e-5):
    m = x.mean(dim=axis, keepdim=True)
    v = x.var(dim=axis, keepdim=True, unbiased=False)
    return (x - m) / torch.sqrt(v + eps)
def _scatter_add(tgt, idx, src, axis=0):
    return tgt.clone().index_add_(axis, idx, src)
def _segment_sum(src, seg, n):
    out = torch.zeros((n,) + src.shape[1:], dtype=src.dtype)
    return out.index_add_(0, seg, src)
def _pad(x, pw):
    flat = tuple(v for pair in reversed(pw) for v in pair)
    return torch.nn.functional.pad(x, flat)

kernel_name = 'interleave_layers__linear_kernel_hedgehog__mixer_channel_mlp__prompt'

def setup_inputs():

    _w = (lambda a: (lambda t: t.to(torch.bfloat16) if t.is_floating_point() else t)(torch.as_tensor(a)))
    def _A_setup():
        B, S, D, H = 1, 2048, 512, 32
        Dh = D // H
        return {
            "x": _w((lambda *s: torch.randn(*s, dtype=torch.bfloat16) * 0.05)(B, S, D)),
            "wqkv": _w((lambda *s: torch.randn(*s, dtype=torch.bfloat16) * 0.4)(D, 3 * D)),
            "w_phi": _w((lambda *s: torch.randn(*s, dtype=torch.bfloat16) * 0.4)(H, Dh, Dh) * (Dh ** -0.5)),
            "wo": _w((lambda *s: torch.randn(*s, dtype=torch.bfloat16) * 0.05)(D, D)),
        }
    _A_in = _A_setup()

    def _B_setup():
        B, S, D, F = 1, 2048, 512, 1792
        return {
            "x": _w((lambda *s: torch.randn(*s, dtype=torch.bfloat16) * 0.05)(B, S, D)),
            "w1": _w((lambda *s: torch.randn(*s, dtype=torch.bfloat16) * 3.2)(D, F) * (D ** -0.5)),
            "b1": _w((lambda *s: torch.randn(*s, dtype=torch.bfloat16) * 0.8)(F)),
            "w2": _w((lambda *s: torch.randn(*s, dtype=torch.bfloat16) * 0.8)(F, D) * (F ** -0.5)),
            "b2": _w((lambda *s: torch.randn(*s, dtype=torch.bfloat16) * 0.8)(D)),
        }
    _B_in = _B_setup()

    return {
        "A_x": _A_in["x"],
        "A_wqkv": _A_in["wqkv"],
        "A_w_phi": _A_in["w_phi"],
        "A_wo": _A_in["wo"],
        "B_x": _B_in["x"],
        "B_w1": _B_in["w1"],
        "B_b1": _B_in["b1"],
        "B_w2": _B_in["w2"],
        "B_b2": _B_in["b2"],
    }

def reference(A_x, A_wqkv, A_w_phi, A_wo, B_x, B_w1, B_b1, B_w2, B_b2):
    def _A_ref(x, wqkv, w_phi, wo):
        B, S, D = x.shape
        H = 32; Dh = D // H
        qkv = (lambda x, *d: x.permute(*d))((x @ wqkv).reshape(B, S, 3, H, Dh), 2, 0, 3, 1, 4)
        q, k, v = qkv[0], qkv[1], qkv[2]
        pq = torch.einsum("bhsd,hdf->bhsf", q, w_phi)
        pk = torch.einsum("bhsd,hdf->bhsf", k, w_phi)
        phi_q = torch.concatenate([_softmax(pq, axis=-1), _softmax(-pq, axis=-1)], axis=-1)
        phi_k = torch.concatenate([_softmax(pk, axis=-1), _softmax(-pk, axis=-1)], axis=-1)
        kv = torch.einsum("bhsf,bhsd->bhfd", phi_k, v)
        z  = torch.einsum("bhsf->bhf", phi_k)
        num = torch.einsum("bhqf,bhfd->bhqd", phi_q, kv)
        den = torch.einsum("bhqf,bhf->bhq", phi_q, z)[..., None] + 1e-6
        o = num / den
        return (lambda x, *d: x.permute(*d))(o, 0, 2, 1, 3).reshape(B, S, D) @ wo

    def _B_ref(x, w1, b1, w2, b2):
        y = _layernorm(x, axis=-1)
        h = _gelu(torch.einsum("bsd,df->bsf", y, w1) + b1)
        return torch.einsum("bsf,fd->bsd", h, w2) + b2

    x = A_x
    x = x + _A_ref(_layernorm(x, axis=-1), A_wqkv, A_w_phi, A_wo)
    x = x + _B_ref(_layernorm(x, axis=-1), B_w1, B_b1, B_w2, B_b2)
    x = x + _A_ref(_layernorm(x, axis=-1), A_wqkv, A_w_phi, A_wo)
    x = x + _B_ref(_layernorm(x, axis=-1), B_w1, B_b1, B_w2, B_b2)
    return x
```

```cpp
#include <hip/hip_runtime.h>
#include <hip/hip_bf16.h>
#include <stdint.h>

typedef unsigned short u16;
__device__ __forceinline__ float bf2f(u16 b){ return __uint_as_float(((unsigned)b)<<16); }
__device__ __forceinline__ u16 f2bf(float f){ __hip_bfloat16 h = __float2bfloat16(f); return *(u16*)&h; }

constexpr int S=2048, D=512, H=32, DH=16, F=1792;

__global__ void k_ln(const float* x, float* y, int times){
  int row = blockIdx.x; int t = threadIdx.x;
  float v[8];
  for(int i=0;i<8;i++) v[i] = x[row*D + t + 64*i];
  for(int it=0; it<times; it++){
    float s=0; for(int i=0;i<8;i++) s+=v[i];
    for(int o=32;o;o>>=1) s += __shfl_xor(s,o);
    float m = s/D;
    float q=0; for(int i=0;i<8;i++){ float d=v[i]-m; q+=d*d; }
    for(int o=32;o;o>>=1) q += __shfl_xor(q,o);
    float r = rsqrtf(q/D + 1e-5f);
    for(int i=0;i<8;i++) v[i] = (v[i]-m)*r;
  }
  for(int i=0;i<8;i++) y[row*D + t + 64*i] = v[i];
}
__global__ void k_cvt(const u16* a, float* b, int n){ int i = blockIdx.x*256+threadIdx.x; if(i<n) b[i]=bf2f(a[i]); }
__global__ void k_cvto(const float* a, u16* b, int n){ int i = blockIdx.x*256+threadIdx.x; if(i<n) b[i]=f2bf(a[i]); }

template<int MODE>
__global__ void k_gemm(const float* A, const u16* W, const u16* bias, const float* res, float* C, int M, int N, int K){
  __shared__ float As[16][17]; __shared__ float Ws[16][17];
  int tx=threadIdx.x, ty=threadIdx.y;
  int row = blockIdx.y*16+ty, col = blockIdx.x*16+tx;
  float acc=0;
  for(int k0=0;k0<K;k0+=16){
    As[ty][tx] = bf2f(f2bf(A[row*K + k0+tx]));
    Ws[ty][tx] = bf2f(W[(k0+ty)*N + col]);
    __syncthreads();
    for(int k=0;k<16;k++) acc += As[ty][k]*Ws[k][tx];
    __syncthreads();
  }
  if(MODE==1||MODE==2) acc += bf2f(bias[col]);
  if(MODE==1){ float x=acc; float u = 0.7978845608028654f*(x+0.044715f*x*x*x); acc = 0.5f*x*(1.f+tanhf(u)); }
  if(MODE==2||MODE==3) acc = bf2f(f2bf(acc + res[row*N+col]));
  C[row*N+col]=acc;
}
__global__ void k_phi(const float* qkv, const u16* wphi, float* phi){
  int idx = blockIdx.x*256+threadIdx.x;
  if(idx>=2*S*H) return;
  int h = idx % H; int row = (idx/H)%S; int w = idx/(H*S);
  const float* src = qkv + row*3*D + w*D + h*DH;
  float p[16];
  for(int f=0;f<16;f++){ float a=0; for(int d=0;d<16;d++) a += bf2f(f2bf(src[d]))*bf2f(wphi[(h*16+d)*16+f]); p[f]=a; }
  float mx=p[0], mn=p[0]; for(int f=1;f<16;f++){ mx=fmaxf(mx,p[f]); mn=fminf(mn,p[f]); }
  float s1=0,s2=0; for(int f=0;f<16;f++){ s1+=__expf(p[f]-mx); s2+=__expf(mn-p[f]); }
  float* o = phi + ((size_t)(w*S+row)*H+h)*32;
  for(int f=0;f<16;f++){ o[f]=bf2f(f2bf(__expf(p[f]-mx)/s1)); o[16+f]=bf2f(f2bf(__expf(mn-p[f])/s2)); }
}
__global__ void k_kv(const float* phi, const float* qkv, float* kv){
  int h = blockIdx.x; int t = threadIdx.x;
  int f = t/17, d = t%17;
  float a=0;
  for(int s=0;s<S;s++){
    float pk = phi[((size_t)(S+s)*H+h)*32+f];
    float v = d<16 ? bf2f(f2bf(qkv[s*3*D + 2*D + h*16 + d])) : 1.f;
    a += pk*v;
  }
  kv[h*544+t]=a;
}
__global__ void k_o(const float* phi, const float* kv, float* o){
  int idx = blockIdx.x*256+threadIdx.x;
  int c = idx % D, row = idx / D; int h = c/16, d = c%16;
  const float* pq = phi + ((size_t)row*H+h)*32;
  float num=0, den=0;
  for(int f=0;f<32;f++){ num += pq[f]*kv[h*544+f*17+d]; den += pq[f]*kv[h*544+f*17+16]; }
  o[idx] = num/(den+1e-6f);
}

extern "C" void kernel_launch(void* const* d_in, const int* in_sizes, int n_in,
                              void* d_out, int out_size, void* d_ws, size_t ws_size,
                              hipStream_t stream) {
  const u16* x = (const u16*)d_in[0]; const u16* wqkv=(const u16*)d_in[1]; const u16* wphi=(const u16*)d_in[2]; const u16* wo=(const u16*)d_in[3];
  const u16* w1=(const u16*)d_in[5]; const u16* b1=(const u16*)d_in[6]; const u16* w2=(const u16*)d_in[7]; const u16* b2=(const u16*)d_in[8];
  float* ws=(float*)d_ws;
  float* X = ws; float* Y = X + S*D; float* QKV = Y + S*D; float* PHI = QKV + S*3*D; float* KV = PHI + 2*S*H*32; float* O = KV + 32*544+64; float* Hh = O + S*D; float* X2 = Hh + S*F;
  k_cvt<<<S*D/256,256,0,stream>>>(x,X,S*D);
  dim3 b(16,16);
  for(int l=0;l<2;l++){
    k_ln<<<S,64,0,stream>>>(X,Y,1);
    k_gemm<0><<<dim3(3*D/16,S/16),b,0,stream>>>(Y,wqkv,nullptr,nullptr,QKV,S,3*D,D);
    k_phi<<<2*S*H/256,256,0,stream>>>(QKV,wphi,PHI);
    k_kv<<<32,544,0,stream>>>(PHI,QKV,KV);
    k_o<<<S*D/256,256,0,stream>>>(PHI,KV,O);
    k_gemm<3><<<dim3(D/16,S/16),b,0,stream>>>(O,wo,nullptr,X,X2,S,D,D);
    k_ln<<<S,64,0,stream>>>(X2,Y,2);
    k_gemm<1><<<dim3(F/16,S/16),b,0,stream>>>(Y,w1,b1,nullptr,Hh,S,F,D);
    k_gemm<2><<<dim3(D/16,S/16),b,0,stream>>>(Hh,w2,b2,X2,X,S,D,F);
  }
  k_cvto<<<S*D/256,256,0,stream>>>(X,(u16*)d_out,S*D);
}
```

```cpp
#include <hip/hip_runtime.h>
#include <stdint.h>

typedef unsigned short u16;
typedef __attribute__((ext_vector_type(8))) __bf16 bf16x8;
typedef __attribute__((ext_vector_type(2))) __bf16 bf16x2;
typedef __attribute__((ext_vector_type(16))) float f32x16;
typedef __attribute__((ext_vector_type(4))) float f32x4;
typedef __attribute__((ext_vector_type(2))) float f32x2;
typedef __attribute__((ext_vector_type(4))) unsigned u32x4;
typedef __attribute__((ext_vector_type(2))) unsigned u32x2;
typedef __attribute__((ext_vector_type(4))) short s16x4;
typedef __amdgpu_buffer_rsrc_t rsrc_t;

#define DEVI __device__ __forceinline__
#define MFMA(a, b, c) __builtin_amdgcn_mfma_f32_32x32x16_bf16(__builtin_bit_cast(bf16x8, a), __builtin_bit_cast(bf16x8, b), c, 0, 0, 0)
#define SB() __builtin_amdgcn_sched_barrier(0)

constexpr int WT_QKV = 0x0;
constexpr int WQ_C   = 101376;
constexpr int WT_O   = 0x190000;
constexpr int WT_1   = 0x210000;
constexpr int WT_2   = 0x3D0000;
constexpr int QN     = 0x600000;
constexpr int KVP    = 0xA00000;
constexpr int KVP_C  = 69632;
constexpr int KVP_L  = 0x120000;
constexpr int OB     = 0xD00000;
constexpr int X1B    = 0x1100000;
constexpr int X2B    = 0x1500000;
constexpr int ST1    = 0x1700000;
constexpr int ST2    = 0x1780000;
constexpr int HB     = 0x1800000;
constexpr int HB_L   = 0x700000;
constexpr int WS_USED = 0x2800000;
#define STAMP_OFF 0x2800000
#define STAMP_N 32

constexpr int LDS_W = 114688;
constexpr int LDS_TOT = 163840;
constexpr int INBOX = 12288;

constexpr float LOG2E = 1.4426950408889634f;
constexpr int SPIN_MAX = 1 << 16;

DEVI unsigned pk2(float a, float b) { f32x2 v = {a, b}; return __builtin_bit_cast(unsigned, __builtin_convertvector(v, bf16x2)); }
DEVI float bflo(unsigned u) { return __uint_as_float(u << 16); }
DEVI float bfhi(unsigned u) { return __uint_as_float(u & 0xffff0000u); }
DEVI float bf2f(u16 b) { return __uint_as_float(((unsigned)b) << 16); }
DEVI void lds_barrier() { asm volatile("s_waitcnt lgkmcnt(0)\n\ts_barrier" ::: "memory"); }
DEVI void vm_drain() { asm volatile("s_waitcnt vmcnt(0)" ::: "memory"); }
DEVI void glds1k(unsigned voff, const void* sbase, unsigned lds_base) {
  asm volatile("s_mov_b32 m0, %2\n\ts_nop 0\n\tglobal_load_lds_dwordx4 %0, %1" :: "v"(voff), "s"(sbase), "s"(lds_base) : "memory", "m0");
}
DEVI void glds4k(unsigned voff, const char* sbase, unsigned lds_base) {
  asm volatile("s_mov_b32 m0, %2\n\ts_nop 0\n\t"
               "global_load_lds_dwordx4 %0, %1\n\t"
               "global_load_lds_dwordx4 %0, %1 offset:1024\n\t"
               "global_load_lds_dwordx4 %0, %1 offset:2048\n\t"
               "global_load_lds_dwordx4 %0, %1 offset:3072"
               :: "v"(voff), "s"(sbase), "s"(lds_base) : "memory", "m0");
}
DEVI float xhalf_sum(float v) {
  auto t = __builtin_amdgcn_permlane32_swap(__float_as_uint(v), __float_as_uint(v), false, false);
  return __uint_as_float(t[0]) + __uint_as_float(t[1]);
}
DEVI u32x4 ldw(rsrc_t rs, int vo, int so) { return __builtin_amdgcn_raw_buffer_load_b128(rs, vo, so, 0); }
DEVI u32x4 ldc(rsrc_t rs, int vo, int so) { return __builtin_amdgcn_raw_buffer_load_b128(rs, vo, so, 16); }
DEVI u32x2 ldc2(rsrc_t rs, int vo, int so) { return __builtin_amdgcn_raw_buffer_load_b64(rs, vo, so, 16); }
DEVI void stc(rsrc_t rs, int vo, int so, u32x4 v) { __builtin_amdgcn_raw_buffer_store_b128(v, rs, vo, so, 16); }
DEVI void stc2(rsrc_t rs, int vo, int so, u32x2 v) { __builtin_amdgcn_raw_buffer_store_b64(v, rs, vo, so, 16); }
DEVI void stp(rsrc_t rs, int vo, int so, u32x4 v) {
  __builtin_amdgcn_raw_buffer_store_b128(v, rs, vo, so, 16);
}
DEVI void stp2(rsrc_t rs, int vo, int so, u32x2 v) {
  __builtin_amdgcn_raw_buffer_store_b64(v, rs, vo, so, 16);
}
DEVI u32x4 ldwk(rsrc_t rs, int vo, int so, int kb) { return ldw(rs, vo + (kb & 3) * 1024, so + (kb >> 2) * 4096); }
DEVI u32x4 ldck(rsrc_t rs, int vo, int so, int kb) { return ldc(rs, vo + (kb & 3) * 1024, so + (kb >> 2) * 4096); }
DEVI float selm(unsigned m, float a, float b) { return __uint_as_float((__float_as_uint(a) & m) | (__float_as_uint(b) & ~m)); }
DEVI unsigned umax3(unsigned a, unsigned b, unsigned c) { return max(max(a, b), c); }
DEVI unsigned chk4(unsigned m, u32x4 f) { return umax3(umax3(m, f[0], f[1]), f[2], f[3]); }
DEVI bool all_valid(unsigned m) { return __builtin_amdgcn_ballot_w64(m == 0xffffffffu) == 0; }
DEVI float fmax3(float a, float b, float c) { return fmaxf(fmaxf(a, b), c); }
DEVI float fmin3(float a, float b, float c) { return fminf(fminf(a, b), c); }

#define STAMP(i)

DEVI void map_block(int b, int& g, int& c) {
  int x = b & 7, j = b >> 3;
  g = 4 * (x >> 1) + (j >> 3); c = 8 * (x & 1) + (j & 7);
}

template <class T>
DEVI void pack16(const T& v, u32x4& a, u32x4& b) {
#pragma unroll
  for (int i = 0; i < 4; i++) { a[i] = pk2(v[2 * i], v[2 * i + 1]); b[i] = pk2(v[8 + 2 * i], v[9 + 2 * i]); }
}

template <int... I> struct iseq {};
template <int N, int... I> struct mkseq : mkseq<N - 1, N - 1, I...> {};
template <int... I> struct mkseq<0, I...> { typedef iseq<I...> type; };
template <class F, int... I> DEVI void sfor_impl(F& f, iseq<I...>) { (f(I), ...); }
template <int N, class F> DEVI void sfor(F f) { sfor_impl(f, typename mkseq<N>::type()); }
#define LAMBDA __attribute__((always_inline))
template <int NS, int D, class RD, class MM, class FILL>
DEVI void stream(RD rd, MM mm, FILL fill) {
  u32x4 ring[D];
#pragma unroll
  for (int i = 0; i < D; i++) ring[i] = rd(i);
  SB();
  sfor<NS>([&](int S) LAMBDA {
    mm(S, ring[S % D]);
    if (S + D < NS) ring[S % D] = rd(S + D);
    fill(S);
    SB();
  });
}

struct Softmax {
  float mx, mn, sp, sn, ep[16], en[16];
  DEVI void step(int i, const f32x16& p, float sc, u32x4 (&ph)[4]) {
    if (i == 0) { mx = fmax3(p[0], p[1], p[2]); mn = fmin3(p[0], p[1], p[2]); mx = fmax3(mx, p[3], p[4]); mn = fmin3(mn, p[3], p[4]); }
    else if (i < 3) { int b = 5 + 4 * (i - 1); mx = fmax3(mx, p[b], p[b + 1]); mn = fmin3(mn, p[b], p[b + 1]); mx = fmax3(mx, p[b + 2], p[b + 3]); mn = fmin3(mn, p[b + 2], p[b + 3]); }
    else if (i == 3) { mx = fmax3(mx, p[13], p[14]); mn = fmin3(mn, p[13], p[14]); mx = fmaxf(mx, p[15]) * -sc; mn = fminf(mn, p[15]) * sc; }
    else if (i < 20) {
      int r = i - 4;
      ep[r] = __builtin_amdgcn_exp2f(fmaf(p[r], sc, mx)); en[r] = __builtin_amdgcn_exp2f(fmaf(p[r], -sc, mn));
      if (r == 1) { sp = ep[0] + ep[1]; sn = en[0] + en[1]; } else if (r > 1) { sp += ep[r]; sn += en[r]; }
    } else if (i == 20) { sp = __builtin_amdgcn_rcpf(sp); sn = __builtin_amdgcn_rcpf(sn); }
    else if (i < 29) {
      int t = i - 21;
      ph[t >> 2][t & 3] = pk2(ep[2 * t] * sp, ep[2 * t + 1] * sp);
      ph[2 + (t >> 2)][t & 3] = pk2(en[2 * t] * sn, en[2 * t + 1] * sn);
    }
  }
};

DEVI u32x4 tr_read(const char* p) {
  typedef __attribute__((address_space(3))) s16x4* lp;
  s16x4 a = __builtin_amdgcn_ds_read_tr16_b64_v4i16((lp)(p));
  s16x4 b = __builtin_amdgcn_ds_read_tr16_b64_v4i16((lp)(p + 256));
  u32x2 ua = __builtin_bit_cast(u32x2, a), ub = __builtin_bit_cast(u32x2, b);
  u32x4 r = {ua[0], ua[1], ub[0], ub[1]};
  return r;
}

DEVI void load_aphi(const u16* wphi, int c, int lane, u32x4 (&aphi)[2]) {
  int i = lane & 31, hk = lane >> 5;
  int hp = (i >> 2) & 1, f = (i & 3) + 4 * (i >> 3);
  const u16* src = wphi + (2 * c + hk) * 256 + f;
#pragma unroll
  for (int s = 0; s < 2; s++)
#pragma unroll
    for (int t = 0; t < 4; t++) {
      unsigned lo = src[(8 * s + 2 * t) * 16], hi = src[(8 * s + 2 * t + 1) * 16];
      aphi[s][t] = (hp == hk) ? (lo | (hi << 16)) : 0u;
    }
}

DEVI u32x4 aug_tok(float mean, float ia, int hk) {
  unsigned mh = pk2(-mean, ia);
  unsigned ml = pk2(-mean - bflo(mh), ia - bfhi(mh));
  u32x4 t;
  t[0] = __builtin_amdgcn_perm(ml, mh, 0x05040100u);
  t[1] = mh;
  t[2] = ml >> 16;
  t[3] = 0;
  if (hk) { t[0] = 0; t[1] = 0; t[2] = 0; }
  return t;
}
DEVI unsigned cs_pack(float cs) { unsigned h = pk2(cs, 0.f); return pk2(cs, cs - bflo(h)); }
DEVI u32x4 aug_w(unsigned csp, unsigned b) {
  u32x4 t;
  t[0] = __builtin_amdgcn_perm(csp, csp, 0x01000100u);
  t[1] = __builtin_amdgcn_perm(b, csp, 0x05040302u);
  t[2] = b & 0xffffu;
  t[3] = 0;
  return t;
}

template <int KVS>
DEVI void kv_tail(const u32x4 (&phk)[4], const f32x16& accv, const char* priv, char* kvs, rsrc_t rs, int kvoff, int g, int tid, int lane, int w) {
  const int tok = lane & 31, hk = lane >> 5;
  int n = tok, Hn = n >> 4, fn = 8 * ((n >> 2) & 1) + (n & 3) + 4 * ((n >> 3) & 1);
  u32x4 sel[2];
#pragma unroll
  for (int s = 0; s < 2; s++)
#pragma unroll
    for (int t = 0; t < 4; t++) {
      int f0 = 8 * s + 2 * t;
      unsigned v = (fn == f0) ? 0x3f80u : ((fn == f0 + 1) ? 0x3f800000u : 0u);
      sel[s][t] = (Hn == hk) ? v : 0u;
    }
  f32x16 t0 = {}, t1 = {};
  t0 = MFMA(phk[0], sel[0], t0); t0 = MFMA(phk[1], sel[1], t0);
  t1 = MFMA(phk[2], sel[0], t1); t1 = MFMA(phk[3], sel[1], t1);
  u32x4 vb0, vb1;
  {
    float vn[16];
#pragma unroll
    for (int m = 0; m < 4; m++) {
      f32x4 r4 = *(const f32x4*)(priv + (8 * m + 4 * hk) * 4);
#pragma unroll
      for (int e = 0; e < 4; e++) vn[4 * m + e] = accv[4 * m + e] * r4[e];
    }
    pack16(vn, vb0, vb1);
  }
  const u32x4 ones = {0x3f803f80u, 0x3f803f80u, 0x3f803f80u, 0x3f803f80u};
  unsigned hm = Hn ? ~0u : 0u;
  asm volatile("" : "+v"(hm));
  char* dst = kvs + w * KVS;
#pragma unroll
  for (int pm = 0; pm < 2; pm++) {
    u32x4 a0, a1; pack16(pm ? t1 : t0, a0, a1);
    f32x16 kv = {}; kv = MFMA(a0, vb0, kv); kv = MFMA(a1, vb1, kv);
    f32x16 z = {}; z = MFMA(a0, ones, z); z = MFMA(a1, ones, z);
    f32x4 o0, o1, z0, z1;
#pragma unroll
    for (int e = 0; e < 4; e++) {
      o0[e] = selm(hm, kv[8 + e], kv[e]); o1[e] = selm(hm, kv[12 + e], kv[4 + e]);
      z0[e] = selm(hm, z[8 + e], z[e]);   z1[e] = selm(hm, z[12 + e], z[4 + e]);
    }
    *(f32x4*)(dst + pm * 2048 + lane * 32) = o0; *(f32x4*)(dst + pm * 2048 + lane * 32 + 16) = o1;
    if ((n & 15) == 0) {
      char* dz = dst + 4096 + ((pm * 2 + Hn) * 2 + hk) * 32;
      *(f32x4*)dz = z0; *(f32x4*)(dz + 16) = z1;
    }
  }
  lds_barrier();
  {
    f32x4 s = *(const f32x4*)(kvs + tid * 16);
#pragma unroll
    for (int k = 1; k < 4; k++) s += *(const f32x4*)(kvs + k * KVS + tid * 16);
    stc(rs, lane * 16, kvoff + (w * 16 + g) * 1024, __builtin_bit_cast(u32x4, s));
    if (tid < 16) {
      f32x4 z = *(const f32x4*)(kvs + 4096 + tid * 16);
#pragma unroll
      for (int k = 1; k < 4; k++) z += *(const f32x4*)(kvs + k * KVS + 4096 + tid * 16);
      stc(rs, tid * 16, kvoff + 65536 + g * 256, __builtin_bit_cast(u32x4, z));
    }
  }
}

__global__ __launch_bounds__(256, 1) void k1(const u16* __restrict__ x, const u16* __restrict__ wqkv, const u16* __restrict__ wphi,
                                             const u16* __restrict__ wo, const u16* __restrict__ w1, const u16* __restrict__ w2, char* ws) {
  __shared__ __attribute__((aligned(16))) char lds[LDS_TOT];
  const int tid = threadIdx.x, lane = tid & 63, w = __builtin_amdgcn_readfirstlane(tid >> 6);
  const int tok = lane & 31, hk = lane >> 5;
  int g, c; map_block(blockIdx.x, g, c);
  rsrc_t rs = __builtin_amdgcn_make_buffer_rsrc(ws, 0, WS_USED, 0x00020000);
  const unsigned lbase = (unsigned)(size_t)lds;
  char* area = lds + w * 32768;
  char* priv = area + 16384;
  char* kvs = lds + 20480;
  char* csp = area + 17408;
  STAMP(0);
  {
    const char* xb = (const char*)(x + (size_t)(128 * g + 32 * w) * 512);
#pragma unroll
    for (int r = 0; r < 32; r++) glds1k((lane ^ (r & 15)) * 16, xb + r * 1024, lbase + w * 32768 + r * 1024);
  }
  const unsigned wvo = (lane >> 2) * 3072 + (lane & 3) * 16;
  const char* wb = (const char*)(wqkv + (size_t)(128 * w) * 1536 + 32 * c);
#pragma unroll
  for (int m = 0; m < 8; m++) glds1k(wvo, wb + m * 16 * 3072, lbase + 131072 + w * 8192 + m * 1024);
  unsigned touch = 0;
#pragma unroll
  for (int m = 0; m < 4; m++) touch |= *(const unsigned*)(wb + (size_t)((m & 1) * 64 + lane) * 3072 + 1024 + (m >> 1) * 1024);
  u32x4 aphi[2]; load_aphi(wphi, c, lane, aphi);
  u32x4 rt[8]; int rt_dst = 0; bool rt_nat = false;
  {
    int id = blockIdx.x * 176 + tid;
    if (tid < 176) {
      const u16* src; int N;
      if (id < 12288) {
        int n8 = id % 192, k8 = id / 192; N = 1536; src = wqkv + (size_t)k8 * 8 * N + n8 * 8;
        int blk = n8 >> 6, cc = (n8 & 63) >> 2, a = n8 & 3;
        int step = 2 * (k8 >> 2) + (k8 & 1), h2 = (k8 >> 1) & 1;
        rt_nat = (blk == 2);
        int i0 = rt_nat ? 8 * a : 16 * (a & 1) + 4 * (a >> 1);
        rt_dst = WT_QKV + cc * WQ_C + (blk * 32 + step) * 1024 + (h2 * 32 + i0) * 16;
      } else if (id < 16384) {
        int q = id - 12288; int n8 = q & 63, k8 = q >> 6; N = 512; src = wo + (size_t)k8 * 8 * N + n8 * 8;
        int cb = n8 >> 3, bb = (n8 >> 2) & 1, a = n8 & 3;
        int step = 2 * (k8 >> 2) + (k8 & 1), h2 = (k8 >> 1) & 1;
        int i0 = 16 * (a & 1) + 4 * (a >> 1);
        int kq = step >> 3;
        rt_dst = WT_O + (((cb * 4 + kq) * 8 + (step & 7)) * 2 + (bb ^ (kq & 1))) * 1024 + (h2 * 32 + i0) * 16;
      } else if (id < 30720) {
        int q = id - 16384; int n8 = q % 224, k8 = q / 224; N = 1792; src = w1 + (size_t)k8 * 8 * N + n8 * 8;
        int cc = n8 / 14, l8 = n8 % 14;
        int step = 2 * (k8 >> 2) + (k8 & 1), h2 = (k8 >> 1) & 1;
        int base = WT_1 + (cc * 32 + step) * 3584;
        if (l8 < 12) { int bb = l8 >> 2, a = l8 & 3; int i0 = 16 * (a & 1) + 4 * (a >> 1); rt_dst = base + bb * 1024 + (h2 * 32 + i0) * 16; }
        else { int i0 = 4 * (l8 - 12); rt_dst = base + 3072 + (h2 * 16 + i0) * 16; }
      } else {
        int q = id - 30720; int n8 = q & 63, k8 = q >> 6; N = 512; src = w2 + (size_t)k8 * 8 * N + n8 * 8;
        int cb = n8 >> 3, bb = (n8 >> 2) & 1, a = n8 & 3;
        int cc = k8 / 14, l8 = k8 % 14;
        int s7 = l8 < 12 ? 2 * (l8 >> 2) + (l8 & 1) : 6, h2 = l8 < 12 ? (l8 >> 1) & 1 : l8 - 12;
        int i0 = 16 * (a & 1) + 4 * (a >> 1);
        int kq = cc >> 2;
        rt_dst = WT_2 + (((cb * 4 + kq) * 28 + s7 * 4 + (cc & 3)) * 2 + (bb ^ (kq & 1))) * 1024 + (h2 * 32 + i0) * 16;
      }
#pragma unroll
      for (int j = 0; j < 8; j++) rt[j] = *(const u32x4*)(src + (size_t)j * N);
    }
  }
  vm_drain();
  asm volatile("" :: "v"(touch));
  STAMP(1);
  u32x4 xf[33];
  {
#pragma unroll
    for (int s = 0; s < 32; s++) {
      int q = 4 * (s >> 1) + (s & 1);
      xf[s] = *(const u32x4*)(area + tok * 1024 + (((q & 15) + 2 * hk) ^ (tok & 15)) * 16 + (q >> 4) * 256);
    }
    asm volatile("s_waitcnt lgkmcnt(0)" ::: "memory");
    SB();
  }
#pragma unroll
  for (int m = 0; m < 16; m++) glds1k(wvo, wb + (m & 7) * 16 * 3072 + 1024 + (m >> 3) * 1024, lbase + w * 32768 + m * 1024);
  int troff_qk, troff_v;
  {
    int ch = (lane >> 4) & 1, q = (lane & 15) >> 2, p = lane & 3;
    int rowoff = (16 * hk + q) * 64;
    troff_qk = rowoff + (16 * (p & 1) + 4 * (2 * ch + (p >> 1))) * 2;
    troff_v = rowoff + (16 * ch + 4 * p) * 2;
  }
  const u32x4 ones = {0x3f803f80u, 0x3f803f80u, 0x3f803f80u, 0x3f803f80u};
  float s1 = 0.f, s2 = 0.f;
  auto stat = [&](int i) LAMBDA {
    const bf16x2 one2 = __builtin_bit_cast(bf16x2, 0x3f803f80u);
    const unsigned u = xf[i >> 2][i & 3]; bf16x2 v = __builtin_bit_cast(bf16x2, u);
    s1 = __builtin_amdgcn_fdot2_f32_bf16(v, one2, s1, false);
    s2 = __builtin_amdgcn_fdot2_f32_bf16(v, v, s2, false);
  };
  auto soff = [](int s) { return (32 * ((s & 7) >> 1) + 8 * (s & 1)) * 64; };
  {
    f32x16 cs = {};
    stream<8, 4>([&](int S) LAMBDA { return tr_read(lds + 131072 + w * 8192 + soff(S) + troff_qk); },
                 [&](int S, const u32x4& a) LAMBDA { cs = MFMA(ones, a, cs); },
                 [&](int S) LAMBDA { stat(2 * S); stat(2 * S + 1); });
    if (hk == 0) *(float*)(csp + tok * 4) = cs[0];
  }
  lds_barrier();
  STAMP(2);
  f32x16 accq = {};
  stream<32, 4>([&](int S) LAMBDA { return tr_read(lds + 131072 + (S >> 3) * 8192 + soff(S) + troff_qk); },
                [&](int S, const u32x4& a) LAMBDA { accq = MFMA(a, xf[S], accq); },
                [&](int S) LAMBDA { stat(16 + 2 * S); stat(17 + 2 * S); });
  vm_drain();
  {
    f32x16 ck = {}, cv = {};
    stream<16, 4>([&](int S) LAMBDA { return tr_read(area + (S >> 3) * 8192 + soff(S) + (S < 8 ? troff_qk : troff_v)); },
                  [&](int S, const u32x4& a) LAMBDA { if (S < 8) ck = MFMA(ones, a, ck); else cv = MFMA(ones, a, cv); },
                  [&](int S) LAMBDA { stat(80 + 3 * S); stat(81 + 3 * S); stat(82 + 3 * S); });
    if (hk == 0) { *(float*)(csp + (32 + tok) * 4) = ck[0]; *(float*)(csp + (64 + tok) * 4) = cv[0]; }
  }
  s1 = xhalf_sum(s1); s2 = xhalf_sum(s2);
  const float mean = s1 * (1.f / 512.f);
  const float rstd = __builtin_amdgcn_rsqf(fmaxf(s2 * (1.f / 512.f) - mean * mean, 0.f) + 1e-5f);
  const float sc = rstd * LOG2E;
  xf[32] = aug_tok(mean, 0.f, hk);
  if (hk == 0) *(float*)(priv + tok * 4) = rstd;
  lds_barrier();
  STAMP(3);
  u32x4 aw[3];
#pragma unroll
  for (int b = 0; b < 3; b++) {
    float s = 0.f;
#pragma unroll
    for (int k = 0; k < 4; k++) s += *(const float*)(lds + k * 32768 + 17408 + (b * 32 + tok) * 4);
    aw[b] = aug_w(cs_pack(s), 0u);
  }
  f32x16 acck = {};
  const u32x4 ff = {~0u, ~0u, ~0u, ~0u};
  stream<33, 4>([&](int S) LAMBDA { return S < 32 ? tr_read(lds + (S >> 3) * 32768 + soff(S) + troff_qk) : aw[1]; },
                [&](int S, const u32x4& a) LAMBDA { acck = MFMA(a, xf[S], acck); if (S == 2) accq = MFMA(aw[0], xf[32], accq); },
                [&](int S) LAMBDA {
                  if (S >= 4 && S < 12) {
                    int m = (S - 4) >> 1, hi = S & 1;
                    u32x4 v;
#pragma unroll
                    for (int t = 0; t < 4; t++) v[t] = __builtin_amdgcn_perm(rt[2 * t + 1][m], rt[2 * t][m], hi ? 0x07060302u : 0x05040100u);
                    int e = 2 * m;
                    int off = rt_nat ? e * 16 : ((e & 3) + 8 * (e >> 2)) * 16;
                    stc(rs, tid < 176 ? rt_dst + off + hi * 16 : WS_USED, 0, v);
                  } else if (S >= 12 && S < 16) {
                    int k = S - 12;
                    stc(rs, tid * 16 + (k & 1) * 4096, (k < 2 ? OB : X1B) + blockIdx.x * 8192, ff);
                  } else if (S == 16) {
                    if (w == 0) stc(rs, lane * 16, ST1 + blockIdx.x * 1024, ff);
                    if (g == 0 && w == 1) { stc(rs, lane * 16, WT_QKV + c * WQ_C + 98304, aw[0]); stc(rs, lane * 16 + 1024, WT_QKV + c * WQ_C + 98304, aw[1]);
                                            stc(rs, lane * 16 + 2048, WT_QKV + c * WQ_C + 98304, aw[2]); }
                  } else if (S >= 18 && S < 26) {
                    int t = S - 18; accq[2 * t] *= sc; accq[2 * t + 1] *= sc;
                  } else if (S == 26) {
                    u32x4 q0, q1; pack16(accq, q0, q1);
                    stc(rs, lane * 16, QN + (blockIdx.x * 4 + w) * 2048, q0); stc(rs, lane * 16 + 1024, QN + (blockIdx.x * 4 + w) * 2048, q1);
                  }
                });
  f32x16 accv = {}, p = {};
  u32x4 phk[4];
  Softmax sm;
  stream<33, 4>([&](int S) LAMBDA { return S < 32 ? tr_read(lds + (S >> 3) * 32768 + 8192 + soff(S) + troff_v) : aw[2]; },
                [&](int S, const u32x4& a) LAMBDA {
                  accv = MFMA(xf[S], a, accv);
                  if (S == 1) { u32x4 b0, b1; pack16(acck, b0, b1); p = MFMA(aphi[0], b0, p); p = MFMA(aphi[1], b1, p); }
                },
                [&](int S) LAMBDA { if (S >= 3) sm.step(S - 3, p, sc, phk); });
  STAMP(4);
  kv_tail<32768>(phk, accv, priv, kvs, rs, KVP + c * KVP_C, g, tid, lane, w);
  STAMP(5);
}

struct Ctx {
  rsrc_t rs; char* ws; char* lds; int tid, lane, w, tok, hk, g, c, cb, th, vo, hoff, ooff; bool ok;
};

#define SPIN_BEGIN { int it_ = 0; while (true) { asm volatile("" ::: "memory");
#define SPIN_END(m) if (all_valid(m) || !cx.ok) break; if (++it_ > SPIN_MAX) { cx.ok = false; break; } __builtin_amdgcn_s_sleep(1); } }

DEVI void red_send(Ctx& cx, const f32x16& a, int j) {
  char* dst = cx.lds + LDS_W + (cx.w ^ j) * INBOX + (j - 1) * 4096 + cx.vo;
#pragma unroll
  for (int m = 0; m < 4; m++) { f32x4 v = {a[4 * m], a[4 * m + 1], a[4 * m + 2], a[4 * m + 3]}; *(f32x4*)(dst + m * 1024) = v; }
}
DEVI void red_recv(Ctx& cx, const f32x16& own, float (&xr)[16]) {
  lds_barrier();
#pragma unroll
  for (int e = 0; e < 16; e++) xr[e] += own[e];
#pragma unroll
  for (int slot = 0; slot < 3; slot++)
#pragma unroll
    for (int m = 0; m < 4; m++) {
      f32x4 v = *(const f32x4*)(cx.lds + LDS_W + cx.w * INBOX + slot * 4096 + m * 1024 + cx.vo);
#pragma unroll
      for (int e = 0; e < 4; e++) xr[4 * m + e] += v[e];
    }
}

DEVI void publish_x(Ctx& cx, float (&xr)[16], int xbuf, int stbuf) {
  const int tb = 4 * cx.g + 2 * cx.th + (cx.w >> 1), blk = 2 * cx.cb + (cx.w & 1);
  u32x4 f0, f1; pack16(xr, f0, f1);
  stp(cx.rs, cx.vo, xbuf + (tb * 32 + 2 * blk) * 1024, f0);
  stp(cx.rs, cx.vo + 1024, xbuf + (tb * 32 + 2 * blk) * 1024, f1);
  float s1 = 0.f, s2 = 0.f;
#pragma unroll
  for (int t = 0; t < 4; t++) {
    xr[2 * t] = bflo(f0[t]); xr[2 * t + 1] = bfhi(f0[t]); xr[8 + 2 * t] = bflo(f1[t]); xr[9 + 2 * t] = bfhi(f1[t]);
  }
#pragma unroll
  for (int r = 0; r < 16; r++) { s1 += xr[r]; s2 = fmaf(xr[r], xr[r], s2); }
  s1 = xhalf_sum(s1); s2 = xhalf_sum(s2);
  if (cx.hk == 0) { u32x2 st = {__float_as_uint(s1), __float_as_uint(s2)}; stp2(cx.rs, cx.tok * 8, stbuf + (tb * 16 + blk) * 256, st); }
}

DEVI void acquire_x(Ctx& cx, int xbuf, int stbuf, u32x4 (&xf)[33], float& mean, float& var) {
  const int tb = 4 * cx.g + cx.w;
  u32x2 st[8];
  SPIN_BEGIN
    unsigned m = 0;
#pragma unroll
    for (int k = 0; k < 8; k++) st[k] = ldc2(cx.rs, cx.tok * 8 + cx.hk * 2048 + k * 256, stbuf + tb * 4096);
#pragma unroll
    for (int k = 0; k < 8; k++) m = umax3(m, st[k][0], st[k][1]);
  SPIN_END(m)
#pragma unroll
  for (int s = 0; s < 32; s++) xf[s] = ldck(cx.rs, cx.vo, xbuf + tb * 32768, s);
  SB();
  float s1 = 0.f, s2 = 0.f;
#pragma unroll
  for (int k = 0; k < 8; k++) { s1 += __uint_as_float(st[k][0]); s2 += __uint_as_float(st[k][1]); }
  s1 = xhalf_sum(s1); s2 = xhalf_sum(s2);
  mean = s1 * (1.f / 512.f);
  var = fmaxf(s2 * (1.f / 512.f) - mean * mean, 0.f);
}
DEVI void check_x(Ctx& cx, int xbuf, u32x4 (&xf)[33], int b) {
  unsigned m = 0;
#pragma unroll
  for (int k = 0; k < 4; k++) m = chk4(m, xf[4 * b + k]);
  if (!all_valid(m)) {
    const int tb = 4 * cx.g + cx.w;
    SPIN_BEGIN
      m = 0;
#pragma unroll
      for (int k = 0; k < 4; k++) xf[4 * b + k] = ldck(cx.rs, cx.vo, xbuf + tb * 32768, 4 * b + k);
#pragma unroll
      for (int k = 0; k < 4; k++) m = chk4(m, xf[4 * b + k]);
    SPIN_END(m)
  }
}

DEVI void fill_ff(Ctx& cx, int off, int nkb, int stride) {
  const u32x4 ff = {~0u, ~0u, ~0u, ~0u};
#pragma unroll
  for (int k = 0; k < nkb; k++) stc(cx.rs, cx.vo, off + k * stride, ff);
}
DEVI void init_x_out(Ctx& cx, int xbuf, int stbuf) {
  const int tb = 4 * cx.g + 2 * cx.th + (cx.w >> 1), blk = 2 * cx.cb + (cx.w & 1);
  fill_ff(cx, xbuf + (tb * 32 + 2 * blk) * 1024, 2, 1024);
  if (cx.lane < 16) { const u32x4 ff = {~0u, ~0u, ~0u, ~0u}; stc(cx.rs, cx.vo, stbuf + (tb * 16 + blk) * 256, ff); }
}

template <int N4>
DEVI void glds_copy(Ctx& cx, int src_off) {
  unsigned lbase = (unsigned)(size_t)cx.lds + cx.w * 4096;
  const char* src = cx.ws + src_off + cx.w * 4096;
  asm volatile("" : "+s"(src), "+s"(lbase));
#pragma unroll
  for (int m = 0; m < N4; m++) glds4k(cx.vo, src + m * 16384, lbase + m * 16384);
}

DEVI void park(Ctx& cx, const float (&xr)[16]) {
  char* p = cx.lds + LDS_W + cx.w * INBOX + 8192 + cx.vo;
#pragma unroll
  for (int m = 0; m < 4; m++) { f32x4 v = {xr[4 * m], xr[4 * m + 1], xr[4 * m + 2], xr[4 * m + 3]}; *(f32x4*)(p + m * 1024) = v; }
}
DEVI void unpark(Ctx& cx, float (&xr)[16]) {
  const char* p = cx.lds + LDS_W + cx.w * INBOX + 8192 + cx.vo;
#pragma unroll
  for (int m = 0; m < 4; m++) { f32x4 v = *(const f32x4*)(p + m * 1024); xr[4 * m] = v[0]; xr[4 * m + 1] = v[1]; xr[4 * m + 2] = v[2]; xr[4 * m + 3] = v[3]; }
}

template <int L>
DEVI void layer(Ctx& cx, const u16* __restrict__ wphi, u16* __restrict__ out, u32x4 (&w2f)[56], float (&xr)[16],
                unsigned (&csp)[4], const unsigned (&b1p)[2], const u16* __restrict__ b2) {
  const int lane = cx.lane, w = cx.w, tid = cx.tid, tok = cx.tok, hk = cx.hk, g = cx.g, c = cx.c;
  char* ws = cx.ws; (void)ws;
  char* sc = cx.lds + LDS_W;
  char* priv = sc + w * INBOX;
  u32x4 wof[16], phq[4];
  u32x4 pa[16], pz[4];
  const int kvb = KVP + L * KVP_L + c * KVP_C;
  u32x4 aphi[2];
  if (L == 0) {
#pragma unroll
    for (int k = 0; k < 16; k++) pa[k] = ldck(cx.rs, cx.vo, kvb + w * 16384, k);
#pragma unroll
    for (int k = 0; k < 4; k++) pz[k] = ldck(cx.rs, cx.vo, kvb + 65536, k);
    u32x4 q0 = ldw(cx.rs, cx.vo, QN + (blockIdx.x * 4 + w) * 2048), q1 = ldw(cx.rs, cx.vo + 1024, QN + (blockIdx.x * 4 + w) * 2048);
    load_aphi(wphi, c, lane, aphi);
#pragma unroll
    for (int s = 0; s < 16; s++) wof[s] = ldwk(cx.rs, cx.vo, WT_O + (cx.cb * 4 + w) * 16384, s);
    SB();
    f32x16 p = {}; p = MFMA(aphi[0], q0, p); p = MFMA(aphi[1], q1, p);
    Softmax sm;
#pragma unroll
    for (int i = 0; i < 29; i++) sm.step(i, p, 1.f, phq);
    SB();
    glds_copy<7>(cx, WT_1 + c * 114688);
  } else {
    load_aphi(wphi, c, lane, aphi);
    vm_drain(); lds_barrier();
    u32x4 xf[33]; float mean, var;
    acquire_x(cx, X2B, ST2, xf, mean, var);
    STAMP(16);
    const float rstd = __builtin_amdgcn_rsqf(var + 1e-5f), scl = rstd * LOG2E;
    xf[32] = aug_tok(mean, 0.f, hk);
    if (hk == 0) *(float*)(priv + tok * 4) = rstd;
    f32x16 acck = {}, accv = {}, p = {};
    u32x4 phk[4];
    Softmax sm;
    check_x(cx, X2B, xf, 0);
    stream<66, 4>([&](int S) LAMBDA { return *(const u32x4*)(cx.lds + (S < 32 ? 32 + S : S == 32 ? 97 : S < 65 ? 64 + S - 33 : 98) * 1024 + cx.vo); },
                  [&](int S, const u32x4& a) LAMBDA {
                    if (S < 33) acck = MFMA(a, xf[S], acck); else accv = MFMA(xf[S - 33], a, accv);
                    if (S == 34) { u32x4 b0, b1; pack16(acck, b0, b1); p = MFMA(aphi[0], b0, p); p = MFMA(aphi[1], b1, p); }
                  },
                  [&](int S) LAMBDA {
                    if (S < 28 && (S & 3) == 3) check_x(cx, X2B, xf, (S + 1) >> 2);
                    if (S >= 36) sm.step(S - 36, p, scl, phk);
                  });
    STAMP(17);
    kv_tail<INBOX>(phk, accv, priv, sc + 1024, cx.rs, kvb, g, tid, lane, w);
    STAMP(18);
#pragma unroll
    for (int s = 0; s < 16; s++) wof[s] = ldwk(cx.rs, cx.vo, WT_O + (cx.cb * 4 + w) * 16384, s);
    f32x16 accq = {};
    stream<33, 4>([&](int S) LAMBDA { return *(const u32x4*)(cx.lds + (S < 32 ? S : 96) * 1024 + cx.vo); },
                  [&](int S, const u32x4& a) LAMBDA { accq = MFMA(a, xf[S], accq); }, [&](int S) LAMBDA {});
    unpark(cx, xr);
    lds_barrier();
    glds_copy<7>(cx, WT_1 + c * 114688);
    {
      u32x4 b0, b1; pack16(accq, b0, b1);
      f32x16 pq = {}; pq = MFMA(aphi[0], b0, pq); pq = MFMA(aphi[1], b1, pq);
      Softmax sq;
#pragma unroll
      for (int i = 0; i < 29; i++) sq.step(i, pq, scl, phq);
    }
    STAMP(19);
    SPIN_BEGIN
      unsigned m = 0;
#pragma unroll
      for (int k = 0; k < 16; k++) pa[k] = ldck(cx.rs, cx.vo, kvb + w * 16384, k);
#pragma unroll
      for (int k = 0; k < 4; k++) pz[k] = ldck(cx.rs, cx.vo, kvb + 65536, k);
#pragma unroll
      for (int k = 0; k < 16; k++) m = chk4(m, pa[k]);
#pragma unroll
      for (int k = 0; k < 4; k++) m = chk4(m, pz[k]);
    SPIN_END(m)
  }
  STAMP(7 + 13 * L);
  {
    f32x4 s = __builtin_bit_cast(f32x4, pa[0]);
#pragma unroll
    for (int k = 1; k < 16; k++) s += __builtin_bit_cast(f32x4, pa[k]);
    f32x4 z = __builtin_bit_cast(f32x4, pz[0]);
#pragma unroll
    for (int k = 1; k < 4; k++) z += __builtin_bit_cast(f32x4, pz[k]);
#pragma unroll
    for (int e = 0; e < 4; e++) { z[e] += __shfl_xor(z[e], 16); z[e] = xhalf_sum(z[e]); }
    char* img = sc + 2048;
    { u32x2 v = {pk2(s[0], s[1]), pk2(s[2], s[3])}; *(u32x2*)(img + tid * 8) = v; }
    if (tid < 16) { u32x2 v = {pk2(z[0], z[1]), pk2(z[2], z[3])}; *(u32x2*)(img + 2048 + tid * 8) = v; }
    if (tid == 16) { u32x4 zz = {0, 0, 0, 0}; *(u32x4*)(img + 2176) = zz; }
    lds_barrier();
    const int i = tok, hp = (i >> 2) & 1, d = (i & 3) + 4 * (i >> 3);
    const bool on = (hp == hk);
    f32x16 num = {}, den = {};
    u32x4 a[4], az[4];
#pragma unroll
    for (int s4 = 0; s4 < 4; s4++) {
      int pm = s4 >> 1, h2 = s4 & 1;
      az[s4] = *(const u32x4*)(img + (on ? 2048 + ((pm * 2 + hp) * 2 + h2) * 16 : 2176));
    }
#pragma unroll
    for (int s4 = 0; s4 < 4; s4++) {
      int pm = s4 >> 1, h2 = s4 & 1;
      a[s4] = *(const u32x4*)(img + (on ? (pm * 64 + h2 * 32 + 16 * hp + d) * 16 : 2176));
    }
    SB();
#pragma unroll
    for (int s4 = 0; s4 < 4; s4++) den = MFMA(az[s4], phq[s4], den);
#pragma unroll
    for (int s4 = 0; s4 < 4; s4++) num = MFMA(a[s4], phq[s4], num);
    float inv = __builtin_amdgcn_rcpf(den[0] + 1e-6f);
    float ov[16];
#pragma unroll
    for (int r = 0; r < 16; r++) ov[r] = num[r] * inv;
    u32x4 f0, f1; pack16(ov, f0, f1);
    stp(cx.rs, cx.vo, OB + L * 0x200000 + cx.ooff, f0);
    stp(cx.rs, cx.vo + 2048, OB + L * 0x200000 + cx.ooff, f1);
    STAMP(8 + 13 * L);
  }
  {
    fill_ff(cx, HB + L * HB_L + cx.hoff, 7, 8192);
    u32x4 of[16];
    const u16* b2p = b2 + 64 * cx.cb + 32 * (w & 1) + 16 * hk;
    asm volatile("" : "+v"(b2p));
    const u32x4 ba = *(const u32x4*)b2p, bb2 = *(const u32x4*)(b2p + 8);
    SPIN_BEGIN
      unsigned m = 0;
#pragma unroll
      for (int k = 0; k < 16; k++) of[k] = ldck(cx.rs, cx.vo, OB + L * 0x200000 + ((2 * g + cx.th) * 4 + w) * 16384, k);
#pragma unroll
      for (int k = 0; k < 16; k++) m = chk4(m, of[k]);
    SPIN_END(m)
    STAMP(9 + 13 * L);
    lds_barrier();
    f32x16 acc[4] = {};
#pragma unroll
    for (int j = 3; j >= 0; j--) {
#pragma unroll
      for (int s = 0; s < 8; s++) {
        acc[j] = MFMA(wof[2 * s + (j & 1)], of[2 * s + (j >> 1)], acc[j]);
        if (s == 2 && j < 3) red_send(cx, acc[j + 1], j + 1);
        SB();
      }
    }
    red_recv(cx, acc[0], xr);
    publish_x(cx, xr, X1B + L * 0x200000, ST1 + L * 0x40000);
#pragma unroll
    for (int t = 0; t < 4; t++) {
      xr[2 * t] += bflo(ba[t]); xr[2 * t + 1] += bfhi(ba[t]); xr[8 + 2 * t] += bflo(bb2[t]); xr[9 + 2 * t] += bfhi(bb2[t]);
    }
    park(cx, xr);
    STAMP(10 + 13 * L);
  }
  const int ahalf = 3072 + (hk * 16 + (lane & 15)) * 16;
  if (L == 0) {
    init_x_out(cx, X2B, ST2);
    init_x_out(cx, X1B + 0x200000, ST1 + 0x40000);
    fill_ff(cx, OB + 0x200000 + cx.ooff, 2, 2048);
    fill_ff(cx, KVP + KVP_L + c * KVP_C + (w * 16 + g) * 1024, 1, 1024);
    if (w == 0 && lane < 16) { const u32x4 ff = {~0u, ~0u, ~0u, ~0u}; stc(cx.rs, cx.vo, KVP + KVP_L + c * KVP_C + 65536 + g * 256, ff); }
    vm_drain(); lds_barrier();
    const u32x4 ones = {0x3f803f80u, 0x3f803f80u, 0x3f803f80u, 0x3f803f80u};
    f32x16 cs = {};
    const int aoff = w < 3 ? w * 1024 + cx.vo : ahalf;
    stream<32, 4>([&](int S) LAMBDA { return *(const u32x4*)(cx.lds + S * 3584 + aoff); },
                  [&](int S, const u32x4& a) LAMBDA { cs = MFMA(ones, a, cs); },
                  [&](int S) LAMBDA { if (S < 28) { w2f[2 * S] = ldwk(cx.rs, cx.vo, WT_2 + (cx.cb * 4 + w) * 57344, 2 * S);
                                             w2f[2 * S + 1] = ldwk(cx.rs, cx.vo, WT_2 + (cx.cb * 4 + w) * 57344, 2 * S + 1); } });
    char* tmp = sc + 3 * INBOX + 4096;
    if (hk == 0) *(unsigned*)(tmp + (tok * 4 + w) * 4) = cs_pack(cs[0]);
    lds_barrier();
    u32x4 t = *(const u32x4*)(tmp + tok * 16);
    csp[0] = t[0]; csp[1] = t[1]; csp[2] = t[2]; csp[3] = t[3];
  } else {
    vm_drain(); lds_barrier();
#pragma unroll
    for (int k = 48; k < 56; k++) w2f[k] = ldwk(cx.rs, cx.vo, WT_2 + (cx.cb * 4 + w) * 57344, k);
    SB();
  }
  {
    u32x4 xf[33]; float mean, var;
    const int xbuf = X1B + L * 0x200000;
    acquire_x(cx, xbuf, ST1 + L * 0x40000, xf, mean, var);
    STAMP(11 + 13 * L);
    const float v1 = var + 1e-5f;
    const float r1 = __builtin_amdgcn_rsqf(v1);
    const float v2 = var * r1 * r1 + 1e-5f;
    const float A = r1 * __builtin_amdgcn_rsqf(v2);
    const float iA = __builtin_amdgcn_rcpf(A);
    xf[32] = aug_tok(mean, iA, hk);
    const float k0 = -2.f * LOG2E * 0.7978845608028654f, k1 = k0 * 0.044715f;
    const float c0 = k0 * A, c1 = k1 * A * A * A;
    const int hbase = HB + L * HB_L + cx.hoff;
    f32x16 acc[2];
    float ge[16]; u32x4 hf0, hf1;
    auto epi = [&](int b, int i) LAMBDA {
      const f32x16& a = acc[b & 1];
      const int r = i >> 1;
      if (b == 3 && r >= 8) return;
      if (!(i & 1)) { float t = a[r] * a[r]; ge[r] = __builtin_amdgcn_exp2f(a[r] * fmaf(t, c1, c0)); }
      else {
        ge[r] = a[r] * __builtin_amdgcn_rcpf(fmaf(ge[r], iA, iA));
        if (r & 1) { unsigned u = pk2(ge[r - 1], ge[r]); if (r < 8) hf0[r >> 1] = u; else hf1[(r - 8) >> 1] = u; }
        if (r == 7) stp(cx.rs, cx.vo, hbase + 2 * b * 8192, hf0);
        if (r == 15) stp(cx.rs, cx.vo, hbase + (2 * b + 1) * 8192, hf1);
      }
    };
    check_x(cx, xbuf, xf, 0);
    stream<132, 4>([&](int S) LAMBDA {
                     const int b = S / 33, s = S % 33;
                     if (s == 32) return aug_w(csp[b], b & 1 ? b1p[b >> 1] >> 16 : b1p[b >> 1]);
                     return *(const u32x4*)(cx.lds + s * 3584 + (b < 3 ? b * 1024 + cx.vo : ahalf));
                   },
                   [&](int S, const u32x4& a) LAMBDA {
                     const int b = S / 33, s = S % 33;
                     if (s == 0) { f32x16 zero = {}; acc[b & 1] = MFMA(a, xf[s], zero); } else acc[b & 1] = MFMA(a, xf[s], acc[b & 1]);
                   },
                   [&](int S) LAMBDA {
                     const int b = S / 33, s = S % 33;
                     if (b == 0) { if (s < 28 && (s & 3) == 3) check_x(cx, xbuf, xf, (s + 1) >> 2); }
                     else if (s >= 1) epi(b - 1, s - 1);
                   });
#pragma unroll
    for (int i = 0; i < 16; i++) epi(3, i);
    STAMP(12 + 13 * L);
    unpark(cx, xr);
    lds_barrier();
    if (L == 0) {
      glds_copy<6>(cx, WT_QKV + c * WQ_C);
      if (w < 3) glds1k(cx.vo, cx.ws + WT_QKV + c * WQ_C + 98304 + w * 1024, (unsigned)(size_t)cx.lds + 98304 + w * 1024);
    }
  }
  {
    const int hb = HB + L * HB_L + ((2 * g + cx.th) * 4 + w) * 57344;
    f32x16 acc[4] = {};
    u32x4 hf[3][8];
    auto hload = [&](int s7) LAMBDA {
#pragma unroll
      for (int k = 0; k < 8; k++) hf[s7 % 3][k] = ldck(cx.rs, cx.vo, hb, s7 * 8 + k);
    };
    hload(0); hload(1);
    SB();
#pragma unroll
    for (int s7 = 0; s7 < 7; s7++) {
      if (s7 + 2 < 7) hload(s7 + 2);
      SB();
      unsigned m = 0;
#pragma unroll
      for (int k = 0; k < 8; k++) m = chk4(m, hf[s7 % 3][k]);
      if (!all_valid(m)) {
        SPIN_BEGIN
          m = 0;
          hload(s7);
#pragma unroll
          for (int k = 0; k < 8; k++) m = chk4(m, hf[s7 % 3][k]);
        SPIN_END(m)
      }
      SB();
#pragma unroll
      for (int j = 3; j >= 0; j--) {
#pragma unroll
        for (int p = 0; p < 4; p++) {
          acc[j] = MFMA(w2f[2 * (4 * s7 + p) + (j & 1)], hf[s7 % 3][2 * p + (j >> 1)], acc[j]);
          if (s7 == 6 && p == 1 && j < 3) red_send(cx, acc[j + 1], j + 1);
        }
        if (s7 == 6) SB();
      }
      SB();
    }
    STAMP(13 + 13 * L);
    red_recv(cx, acc[0], xr);
    if (L == 0) { publish_x(cx, xr, X2B, ST2); park(cx, xr); }
    else {
      u32x4 f0, f1; pack16(xr, f0, f1);
      u16* dst = out + (size_t)(128 * g + 64 * cx.th + 32 * (w >> 1) + tok) * 512 + 64 * cx.cb + 32 * (w & 1) + 16 * hk;
      *(u32x4*)dst = f0; *(u32x4*)(dst + 8) = f1;
    }
    STAMP(14 + 13 * L);
  }
}

__global__ __launch_bounds__(256, 1) void k2(const u16* __restrict__ x, const u16* __restrict__ wphi, const u16* __restrict__ b1,
                                             const u16* __restrict__ b2, char* ws, u16* __restrict__ out) {
  __shared__ __attribute__((aligned(16))) char lds[LDS_TOT];
  Ctx cx;
  cx.ws = ws; cx.lds = lds; cx.tid = threadIdx.x; cx.lane = cx.tid & 63; cx.w = __builtin_amdgcn_readfirstlane(cx.tid >> 6);
  cx.tok = cx.lane & 31; cx.hk = cx.lane >> 5; cx.vo = cx.lane * 16; cx.ok = true;
  map_block(blockIdx.x, cx.g, cx.c);
  cx.cb = 4 * (cx.c >> 3) + (cx.c & 3); cx.th = (cx.c >> 2) & 1;
  cx.rs = __builtin_amdgcn_make_buffer_rsrc(ws, 0, WS_USED, 0x00020000);
  {
    const int tp = 2 * cx.g + (cx.w >> 1), kq = cx.c >> 2, tx = (cx.w & 1) ^ (kq >> 1);
    cx.hoff = ((tp * 4 + kq) * 56 + (cx.c & 3) * 2 + tx) * 1024;
    cx.ooff = ((tp * 4 + kq) * 16 + (cx.c & 3) * 4 + tx) * 1024;
  }
  const int lane = cx.lane, w = cx.w;
  STAMP(6);
  float xr[16]; unsigned csp[4], b1p[2];
  {
    const int col = 64 * cx.cb + 32 * (w & 1) + 16 * cx.hk;
    const u16* src = x + (size_t)(128 * cx.g + 64 * cx.th + 32 * (w >> 1) + cx.tok) * 512 + col;
    u32x4 a = *(const u32x4*)src, b = *(const u32x4*)(src + 8);
#pragma unroll
    for (int t = 0; t < 4; t++) {
      xr[2 * t] = bflo(a[t]); xr[2 * t + 1] = bfhi(a[t]); xr[8 + 2 * t] = bflo(b[t]); xr[9 + 2 * t] = bfhi(b[t]);
    }
    const int i = cx.tok, pi = 16 * ((i >> 2) & 1) + (i & 3) + 4 * (i >> 3), ph = 8 * ((i >> 2) & 1) + (i & 3) + 4 * ((i >> 3) & 1);
    const u16* bp = b1 + 112 * cx.c;
    b1p[0] = bp[pi] | ((unsigned)bp[32 + pi] << 16);
    b1p[1] = bp[64 + pi] | ((unsigned)bp[96 + ph] << 16);
  }
  u32x4 w2f[56];
  layer<0>(cx, wphi, out, w2f, xr, csp, b1p, b2);
  asm volatile("" : "+v"(cx.tid), "+v"(cx.lane), "+v"(cx.tok), "+v"(cx.hk), "+v"(cx.vo));
  asm volatile("" : "+v"(csp[0]), "+v"(csp[1]), "+v"(csp[2]), "+v"(csp[3]), "+v"(b1p[0]), "+v"(b1p[1]));
  layer<1>(cx, wphi, out, w2f, xr, csp, b1p, b2);
}

extern "C" void kernel_launch(void* const* d_in, const int* in_sizes, int n_in, void* d_out, int out_size, void* d_ws, size_t ws_size,
                              hipStream_t stream) {
  const u16* x = (const u16*)d_in[0]; const u16* wqkv = (const u16*)d_in[1]; const u16* wphi = (const u16*)d_in[2];
  const u16* wo = (const u16*)d_in[3]; const u16* w1 = (const u16*)d_in[5]; const u16* b1 = (const u16*)d_in[6];
  const u16* w2 = (const u16*)d_in[7]; const u16* b2 = (const u16*)d_in[8];
  k1<<<256, 256, 0, stream>>>(x, wqkv, wphi, wo, w1, w2, (char*)d_ws);
  k2<<<256, 256, 0, stream>>>(x, wphi, b1, b2, (char*)d_ws, (u16*)d_out);
}
```

```cpp
#include <hip/hip_runtime.h>
#include <stdint.h>

typedef unsigned short u16;
typedef __attribute__((ext_vector_type(8))) __bf16 bf16x8;
typedef __attribute__((ext_vector_type(2))) __bf16 bf16x2;
typedef __attribute__((ext_vector_type(16))) float f32x16;
typedef __attribute__((ext_vector_type(4))) float f32x4;
typedef __attribute__((ext_vector_type(2))) float f32x2;
typedef __attribute__((ext_vector_type(4))) unsigned u32x4;
typedef __attribute__((ext_vector_type(2))) unsigned u32x2;
typedef __attribute__((ext_vector_type(4))) short s16x4;
typedef __amdgpu_buffer_rsrc_t rsrc_t;

#define DEVI __device__ __forceinline__
#define MFMA(a, b, c) __builtin_amdgcn_mfma_f32_32x32x16_bf16(__builtin_bit_cast(bf16x8, a), __builtin_bit_cast(bf16x8, b), c, 0, 0, 0)
#define SB() __builtin_amdgcn_sched_barrier(0)

constexpr int WT_QKV = 0x0;
constexpr int WQ_C   = 101376;
constexpr int WT_O   = 0x190000;
constexpr int WT_1   = 0x210000;
constexpr int WT_2   = 0x3D0000;
constexpr int QN     = 0x600000;
constexpr int KVP    = 0xA00000;
constexpr int KVP_C  = 69632;
constexpr int KVP_L  = 0x120000;
constexpr int OB     = 0xD00000;
constexpr int X1B    = 0x1100000;
constexpr int X2B    = 0x1500000;
constexpr int ST1    = 0x1700000;
constexpr int ST2    = 0x1780000;
constexpr int HB     = 0x1800000;
constexpr int HB_L   = 0x700000;
constexpr int WS_USED = 0x2800000;
#define STAMP_OFF 0x2800000
#define STAMP_N 32

constexpr int LDS_W = 114688;
constexpr int LDS_TOT = 163840;
constexpr int INBOX = 12288;

constexpr float LOG2E = 1.4426950408889634f;
constexpr int SPIN_MAX = 1 << 16;

DEVI unsigned pk2(float a, float b) { f32x2 v = {a, b}; return __builtin_bit_cast(unsigned, __builtin_convertvector(v, bf16x2)); }
DEVI float bflo(unsigned u) { return __uint_as_float(u << 16); }
DEVI float bfhi(unsigned u) { return __uint_as_float(u & 0xffff0000u); }
DEVI float bf2f(u16 b) { return __uint_as_float(((unsigned)b) << 16); }
DEVI void lds_barrier() { asm volatile("s_waitcnt lgkmcnt(0)\n\ts_barrier" ::: "memory"); }
DEVI void vm_drain() { asm volatile("s_waitcnt vmcnt(0)" ::: "memory"); }
DEVI void glds1k(unsigned voff, const void* sbase, unsigned lds_base) {
  asm volatile("s_mov_b32 m0, %2\n\ts_nop 0\n\tglobal_load_lds_dwordx4 %0, %1" :: "v"(voff), "s"(sbase), "s"(lds_base) : "memory", "m0");
}
DEVI void glds4k(unsigned voff, const char* sbase, unsigned lds_base) {
  asm volatile("s_mov_b32 m0, %2\n\ts_nop 0\n\t"
               "global_load_lds_dwordx4 %0, %1\n\t"
               "global_load_lds_dwordx4 %0, %1 offset:1024\n\t"
               "global_load_lds_dwordx4 %0, %1 offset:2048\n\t"
               "global_load_lds_dwordx4 %0, %1 offset:3072"
               :: "v"(voff), "s"(sbase), "s"(lds_base) : "memory", "m0");
}
DEVI float xhalf_sum(float v) {
  auto t = __builtin_amdgcn_permlane32_swap(__float_as_uint(v), __float_as_uint(v), false, false);
  return __uint_as_float(t[0]) + __uint_as_float(t[1]);
}
DEVI u32x4 ldw(rsrc_t rs, int vo, int so) { return __builtin_amdgcn_raw_buffer_load_b128(rs, vo, so, 0); }
DEVI u32x4 ldc(rsrc_t rs, int vo, int so) { return __builtin_amdgcn_raw_buffer_load_b128(rs, vo, so, 16); }
DEVI u32x2 ldc2(rsrc_t rs, int vo, int so) { return __builtin_amdgcn_raw_buffer_load_b64(rs, vo, so, 16); }
DEVI void stc(rsrc_t rs, int vo, int so, u32x4 v) { __builtin_amdgcn_raw_buffer_store_b128(v, rs, vo, so, 16); }
DEVI void stc2(rsrc_t rs, int vo, int so, u32x2 v) { __builtin_amdgcn_raw_buffer_store_b64(v, rs, vo, so, 16); }
DEVI void stp(rsrc_t rs, int vo, int so, u32x4 v) {
  __builtin_amdgcn_raw_buffer_store_b128(v, rs, vo, so, 16);
}
DEVI void stp2(rsrc_t rs, int vo, int so, u32x2 v) {
  __builtin_amdgcn_raw_buffer_store_b64(v, rs, vo, so, 16);
}
DEVI u32x4 ldwk(rsrc_t rs, int vo, int so, int kb) { return ldw(rs, vo + (kb & 3) * 1024, so + (kb >> 2) * 4096); }
DEVI u32x4 ldck(rsrc_t rs, int vo, int so, int kb) { return ldc(rs, vo + (kb & 3) * 1024, so + (kb >> 2) * 4096); }
DEVI float selm(unsigned m, float a, float b) { return __uint_as_float((__float_as_uint(a) & m) | (__float_as_uint(b) & ~m)); }
DEVI unsigned umax3(unsigned a, unsigned b, unsigned c) { return max(max(a, b), c); }
DEVI unsigned chk4(unsigned m, u32x4 f) { return umax3(umax3(m, f[0], f[1]), f[2], f[3]); }
DEVI bool all_valid(unsigned m) { return __builtin_amdgcn_ballot_w64(m == 0xffffffffu) == 0; }
DEVI float fmax3(float a, float b, float c) { return fmaxf(fmaxf(a, b), c); }
DEVI float fmin3(float a, float b, float c) { return fminf(fminf(a, b), c); }

#define STAMP(i)
#define CSTAMP(i)

DEVI void map_block(int b, int& g, int& c) {
  int x = b & 7, j = b >> 3;
  g = 4 * (x >> 1) + (j >> 3); c = 8 * (x & 1) + (j & 7);
}

template <class T>
DEVI void pack16(const T& v, u32x4& a, u32x4& b) {
#pragma unroll
  for (int i = 0; i < 4; i++) { a[i] = pk2(v[2 * i], v[2 * i + 1]); b[i] = pk2(v[8 + 2 * i], v[9 + 2 * i]); }
}

template <int... I> struct iseq {};
template <int N, int... I> struct mkseq : mkseq<N - 1, N - 1, I...> {};
template <int... I> struct mkseq<0, I...> { typedef iseq<I...> type; };
template <class F, int... I> DEVI void sfor_impl(F& f, iseq<I...>) { (f(I), ...); }
template <int N, class F> DEVI void sfor(F f) { sfor_impl(f, typename mkseq<N>::type()); }
#define LAMBDA __attribute__((always_inline))
template <int NS, int D, class RD, class MM, class FILL>
DEVI void stream(RD rd, MM mm, FILL fill) {
  u32x4 ring[D];
#pragma unroll
  for (int i = 0; i < D; i++) ring[i] = rd(i);
  SB();
  sfor<NS>([&](int S) LAMBDA {
    mm(S, ring[S % D]);
    if (S + D < NS) ring[S % D] = rd(S + D);
    fill(S);
    SB();
  });
}

struct Softmax {
  float mx, mn, sp, sn, ep[16], en[16];
  DEVI void step(int i, const f32x16& p, float sc, u32x4 (&ph)[4]) {
    if (i == 0) { mx = fmax3(p[0], p[1], p[2]); mn = fmin3(p[0], p[1], p[2]); mx = fmax3(mx, p[3], p[4]); mn = fmin3(mn, p[3], p[4]); }
    else if (i < 3) { int b = 5 + 4 * (i - 1); mx = fmax3(mx, p[b], p[b + 1]); mn = fmin3(mn, p[b], p[b + 1]); mx = fmax3(mx, p[b + 2], p[b + 3]); mn = fmin3(mn, p[b + 2], p[b + 3]); }
    else if (i == 3) { mx = fmax3(mx, p[13], p[14]); mn = fmin3(mn, p[13], p[14]); mx = fmaxf(mx, p[15]) * -sc; mn = fminf(mn, p[15]) * sc; }
    else if (i < 20) {
      int r = i - 4;
      ep[r] = __builtin_amdgcn_exp2f(fmaf(p[r], sc, mx)); en[r] = __builtin_amdgcn_exp2f(fmaf(p[r], -sc, mn));
      if (r == 1) { sp = ep[0] + ep[1]; sn = en[0] + en[1]; } else if (r > 1) { sp += ep[r]; sn += en[r]; }
    } else if (i == 20) { sp = __builtin_amdgcn_rcpf(sp); sn = __builtin_amdgcn_rcpf(sn); }
    else if (i < 29) {
      int t = i - 21;
      ph[t >> 2][t & 3] = pk2(ep[2 * t] * sp, ep[2 * t + 1] * sp);
      ph[2 + (t >> 2)][t & 3] = pk2(en[2 * t] * sn, en[2 * t + 1] * sn);
    }
  }
};

DEVI u32x4 tr_read(const char* p) {
  typedef __attribute__((address_space(3))) s16x4* lp;
  s16x4 a = __builtin_amdgcn_ds_read_tr16_b64_v4i16((lp)(p));
  s16x4 b = __builtin_amdgcn_ds_read_tr16_b64_v4i16((lp)(p + 256));
  u32x2 ua = __builtin_bit_cast(u32x2, a), ub = __builtin_bit_cast(u32x2, b);
  u32x4 r = {ua[0], ua[1], ub[0], ub[1]};
  return r;
}

DEVI void load_aphi(const u16* wphi, int c, int lane, u32x4 (&aphi)[2]) {
  int i = lane & 31, hk = lane >> 5;
  int hp = (i >> 2) & 1, f = (i & 3) + 4 * (i >> 3);
  const u16* src = wphi + (2 * c + hk) * 256 + f;
#pragma unroll
  for (int s = 0; s < 2; s++)
#pragma unroll
    for (int t = 0; t < 4; t++) {
      unsigned lo = src[(8 * s + 2 * t) * 16], hi = src[(8 * s + 2 * t + 1) * 16];
      aphi[s][t] = (hp == hk) ? (lo | (hi << 16)) : 0u;
    }
}

DEVI u32x4 aug_tok(float mean, float ia, int hk) {
  unsigned mh = pk2(-mean, ia);
  unsigned ml = pk2(-mean - bflo(mh), ia - bfhi(mh));
  u32x4 t;
  t[0] = __builtin_amdgcn_perm(ml, mh, 0x05040100u);
  t[1] = mh;
  t[2] = ml >> 16;
  t[3] = 0;
  if (hk) { t[0] = 0; t[1] = 0; t[2] = 0; }
  return t;
}
DEVI unsigned cs_pack(float cs) { unsigned h = pk2(cs, 0.f); return pk2(cs, cs - bflo(h)); }
DEVI u32x4 aug_w(unsigned csp, unsigned b) {
  u32x4 t;
  t[0] = __builtin_amdgcn_perm(csp, csp, 0x01000100u);
  t[1] = __builtin_amdgcn_perm(b, csp, 0x05040302u);
  t[2] = b & 0xffffu;
  t[3] = 0;
  return t;
}

template <int KVS>
DEVI void kv_tail(const u32x4 (&phk)[4], const f32x16& accv, const char* priv, char* kvs, rsrc_t rs, int kvoff, int g, int tid, int lane, int w) {
  const int tok = lane & 31, hk = lane >> 5;
  int n = tok, Hn = n >> 4, fn = 8 * ((n >> 2) & 1) + (n & 3) + 4 * ((n >> 3) & 1);
  u32x4 sel[2];
#pragma unroll
  for (int s = 0; s < 2; s++)
#pragma unroll
    for (int t = 0; t < 4; t++) {
      int f0 = 8 * s + 2 * t;
      unsigned v = (fn == f0) ? 0x3f80u : ((fn == f0 + 1) ? 0x3f800000u : 0u);
      sel[s][t] = (Hn == hk) ? v : 0u;
    }
  f32x16 t0 = {}, t1 = {};
  t0 = MFMA(phk[0], sel[0], t0); t0 = MFMA(phk[1], sel[1], t0);
  t1 = MFMA(phk[2], sel[0], t1); t1 = MFMA(phk[3], sel[1], t1);
  u32x4 vb0, vb1;
  {
    float vn[16];
#pragma unroll
    for (int m = 0; m < 4; m++) {
      f32x4 r4 = *(const f32x4*)(priv + (8 * m + 4 * hk) * 4);
#pragma unroll
      for (int e = 0; e < 4; e++) vn[4 * m + e] = accv[4 * m + e] * r4[e];
    }
    pack16(vn, vb0, vb1);
  }
  const u32x4 ones = {0x3f803f80u, 0x3f803f80u, 0x3f803f80u, 0x3f803f80u};
  unsigned hm = Hn ? ~0u : 0u;
  asm volatile("" : "+v"(hm));
  char* dst = kvs + w * KVS;
#pragma unroll
  for (int pm = 0; pm < 2; pm++) {
    u32x4 a0, a1; pack16(pm ? t1 : t0, a0, a1);
    f32x16 kv = {}; kv = MFMA(a0, vb0, kv); kv = MFMA(a1, vb1, kv);
    f32x16 z = {}; z = MFMA(a0, ones, z); z = MFMA(a1, ones, z);
    f32x4 o0, o1, z0, z1;
#pragma unroll
    for (int e = 0; e < 4; e++) {
      o0[e] = selm(hm, kv[8 + e], kv[e]); o1[e] = selm(hm, kv[12 + e], kv[4 + e]);
      z0[e] = selm(hm, z[8 + e], z[e]);   z1[e] = selm(hm, z[12 + e], z[4 + e]);
    }
    *(f32x4*)(dst + pm * 2048 + lane * 32) = o0; *(f32x4*)(dst + pm * 2048 + lane * 32 + 16) = o1;
    if ((n & 15) == 0) {
      char* dz = dst + 4096 + ((pm * 2 + Hn) * 2 + hk) * 32;
      *(f32x4*)dz = z0; *(f32x4*)(dz + 16) = z1;
    }
  }
  lds_barrier();
  {
    f32x4 s = *(const f32x4*)(kvs + tid * 16);
#pragma unroll
    for (int k = 1; k < 4; k++) s += *(const f32x4*)(kvs + k * KVS + tid * 16);
    stc(rs, lane * 16, kvoff + (w * 16 + g) * 1024, __builtin_bit_cast(u32x4, s));
    if (tid < 16) {
      f32x4 z = *(const f32x4*)(kvs + 4096 + tid * 16);
#pragma unroll
      for (int k = 1; k < 4; k++) z += *(const f32x4*)(kvs + k * KVS + 4096 + tid * 16);
      stc(rs, tid * 16, kvoff + 65536 + g * 256, __builtin_bit_cast(u32x4, z));
    }
  }
}

__global__ __launch_bounds__(256, 1) void k1(const u16* __restrict__ x, const u16* __restrict__ wqkv, const u16* __restrict__ wphi,
                                             const u16* __restrict__ wo, const u16* __restrict__ w1, const u16* __restrict__ w2, char* ws) {
  __shared__ __attribute__((aligned(16))) char lds[LDS_TOT];
  const int tid = threadIdx.x, lane = tid & 63, w = __builtin_amdgcn_readfirstlane(tid >> 6);
  const int tok = lane & 31, hk = lane >> 5;
  int g, c; map_block(blockIdx.x, g, c);
  rsrc_t rs = __builtin_amdgcn_make_buffer_rsrc(ws, 0, WS_USED, 0x00020000);
  const unsigned lbase = (unsigned)(size_t)lds;
  char* area = lds + w * 32768;
  char* priv = area + 16384;
  char* kvs = lds + 20480;
  char* csp = area + 17408;
  STAMP(0);
  {
    const char* xb = (const char*)(x + (size_t)(128 * g + 32 * w) * 512);
#pragma unroll
    for (int r = 0; r < 32; r++) glds1k((lane ^ (r & 15)) * 16, xb + r * 1024, lbase + w * 32768 + r * 1024);
  }
  const unsigned wvo = (lane >> 2) * 3072 + (lane & 3) * 16;
  const char* wb = (const char*)(wqkv + (size_t)(128 * w) * 1536 + 32 * c);
#pragma unroll
  for (int m = 0; m < 8; m++) glds1k(wvo, wb + m * 16 * 3072, lbase + 131072 + w * 8192 + m * 1024);
  unsigned touch = 0;
#pragma unroll
  for (int m = 0; m < 4; m++) touch |= *(const unsigned*)(wb + (size_t)((m & 1) * 64 + lane) * 3072 + 1024 + (m >> 1) * 1024);
  u32x4 aphi[2]; load_aphi(wphi, c, lane, aphi);
  u32x4 rt[8]; int rt_dst = 0; bool rt_nat = false;
  {
    int id = blockIdx.x * 176 + tid;
    if (tid < 176) {
      const u16* src; int N;
      if (id < 12288) {
        int n8 = id % 192, k8 = id / 192; N = 1536; src = wqkv + (size_t)k8 * 8 * N + n8 * 8;
        int blk = n8 >> 6, cc = (n8 & 63) >> 2, a = n8 & 3;
        int step = 2 * (k8 >> 2) + (k8 & 1), h2 = (k8 >> 1) & 1;
        rt_nat = (blk == 2);
        int i0 = rt_nat ? 8 * a : 16 * (a & 1) + 4 * (a >> 1);
        rt_dst = WT_QKV + cc * WQ_C + (blk * 32 + step) * 1024 + (h2 * 32 + i0) * 16;
      } else if (id < 16384) {
        int q = id - 12288; int n8 = q & 63, k8 = q >> 6; N = 512; src = wo + (size_t)k8 * 8 * N + n8 * 8;
        int cb = n8 >> 3, bb = (n8 >> 2) & 1, a = n8 & 3;
        int step = 2 * (k8 >> 2) + (k8 & 1), h2 = (k8 >> 1) & 1;
        int i0 = 16 * (a & 1) + 4 * (a >> 1);
        int kq = step >> 3;
        rt_dst = WT_O + (((cb * 4 + kq) * 8 + (step & 7)) * 2 + (bb ^ (kq & 1))) * 1024 + (h2 * 32 + i0) * 16;
      } else if (id < 30720) {
        int q = id - 16384; int n8 = q % 224, k8 = q / 224; N = 1792; src = w1 + (size_t)k8 * 8 * N + n8 * 8;
        int cc = n8 / 14, l8 = n8 % 14;
        int step = 2 * (k8 >> 2) + (k8 & 1), h2 = (k8 >> 1) & 1;
        int base = WT_1 + (cc * 32 + step) * 3584;
        if (l8 < 12) { int bb = l8 >> 2, a = l8 & 3; int i0 = 16 * (a & 1) + 4 * (a >> 1); rt_dst = base + bb * 1024 + (h2 * 32 + i0) * 16; }
        else { int i0 = 4 * (l8 - 12); rt_dst = base + 3072 + (h2 * 16 + i0) * 16; }
      } else {
        int q = id - 30720; int n8 = q & 63, k8 = q >> 6; N = 512; src = w2 + (size_t)k8 * 8 * N + n8 * 8;
        int cb = n8 >> 3, bb = (n8 >> 2) & 1, a = n8 & 3;
        int cc = k8 / 14, l8 = k8 % 14;
        int s7 = l8 < 12 ? 2 * (l8 >> 2) + (l8 & 1) : 6, h2 = l8 < 12 ? (l8 >> 1) & 1 : l8 - 12;
        int i0 = 16 * (a & 1) + 4 * (a >> 1);
        int kq = cc >> 2;
        rt_dst = WT_2 + (((cb * 4 + kq) * 28 + s7 * 4 + (cc & 3)) * 2 + (bb ^ (kq & 1))) * 1024 + (h2 * 32 + i0) * 16;
      }
#pragma unroll
      for (int j = 0; j < 8; j++) rt[j] = *(const u32x4*)(src + (size_t)j * N);
    }
  }
  vm_drain();
  asm volatile("" :: "v"(touch));
  STAMP(1);
  u32x4 xf[33];
  {
#pragma unroll
    for (int s = 0; s < 32; s++) {
      int q = 4 * (s >> 1) + (s & 1);
      xf[s] = *(const u32x4*)(area + tok * 1024 + (((q & 15) + 2 * hk) ^ (tok & 15)) * 16 + (q >> 4) * 256);
    }
    asm volatile("s_waitcnt lgkmcnt(0)" ::: "memory");
    SB();
  }
#pragma unroll
  for (int m = 0; m < 16; m++) glds1k(wvo, wb + (m & 7) * 16 * 3072 + 1024 + (m >> 3) * 1024, lbase + w * 32768 + m * 1024);
  int troff_qk, troff_v;
  {
    int ch = (lane >> 4) & 1, q = (lane & 15) >> 2, p = lane & 3;
    int rowoff = (16 * hk + q) * 64;
    troff_qk = rowoff + (16 * (p & 1) + 4 * (2 * ch + (p >> 1))) * 2;
    troff_v = rowoff + (16 * ch + 4 * p) * 2;
  }
  const u32x4 ones = {0x3f803f80u, 0x3f803f80u, 0x3f803f80u, 0x3f803f80u};
  float s1 = 0.f, s2 = 0.f;
  auto stat = [&](int i) LAMBDA {
    const bf16x2 one2 = __builtin_bit_cast(bf16x2, 0x3f803f80u);
    const unsigned u = xf[i >> 2][i & 3]; bf16x2 v = __builtin_bit_cast(bf16x2, u);
    s1 = __builtin_amdgcn_fdot2_f32_bf16(v, one2, s1, false);
    s2 = __builtin_amdgcn_fdot2_f32_bf16(v, v, s2, false);
  };
  auto soff = [](int s) { return (32 * ((s & 7) >> 1) + 8 * (s & 1)) * 64; };
  {
    f32x16 cs = {};
    stream<8, 4>([&](int S) LAMBDA { return tr_read(lds + 131072 + w * 8192 + soff(S) + troff_qk); },
                 [&](int S, const u32x4& a) LAMBDA { cs = MFMA(ones, a, cs); },
                 [&](int S) LAMBDA { stat(2 * S); stat(2 * S + 1); });
    if (hk == 0) *(float*)(csp + tok * 4) = cs[0];
  }
  lds_barrier();
  STAMP(2);
  f32x16 accq = {};
  stream<32, 4>([&](int S) LAMBDA { return tr_read(lds + 131072 + (S >> 3) * 8192 + soff(S) + troff_qk); },
                [&](int S, const u32x4& a) LAMBDA { accq = MFMA(a, xf[S], accq); },
                [&](int S) LAMBDA { stat(16 + 2 * S); stat(17 + 2 * S); });
  vm_drain();
  {
    f32x16 ck = {}, cv = {};
    stream<16, 4>([&](int S) LAMBDA { return tr_read(area + (S >> 3) * 8192 + soff(S) + (S < 8 ? troff_qk : troff_v)); },
                  [&](int S, const u32x4& a) LAMBDA { if (S < 8) ck = MFMA(ones, a, ck); else cv = MFMA(ones, a, cv); },
                  [&](int S) LAMBDA { stat(80 + 3 * S); stat(81 + 3 * S); stat(82 + 3 * S); });
    if (hk == 0) { *(float*)(csp + (32 + tok) * 4) = ck[0]; *(float*)(csp + (64 + tok) * 4) = cv[0]; }
  }
  s1 = xhalf_sum(s1); s2 = xhalf_sum(s2);
  const float mean = s1 * (1.f / 512.f);
  const float rstd = __builtin_amdgcn_rsqf(fmaxf(s2 * (1.f / 512.f) - mean * mean, 0.f) + 1e-5f);
  const float sc = rstd * LOG2E;
  xf[32] = aug_tok(mean, 0.f, hk);
  if (hk == 0) *(float*)(priv + tok * 4) = rstd;
  lds_barrier();
  STAMP(3);
  u32x4 aw[3];
#pragma unroll
  for (int b = 0; b < 3; b++) {
    float s = 0.f;
#pragma unroll
    for (int k = 0; k < 4; k++) s += *(const float*)(lds + k * 32768 + 17408 + (b * 32 + tok) * 4);
    aw[b] = aug_w(cs_pack(s), 0u);
  }
  f32x16 acck = {};
  const u32x4 ff = {~0u, ~0u, ~0u, ~0u};
  stream<33, 4>([&](int S) LAMBDA { return S < 32 ? tr_read(lds + (S >> 3) * 32768 + soff(S) + troff_qk) : aw[1]; },
                [&](int S, const u32x4& a) LAMBDA { acck = MFMA(a, xf[S], acck); if (S == 2) accq = MFMA(aw[0], xf[32], accq); },
                [&](int S) LAMBDA {
                  if (S >= 4 && S < 12) {
                    int m = (S - 4) >> 1, hi = S & 1;
                    u32x4 v;
#pragma unroll
                    for (int t = 0; t < 4; t++) v[t] = __builtin_amdgcn_perm(rt[2 * t + 1][m], rt[2 * t][m], hi ? 0x07060302u : 0x05040100u);
                    int e = 2 * m;
                    int off = rt_nat ? e * 16 : ((e & 3) + 8 * (e >> 2)) * 16;
                    stc(rs, tid < 176 ? rt_dst + off + hi * 16 : WS_USED, 0, v);
                  } else if (S >= 12 && S < 16) {
                    int k = S - 12;
                    stc(rs, tid * 16 + (k & 1) * 4096, (k < 2 ? OB : X1B) + blockIdx.x * 8192, ff);
                  } else if (S == 16) {
                    if (w == 0) stc(rs, lane * 16, ST1 + blockIdx.x * 1024, ff);
                    if (g == 0 && w == 1) { stc(rs, lane * 16, WT_QKV + c * WQ_C + 98304, aw[0]); stc(rs, lane * 16 + 1024, WT_QKV + c * WQ_C + 98304, aw[1]);
                                            stc(rs, lane * 16 + 2048, WT_QKV + c * WQ_C + 98304, aw[2]); }
                  } else if (S >= 18 && S < 26) {
                    int t = S - 18; accq[2 * t] *= sc; accq[2 * t + 1] *= sc;
                  } else if (S == 26) {
                    u32x4 q0, q1; pack16(accq, q0, q1);
                    stc(rs, lane * 16, QN + (blockIdx.x * 4 + w) * 2048, q0); stc(rs, lane * 16 + 1024, QN + (blockIdx.x * 4 + w) * 2048, q1);
                  }
                });
  f32x16 accv = {}, p = {};
  u32x4 phk[4];
  Softmax sm;
  stream<33, 4>([&](int S) LAMBDA { return S < 32 ? tr_read(lds + (S >> 3) * 32768 + 8192 + soff(S) + troff_v) : aw[2]; },
                [&](int S, const u32x4& a) LAMBDA {
                  accv = MFMA(xf[S], a, accv);
                  if (S == 1) { u32x4 b0, b1; pack16(acck, b0, b1); p = MFMA(aphi[0], b0, p); p = MFMA(aphi[1], b1, p); }
                },
                [&](int S) LAMBDA { if (S >= 3) sm.step(S - 3, p, sc, phk); });
  STAMP(4);
  kv_tail<32768>(phk, accv, priv, kvs, rs, KVP + c * KVP_C, g, tid, lane, w);
  STAMP(5);
}

struct Ctx {
  rsrc_t rs; char* ws; char* lds; int tid, lane, w, tok, hk, g, c, cb, th, vo, hoff, ooff; bool ok;
};

#define SPIN_BEGIN { int it_ = 0; while (true) { asm volatile("" ::: "memory");
#define SPIN_END(m) if (all_valid(m) || !cx.ok) break; if (++it_ > SPIN_MAX) { cx.ok = false; break; } __builtin_amdgcn_s_sleep(1); } }

DEVI void red_send(Ctx& cx, const f32x16& a, int j) {
  char* dst = cx.lds + LDS_W + (cx.w ^ j) * INBOX + (j - 1) * 4096 + cx.vo;
#pragma unroll
  for (int m = 0; m < 4; m++) { f32x4 v = {a[4 * m], a[4 * m + 1], a[4 * m + 2], a[4 * m + 3]}; *(f32x4*)(dst + m * 1024) = v; }
}
DEVI void red_recv(Ctx& cx, const f32x16& own, float (&xr)[16]) {
  lds_barrier();
#pragma unroll
  for (int e = 0; e < 16; e++) xr[e] += own[e];
#pragma unroll
  for (int slot = 0; slot < 3; slot++)
#pragma unroll
    for (int m = 0; m < 4; m++) {
      f32x4 v = *(const f32x4*)(cx.lds + LDS_W + cx.w * INBOX + slot * 4096 + m * 1024 + cx.vo);
#pragma unroll
      for (int e = 0; e < 4; e++) xr[4 * m + e] += v[e];
    }
}

DEVI void publish_x(Ctx& cx, float (&xr)[16], int xbuf, int stbuf) {
  const int tb = 4 * cx.g + 2 * cx.th + (cx.w >> 1), blk = 2 * cx.cb + (cx.w & 1);
  u32x4 f0, f1; pack16(xr, f0, f1);
  stp(cx.rs, cx.vo, xbuf + (tb * 32 + 2 * blk) * 1024, f0);
  stp(cx.rs, cx.vo + 1024, xbuf + (tb * 32 + 2 * blk) * 1024, f1);
  float s1 = 0.f, s2 = 0.f;
#pragma unroll
  for (int t = 0; t < 4; t++) {
    xr[2 * t] = bflo(f0[t]); xr[2 * t + 1] = bfhi(f0[t]); xr[8 + 2 * t] = bflo(f1[t]); xr[9 + 2 * t] = bfhi(f1[t]);
  }
#pragma unroll
  for (int r = 0; r < 16; r++) { s1 += xr[r]; s2 = fmaf(xr[r], xr[r], s2); }
  s1 = xhalf_sum(s1); s2 = xhalf_sum(s2);
  if (cx.hk == 0) { u32x2 st = {__float_as_uint(s1), __float_as_uint(s2)}; stp2(cx.rs, cx.tok * 8, stbuf + (tb * 16 + blk) * 256, st); }
}

DEVI void acquire_x(Ctx& cx, int xbuf, int stbuf, u32x4 (&xf)[33], float& mean, float& var) {
  const int tb = 4 * cx.g + cx.w;
  u32x2 st[8];
  SPIN_BEGIN
    unsigned m = 0;
#pragma unroll
    for (int k = 0; k < 8; k++) st[k] = ldc2(cx.rs, cx.tok * 8 + cx.hk * 2048 + k * 256, stbuf + tb * 4096);
#pragma unroll
    for (int k = 0; k < 8; k++) m = umax3(m, st[k][0], st[k][1]);
  SPIN_END(m)
#pragma unroll
  for (int s = 0; s < 32; s++) xf[s] = ldck(cx.rs, cx.vo, xbuf + tb * 32768, s);
  SB();
  float s1 = 0.f, s2 = 0.f;
#pragma unroll
  for (int k = 0; k < 8; k++) { s1 += __uint_as_float(st[k][0]); s2 += __uint_as_float(st[k][1]); }
  s1 = xhalf_sum(s1); s2 = xhalf_sum(s2);
  mean = s1 * (1.f / 512.f);
  var = fmaxf(s2 * (1.f / 512.f) - mean * mean, 0.f);
}
DEVI void check_x(Ctx& cx, int xbuf, u32x4 (&xf)[33], int b) {
  unsigned m = 0;
#pragma unroll
  for (int k = 0; k < 4; k++) m = chk4(m, xf[4 * b + k]);
  if (!all_valid(m)) {
    const int tb = 4 * cx.g + cx.w;
    SPIN_BEGIN
      m = 0;
#pragma unroll
      for (int k = 0; k < 4; k++) xf[4 * b + k] = ldck(cx.rs, cx.vo, xbuf + tb * 32768, 4 * b + k);
#pragma unroll
      for (int k = 0; k < 4; k++) m = chk4(m, xf[4 * b + k]);
    SPIN_END(m)
  }
}

DEVI void fill_ff(Ctx& cx, int off, int nkb, int stride) {
  const u32x4 ff = {~0u, ~0u, ~0u, ~0u};
#pragma unroll
  for (int k = 0; k < nkb; k++) stc(cx.rs, cx.vo, off + k * stride, ff);
}
DEVI void init_x_out(Ctx& cx, int xbuf, int stbuf) {
  const int tb = 4 * cx.g + 2 * cx.th + (cx.w >> 1), blk = 2 * cx.cb + (cx.w & 1);
  fill_ff(cx, xbuf + (tb * 32 + 2 * blk) * 1024, 2, 1024);
  if (cx.lane < 16) { const u32x4 ff = {~0u, ~0u, ~0u, ~0u}; stc(cx.rs, cx.vo, stbuf + (tb * 16 + blk) * 256, ff); }
}

template <int N4>
DEVI void glds_copy(Ctx& cx, int src_off) {
  unsigned lbase = (unsigned)(size_t)cx.lds + cx.w * 4096;
  const char* src = cx.ws + src_off + cx.w * 4096;
  asm volatile("" : "+s"(src), "+s"(lbase));
#pragma unroll
  for (int m = 0; m < N4; m++) glds4k(cx.vo, src + m * 16384, lbase + m * 16384);
}

DEVI void park(Ctx& cx, const float (&xr)[16]) {
  char* p = cx.lds + LDS_W + cx.w * INBOX + 8192 + cx.vo;
#pragma unroll
  for (int m = 0; m < 4; m++) { f32x4 v = {xr[4 * m], xr[4 * m + 1], xr[4 * m + 2], xr[4 * m + 3]}; *(f32x4*)(p + m * 1024) = v; }
}
DEVI void unpark(Ctx& cx, float (&xr)[16]) {
  const char* p = cx.lds + LDS_W + cx.w * INBOX + 8192 + cx.vo;
#pragma unroll
  for (int m = 0; m < 4; m++) { f32x4 v = *(const f32x4*)(p + m * 1024); xr[4 * m] = v[0]; xr[4 * m + 1] = v[1]; xr[4 * m + 2] = v[2]; xr[4 * m + 3] = v[3]; }
}

template <int L>
DEVI void layer(Ctx& cx, const u16* __restrict__ wphi, u16* __restrict__ out, u32x4 (&w2f)[56], float (&xr)[16],
                unsigned (&csp)[4], const unsigned (&b1p)[2], const u16* __restrict__ b2) {
  const int lane = cx.lane, w = cx.w, tid = cx.tid, tok = cx.tok, hk = cx.hk, g = cx.g, c = cx.c;
  char* ws = cx.ws; (void)ws;
  char* sc = cx.lds + LDS_W;
  char* priv = sc + w * INBOX;
  u32x4 wof[16], phq[4];
  u32x4 pa[16], pz[4];
  const int kvb = KVP + L * KVP_L + c * KVP_C;
  u32x4 aphi[2];
  if (L == 0) {
#pragma unroll
    for (int k = 0; k < 16; k++) pa[k] = ldck(cx.rs, cx.vo, kvb + w * 16384, k);
#pragma unroll
    for (int k = 0; k < 4; k++) pz[k] = ldck(cx.rs, cx.vo, kvb + 65536, k);
    u32x4 q0 = ldw(cx.rs, cx.vo, QN + (blockIdx.x * 4 + w) * 2048), q1 = ldw(cx.rs, cx.vo + 1024, QN + (blockIdx.x * 4 + w) * 2048);
    load_aphi(wphi, c, lane, aphi);
#pragma unroll
    for (int s = 0; s < 16; s++) wof[s] = ldwk(cx.rs, cx.vo, WT_O + (cx.cb * 4 + w) * 16384, s);
    SB();
    f32x16 p = {}; p = MFMA(aphi[0], q0, p); p = MFMA(aphi[1], q1, p);
    Softmax sm;
#pragma unroll
    for (int i = 0; i < 29; i++) sm.step(i, p, 1.f, phq);
    SB();
    glds_copy<7>(cx, WT_1 + c * 114688);
  } else {
    load_aphi(wphi, c, lane, aphi);
    vm_drain(); lds_barrier();
    u32x4 xf[33]; float mean, var;
    acquire_x(cx, X2B, ST2, xf, mean, var);
    STAMP(16);
    const float rstd = __builtin_amdgcn_rsqf(var + 1e-5f), scl = rstd * LOG2E;
    xf[32] = aug_tok(mean, 0.f, hk);
    if (hk == 0) *(float*)(priv + tok * 4) = rstd;
    f32x16 acck = {}, accv = {}, p = {};
    u32x4 phk[4];
    Softmax sm;
    check_x(cx, X2B, xf, 0);
    stream<66, 4>([&](int S) LAMBDA { return *(const u32x4*)(cx.lds + (S < 32 ? 32 + S : S == 32 ? 97 : S < 65 ? 64 + S - 33 : 98) * 1024 + cx.vo); },
                  [&](int S, const u32x4& a) LAMBDA {
                    if (S < 33) acck = MFMA(a, xf[S], acck); else accv = MFMA(xf[S - 33], a, accv);
                    if (S == 34) { u32x4 b0, b1; pack16(acck, b0, b1); p = MFMA(aphi[0], b0, p); p = MFMA(aphi[1], b1, p); }
                  },
                  [&](int S) LAMBDA {
                    if (S < 28 && (S & 3) == 3) check_x(cx, X2B, xf, (S + 1) >> 2);
                    if (S >= 36) sm.step(S - 36, p, scl, phk);
                  });
    STAMP(17);
    kv_tail<INBOX>(phk, accv, priv, sc + 1024, cx.rs, kvb, g, tid, lane, w);
    STAMP(18);
#pragma unroll
    for (int s = 0; s < 16; s++) wof[s] = ldwk(cx.rs, cx.vo, WT_O + (cx.cb * 4 + w) * 16384, s);
    f32x16 accq = {};
    stream<33, 4>([&](int S) LAMBDA { return *(const u32x4*)(cx.lds + (S < 32 ? S : 96) * 1024 + cx.vo); },
                  [&](int S, const u32x4& a) LAMBDA { accq = MFMA(a, xf[S], accq); }, [&](int S) LAMBDA {});
    unpark(cx, xr);
    lds_barrier();
    glds_copy<7>(cx, WT_1 + c * 114688);
    {
      u32x4 b0, b1; pack16(accq, b0, b1);
      f32x16 pq = {}; pq = MFMA(aphi[0], b0, pq); pq = MFMA(aphi[1], b1, pq);
      Softmax sq;
#pragma unroll
      for (int i = 0; i < 29; i++) sq.step(i, pq, scl, phq);
    }
    STAMP(19);
    SPIN_BEGIN
      unsigned m = 0;
#pragma unroll
      for (int k = 0; k < 16; k++) pa[k] = ldck(cx.rs, cx.vo, kvb + w * 16384, k);
#pragma unroll
      for (int k = 0; k < 4; k++) pz[k] = ldck(cx.rs, cx.vo, kvb + 65536, k);
#pragma unroll
      for (int k = 0; k < 16; k++) m = chk4(m, pa[k]);
#pragma unroll
      for (int k = 0; k < 4; k++) m = chk4(m, pz[k]);
    SPIN_END(m)
  }
  STAMP(7 + 13 * L);
  {
    f32x4 s = __builtin_bit_cast(f32x4, pa[0]);
#pragma unroll
    for (int k = 1; k < 16; k++) s += __builtin_bit_cast(f32x4, pa[k]);
    f32x4 z = __builtin_bit_cast(f32x4, pz[0]);
#pragma unroll
    for (int k = 1; k < 4; k++) z += __builtin_bit_cast(f32x4, pz[k]);
#pragma unroll
    for (int e = 0; e < 4; e++) { z[e] += __shfl_xor(z[e], 16); z[e] = xhalf_sum(z[e]); }
    char* img = sc + 2048;
    { u32x2 v = {pk2(s[0], s[1]), pk2(s[2], s[3])}; *(u32x2*)(img + tid * 8) = v; }
    if (tid < 16) { u32x2 v = {pk2(z[0], z[1]), pk2(z[2], z[3])}; *(u32x2*)(img + 2048 + tid * 8) = v; }
    if (tid == 16) { u32x4 zz = {0, 0, 0, 0}; *(u32x4*)(img + 2176) = zz; }
    lds_barrier();
    const int i = tok, hp = (i >> 2) & 1, d = (i & 3) + 4 * (i >> 3);
    const bool on = (hp == hk);
    f32x16 num = {}, den = {};
    u32x4 a[4], az[4];
#pragma unroll
    for (int s4 = 0; s4 < 4; s4++) {
      int pm = s4 >> 1, h2 = s4 & 1;
      az[s4] = *(const u32x4*)(img + (on ? 2048 + ((pm * 2 + hp) * 2 + h2) * 16 : 2176));
    }
#pragma unroll
    for (int s4 = 0; s4 < 4; s4++) {
      int pm = s4 >> 1, h2 = s4 & 1;
      a[s4] = *(const u32x4*)(img + (on ? (pm * 64 + h2 * 32 + 16 * hp + d) * 16 : 2176));
    }
    SB();
#pragma unroll
    for (int s4 = 0; s4 < 4; s4++) den = MFMA(az[s4], phq[s4], den);
#pragma unroll
    for (int s4 = 0; s4 < 4; s4++) num = MFMA(a[s4], phq[s4], num);
    float inv = __builtin_amdgcn_rcpf(den[0] + 1e-6f);
    float ov[16];
#pragma unroll
    for (int r = 0; r < 16; r++) ov[r] = num[r] * inv;
    u32x4 f0, f1; pack16(ov, f0, f1);
    stp(cx.rs, cx.vo, OB + L * 0x200000 + cx.ooff, f0);
    stp(cx.rs, cx.vo + 2048, OB + L * 0x200000 + cx.ooff, f1);
    STAMP(8 + 13 * L);
  }
  {
    fill_ff(cx, HB + L * HB_L + cx.hoff, 7, 8192);
    u32x4 of[16];
    const u16* b2p = b2 + 64 * cx.cb + 32 * (w & 1) + 16 * hk;
    asm volatile("" : "+v"(b2p));
    const u32x4 ba = *(const u32x4*)b2p, bb2 = *(const u32x4*)(b2p + 8);
    SPIN_BEGIN
      unsigned m = 0;
#pragma unroll
      for (int k = 0; k < 16; k++) of[k] = ldck(cx.rs, cx.vo, OB + L * 0x200000 + ((2 * g + cx.th) * 4 + w) * 16384, k);
#pragma unroll
      for (int k = 0; k < 16; k++) m = chk4(m, of[k]);
    SPIN_END(m)
    STAMP(9 + 13 * L);
    lds_barrier();
    f32x16 acc[4] = {};
#pragma unroll
    for (int j = 3; j >= 0; j--) {
#pragma unroll
      for (int s = 0; s < 8; s++) {
        acc[j] = MFMA(wof[2 * s + (j & 1)], of[2 * s + (j >> 1)], acc[j]);
        if (s == 2 && j < 3) red_send(cx, acc[j + 1], j + 1);
        SB();
      }
    }
    red_recv(cx, acc[0], xr);
    publish_x(cx, xr, X1B + L * 0x200000, ST1 + L * 0x40000);
#pragma unroll
    for (int t = 0; t < 4; t++) {
      xr[2 * t] += bflo(ba[t]); xr[2 * t + 1] += bfhi(ba[t]); xr[8 + 2 * t] += bflo(bb2[t]); xr[9 + 2 * t] += bfhi(bb2[t]);
    }
    park(cx, xr);
    STAMP(10 + 13 * L);
  }
  const int ahalf = 3072 + (hk * 16 + (lane & 15)) * 16;
  if (L == 0) {
    init_x_out(cx, X2B, ST2);
    init_x_out(cx, X1B + 0x200000, ST1 + 0x40000);
    fill_ff(cx, OB + 0x200000 + cx.ooff, 2, 2048);
    fill_ff(cx, KVP + KVP_L + c * KVP_C + (w * 16 + g) * 1024, 1, 1024);
    if (w == 0 && lane < 16) { const u32x4 ff = {~0u, ~0u, ~0u, ~0u}; stc(cx.rs, cx.vo, KVP + KVP_L + c * KVP_C + 65536 + g * 256, ff); }
    vm_drain(); lds_barrier();
    const u32x4 ones = {0x3f803f80u, 0x3f803f80u, 0x3f803f80u, 0x3f803f80u};
    f32x16 cs = {};
    const int aoff = w < 3 ? w * 1024 + cx.vo : ahalf;
    stream<32, 4>([&](int S) LAMBDA { return *(const u32x4*)(cx.lds + S * 3584 + aoff); },
                  [&](int S, const u32x4& a) LAMBDA { cs = MFMA(ones, a, cs); },
                  [&](int S) LAMBDA { if (S < 28) { w2f[2 * S] = ldwk(cx.rs, cx.vo, WT_2 + (cx.cb * 4 + w) * 57344, 2 * S);
                                             w2f[2 * S + 1] = ldwk(cx.rs, cx.vo, WT_2 + (cx.cb * 4 + w) * 57344, 2 * S + 1); } });
    char* tmp = sc + 3 * INBOX + 4096;
    if (hk == 0) *(unsigned*)(tmp + (tok * 4 + w) * 4) = cs_pack(cs[0]);
    lds_barrier();
    u32x4 t = *(const u32x4*)(tmp + tok * 16);
    csp[0] = t[0]; csp[1] = t[1]; csp[2] = t[2]; csp[3] = t[3];
  } else {
    vm_drain(); lds_barrier();
#pragma unroll
    for (int k = 48; k < 56; k++) w2f[k] = ldwk(cx.rs, cx.vo, WT_2 + (cx.cb * 4 + w) * 57344, k);
    SB();
  }
  {
    u32x4 xf[33]; float mean, var;
    const int xbuf = X1B + L * 0x200000;
    acquire_x(cx, xbuf, ST1 + L * 0x40000, xf, mean, var);
    STAMP(11 + 13 * L); if (L == 0) CSTAMP(30);
    const float v1 = var + 1e-5f;
    const float r1 = __builtin_amdgcn_rsqf(v1);
    const float v2 = var * r1 * r1 + 1e-5f;
    const float A = r1 * __builtin_amdgcn_rsqf(v2);
    const float iA = __builtin_amdgcn_rcpf(A);
    xf[32] = aug_tok(mean, iA, hk);
    const float k0 = -2.f * LOG2E * 0.7978845608028654f, k1 = k0 * 0.044715f;
    const float c0 = k0 * A, c1 = k1 * A * A * A;
    const int hbase = HB + L * HB_L + cx.hoff;
    f32x16 acc[2];
    float ge[16]; u32x4 hf0, hf1;
    auto epi = [&](int b, int i) LAMBDA {
      const f32x16& a = acc[b & 1];
      const int r = i >> 1;
      if (b == 3 && r >= 8) return;
      if (!(i & 1)) { float t = a[r] * a[r]; ge[r] = __builtin_amdgcn_exp2f(a[r] * fmaf(t, c1, c0)); }
      else {
        ge[r] = a[r] * __builtin_amdgcn_rcpf(fmaf(ge[r], iA, iA));
        if (r & 1) { unsigned u = pk2(ge[r - 1], ge[r]); if (r < 8) hf0[r >> 1] = u; else hf1[(r - 8) >> 1] = u; }
        if (r == 7) stp(cx.rs, cx.vo, hbase + 2 * b * 8192, hf0);
        if (r == 15) stp(cx.rs, cx.vo, hbase + (2 * b + 1) * 8192, hf1);
      }
    };
    check_x(cx, xbuf, xf, 0);
    stream<132, 4>([&](int S) LAMBDA {
                     const int b = S / 33, s = S % 33;
                     if (s == 32) return aug_w(csp[b], b & 1 ? b1p[b >> 1] >> 16 : b1p[b >> 1]);
                     return *(const u32x4*)(cx.lds + s * 3584 + (b < 3 ? b * 1024 + cx.vo : ahalf));
                   },
                   [&](int S, const u32x4& a) LAMBDA {
                     const int b = S / 33, s = S % 33;
                     if (s == 0) { f32x16 zero = {}; acc[b & 1] = MFMA(a, xf[s], zero); } else acc[b & 1] = MFMA(a, xf[s], acc[b & 1]);
                   },
                   [&](int S) LAMBDA {
                     const int b = S / 33, s = S % 33;
                     if (b == 0) { if (s < 28 && (s & 3) == 3) check_x(cx, xbuf, xf, (s + 1) >> 2); }
                     else if (s >= 1) epi(b - 1, s - 1);
                   });
#pragma unroll
    for (int i = 0; i < 16; i++) epi(3, i);
    STAMP(12 + 13 * L); if (L == 0) CSTAMP(31);
    unpark(cx, xr);
    lds_barrier();
    if (L == 0) {
      glds_copy<6>(cx, WT_QKV + c * WQ_C);
      if (w < 3) glds1k(cx.vo, cx.ws + WT_QKV + c * WQ_C + 98304 + w * 1024, (unsigned)(size_t)cx.lds + 98304 + w * 1024);
    }
  }
  {
    const int hb = HB + L * HB_L + ((2 * g + cx.th) * 4 + w) * 57344;
    f32x16 acc[4] = {};
    u32x4 hf[3][8];
    auto hload = [&](int s7) LAMBDA {
#pragma unroll
      for (int k = 0; k < 8; k++) hf[s7 % 3][k] = ldck(cx.rs, cx.vo, hb, s7 * 8 + k);
    };
    hload(0); hload(1);
    SB();
#pragma unroll
    for (int s7 = 0; s7 < 7; s7++) {
      if (s7 + 2 < 7) hload(s7 + 2);
      SB();
      unsigned m = 0;
#pragma unroll
      for (int k = 0; k < 8; k++) m = chk4(m, hf[s7 % 3][k]);
      if (!all_valid(m)) {
        SPIN_BEGIN
          m = 0;
          hload(s7);
#pragma unroll
          for (int k = 0; k < 8; k++) m = chk4(m, hf[s7 % 3][k]);
        SPIN_END(m)
      }
      SB();
#pragma unroll
      for (int j = 3; j >= 0; j--) {
#pragma unroll
        for (int p = 0; p < 4; p++) {
          acc[j] = MFMA(w2f[2 * (4 * s7 + p) + (j & 1)], hf[s7 % 3][2 * p + (j >> 1)], acc[j]);
          if (s7 == 6 && p == 1 && j < 3) red_send(cx, acc[j + 1], j + 1);
        }
        if (s7 == 6) SB();
      }
      SB();
    }
    STAMP(13 + 13 * L);
    red_recv(cx, acc[0], xr);
    if (L == 0) { publish_x(cx, xr, X2B, ST2); park(cx, xr); }
    else {
      u32x4 f0, f1; pack16(xr, f0, f1);
      u16* dst = out + (size_t)(128 * g + 64 * cx.th + 32 * (w >> 1) + tok) * 512 + 64 * cx.cb + 32 * (w & 1) + 16 * hk;
      *(u32x4*)dst = f0; *(u32x4*)(dst + 8) = f1;
    }
    STAMP(14 + 13 * L); if (L == 1) CSTAMP(29);
  }
}

__global__ __launch_bounds__(256, 1) void k2(const u16* __restrict__ x, const u16* __restrict__ wphi, const u16* __restrict__ b1,
                                             const u16* __restrict__ b2, char* ws, u16* __restrict__ out) {
  __shared__ __attribute__((aligned(16))) char lds[LDS_TOT];
  Ctx cx;
  cx.ws = ws; cx.lds = lds; cx.tid = threadIdx.x; cx.lane = cx.tid & 63; cx.w = __builtin_amdgcn_readfirstlane(cx.tid >> 6);
  cx.tok = cx.lane & 31; cx.hk = cx.lane >> 5; cx.vo = cx.lane * 16; cx.ok = true;
  map_block(blockIdx.x, cx.g, cx.c);
  cx.cb = 4 * (cx.c >> 3) + (cx.c & 3); cx.th = (cx.c >> 2) & 1;
  cx.rs = __builtin_amdgcn_make_buffer_rsrc(ws, 0, WS_USED, 0x00020000);
  {
    const int tp = 2 * cx.g + (cx.w >> 1), kq = cx.c >> 2, tx = (cx.w & 1) ^ (kq >> 1);
    cx.hoff = ((tp * 4 + kq) * 56 + (cx.c & 3) * 2 + tx) * 1024;
    cx.ooff = ((tp * 4 + kq) * 16 + (cx.c & 3) * 4 + tx) * 1024;
  }
  const int lane = cx.lane, w = cx.w;
  STAMP(6); CSTAMP(28);
  float xr[16]; unsigned csp[4], b1p[2];
  {
    const int col = 64 * cx.cb + 32 * (w & 1) + 16 * cx.hk;
    const u16* src = x + (size_t)(128 * cx.g + 64 * cx.th + 32 * (w >> 1) + cx.tok) * 512 + col;
    u32x4 a = *(const u32x4*)src, b = *(const u32x4*)(src + 8);
#pragma unroll
    for (int t = 0; t < 4; t++) {
      xr[2 * t] = bflo(a[t]); xr[2 * t + 1] = bfhi(a[t]); xr[8 + 2 * t] = bflo(b[t]); xr[9 + 2 * t] = bfhi(b[t]);
    }
    const int i = cx.tok, pi = 16 * ((i >> 2) & 1) + (i & 3) + 4 * (i >> 3), ph = 8 * ((i >> 2) & 1) + (i & 3) + 4 * ((i >> 3) & 1);
    const u16* bp = b1 + 112 * cx.c;
    b1p[0] = bp[pi] | ((unsigned)bp[32 + pi] << 16);
    b1p[1] = bp[64 + pi] | ((unsigned)bp[96 + ph] << 16);
  }
  u32x4 w2f[56];
  layer<0>(cx, wphi, out, w2f, xr, csp, b1p, b2);
  asm volatile("" : "+v"(cx.tid), "+v"(cx.lane), "+v"(cx.tok), "+v"(cx.hk), "+v"(cx.vo));
  asm volatile("" : "+v"(csp[0]), "+v"(csp[1]), "+v"(csp[2]), "+v"(csp[3]), "+v"(b1p[0]), "+v"(b1p[1]));
  layer<1>(cx, wphi, out, w2f, xr, csp, b1p, b2);
}

extern "C" void kernel_launch(void* const* d_in, const int* in_sizes, int n_in, void* d_out, int out_size, void* d_ws, size_t ws_size,
                              hipStream_t stream) {
  const u16* x = (const u16*)d_in[0]; const u16* wqkv = (const u16*)d_in[1]; const u16* wphi = (const u16*)d_in[2];
  const u16* wo = (const u16*)d_in[3]; const u16* w1 = (const u16*)d_in[5]; const u16* b1 = (const u16*)d_in[6];
  const u16* w2 = (const u16*)d_in[7]; const u16* b2 = (const u16*)d_in[8];
  k1<<<256, 256, 0, stream>>>(x, wqkv, wphi, wo, w1, w2, (char*)d_ws);
  k2<<<256, 256, 0, stream>>>(x, wphi, b1, b2, (char*)d_ws, (u16*)d_out);
}
```

```cpp
#include <hip/hip_runtime.h>
#include <stdint.h>

typedef unsigned short u16;
typedef __attribute__((ext_vector_type(8))) __bf16 bf16x8;
typedef __attribute__((ext_vector_type(2))) __bf16 bf16x2;
typedef __attribute__((ext_vector_type(16))) float f32x16;
typedef __attribute__((ext_vector_type(4))) float f32x4;
typedef __attribute__((ext_vector_type(2))) float f32x2;
typedef __attribute__((ext_vector_type(4))) unsigned u32x4;
typedef __attribute__((ext_vector_type(2))) unsigned u32x2;
typedef __attribute__((ext_vector_type(4))) short s16x4;
typedef __amdgpu_buffer_rsrc_t rsrc_t;
typedef __attribute__((address_space(3))) void* ldsp;

#define DEVI __device__ __forceinline__
#define LAMBDA __attribute__((always_inline))
#define MFMA(a, b, c) __builtin_amdgcn_mfma_f32_32x32x16_bf16(__builtin_bit_cast(bf16x8, a), __builtin_bit_cast(bf16x8, b), c, 0, 0, 0)
#define MFMA16(a, b, c) __builtin_amdgcn_mfma_f32_16x16x32_bf16(__builtin_bit_cast(bf16x8, a), __builtin_bit_cast(bf16x8, b), c, 0, 0, 0)
#define SB() __builtin_amdgcn_sched_barrier(0)
#define RFL(v) __builtin_amdgcn_readfirstlane(v)
#define BALLOT(p) __builtin_amdgcn_ballot_w64(p)

constexpr int WT_QKV = 0x0, WQ_C = 98304;
constexpr int WT_O   = 0x180000;
constexpr int WT_1   = 0x200000, W1_C = 114688;
constexpr int CS1    = 0x3C0000;
constexpr int WT_2   = 0x400000;
constexpr int MBX    = 0x5C0000;
constexpr int TOKR   = 0x5E0000;
constexpr int TOKC   = 0x5E1000;
constexpr int KVA    = 0x600000, KV_C = 0xA000, KV_L = 0xA0000, KV_Z = 0x8000, KV_F = 0x8800;
constexpr int GRP    = 0x800000, G_S = 0x200000, G_L = 0xC0000;
constexpr int G_O = 0, G_X1 = 0x20000, G_H = 0x40000, G_ST1 = 0xB0000, G_FO = 0xB4000, G_FH = 0xB4400, G_FX1 = 0xB4800;
constexpr int G_X2 = 0x180000, G_ST2 = 0x1A0000, G_FX2 = 0x1A4000;
constexpr int WS_USED = 0x2800000;
constexpr int OOB = 0x40000000;
#define STAMP_OFF 0x2800000
#define STAMP_N 48

constexpr int INBOX = 12288;
constexpr int C_V = 4352, C_IMG = 6656, C_ZERO = 7680, C_PARK = 8192;
constexpr float LOG2E = 1.4426950408889634f;
constexpr int SPIN_MAX = 1 << 16;

DEVI unsigned pk2(float a, float b) { f32x2 v = {a, b}; return __builtin_bit_cast(unsigned, __builtin_convertvector(v, bf16x2)); }
DEVI float bflo(unsigned u) { return __uint_as_float(u << 16); }
DEVI float bfhi(unsigned u) { return __uint_as_float(u & 0xffff0000u); }
DEVI void lds_barrier() { asm volatile("s_waitcnt lgkmcnt(0)\n\ts_barrier" ::: "memory"); }
DEVI void bar() { asm volatile("s_barrier" ::: "memory"); }
template <int N> DEVI void vm_wait() { asm volatile("s_waitcnt vmcnt(%0)" :: "n"(N) : "memory"); }
template <int OFF> DEVI void gldso(unsigned voff, const void* sbase, unsigned lds_base) {
  asm volatile("s_mov_b32 m0, %2\n\ts_nop 0\n\tglobal_load_lds_dwordx4 %0, %1 offset:%3" :: "v"(voff), "s"(sbase), "s"(lds_base), "n"(OFF) : "memory", "m0");
}
DEVI void gldsc4(unsigned voff, const void* sbase, unsigned lds_base) {
  asm volatile("s_mov_b32 m0, %2\n\ts_nop 0\n\tglobal_load_lds_dwordx4 %0, %1 sc1" :: "v"(voff), "s"(sbase), "s"(lds_base) : "memory", "m0");
}
DEVI void gldsc1(unsigned voff, const void* sbase, unsigned lds_base) {
  asm volatile("s_mov_b32 m0, %2\n\ts_nop 0\n\tglobal_load_lds_dword %0, %1 sc1" :: "v"(voff), "s"(sbase), "s"(lds_base) : "memory", "m0");
}
DEVI float xhalf_sum(float v) {
  auto t = __builtin_amdgcn_permlane32_swap(__float_as_uint(v), __float_as_uint(v), false, false);
  return __uint_as_float(t[0]) + __uint_as_float(t[1]);
}
DEVI u32x4 ldc(rsrc_t rs, int vo, int so) { return __builtin_amdgcn_raw_buffer_load_b128(rs, vo, so, 16); }
DEVI u32x2 ldc2(rsrc_t rs, int vo, int so) { return __builtin_amdgcn_raw_buffer_load_b64(rs, vo, so, 16); }
DEVI unsigned ldc1(rsrc_t rs, int vo, int so) { return __builtin_amdgcn_raw_buffer_load_b32(rs, vo, so, 16); }
DEVI void stc(rsrc_t rs, int vo, int so, u32x4 v) { __builtin_amdgcn_raw_buffer_store_b128(v, rs, vo, so, 16); }
DEVI void stc2(rsrc_t rs, int vo, int so, u32x2 v) { __builtin_amdgcn_raw_buffer_store_b64(v, rs, vo, so, 16); }
DEVI void stc1(rsrc_t rs, int vo, int so, unsigned v) { __builtin_amdgcn_raw_buffer_store_b32(v, rs, vo, so, 16); }
#define GST 16
DEVI void stg(rsrc_t rs, int vo, int so, u32x4 v) { __builtin_amdgcn_raw_buffer_store_b128(v, rs, vo, so, GST); }
DEVI void stg2(rsrc_t rs, int vo, int so, u32x2 v) { __builtin_amdgcn_raw_buffer_store_b64(v, rs, vo, so, GST); }
DEVI void stg1(rsrc_t rs, int vo, int so, unsigned v) { __builtin_amdgcn_raw_buffer_store_b32(v, rs, vo, so, GST); }
DEVI u32x4 ldck(rsrc_t rs, int vo, int so, int kb) { return ldc(rs, vo + (kb & 3) * 1024, so + (kb >> 2) * 4096); }
template <int KB> DEVI void dma(rsrc_t rs, char* dst, int vo, int so) {
  __builtin_amdgcn_raw_ptr_buffer_load_lds(rs, (ldsp)(dst + (KB >> 2) * 4096), 16, vo, so + (KB >> 2) * 4096, (KB & 3) * 1024, 16);
}
DEVI float lsel(unsigned long long m, float t, float f) { float r; asm("v_cndmask_b32_e64 %0, %1, %2, %3" : "=v"(r) : "v"(f), "v"(t), "s"(m)); return r; }
DEVI float fmax3(float a, float b, float c) { return fmaxf(fmaxf(a, b), c); }
DEVI float fmin3(float a, float b, float c) { return fminf(fminf(a, b), c); }
DEVI unsigned sum4(unsigned s, const u32x4& f) { s = (s + f[0] + f[1]) + f[2] + f[3]; asm volatile("" : "+v"(s)); return s; }
DEVI unsigned wsum(unsigned v) {
  v += __builtin_amdgcn_update_dpp(0u, v, 0x111, 0xf, 0xf, false);
  v += __builtin_amdgcn_update_dpp(0u, v, 0x112, 0xf, 0xf, false);
  v += __builtin_amdgcn_update_dpp(0u, v, 0x114, 0xf, 0xf, false);
  v += __builtin_amdgcn_update_dpp(0u, v, 0x118, 0xf, 0xf, false);
  v += __builtin_amdgcn_update_dpp(0u, v, 0x142, 0xa, 0xf, false);
  v += __builtin_amdgcn_update_dpp(0u, v, 0x143, 0xc, 0xf, false);
  return v;
}

#define FST(sel, i)
#define STAMP(i)
#define CSTAMP(i)
#define COUNT(i)

template <class T>
DEVI void pack16(const T& v, u32x4& a, u32x4& b) {
#pragma unroll
  for (int i = 0; i < 4; i++) { a[i] = pk2(v[2 * i], v[2 * i + 1]); b[i] = pk2(v[8 + 2 * i], v[9 + 2 * i]); }
}
template <int I> struct ic { static constexpr int v = I; constexpr operator int() const { return I; } };
template <int... I> struct iseq {};
template <int N, int... I> struct mkseq : mkseq<N - 1, N - 1, I...> {};
template <int... I> struct mkseq<0, I...> { typedef iseq<I...> type; };
template <class F, int... I> DEVI void sfor_impl(F& f, iseq<I...>) { (f(ic<I>()), ...); }
template <int N, class F> DEVI void sfor(F f) { sfor_impl(f, typename mkseq<N>::type()); }

struct Softmax {
  float mx, mn, sp, sn, ep[16], en[16];
  DEVI void step(int i, const f32x16& p, float sc, u32x4 (&ph)[4]) {
    if (i == 0) { mx = fmax3(p[0], p[1], p[2]); mn = fmin3(p[0], p[1], p[2]); mx = fmax3(mx, p[3], p[4]); mn = fmin3(mn, p[3], p[4]); }
    else if (i < 3) { int b = 5 + 4 * (i - 1); mx = fmax3(mx, p[b], p[b + 1]); mn = fmin3(mn, p[b], p[b + 1]); mx = fmax3(mx, p[b + 2], p[b + 3]); mn = fmin3(mn, p[b + 2], p[b + 3]); }
    else if (i == 3) { mx = fmax3(mx, p[13], p[14]); mn = fmin3(mn, p[13], p[14]); mx = fmaxf(mx, p[15]) * -sc; mn = fminf(mn, p[15]) * sc; }
    else if (i < 20) {
      int r = i - 4;
      ep[r] = __builtin_amdgcn_exp2f(fmaf(p[r], sc, mx)); en[r] = __builtin_amdgcn_exp2f(fmaf(p[r], -sc, mn));
      if (r == 2) { sp = ep[0] + ep[1]; sn = en[0] + en[1]; } else if (r > 2) { sp += ep[r - 1]; sn += en[r - 1]; }
    } else if (i == 20) { sp = __builtin_amdgcn_rcpf(sp + ep[15]); sn = __builtin_amdgcn_rcpf(sn + en[15]); }
    else if (i < 29) {
      int t = i - 21;
      ph[t >> 2][t & 3] = pk2(ep[2 * t] * sp, ep[2 * t + 1] * sp);
      ph[2 + (t >> 2)][t & 3] = pk2(en[2 * t] * sn, en[2 * t + 1] * sn);
    }
  }
};

template <int C> DEVI u32x4 tr_readc(const char* p) {
  typedef __attribute__((address_space(3))) s16x4* lp;
  s16x4 a = __builtin_amdgcn_ds_read_tr16_b64_v4i16((lp)(p));
  s16x4 b = __builtin_amdgcn_ds_read_tr16_b64_v4i16((lp)(p + C));
  u32x2 ua = __builtin_bit_cast(u32x2, a), ub = __builtin_bit_cast(u32x2, b);
  u32x4 r = {ua[0], ua[1], ub[0], ub[1]};
  return r;
}
DEVI u32x4 tr_read2(const char* p, const char* p2) {
  typedef __attribute__((address_space(3))) s16x4* lp;
  s16x4 a = __builtin_amdgcn_ds_read_tr16_b64_v4i16((lp)(p));
  s16x4 b = __builtin_amdgcn_ds_read_tr16_b64_v4i16((lp)(p2));
  u32x2 ua = __builtin_bit_cast(u32x2, a), ub = __builtin_bit_cast(u32x2, b);
  u32x4 r = {ua[0], ua[1], ub[0], ub[1]};
  return r;
}

DEVI u32x4 aug_tok(float mean, float ia, int hk) {
  unsigned mh = pk2(-mean, ia);
  unsigned ml = pk2(-mean - bflo(mh), ia - bfhi(mh));
  u32x4 t;
  t[0] = __builtin_amdgcn_perm(ml, mh, 0x05040100u);
  t[1] = mh;
  t[2] = ml >> 16;
  t[3] = 0;
  if (hk) { t[0] = 0; t[1] = 0; t[2] = 0; }
  return t;
}
DEVI unsigned cs_pack(float cs) { unsigned h = pk2(cs, 0.f); return pk2(cs, cs - bflo(h)); }
DEVI u32x4 aug_w(unsigned csp, unsigned b) {
  u32x4 t;
  t[0] = __builtin_amdgcn_perm(csp, csp, 0x01000100u);
  t[1] = __builtin_amdgcn_perm(b, csp, 0x05040302u);
  t[2] = b & 0xffffu;
  t[3] = 0;
  return t;
}

struct Cx {
  rsrc_t rs; char* ws; char *A, *B, *C;
  int lane, w, tok, hk, g, c, cb, th, vo, gb;
  int fv16, fv8, f63, oo, ho, fo, xo, so, fxo;
  unsigned N, tk_kv, tk_oh, tk_x;
  int budget;
};

DEVI void put_flag(Cx& cx, int so, int i) { stc1(cx.rs, cx.f63, so, cx.N + i); }
DEVI void put_final(Cx& cx, int so, unsigned sum) { u32x2 v = {cx.N + 7, wsum(sum) + cx.N}; stc2(cx.rs, cx.f63, so, v); }
DEVI void put_flag_g(Cx& cx, int so, int i) { stg1(cx.rs, cx.f63, so, cx.N + i); }
DEVI void put_final_g(Cx& cx, int so, unsigned sum) { u32x2 v = {cx.N + 7, wsum(sum) + cx.N}; stg2(cx.rs, cx.f63, so, v); }
template <int NP> DEVI bool flag_ok(unsigned f, unsigned tk, int i) {
  return (unsigned)(BALLOT(f - tk - i >= 8u - i) & ((1ull << NP) - 1)) == 0;
}
template <int NP> DEVI void poll_wait(Cx& cx, unsigned a, int so, unsigned tk, int i, int cnt) {
  unsigned t; int s;
  asm volatile("v_sub_u32 %0, %3, %4\n\t"
               "v_subrev_u32 %0, %10, %0\n\t"
               "v_cmp_le_u32 vcc, %8, %0\n\t"
               "s_and_b32 %1, vcc_lo, %9\n\t"
               "s_cbranch_scc0 2f\n"
               "1:\n\t"
               "s_cmp_eq_u32 %2, 0\n\t"
               "s_cbranch_scc1 2f\n\t"
               "buffer_load_dword %0, %5, %6, %7 offen sc1\n\t"
               "s_sub_u32 %2, %2, 1\n\t"
               "s_waitcnt vmcnt(0)\n\t"
               "v_sub_u32 %0, %0, %4\n\t"
               "v_subrev_u32 %0, %10, %0\n\t"
               "v_cmp_le_u32 vcc, %8, %0\n\t"
               "s_and_b32 %1, vcc_lo, %9\n\t"
               "s_cbranch_scc1 1b\n"
               "2:"
               : "=&v"(t), "=&s"(s), "+s"(cx.budget)
               : "v"(a), "v"(tk), "v"(NP == 16 ? cx.fv16 : cx.fv8), "s"(cx.rs), "s"(so), "n"(8 - i), "n"((1 << NP) - 1), "n"(i)
               : "vcc", "scc", "memory");
}
template <int NP> DEVI bool validate(Cx& cx, unsigned sum, u32x2 fc, int so, unsigned tk, int cnt) {
  int s; unsigned f = fc[0], k = fc[1];
  asm volatile("v_sub_u32 %0, %0, %4\n\t"
               "v_cmp_ne_u32 vcc, 7, %0\n\t"
               "s_and_b32 %2, vcc_lo, %8\n\t"
               "s_cbranch_scc0 2f\n"
               "1:\n\t"
               "s_cmp_eq_u32 %3, 0\n\t"
               "s_cbranch_scc1 2f\n\t"
               "buffer_load_dword %0, %5, %6, %7 offen sc1\n\t"
               "buffer_load_dword %1, %5, %6, %7 offen offset:4 sc1\n\t"
               "s_sub_u32 %3, %3, 1\n\t"
               "s_waitcnt vmcnt(0)\n\t"
               "v_sub_u32 %0, %0, %4\n\t"
               "v_cmp_ne_u32 vcc, 7, %0\n\t"
               "s_and_b32 %2, vcc_lo, %8\n\t"
               "s_cbranch_scc1 1b\n"
               "2:"
               : "+v"(f), "+v"(k), "=&s"(s), "+s"(cx.budget)
               : "v"(tk), "v"(NP == 16 ? cx.fv16 : cx.fv8), "s"(cx.rs), "s"(so), "n"((1 << NP) - 1)
               : "vcc", "scc", "memory");
  const unsigned r = __builtin_amdgcn_readlane(wsum(sum - k + tk), 63);
  if (r == 0 || cx.budget < 64) return true;
  COUNT(cnt);
  cx.budget -= 64;
  return false;
}
DEVI void mail_wait(Cx& cx, unsigned a, int so, unsigned v) {
  int s;
  asm volatile("v_cmp_ne_u32 vcc, %3, %0\n\t"
               "s_and_b32 %1, vcc_lo, 0xffff\n\t"
               "s_cbranch_scc0 2f\n"
               "1:\n\t"
               "s_cmp_eq_u32 %2, 0\n\t"
               "s_cbranch_scc1 2f\n\t"
               "buffer_load_dword %0, %4, %5, %6 offen sc1\n\t"
               "s_sub_u32 %2, %2, 1\n\t"
               "s_waitcnt vmcnt(0)\n\t"
               "v_cmp_ne_u32 vcc, %3, %0\n\t"
               "s_and_b32 %1, vcc_lo, 0xffff\n\t"
               "s_cbranch_scc1 1b\n"
               "2:"
               : "+v"(a), "=&s"(s), "+s"(cx.budget)
               : "s"(v), "v"(cx.fv16), "s"(cx.rs), "s"(so)
               : "vcc", "scc", "memory");
}
DEVI void kv_write_v(Cx& cx, const f32x16& accv, float rstd) {
  float vn[16];
#pragma unroll
  for (int r = 0; r < 16; r++) vn[r] = accv[r] * rstd;
  u32x4 v0, v1; pack16(vn, v0, v1);
  char* p = cx.C + 2 * cx.hk * INBOX + C_V + (32 * cx.w + cx.tok) * 16;
  *(u32x4*)p = v0; *(u32x4*)(p + INBOX + 128) = v1;
}
template <int VMW>
DEVI void kv_tail(Cx& cx, const u32x4 (&phk)[4], int kvb) {
  char* p = cx.C + 2 * cx.hk * INBOX + (32 * cx.w + cx.tok) * 16;
  *(u32x4*)p = phk[0]; *(u32x4*)(p + 2176) = phk[1]; *(u32x4*)(p + INBOX) = phk[2]; *(u32x4*)(p + INBOX + 2176) = phk[3];
  const int G = cx.lane >> 4, d = cx.lane & 15, q4 = d >> 2, p4 = cx.lane & 3;
  const int ro = (4 * G + q4) * 16 + (p4 & 1) * 8;
  const char* pa = cx.C + cx.w * INBOX + (p4 >> 1) * 2176 + ro;
  const char* pb = cx.C + ((cx.w & 2) + (p4 >> 1)) * INBOX + C_V + (p4 >> 1) * 128 + ro;
  if (VMW >= 0) vm_wait<(VMW >= 0 ? VMW : 0)>();
  lds_barrier();
  u32x4 a[4], b[4];
#pragma unroll
  for (int t = 0; t < 4; t++) { a[t] = tr_readc<256>(pa + t * 512); b[t] = tr_readc<256>(pb + t * 512); }
  const u32x4 ones = {0x3f803f80u, 0x3f803f80u, 0x3f803f80u, 0x3f803f80u};
  f32x4 kv = {}, z = {};
#pragma unroll
  for (int t = 0; t < 4; t++) { kv = MFMA16(a[t], b[t], kv); z = MFMA16(a[t], ones, z); }
  const u32x2 k2 = {pk2(kv[0], kv[1]), pk2(kv[2], kv[3])}, z2 = {pk2(z[0], z[1]), pk2(z[2], z[3])};
  const int e = cx.w * 16 + cx.g;
  stc2(cx.rs, (G >> 1) * 256 + d * 16 + (G & 1) * 8, kvb + e * 512, k2);
  stc2(cx.rs, d == 0 ? G * 8 : OOB, kvb + KV_Z + e * 32, z2);
  put_final(cx, kvb + KV_F + e * 8, k2[0] + k2[1] + (d == 0 ? z2[0] + z2[1] : 0u));
}
struct KvIn { u32x4 pa[8], pz; u32x2 fc; };
DEVI void kv_load(Cx& cx, KvIn& k, int kvb) {
#pragma unroll
  for (int m = 0; m < 8; m++) k.pa[m] = ldck(cx.rs, cx.vo, kvb + cx.w * 8192, m);
  k.pz = ldc(cx.rs, cx.hk ? OOB : cx.vo, kvb + KV_Z + cx.w * 512);
  k.fc = ldc2(cx.rs, cx.fv16, kvb + KV_F + cx.w * 128);
}

#define NW2_A 4

DEVI void stage1a(Cx& cx, const u16* __restrict__ x, const u16* __restrict__ wqkv, const u16* __restrict__ wphi,
                  const u16* __restrict__ wo, const u16* __restrict__ w1, const u16* __restrict__ w2,
                  u32x4 (&phq)[4], unsigned (&csw)[3], u32x4 (&aphi)[2], KvIn& kin, u32x4 (&w2f)[56]) {
  const int lane = cx.lane, w = cx.w, tok = cx.tok, hk = cx.hk, g = cx.g, c = cx.c;
  const int rot = (c + 2 * g) & 7;
  const rsrc_t rs = cx.rs;
  const unsigned lA = (unsigned)(size_t)cx.A, lB = (unsigned)(size_t)cx.B, lC = (unsigned)(size_t)cx.C;
  char* xs = cx.C + w * INBOX;
  char* stg = cx.B + 32768 + w * 4096;
  const unsigned lstg = lB + 32768 + w * 4096;
  unsigned vx[4];
#pragma unroll
  for (int i = 0; i < 4; i++) vx[i] = (8 * i + (lane >> 3)) * 1024 + (((lane & 7) ^ ((4 * i + (lane >> 4)) & 7)) * 16) - i * 1024;
  const char* xb = (const char*)(x + (size_t)(128 * g + 32 * w) * 512);
  const unsigned xl = lC + w * INBOX;
  const unsigned wvo = (lane >> 2) * 3072 + (lane & 3) * 16;
  const char* wb = (const char*)(wqkv + (size_t)(16 * w) * 1536 + 32 * c);
  auto cload = [&](int j, int i) LAMBDA {
    const int cj = (j + rot) & 7;
    if (i < 4) {
      const char* sb = xb + cj * 128;
      if (i == 0) gldso<0>(vx[0], sb, xl + (j % 3) * 4096);
      if (i == 1) gldso<1024>(vx[1], sb, xl + (j % 3) * 4096);
      if (i == 2) gldso<2048>(vx[2], sb, xl + (j % 3) * 4096);
      if (i == 3) gldso<3072>(vx[3], sb, xl + (j % 3) * 4096);
    } else {
      const char* sb = wb + cj * (64 * 3072);
      if (i == 4) gldso<0>(wvo + 1024, sb, lA + j * 8192 + w * 2048);
      if (i == 5) gldso<1024>(wvo + 1024, sb, lA + j * 8192 + w * 2048);
      if (i == 6) gldso<0>(wvo, sb, lB + j * 4096 + w * 1024);
    }
  };
#pragma unroll
  for (int i = 0; i < 6; i++) cload(0, i);
  const unsigned tvo = lane < 16 ? TOKR + g * 128 + lane * 4 : TOKC + cx.cb * 128 + ((lane - 16) & 31) * 4;
  const unsigned mvo = MBX + (g * 16 + c) * 512 + (lane & 31) * 16;
  gldsc1(tvo, cx.ws, lB + 5 * 4096 + w * 1024);
  gldsc4(mvo, cx.ws, lB + 6 * 4096 + w * 1024);
#pragma unroll
  for (int i = 0; i < 6; i++) cload(1, i);
  const int ch = (lane >> 4) & 1, q4 = (lane & 15) >> 2, p4 = lane & 3;
  const int cperm = (16 * (p4 & 1) + 4 * (2 * ch + (p4 >> 1))) * 2;
  int r_tr, r_a, r_b, r_c, r_dst, r_d0, r_d1, r_cs = OOB; unsigned r_dv = lane * 16; bool r_half = false;
  {
    const int lj = g * 4 + w, cb = cx.cb, th = cx.th;
    const u16* src; unsigned v0; int s0, s1;
    if (lj < 24) {
      const int bb = lj >> 3, m = lj & 7;
      src = w1 + (size_t)(64 * m) * 1792 + 112 * c + 32 * bb;
      v0 = (lane >> 2) * 3584 + (lane & 3) * 16; s0 = 16 * 3584; s1 = 32 * 3584;
      r_tr = (16 * hk + q4) * 64 + cperm; r_a = 512; r_b = 2048; r_c = 256;
      r_d0 = bb < 2 ? 2048 : 1536; r_d1 = 2 * r_d0;
      r_dst = WT_1 + c * W1_C + (bb < 2 ? bb * 1024 : 65536) + 4 * m * r_d0;
      r_cs = CS1 + (c * 4 + bb) * 1024 + m * 4;
    } else if (lj < 28) {
      const int m4 = lj - 24;
      src = w1 + (size_t)(128 * m4) * 1792 + 112 * c + 96;
      v0 = (lane >> 1) * 3584 + (lane & 1) * 16; s0 = 32 * 3584; s1 = 64 * 3584;
      r_tr = (8 * ch + 16 * hk + q4) * 32 + (8 * (p4 & 1) + 4 * (p4 >> 1)) * 2; r_a = 1024; r_b = 2048; r_c = 128;
      r_dst = WT_1 + c * W1_C + 65536 + 8 * m4 * 1536 + 1024; r_d0 = 3072; r_d1 = 6144;
      r_dv = ch * 1536 + (hk * 16 + (lane & 15)) * 16;
      r_cs = CS1 + (c * 4 + 3) * 1024 + m4 * 8; r_half = true;
    } else {
      v0 = (lane >> 3) * 1024 + (lane & 7) * 16; s0 = 8192; s1 = 16384;
      r_tr = (16 * hk + q4) * 128 + cperm; r_b = 1024; r_c = 512; r_d0 = 1024;
      int kq;
      if (lj < 36) {
        const int rg = 8 * th + lj - 28;
        kq = rg >> 2;
        src = wo + (size_t)(32 * rg) * 512 + 64 * cb;
        r_dst = WT_O + ((cb * 4 + kq) * 8 + ((2 * rg) & 7)) * 2048; r_d1 = 2048;
      } else if (lj < 60) {
        const int u = lj - 36, cc = 8 * th + (u & 7), blk = u >> 3;
        kq = cc >> 2;
        src = w2 + (size_t)(112 * cc + 32 * blk) * 512 + 64 * cb;
        r_dst = WT_2 + ((cb * 4 + kq) * 28 + 8 * blk + (cc & 3)) * 2048; r_d1 = 8192;
      } else {
        const int cc = 8 * th + 2 * (lj - 60);
        kq = cc >> 2;
        src = w2 + (size_t)(112 * cc + 96) * 512 + 64 * cb; s1 = 112 * 1024;
        r_tr = (8 * hk + q4) * 128 + cperm; r_b = 2048;
        r_dst = WT_2 + ((cb * 4 + kq) * 28 + 24 + (cc & 3)) * 2048; r_d1 = 2048;
      }
      r_tr += (kq & 1) * 64; r_a = (kq & 1) ? -64 : 64;
    }
    r_dst = RFL(r_dst); r_d0 = RFL(r_d0); r_d1 = RFL(r_d1); r_cs = RFL(r_cs);
    gldso<0>(v0, src, lstg); gldso<1024>(v0 + s0 - 1024, src, lstg); gldso<2048>(v0 + s1 - 2048, src, lstg); gldso<3072>(v0 + s0 + s1 - 3072, src, lstg);
  }
  gldso<0>(lane * 16, wphi + c * 512, lB + 7 * 4096 + w * 1024);
  const int xro = tok * 128 + (((2 * hk) ^ ((tok >> 1) & 7)) * 16);
  const char* wk = cx.A + hk * 2048 + q4 * 64 + cperm;
  const char* wq = cx.B + hk * 1024 + q4 * 64 + cperm;
  const unsigned on1 = (ch == ((lane >> 2) & 1)) ? 0x3f803f80u : 0u;
  const u32x4 sel = {on1, on1, on1, on1};
  const int mdst = w != 0 || lane >= 48 ? OOB
                 : lane < 16 ? MBX + (g * 16 + lane) * 512 + c * 8
                             : MBX + (((lane - 16) & 15) * 16 + ((c & ~4) | (((lane - 16) >> 4) << 2))) * 512 + (16 + g + 16 * cx.th) * 8;
  unsigned N = 0, sent = 0;
  u32x4 xf[33], ka[3], va[3], rf[4];
  f32x16 acck = {}, accv = {}, gram = {};
  f32x4 csk = {}, csv = {}, s1v = {};
  auto rdx = [&](int n) LAMBDA { xf[n] = *(const u32x4*)(xs + ((n >> 2) % 3) * 4096 + (xro ^ ((4 * ((n >> 1) & 1) + (n & 1)) * 16))); };
  auto rdk = [&](int n) LAMBDA { ka[n % 3] = tr_readc<256>(wk + (n >> 1) * 4096 + (n & 1) * 512); };
  auto rdv = [&](int n) LAMBDA { va[n % 3] = tr_readc<256>(wk + 1024 + (n >> 1) * 4096 + (n & 1) * 512); };
  auto send = [&](int slot, bool first) LAMBDA {
    const unsigned t = *(const unsigned*)(stg + slot * 256 + lane * 4);
    const u32x2 m = {t, N};
    stc2(rs, first || t != sent ? mdst : OOB, 0, m);
    sent = t;
  };
  vm_wait<13>(); bar(); STAMP(1); FST(1, 0);
  rdx(0); rdk(0); rdv(0); rdx(1); rdk(1); rdv(1);
  SB();
  float s2 = 0.f;
  sfor<32>([&](auto nn) LAMBDA {
    constexpr int n = nn, j = n >> 2, s = n & 3;
    if (s == 2 && j < 7) {
      constexpr int cnt[7] = {11, 7, 11, 6, 6, 7, 3};
      FST(1, 2 * j + 1); vm_wait<cnt[j < 7 ? j : 0]>(); bar(); FST(1, 2 * j + 2);
    }
    acck = MFMA(ka[n % 3], xf[n], acck); SB();
    if (n + 2 < 32) rdx(n + 2);
    if (s < 2 && j < 6) cload(j + 2, 3 * s);
    if (n == 14 || n == 18 || n == 22) gldsc1(tvo, cx.ws, lstg + (j & 1) * 256);
    if (n == 26) gldsc4(mvo, cx.ws, lstg + 1024);
    SB();
    csk = MFMA16(sel, ka[n % 3], csk); SB();
    if (n + 2 < 32) rdk(n + 2);
    if (n == 18 || n == 22 || n == 26) send((j - 1) & 1, n == 18);
    if (j >= 6) cload(n - 24, 6);
    SB();
    accv = MFMA(va[n % 3], xf[n], accv); SB();
    if (s < 2 && j < 6) cload(j + 2, 3 * s + 1);
    if (n == 2) {
      const unsigned t = *(const unsigned*)(cx.B + 5 * 4096 + w * 1024 + lane * 4);
      const unsigned m = *(const unsigned*)(cx.B + 6 * 4096 + w * 1024 + lane * 8);
      N = __builtin_amdgcn_readlane(t, c) + 64;
      while (BALLOT(t - N < 64u || m - N < 64u) != 0) N += 64;
      stc1(rs, w == 0 && (lane == c || lane == 16 + 16 * cx.th + g) ? (int)tvo : OOB, 0, N);
    }
    if (n == 6) {
      const char* st = stg + r_tr;
      rf[0] = tr_read2(st, st + r_c); rf[1] = tr_read2(st + r_a, st + r_a + r_c);
      rf[2] = tr_read2(st + r_b, st + r_b + r_c); rf[3] = tr_read2(st + r_a + r_b, st + r_a + r_b + r_c);
    }
    if (n == 7) {
      stc(rs, r_dv, r_dst, rf[0]); stc(rs, r_dv, r_dst + r_d0, rf[1]); stc(rs, r_dv, r_dst + r_d1, rf[2]); stc(rs, r_dv, r_dst + r_d0 + r_d1, rf[3]);
    }
    SB();
    csv = MFMA16(sel, va[n % 3], csv); SB();
    if (n + 2 < 32) rdv(n + 2);
    SB();
    if (n == 0) { gram = MFMA(xf[0], xf[0], gram); s1v = MFMA16(sel, xf[0], s1v); SB(); }
    if (n < 31) { gram = MFMA(xf[n + 1], xf[n + 1], gram); SB(); }
    if (s < 2 && j < 6) cload(j + 2, 3 * s + 2);
    if (n == 7) {
      const unsigned o2 = r_half ? 0x3f803f80u : on1;
      const u32x4 sl = {o2, o2, o2, o2};
      f32x4 ca = {}, cb2 = {};
      ca = MFMA16(sl, rf[0], ca); cb2 = MFMA16(sl, rf[2], cb2); ca = MFMA16(sl, rf[1], ca); cb2 = MFMA16(sl, rf[3], cb2);
      stc1(rs, r_half ? tok * 32 + hk * 4 : (hk ? OOB : tok * 32), r_cs, __float_as_uint(r_half ? (hk ? cb2[0] : ca[0]) : ca[0] + cb2[0]));
    }
    if (n == 8) {
      const char* ap = cx.B + 7 * 4096 + w * 1024 + hk * 512 + q4 * 32 + (2 * ch + (p4 >> 1)) * 8;
      aphi[0] = tr_readc<128>(ap); aphi[1] = tr_readc<128>(ap + 256);
    }
    if (n == 11) {
      const bool on = ((lane >> 2) & 1) == hk;
#pragma unroll
      for (int t = 0; t < 4; t++) { aphi[0][t] = on ? aphi[0][t] : 0u; aphi[1][t] = on ? aphi[1][t] : 0u; }
      asm volatile("" : "+v"(aphi[0]), "+v"(aphi[1]));
    }
    if (n == 31) {
      float t8[8], t4[4];
#pragma unroll
      for (int r = 0; r < 8; r++) t8[r] = lsel(0xFFFF0000FFFF0000ull, gram[8 + r], gram[r]);
#pragma unroll
      for (int r = 0; r < 4; r++) t4[r] = lsel(0xFF00FF00FF00FF00ull, t8[4 + r], t8[r]);
      const float u0 = lsel(0xCCCCCCCCCCCCCCCCull, t4[2], t4[0]), u1 = lsel(0xCCCCCCCCCCCCCCCCull, t4[3], t4[1]);
      s2 = lsel(0xAAAAAAAAAAAAAAAAull, u1, u0);
      s2 = xhalf_sum(((tok >> 2) & 1) == hk ? s2 : 0.f);
    }
    SB();
    if (n < 31) { s1v = MFMA16(sel, xf[n + 1], s1v); SB(); }
    if (n < 31) asm volatile("" : "+a"(acck), "+a"(accv), "+a"(csk), "+a"(csv), "+a"(s1v), "+a"(gram));
    SB();
  });
  STAMP(2); FST(1, 15);
  const float mean = s1v[0] * (1.f / 512.f);
  xf[32] = aug_tok(mean, 0.f, hk);
  csw[1] = cs_pack(csk[0]);
  acck = MFMA(aug_w(csw[1], 0u), xf[32], acck);
  csw[2] = cs_pack(csv[0]);
  accv = MFMA(aug_w(csw[2], 0u), xf[32], accv);
  const float rstd = __builtin_amdgcn_rsqf(fmaxf(s2 * (1.f / 512.f) - mean * mean, 0.f) + 1e-5f);
  const float sc = rstd * LOG2E;
  f32x16 p = {};
  { u32x4 b0, b1; pack16(acck, b0, b1); p = MFMA(aphi[0], b0, p); p = MFMA(aphi[1], b1, p); }
  SB();
  const int js = ((g >> 1) - rot) & 7;
  u32x4 wf0, wf1;
  { const char* wsrc = wk + (w & 1) * 1024 + js * 8192 + (g & 1) * 4096; wf0 = tr_readc<256>(wsrc); wf1 = tr_readc<256>(wsrc + 512); }
  char* tab = cx.B + 32768 + 2048;
  if (w == 0) {
    vm_wait<7>();
    u32x2 m = *(const u32x2*)(stg + 1024 + lane * 8);
    while (true) {
      const bool valid = m[0] == N;
      if (BALLOT(valid && m[1] != sent && lane < 48) != 0) {
        const u32x2 r = {m[1], N};
        stc2(rs, valid && m[1] != sent ? mdst : OOB, 0, r);
        sent = valid ? m[1] : sent;
        COUNT(40);
      }
      if (BALLOT(!valid && lane < 48) == 0) break;
      if (cx.budget == 0) break;
      cx.budget--;
      COUNT(41);
      asm volatile("" ::: "memory");
      m = ldc2(rs, lane * 8, MBX + (g * 16 + c) * 512);
    }
    *(unsigned*)(tab + lane * 4) = lane < 48 ? m[1] : 0u;
    if (lane == 0) *(int*)(tab + 256) = cx.budget;
  }
  lds_barrier();
  STAMP(3);
  {
    const int e = lane >> 1;
    cx.N = RFL(*(const unsigned*)(tab + c * 4));
    cx.budget = RFL(*(const int*)(tab + 256));
    cx.tk_kv = *(const unsigned*)(tab + (lane < 16 ? 16 + 16 * cx.th + lane : 63) * 4);
    cx.tk_oh = *(const unsigned*)(tab + (lane < 8 ? 4 * w + e : 63) * 4);
    cx.tk_x = *(const unsigned*)(tab + (lane < 16 ? 8 * (e >> 2) + 4 * (w >> 1) + (e & 3) : 63) * 4);
  }
  const int kvb = KVA + c * KV_C;
  char* dA = cx.A + w * 16384;
  const int sA = WT_1 + c * W1_C + w * 16384;
  {
    Softmax sm; u32x4 phk[4];
    sfor<29>([&](auto ii) LAMBDA {
      constexpr int i = ii;
      sm.step(i, p, sc, phk);
      if (i < 10) dma<(i < 10 ? i : 0)>(rs, dA, cx.vo, sA);
      if (i == 2) kv_write_v(cx, accv, rstd);
      SB();
    });
    STAMP(4);
    kv_tail<11>(cx, phk, kvb);
  }
  STAMP(5);
  {
    f32x16 accq = {}; f32x4 csq = {};
    u32x4 qa[3];
    auto rdq = [&](int n) LAMBDA { qa[n % 3] = tr_readc<256>(wq + (n >> 1) * 2048 + (n & 1) * 512); };
    rdq(0); rdq(1); rdq(2);
    const int wd = WT_QKV + c * WQ_C;
    unsigned pl = 0;
    SB();
    sfor<32>([&](auto nn) LAMBDA {
      constexpr int n = nn;
      accq = MFMA(qa[n % 3], xf[n], accq); SB();
      if (n == 0 && w == 2) { const char* wsrc = wq + js * 4096 + (g & 1) * 2048; wf0 = tr_readc<256>(wsrc); wf1 = tr_readc<256>(wsrc + 512); }
      if (n == 3) {
        const int so = w < 2 ? wd + g * 4096 + w * 1024 : w == 2 ? wd + 65536 + g * 2048 : OOB;
        stc(rs, cx.vo, so, wf0); stc(rs, cx.vo, so + (w < 2 ? 2048 : 1024), wf1);
      }
      if (n < 6) dma<(n < 6 ? 10 + n : 0)>(rs, dA, cx.vo, sA);
      else if (n - 6 < NW2_A) w2f[n - 6] = ldck(rs, cx.vo, WT_2 + (cx.cb * 4 + w) * 57344, n - 6);
      if (n == 10) pl = ldc1(rs, cx.fv16, kvb + KV_F + w * 128);
      if (n == 20) { poll_wait<16>(cx, pl, kvb + KV_F + w * 128, cx.tk_kv, 7, 32); STAMP(6); kv_load(cx, kin, kvb); }
      SB();
      csq = MFMA16(sel, qa[n % 3], csq); SB();
      if (n + 3 < 32) rdq(n + 3);
      asm volatile("" : "+a"(accq), "+a"(csq));
      SB();
    });
    csw[0] = cs_pack(csq[0]);
    accq = MFMA(aug_w(csw[0], 0u), xf[32], accq);
    u32x4 b0, b1; pack16(accq, b0, b1);
    f32x16 pq = {}; pq = MFMA(aphi[0], b0, pq); pq = MFMA(aphi[1], b1, pq);
    Softmax sq;
#pragma unroll
    for (int i = 0; i < 29; i++) sq.step(i, pq, sc, phq);
  }
}

DEVI void red_send(Cx& cx, const f32x16& a, int j) {
  char* dst = cx.C + (cx.w ^ j) * INBOX + (j - 1) * 4096 + cx.vo;
#pragma unroll
  for (int m = 0; m < 4; m++) { f32x4 v = {a[4 * m], a[4 * m + 1], a[4 * m + 2], a[4 * m + 3]}; *(f32x4*)(dst + m * 1024) = v; }
}
DEVI void red_recv(Cx& cx, const f32x16& own, float (&xr)[16]) {
  f32x4 v[12];
#pragma unroll
  for (int k = 0; k < 12; k++) v[k] = *(const f32x4*)(cx.C + cx.w * INBOX + ((k % 3) * 4 + k / 3) * 1024 + cx.vo);
#pragma unroll
  for (int m = 0; m < 4; m++)
#pragma unroll
    for (int e = 0; e < 4; e++) xr[4 * m + e] = (own[4 * m + e] + v[3 * m][e]) + (v[3 * m + 1][e] + v[3 * m + 2][e]);
}
DEVI void unpack16(const u32x4& a, const u32x4& b, float (&x)[16]) {
#pragma unroll
  for (int t = 0; t < 4; t++) { x[2 * t] = bflo(a[t]); x[2 * t + 1] = bfhi(a[t]); x[8 + 2 * t] = bflo(b[t]); x[9 + 2 * t] = bfhi(b[t]); }
}
DEVI void publish_x(Cx& cx, float (&xr)[16], int xb, int sb, int fb) {
  u32x4 f0, f1; pack16(xr, f0, f1);
  stg(cx.rs, cx.vo, xb + cx.xo, f0);
  stg(cx.rs, cx.vo + 1024, xb + cx.xo, f1);
  SB();
  unpack16(f0, f1, xr);
  float s1 = xr[0] + xr[1], s2 = xr[0] * xr[0], t1 = xr[2] + xr[3], t2 = xr[1] * xr[1];
#pragma unroll
  for (int r = 2; r < 16; r += 2) { s2 = fmaf(xr[r], xr[r], s2); t2 = fmaf(xr[r + 1], xr[r + 1], t2); if (r >= 4) { s1 += xr[r]; t1 += xr[r + 1]; } }
  s1 = xhalf_sum(s1 + t1); s2 = xhalf_sum(s2 + t2);
  const u32x2 st = {__float_as_uint(s1), __float_as_uint(s2)};
  stg2(cx.rs, cx.hk ? OOB : cx.vo, sb + cx.so, st);
  put_final_g(cx, fb + cx.fxo, sum4(sum4(cx.hk ? 0u : st[0] + st[1], f0), f1));
}
DEVI void park(Cx& cx, const float (&xr)[16]) {
  char* p = cx.C + cx.w * INBOX + C_PARK + cx.vo;
#pragma unroll
  for (int m = 0; m < 4; m++) { f32x4 v = {xr[4 * m], xr[4 * m + 1], xr[4 * m + 2], xr[4 * m + 3]}; *(f32x4*)(p + m * 1024) = v; }
}
DEVI f32x16 unpark(Cx& cx) {
  const char* p = cx.C + cx.w * INBOX + C_PARK + cx.vo;
  f32x16 r;
#pragma unroll
  for (int m = 0; m < 4; m++) { f32x4 v = *(const f32x4*)(p + m * 1024); r[4 * m] = v[0]; r[4 * m + 1] = v[1]; r[4 * m + 2] = v[2]; r[4 * m + 3] = v[3]; }
  return r;
}
DEVI f32x16 tovec(const float (&x)[16]) {
  f32x16 r;
#pragma unroll
  for (int e = 0; e < 16; e++) r[e] = x[e];
  return r;
}
#define UNLIKELY(x) __builtin_expect(!!(x), 0)

template <class AUG, class WIN>
DEVI void xpass(Cx& cx, int xb, int sb, int fb, u32x4 (&xf)[33], f32x16& a0, f32x16& a1, int sti, AUG aug, WIN win) {
  const rsrc_t rs = cx.rs;
  xb += cx.w * 32768; sb += cx.w * 4096; fb += cx.w * 128;
  poll_wait<16>(cx, ldc1(rs, cx.fv16, fb), fb, cx.tk_x, 7, 45);
  STAMP(sti); if (sti != 15) FST(sti == 11 ? 4 : 5, 0);
  u32x4 st[4], ra[3], rb[3], w0, w1; u32x2 fc;
  auto ld = [&](int q) LAMBDA {
    if (q < 24) xf[q] = ldck(rs, cx.vo, xb, q);
    else if (q < 28) st[q - 24] = ldck(rs, cx.vo, sb, q - 24);
    else if (q < 36) xf[q - 4] = ldck(rs, cx.vo, xb, q - 4);
    else fc = ldc2(rs, cx.fv16, fb);
  };
  auto stats = [&]() LAMBDA {
    float s1 = 0.f, s2 = 0.f;
#pragma unroll
    for (int k = 0; k < 4; k++) { s1 += __uint_as_float(st[k][0]) + __uint_as_float(st[k][2]); s2 += __uint_as_float(st[k][1]) + __uint_as_float(st[k][3]); }
    const float mean = xhalf_sum(s1) * (1.f / 512.f);
    aug(mean, fmaxf(xhalf_sum(s2) * (1.f / 512.f) - mean * mean, 0.f), w0, w1);
  };
  const char* pa = cx.A + cx.vo;
#pragma unroll
  for (int q = 0; q < 10; q++) ld(q);
  SB();
  bar();
#pragma unroll
  for (int n = 0; n < 3; n++) { ra[n] = *(const u32x4*)(pa + n * 2048); rb[n] = *(const u32x4*)(pa + n * 2048 + 1024); }
  SB();
  unsigned sum = 0;
  const f32x16 zero = {};
  sfor<32>([&](auto nn) LAMBDA {
    constexpr int n = nn;
    if (n % 8 == 0 && sti != 15) FST(sti == 11 ? 4 : 5, 1 + n / 8);
    a0 = MFMA(ra[n % 3], xf[n], n ? a0 : zero); SB();
    if (n + 3 < 32) ra[n % 3] = *(const u32x4*)(pa + (n + 3) * 2048);
    if (n + 10 < 37) ld(n + 10); else win(ic<(n < 27 ? 0 : 2 * (n - 27))>());
    if (n == 25) sum = sum4(sum4(sum, st[0]), st[1]);
    if (n == 26) sum = sum4(sum4(sum, st[2]), st[3]);
    SB();
    a1 = MFMA(rb[n % 3], xf[n], n ? a1 : zero); SB();
    if (n + 3 < 32) rb[n % 3] = *(const u32x4*)(pa + (n + 3) * 2048 + 1024);
    sum = sum4(sum, xf[n]);
    if (n == 27) stats();
    if (n >= 27) win(ic<(n < 27 ? 0 : 2 * (n - 27) + 1)>());
    SB();
    if (n == 29) { a0 = MFMA(w0, xf[32], a0); a1 = MFMA(w1, xf[32], a1); SB(); }
  });
  if (sti != 15) FST(sti == 11 ? 4 : 5, 5);
  asm volatile("" : "+a"(a0), "+a"(a1));
  if (UNLIKELY(!validate<16>(cx, sum, fc, fb, cx.tk_x, 46))) {
    do {
      asm volatile("" ::: "memory");
#pragma unroll
      for (int q = 0; q < 37; q++) ld(q);
      sum = 0;
#pragma unroll
      for (int q = 0; q < 32; q++) sum = sum4(sum, xf[q]);
#pragma unroll
      for (int q = 0; q < 4; q++) sum = sum4(sum, st[q]);
    } while (!validate<16>(cx, sum, fc, fb, cx.tk_x, 46));
    stats();
    a0 = MFMA(w0, xf[32], zero); a1 = MFMA(w1, xf[32], zero);
#pragma unroll
    for (int n = 0; n < 32; n++) {
      a0 = MFMA(*(const u32x4*)(pa + n * 2048), xf[n], a0); a1 = MFMA(*(const u32x4*)(pa + n * 2048 + 1024), xf[n], a1);
      SB();
    }
  }
}

struct Gelu {
  float iA, c0, c1, a[4], u[2], e[2], r[2], g;
  u32x4 f; unsigned sum;
  template <int K0, int K1, int K, int H>
  DEVI void tick(Cx& cx, const f32x16& lo, const f32x16& hi, int hb) {
    if (H == 0) {
      if (K >= K0 && K < K1) { a[K & 3] = (K & 16) ? hi[K & 15] : lo[K & 15]; u[K & 1] = fmaf(a[K & 3] * a[K & 3], c1, c0); }
      if (K - 2 >= K0 && K - 2 < K1) r[K & 1] = __builtin_amdgcn_rcpf(fmaf(e[K & 1], iA, iA));
    } else {
      if (K - 1 >= K0 && K - 1 < K1) e[(K - 1) & 1] = __builtin_amdgcn_exp2f(a[(K - 1) & 3] * u[(K - 1) & 1]);
      if (K - 3 >= K0 && K - 3 < K1) {
        constexpr int v = K - 3;
        const float y = a[v & 3] * r[v & 1];
        if (v & 1) f[(v >> 1) & 3] = pk2(g, y); else { g = y; asm volatile("" : "+v"(g)); }
        if ((v & 7) == 7) { stg(cx.rs, cx.vo, hb + (v >> 3) * 8192, f); sum = sum4(sum, f); }
      }
    }
  }
};

#define NW2_B 6
#define NW2_C 10
#define NW2_D 32

template <int L>
DEVI void layer(Cx& cx, const u16* __restrict__ x, const u16* __restrict__ b1, const u16* __restrict__ b2, u16* __restrict__ out,
                u32x4 (&phq)[4], const unsigned (&csw)[3], const u32x4 (&aphi)[2], KvIn& kin, u32x4 (&w2f)[56],
                unsigned (&csp)[4], unsigned (&b1p)[2]) {
  const int lane = cx.lane, w = cx.w, tok = cx.tok, hk = cx.hk, g = cx.g, c = cx.c;
  const rsrc_t rs = cx.rs;
  const int gl = cx.gb + L * G_L;
  const int kvb = KVA + L * KV_L + c * KV_C;
  const int w2b = WT_2 + (cx.cb * 4 + w) * 57344;
  const f32x16 zero = {};
  constexpr int SO = 14 * L;
  u32x4 wof[16], xa, xb2, ba, bb;
  const int col = 64 * cx.cb + 32 * (w & 1) + 16 * hk;
  const size_t xrow = (size_t)(128 * g + 64 * cx.th + 32 * (w >> 1) + tok) * 512 + col;
  if (L == 1) {
    u32x4 xf[33]; f32x16 acck, accv; float rstd;
    xpass(cx, cx.gb + G_X2, cx.gb + G_ST2, cx.gb + G_FX2, xf, acck, accv, 15,
          [&](float mean, float var, u32x4& w0, u32x4& w1) LAMBDA {
            rstd = __builtin_amdgcn_rsqf(var + 1e-5f);
            xf[32] = aug_tok(mean, 0.f, hk); w0 = aug_w(csw[1], 0u); w1 = aug_w(csw[2], 0u);
          },
          [&](auto) LAMBDA {});
    STAMP(16);
    const float sc = rstd * LOG2E;
    f32x16 p;
    { u32x4 b0, b1; pack16(acck, b0, b1); p = MFMA(aphi[0], b0, zero); p = MFMA(aphi[1], b1, p); }
    kv_write_v(cx, accv, rstd);
    SB();
    {
      Softmax sm; u32x4 phk[4];
      sfor<29>([&](auto ii) LAMBDA {
        constexpr int i = ii;
        sm.step(i, p, sc, phk);
        SB();
      });
      STAMP(17);
      kv_tail<-1>(cx, phk, kvb);
    }
    STAMP(18);
    f32x16 accq = MFMA(aug_w(csw[0], 0u), xf[32], zero);
    u32x4 qa[3];
    const char* pq = cx.B + cx.vo;
#pragma unroll
    for (int n = 0; n < 3; n++) qa[n] = *(const u32x4*)(pq + n * 1024);
    char* dA = cx.A + w * 16384;
    const int sA = WT_1 + c * W1_C + w * 16384;
    SB();
    sfor<32>([&](auto nn) LAMBDA {
      constexpr int n = nn;
      accq = MFMA(qa[n % 3], xf[n], accq); SB();
      if (n + 3 < 32) qa[n % 3] = *(const u32x4*)(pq + (n + 3) * 1024);
      if ((n & 1) == 0) dma<n / 2>(rs, dA, cx.vo, sA);
      SB();
    });
    unsigned pl = ldc1(rs, cx.fv16, kvb + KV_F + w * 128);
    u32x4 b0, b1; pack16(accq, b0, b1);
    f32x16 pq2 = MFMA(aphi[0], b0, zero); pq2 = MFMA(aphi[1], b1, pq2);
    Softmax sq;
    sfor<29>([&](auto ii) LAMBDA {
      constexpr int i = ii;
      sq.step(i, pq2, sc, phq);
      if (i == 12) { SB(); poll_wait<16>(cx, pl, kvb + KV_F + w * 128, cx.tk_kv, 7, 34); STAMP(19); kv_load(cx, kin, kvb); }
      SB();
    });
  }
  asm volatile("" : "+v"(phq[0]), "+v"(phq[1]), "+v"(phq[2]), "+v"(phq[3]));
#pragma unroll
  for (int s = 0; s < 16; s++) wof[s] = ldck(rs, cx.vo, WT_O + (cx.cb * 4 + w) * 16384, s);
  if (L == 0) { xa = *(const u32x4*)(x + xrow); xb2 = *(const u32x4*)(x + xrow + 8); }
  ba = *(const u32x4*)(b2 + col); bb = *(const u32x4*)(b2 + col + 8);
  u32x4 cs[4]; u16 b1v[4];
  if (L == 0) {
#pragma unroll
    for (int k = 0; k < 4; k++) cs[k] = ldc(rs, tok * 32 + hk * 16 + k * 1024, CS1 + c * 4096);
    const int pi = 16 * ((tok >> 2) & 1) + (tok & 3) + 4 * (tok >> 3), ph = 8 * ((tok >> 2) & 1) + (tok & 3) + 4 * ((tok >> 3) & 1);
    const u16* bp = b1 + 112 * c;
    b1v[0] = bp[pi]; b1v[1] = bp[32 + pi]; b1v[2] = bp[64 + pi]; b1v[3] = bp[96 + ph];
#pragma unroll
    for (int k = NW2_A; k < NW2_B; k++) w2f[k] = ldck(rs, cx.vo, w2b, k);
  }
  SB();
  STAMP(7 + SO);
  {
    const int G = lane >> 4, d = lane & 15;
    u32x4 idn;
#pragma unroll
    for (int t = 0; t < 4; t++) { const int k = 8 * (G & 1) + 2 * t; idn[t] = k == d ? 0x3f80u : k + 1 == d ? 0x3f800000u : 0u; }
    f32x4 kv; unsigned sum;
    auto red = [&]() LAMBDA {
      f32x4 k0 = {}, k1 = {}; sum = 0;
#pragma unroll
      for (int m = 0; m < 8; m += 2) {
        k0 = MFMA16(idn, kin.pa[m], k0); k1 = MFMA16(idn, kin.pa[m + 1], k1);
        sum = sum4(sum4(sum, kin.pa[m]), kin.pa[m + 1]);
      }
      sum = sum4(sum, kin.pz); kv = k0 + k1;
    };
    red();
    asm volatile("" : "+v"(kv));
    if (UNLIKELY(!validate<16>(cx, sum, kin.fc, kvb + KV_F + w * 128, cx.tk_kv, 35))) {
      do { asm volatile("" ::: "memory"); kv_load(cx, kin, kvb); red(); } while (!validate<16>(cx, sum, kin.fc, kvb + KV_F + w * 128, cx.tk_kv, 35));
    }
    char* img = cx.C + w * INBOX + C_IMG;
    { const u32x2 v = {pk2(kv[0], kv[1]), pk2(kv[2], kv[3])}; *(u32x2*)(img + d * 32 + G * 8) = v; }
    *(u32x4*)(img + 512 + cx.vo) = kin.pz;
    lds_barrier();
    const int hp = (tok >> 2) & 1, dd = (tok & 3) + 4 * (tok >> 3);
    const char* ap = cx.C + (hp == hk ? 2 * hp * INBOX + C_IMG + dd * 32 : C_ZERO);
    const int zo = hp == hk ? 512 : 0;
    u32x4 a[4], az[4];
#pragma unroll
    for (int s4 = 0; s4 < 4; s4++) az[s4] = *(const u32x4*)(ap + zo + (s4 >> 1) * INBOX + (s4 & 1) * 16);
#pragma unroll
    for (int s4 = 0; s4 < 4; s4++) a[s4] = *(const u32x4*)(ap + (s4 >> 1) * INBOX + (s4 & 1) * 16);
    if (L == 0) stc1(rs, w == 0 && lane < 16 ? lane * 8192 : OOB, MBX + c * 512 + (48 + g) * 8, cx.tk_kv + 1);
    SB();
    f32x16 num = {}, den = {};
#pragma unroll
    for (int s4 = 0; s4 < 4; s4++) den = MFMA(az[s4], phq[s4], den);
#pragma unroll
    for (int s4 = 0; s4 < 4; s4++) num = MFMA(a[s4], phq[s4], num);
    float ds[4];
#pragma unroll
    for (int r = 0; r < 4; r++) ds[r] = (den[r] + den[4 + r]) + (den[8 + r] + den[12 + r]);
    const float inv = __builtin_amdgcn_rcpf(((ds[0] + ds[1]) + (ds[2] + ds[3])) + 1e-6f);
    float ov[16];
#pragma unroll
    for (int r = 0; r < 16; r++) ov[r] = num[r] * inv;
    u32x4 f0, f1; pack16(ov, f0, f1);
    stg(rs, cx.vo, gl + G_O + cx.oo, f0);
    stg(rs, cx.vo + 1024, gl + G_O + cx.oo, f1);
    put_final_g(cx, gl + G_FO + cx.fo, sum4(sum4(0u, f0), f1));
    STAMP(8 + SO);
  }
  float xr[16];
  {
    const int ob = gl + G_O + (cx.th * 4 + w) * 16384, fb = gl + G_FO + (cx.th * 4 + w) * 128;
    f32x16 acc[4];
    if (L == 0) {
      unpack16(xa, xb2, xr); acc[0] = tovec(xr);
#pragma unroll
      for (int k = 0; k < 4; k++) {
        const f32x4 v = __builtin_bit_cast(f32x4, cs[k]);
        csp[k] = cs_pack(xhalf_sum((v[0] + v[1]) + (v[2] + v[3])));
      }
      b1p[0] = b1v[0] | ((unsigned)b1v[1] << 16); b1p[1] = b1v[2] | ((unsigned)b1v[3] << 16);
    } else acc[0] = unpark(cx);
    SB();
    poll_wait<8>(cx, ldc1(rs, cx.fv8, fb), fb, cx.tk_oh, 7, 36);
    STAMP(9 + SO);
    u32x4 of[16]; u32x2 fc;
#pragma unroll
    for (int k = 0; k < 10; k++) of[k ^ 8] = ldck(rs, cx.vo, ob, k ^ 8);
    SB();
    lds_barrier();
    char* dB = cx.B + w * 12288;
    const int sB = WT_1 + c * W1_C + 65536 + w * 12288;
    unsigned sum = 0;
    sfor<32>([&](auto nn) LAMBDA {
      constexpr int n = nn, s = (n >> 1) & 7, j = (n < 16 ? 3 : 1) - (n & 1);
      acc[j] = MFMA(wof[2 * s + (j & 1)], of[8 * (j >> 1) + s], s == 0 && j ? zero : acc[j]); SB();
      if (n < 6) of[(n + 10) ^ 8] = ldck(rs, cx.vo, ob, (n + 10) ^ 8);
      if (n == 6) fc = ldc2(rs, cx.fv8, fb);
      if (n >= 7 && n < 19) dma<(n >= 7 && n < 19 ? n - 7 : 0)>(rs, dB, cx.vo, sB);
      if (L == 0 && n >= 19 && NW2_B + n - 19 < NW2_C) w2f[NW2_B + n - 19] = ldck(rs, cx.vo, w2b, NW2_B + n - 19);
      if (n & 1) sum = sum4(sum, of[8 * (j >> 1) + s]);
      if (n == 18) red_send(cx, acc[3], 3);
      if (n == 22) red_send(cx, acc[2], 2);
      SB();
    });
    red_send(cx, acc[1], 1);
    asm volatile("" : "+a"(acc[0]));
    if (UNLIKELY(!validate<8>(cx, sum, fc, fb, cx.tk_oh, 37))) {
      do {
        asm volatile("" ::: "memory");
        sum = 0;
#pragma unroll
        for (int k = 0; k < 16; k++) { of[k] = ldck(rs, cx.vo, ob, k); sum = sum4(sum, of[k]); }
        fc = ldc2(rs, cx.fv8, fb);
      } while (!validate<8>(cx, sum, fc, fb, cx.tk_oh, 37));
      if (L == 0) { xa = *(const u32x4*)(x + xrow); xb2 = *(const u32x4*)(x + xrow + 8); }
      else { xa = ldc(rs, cx.vo, cx.gb + G_X2 + cx.xo); xb2 = ldc(rs, cx.vo + 1024, cx.gb + G_X2 + cx.xo); }
      unpack16(xa, xb2, xr); acc[0] = tovec(xr); acc[1] = zero; acc[2] = zero; acc[3] = zero;
#pragma unroll
      for (int n = 0; n < 32; n++) acc[n >> 3] = MFMA(wof[2 * (n & 7) + ((n >> 3) & 1)], of[8 * (n >> 4) + (n & 7)], acc[n >> 3]);
      red_send(cx, acc[1], 1); red_send(cx, acc[2], 2); red_send(cx, acc[3], 3);
    }
    lds_barrier();
    red_recv(cx, acc[0], xr);
    publish_x(cx, xr, gl + G_X1, gl + G_ST1, gl + G_FX1);
    float bv[16]; unpack16(ba, bb, bv);
#pragma unroll
    for (int r = 0; r < 16; r++) xr[r] += bv[r];
    park(cx, xr);
    STAMP(10 + SO);
  }
  {
    u32x4 xf[33]; f32x16 h0, h1, h2, h3;
    Gelu ge;
    xpass(cx, gl + G_X1, gl + G_ST1, gl + G_FX1, xf, h0, h1, 11 + SO,
          [&](float mean, float var, u32x4& w0, u32x4& w1) LAMBDA {
            const float r1 = __builtin_amdgcn_rsqf(var + 1e-5f);
            const float A = r1 * __builtin_amdgcn_rsqf(var * r1 * r1 + 1e-5f);
            ge.iA = __builtin_amdgcn_rcpf(A);
            xf[32] = aug_tok(mean, ge.iA, hk);
            const float k0 = -2.f * LOG2E * 0.7978845608028654f, k1 = k0 * 0.044715f;
            ge.c0 = k0 * A; ge.c1 = k1 * A * A * A;
            w0 = aug_w(csp[0], b1p[0]); w1 = aug_w(csp[1], b1p[0] >> 16);
          },
          [&](auto ii) LAMBDA { constexpr int k = NW2_C + ii.v; if (L == 0 && k < NW2_D) w2f[k] = ldck(rs, cx.vo, w2b, k); });
    STAMP(12 + SO);
    const int hs = gl + G_H + cx.ho, hfo = gl + G_FH + cx.fo;
    const int hb = gl + G_H + (cx.th * 4 + w) * 57344, fb = gl + G_FH + (cx.th * 4 + w) * 128;
    ge.sum = 0;
    h2 = MFMA(aug_w(csp[2], b1p[1]), xf[32], zero); h3 = MFMA(aug_w(csp[3], b1p[1] >> 16), xf[32], zero);
    const char* pb = cx.B + cx.vo;
    const char* ph = cx.B + 1024 + (hk * 16 + (lane & 15)) * 16;
    u32x4 ra[3], rb[3], hf[56]; u32x2 fc;
#pragma unroll
    for (int n = 0; n < 3; n++) { ra[n] = *(const u32x4*)(pb + n * 1536); rb[n] = *(const u32x4*)(ph + n * 1536); }
    SB();
    unsigned pl = 0, sum = 0;
    sfor<64>([&](auto gg) LAMBDA {
      constexpr int G = gg, n = G >> 1;
      if (G % 8 == 0) FST(4 + L, 6 + G / 8);
      if (G & 1) { h3 = MFMA(rb[n % 3], xf[n], h3); SB(); if (n + 3 < 32) rb[n % 3] = *(const u32x4*)(ph + (n + 3) * 1536); }
      else       { h2 = MFMA(ra[n % 3], xf[n], h2); SB(); if (n + 3 < 32) ra[n % 3] = *(const u32x4*)(pb + (n + 3) * 1536); }
      if (G < 35) { ge.tick<0, 32, G, 0>(cx, h0, h1, hs); ge.tick<0, 32, G, 1>(cx, h0, h1, hs); }
      if (L == 0 && G % 2 == 0 && NW2_C + 10 + G / 2 < NW2_D) w2f[NW2_C + 10 + G / 2] = ldck(rs, cx.vo, w2b, NW2_C + 10 + G / 2);
      if (G == 18) put_flag_g(cx, hfo, 1);
      if (G == 34) put_flag_g(cx, hfo, 3);
      if (G == 26) pl = ldc1(rs, cx.fv8, fb);
      if (G == 40) { SB(); poll_wait<8>(cx, pl, fb, cx.tk_oh, 1, 38); STAMP(13 + SO); }
      if (G >= 40 && G < 56) { constexpr int k = 2 * ((G - 40) & 7) + (G >= 48); hf[k] = ldck(rs, cx.vo, hb, k); }
      if (G == 48) pl = ldc1(rs, cx.fv8, fb);
      SB();
    });
    f32x16 acc[4];
    FST(4 + L, 14);
    acc[0] = unpark(cx);
    poll_wait<8>(cx, pl, fb, cx.tk_oh, 3, 39);
    lds_barrier();
    STAMP(14 + SO); FST(4 + L, 15);
    unsigned msg = 0;
    char* dA = cx.A + w * 16384; char* dB = cx.B + w * 8192;
    const int sA = WT_QKV + c * WQ_C + w * 16384, sB = WT_QKV + c * WQ_C + 65536 + w * 8192;
    sfor<112>([&](auto gg) LAMBDA {
      constexpr int G = gg;
      constexpr int q = G < 64 ? (G >> 1) & 15 : G < 96 ? G >> 2 : 24 + (G & 3);
      constexpr int j = G < 64 ? (G & 1) + 2 * (G >> 5) : G < 96 ? G & 3 : 3 - ((G >> 2) & 3);
      if (G % 8 == 0) FST(2 + L, G / 8);
      acc[j] = MFMA(w2f[2 * q + (j & 1)], hf[2 * q + (j >> 1)], q == 0 && j ? zero : acc[j]); SB();
      if (G < 27) { ge.tick<32, 56, 32 + G, 0>(cx, h2, h3, hs); ge.tick<32, 56, 32 + G, 1>(cx, h2, h3, hs); }
      if (G == 18) put_flag_g(cx, hfo, 5);
      if (G == 27) put_final_g(cx, hfo, ge.sum);
      if (G < 16) { constexpr int k = 16 + 2 * (G & 7) + (G >= 8); hf[k] = ldck(rs, cx.vo, hb, k); }
      if (L == 0 && G >= 16 && NW2_D + G - 16 < 56) w2f[NW2_D + G - 16] = ldck(rs, cx.vo, w2b, NW2_D + G - 16);
      if (L == 1 && G >= 69 && G < 77) w2f[G - 21] = ldck(rs, cx.vo, w2b, G - 21);
      if (G == 32) pl = ldc1(rs, cx.fv8, fb);
      if (G == 42) { SB(); poll_wait<8>(cx, pl, fb, cx.tk_oh, 5, 42); }
      if (G >= 42 && G < 58) hf[G - 10] = ldck(rs, cx.vo, hb, G - 10);
      if (G == 48) pl = ldc1(rs, cx.fv8, fb);
      if (G == 60) { SB(); poll_wait<8>(cx, pl, fb, cx.tk_oh, 7, 43); }
      if (G >= 60 && G < 68) hf[G - 12] = ldck(rs, cx.vo, hb, G - 12);
      if (G == 68) fc = ldc2(rs, cx.fv8, fb);
      if (L == 0) {
        if (G == 59) msg = ldc1(rs, cx.fv16, MBX + (g * 16 + c) * 512 + 384);
        if (G == 72) { SB(); mail_wait(cx, msg, MBX + (g * 16 + c) * 512 + 384, cx.N + 1); }
        if (G >= 72 && G < 88) dma<(G >= 72 && G < 88 ? G - 72 : 0)>(rs, dA, cx.vo, sA);
        if (G >= 88 && G < 96) dma<(G >= 88 && G < 96 ? G - 88 : 0)>(rs, dB, cx.vo, sB);
      }
      if (G < 96 ? (j & 1) : !(j & 1)) sum = sum4(sum, hf[2 * q + (j >> 1)]);
      if (G == 102) red_send(cx, acc[3], 3);
      if (G == 106) red_send(cx, acc[2], 2);
      if (G == 110) red_send(cx, acc[1], 1);
      SB();
    });
      FST(2 + L, 14);
    asm volatile("" : "+a"(acc[0]));
    if (UNLIKELY(!validate<8>(cx, sum, fc, fb, cx.tk_oh, 44))) {
      do {
        asm volatile("" ::: "memory");
        xa = ldc(rs, cx.vo, gl + G_X1 + cx.xo); xb2 = ldc(rs, cx.vo + 1024, gl + G_X1 + cx.xo);
        float bv[16]; unpack16(*(const u32x4*)(b2 + col), *(const u32x4*)(b2 + col + 8), bv);
        unpack16(xa, xb2, xr);
#pragma unroll
        for (int r = 0; r < 16; r++) acc[0][r] = xr[r] + bv[r];
        acc[1] = zero; acc[2] = zero; acc[3] = zero; sum = 0;
#pragma unroll
        for (int k = 0; k < 56; k++) {
          const u32x4 f = ldck(rs, cx.vo, hb, k);
          sum = sum4(sum, f);
          acc[2 * (k & 1)] = MFMA(w2f[k & ~1], f, acc[2 * (k & 1)]); acc[2 * (k & 1) + 1] = MFMA(w2f[k | 1], f, acc[2 * (k & 1) + 1]);
          if ((k & 7) == 7) SB();
        }
        fc = ldc2(rs, cx.fv8, fb);
      } while (!validate<8>(cx, sum, fc, fb, cx.tk_oh, 44));
      red_send(cx, acc[1], 1); red_send(cx, acc[2], 2); red_send(cx, acc[3], 3);
    }
    lds_barrier();
    FST(2 + L, 15);
    if (L == 0) {
      red_recv(cx, acc[0], xr);
      publish_x(cx, xr, cx.gb + G_X2, cx.gb + G_ST2, cx.gb + G_FX2);
      park(cx, xr);
      STAMP(20);
    } else {
      red_recv(cx, acc[0], xr);
      u32x4 f0, f1; pack16(xr, f0, f1);
      *(u32x4*)(out + xrow) = f0; *(u32x4*)(out + xrow + 8) = f1;
      STAMP(29);
    }
  }
}

__global__ __launch_bounds__(256, 1) void fused(const u16* __restrict__ x, const u16* __restrict__ wqkv, const u16* __restrict__ wphi,
                                                const u16* __restrict__ wo, const u16* __restrict__ w1, const u16* __restrict__ b1,
                                                const u16* __restrict__ w2, const u16* __restrict__ b2, char* ws, u16* __restrict__ out) {
  __shared__ __attribute__((aligned(16))) char ldsA[65536];
  __shared__ __attribute__((aligned(16))) char ldsB[49152];
  __shared__ __attribute__((aligned(16))) char ldsC[49152];
  Cx cx;
  STAMP(0); CSTAMP(30);
  cx.ws = ws; cx.A = ldsA; cx.B = ldsB; cx.C = ldsC;
  cx.lane = threadIdx.x & 63; cx.w = RFL(threadIdx.x >> 6);
  cx.tok = cx.lane & 31; cx.hk = cx.lane >> 5; cx.vo = cx.lane * 16; cx.budget = SPIN_MAX;
  { const int b = blockIdx.x, xx = b & 7, j = b >> 3; cx.g = 4 * (xx >> 1) + (j >> 3); cx.c = 8 * (xx & 1) + (j & 7); }
  cx.cb = 4 * (cx.c >> 3) + (cx.c & 3); cx.th = (cx.c >> 2) & 1;
  cx.rs = __builtin_amdgcn_make_buffer_rsrc(ws, 0, WS_USED, 0x00020000);
  cx.gb = GRP + cx.g * G_S;
  cx.fv16 = cx.lane < 16 ? cx.lane * 8 : OOB; cx.fv8 = cx.lane < 8 ? cx.lane * 8 : OOB; cx.f63 = cx.lane == 63 ? 0 : OOB;
  {
    const int w = cx.w, kq = cx.c >> 2, tx = (w & 1) ^ (kq >> 1), ln = (w >> 1) * 4 + kq, tb = 2 * cx.th + (w >> 1), blk = 2 * cx.cb + (w & 1);
    cx.oo = (ln * 16 + tx * 8 + (cx.c & 3) * 2) * 1024;
    cx.ho = (ln * 56 + (cx.c & 3) * 2 + tx) * 1024;
    cx.fo = ln * 128 + ((cx.c & 3) * 2 + (w & 1)) * 8;
    cx.xo = (tb * 32 + 2 * blk) * 1024;
    cx.so = tb * 4096 + cx.cb * 512 + (w & 1) * 8;
    cx.fxo = tb * 128 + blk * 8;
  }
  u32x4 phq[4], aphi[2], w2f[56]; unsigned csw[3], csp[4], b1p[2]; KvIn kin;
  stage1a(cx, x, wqkv, wphi, wo, w1, w2, phq, csw, aphi, kin, w2f);
  layer<0>(cx, x, b1, b2, out, phq, csw, aphi, kin, w2f, csp, b1p);
  asm volatile("" : "+v"(cx.lane), "+v"(cx.tok), "+v"(cx.hk), "+v"(cx.vo));
  asm volatile("" : "+v"(csp[0]), "+v"(csp[1]), "+v"(csp[2]), "+v"(csp[3]), "+v"(b1p[0]), "+v"(b1p[1]), "+v"(csw[0]), "+v"(csw[1]), "+v"(csw[2]));
#pragma unroll
  for (int k = 0; k < 48; k++) asm volatile("" : "+a"(w2f[k]));
  layer<1>(cx, x, b1, b2, out, phq, csw, aphi, kin, w2f, csp, b1p);
}

extern "C" void kernel_launch(void* const* d_in, const int* in_sizes, int n_in, void* d_out, int out_size, void* d_ws, size_t ws_size,
                              hipStream_t stream) {
  fused<<<256, 256, 0, stream>>>((const u16*)d_in[0], (const u16*)d_in[1], (const u16*)d_in[2], (const u16*)d_in[3], (const u16*)d_in[5],
                                 (const u16*)d_in[6], (const u16*)d_in[7], (const u16*)d_in[8], (char*)d_ws, (u16*)d_out);
}
```

```cpp
#include <hip/hip_runtime.h>
#include <stdint.h>

typedef unsigned short u16;
typedef __attribute__((ext_vector_type(8))) __bf16 bf16x8;
typedef __attribute__((ext_vector_type(2))) __bf16 bf16x2;
typedef __attribute__((ext_vector_type(16))) float f32x16;
typedef __attribute__((ext_vector_type(4))) float f32x4;
typedef __attribute__((ext_vector_type(2))) float f32x2;
typedef __attribute__((ext_vector_type(4))) unsigned u32x4;
typedef __attribute__((ext_vector_type(2))) unsigned u32x2;
typedef __attribute__((ext_vector_type(4))) short s16x4;
typedef __amdgpu_buffer_rsrc_t rsrc_t;
typedef __attribute__((address_space(3))) void* ldsp;

#define DEVI __device__ __forceinline__
#define LAMBDA __attribute__((always_inline))
#define MFMA(a, b, c) __builtin_amdgcn_mfma_f32_32x32x16_bf16(__builtin_bit_cast(bf16x8, a), __builtin_bit_cast(bf16x8, b), c, 0, 0, 0)
#define MFMA16(a, b, c) __builtin_amdgcn_mfma_f32_16x16x32_bf16(__builtin_bit_cast(bf16x8, a), __builtin_bit_cast(bf16x8, b), c, 0, 0, 0)
#define SB() __builtin_amdgcn_sched_barrier(0)
#define RFL(v) __builtin_amdgcn_readfirstlane(v)
#define BALLOT(p) __builtin_amdgcn_ballot_w64(p)

constexpr int WT_QKV = 0x0, WQ_C = 98304;
constexpr int WT_O   = 0x180000;
constexpr int WT_1   = 0x200000, W1_C = 114688;
constexpr int CS1    = 0x3C0000;
constexpr int WT_2   = 0x400000;
constexpr int MBX    = 0x5C0000;
constexpr int TOKR   = 0x5E0000;
constexpr int TOKC   = 0x5E1000;
constexpr int KVA    = 0x600000, KV_C = 0xC000, KV_L = 0xC0000, KV_F = 0x8000;
constexpr int GRP    = 0x800000, G_S = 0x200000, G_L = 0xC0000;
constexpr int G_O = 0, G_X1 = 0x20000, G_H = 0x40000, G_ST1 = 0xB0000, G_FO = 0xB4000, G_FH = 0xB6000, G_FX1 = 0xBE000;
constexpr int FS = 128, FHL = 0x2000;
constexpr int G_X2 = 0x180000, G_ST2 = 0x1A0000, G_FX2 = 0x1A4000;
constexpr int WS_USED = 0x2800000;
constexpr int OOB = 0x40000000;
#define STAMP_OFF 0x2800000
#define STAMP_N 64

constexpr int INBOX = 12288;
constexpr int C_V = 4352, C_IMG = 6656, C_ZERO = 7680, C_PARK = 8192;
constexpr float LOG2E = 1.4426950408889634f;
constexpr int SPIN_MAX = 1 << 16;

DEVI unsigned pk2(float a, float b) { f32x2 v = {a, b}; return __builtin_bit_cast(unsigned, __builtin_convertvector(v, bf16x2)); }
DEVI float bflo(unsigned u) { return __uint_as_float(u << 16); }
DEVI float bfhi(unsigned u) { return __uint_as_float(u & 0xffff0000u); }
DEVI void lds_barrier() { asm volatile("s_waitcnt lgkmcnt(0)\n\ts_barrier" ::: "memory"); }
DEVI void bar() { asm volatile("s_barrier" ::: "memory"); }
template <int N> DEVI void vm_wait() { asm volatile("s_waitcnt vmcnt(%0)" :: "n"(N) : "memory"); }
template <int OFF> DEVI void gldso(unsigned voff, const void* sbase, unsigned lds_base) {
  asm volatile("s_mov_b32 m0, %2\n\ts_nop 0\n\tglobal_load_lds_dwordx4 %0, %1 offset:%3" :: "v"(voff), "s"(sbase), "s"(lds_base), "n"(OFF) : "memory", "m0");
}
DEVI void gldsc4(unsigned voff, const void* sbase, unsigned lds_base) {
  asm volatile("s_mov_b32 m0, %2\n\ts_nop 0\n\tglobal_load_lds_dwordx4 %0, %1 sc1" :: "v"(voff), "s"(sbase), "s"(lds_base) : "memory", "m0");
}
DEVI void gldsc1(unsigned voff, const void* sbase, unsigned lds_base) {
  asm volatile("s_mov_b32 m0, %2\n\ts_nop 0\n\tglobal_load_lds_dword %0, %1 sc1" :: "v"(voff), "s"(sbase), "s"(lds_base) : "memory", "m0");
}
DEVI float xhalf_sum(float v) {
  auto t = __builtin_amdgcn_permlane32_swap(__float_as_uint(v), __float_as_uint(v), false, false);
  return __uint_as_float(t[0]) + __uint_as_float(t[1]);
}
DEVI u32x4 ldc(rsrc_t rs, int vo, int so) { return __builtin_amdgcn_raw_buffer_load_b128(rs, vo, so, 16); }
DEVI u32x2 ldc2(rsrc_t rs, int vo, int so) { return __builtin_amdgcn_raw_buffer_load_b64(rs, vo, so, 16); }
DEVI unsigned ldc1(rsrc_t rs, int vo, int so) { return __builtin_amdgcn_raw_buffer_load_b32(rs, vo, so, 16); }
DEVI void stc(rsrc_t rs, int vo, int so, u32x4 v) { __builtin_amdgcn_raw_buffer_store_b128(v, rs, vo, so, 16); }
DEVI void stc2(rsrc_t rs, int vo, int so, u32x2 v) { __builtin_amdgcn_raw_buffer_store_b64(v, rs, vo, so, 16); }
DEVI void stc1(rsrc_t rs, int vo, int so, unsigned v) { __builtin_amdgcn_raw_buffer_store_b32(v, rs, vo, so, 16); }
DEVI u32x4 ldck(rsrc_t rs, int vo, int so, int kb) { return ldc(rs, vo + (kb & 3) * 1024, so + (kb >> 2) * 4096); }
template <int KB> DEVI void dma(rsrc_t rs, char* dst, int vo, int so) {
  __builtin_amdgcn_raw_ptr_buffer_load_lds(rs, (ldsp)(dst + (KB >> 2) * 4096), 16, vo, so + (KB >> 2) * 4096, (KB & 3) * 1024, 16);
}
DEVI float lsel(unsigned long long m, float t, float f) { float r; asm("v_cndmask_b32_e64 %0, %1, %2, %3" : "=v"(r) : "v"(f), "v"(t), "s"(m)); return r; }
DEVI float fmax3(float a, float b, float c) { return fmaxf(fmaxf(a, b), c); }
DEVI float fmin3(float a, float b, float c) { return fminf(fminf(a, b), c); }
DEVI unsigned sum4(unsigned s, const u32x4& f) { s = (s + f[0] + f[1]) + f[2] + f[3]; asm volatile("" : "+v"(s)); return s; }
DEVI unsigned wsum(unsigned v) {
  v += __builtin_amdgcn_update_dpp(0u, v, 0x111, 0xf, 0xf, false);
  v += __builtin_amdgcn_update_dpp(0u, v, 0x112, 0xf, 0xf, false);
  v += __builtin_amdgcn_update_dpp(0u, v, 0x114, 0xf, 0xf, false);
  v += __builtin_amdgcn_update_dpp(0u, v, 0x118, 0xf, 0xf, false);
  v += __builtin_amdgcn_update_dpp(0u, v, 0x142, 0xa, 0xf, false);
  v += __builtin_amdgcn_update_dpp(0u, v, 0x143, 0xc, 0xf, false);
  return v;
}

#define FINE 0
#define FST(sel, i)
#define STAMP(i)
#define CSTAMP(i)
#define COUNT(i)
#define BUD(i)

template <class T>
DEVI void pack16(const T& v, u32x4& a, u32x4& b) {
#pragma unroll
  for (int i = 0; i < 4; i++) { a[i] = pk2(v[2 * i], v[2 * i + 1]); b[i] = pk2(v[8 + 2 * i], v[9 + 2 * i]); }
}
template <int I> struct ic { static constexpr int v = I; constexpr operator int() const { return I; } };
template <int... I> struct iseq {};
template <int N, int... I> struct mkseq : mkseq<N - 1, N - 1, I...> {};
template <int... I> struct mkseq<0, I...> { typedef iseq<I...> type; };
template <class F, int... I> DEVI void sfor_impl(F& f, iseq<I...>) { (f(ic<I>()), ...); }
template <int N, class F> DEVI void sfor(F f) { sfor_impl(f, typename mkseq<N>::type()); }

struct Softmax {
  float mx, mn, sp, sn, ep[16], en[16];
  DEVI void step(int i, const f32x16& p, float sc, u32x4 (&ph)[4]) {
    if (i == 0) { mx = fmax3(p[0], p[1], p[2]); mn = fmin3(p[0], p[1], p[2]); mx = fmax3(mx, p[3], p[4]); mn = fmin3(mn, p[3], p[4]); }
    else if (i < 3) { int b = 5 + 4 * (i - 1); mx = fmax3(mx, p[b], p[b + 1]); mn = fmin3(mn, p[b], p[b + 1]); mx = fmax3(mx, p[b + 2], p[b + 3]); mn = fmin3(mn, p[b + 2], p[b + 3]); }
    else if (i == 3) { mx = fmax3(mx, p[13], p[14]); mn = fmin3(mn, p[13], p[14]); mx = fmaxf(mx, p[15]) * -sc; mn = fminf(mn, p[15]) * sc; }
    else if (i < 20) {
      int r = i - 4;
      ep[r] = __builtin_amdgcn_exp2f(fmaf(p[r], sc, mx)); en[r] = __builtin_amdgcn_exp2f(fmaf(p[r], -sc, mn));
      if (r == 2) { sp = ep[0] + ep[1]; sn = en[0] + en[1]; } else if (r > 2) { sp += ep[r - 1]; sn += en[r - 1]; }
    } else if (i == 20) { sp = __builtin_amdgcn_rcpf(sp + ep[15]); sn = __builtin_amdgcn_rcpf(sn + en[15]); }
    else if (i < 29) {
      int t = i - 21;
      ph[t >> 2][t & 3] = pk2(ep[2 * t] * sp, ep[2 * t + 1] * sp);
      ph[2 + (t >> 2)][t & 3] = pk2(en[2 * t] * sn, en[2 * t + 1] * sn);
    }
  }
};

template <int C> DEVI u32x4 tr_readc(const char* p) {
  typedef __attribute__((address_space(3))) s16x4* lp;
  s16x4 a = __builtin_amdgcn_ds_read_tr16_b64_v4i16((lp)(p));
  s16x4 b = __builtin_amdgcn_ds_read_tr16_b64_v4i16((lp)(p + C));
  u32x2 ua = __builtin_bit_cast(u32x2, a), ub = __builtin_bit_cast(u32x2, b);
  u32x4 r = {ua[0], ua[1], ub[0], ub[1]};
  return r;
}
DEVI u32x4 tr_read2(const char* p, const char* p2) {
  typedef __attribute__((address_space(3))) s16x4* lp;
  s16x4 a = __builtin_amdgcn_ds_read_tr16_b64_v4i16((lp)(p));
  s16x4 b = __builtin_amdgcn_ds_read_tr16_b64_v4i16((lp)(p2));
  u32x2 ua = __builtin_bit_cast(u32x2, a), ub = __builtin_bit_cast(u32x2, b);
  u32x4 r = {ua[0], ua[1], ub[0], ub[1]};
  return r;
}

DEVI u32x4 aug_tok(float mean, float ia, int hk) {
  unsigned mh = pk2(-mean, ia);
  unsigned ml = pk2(-mean - bflo(mh), ia - bfhi(mh));
  u32x4 t;
  t[0] = __builtin_amdgcn_perm(ml, mh, 0x05040100u);
  t[1] = mh;
  t[2] = ml >> 16;
  t[3] = 0;
  if (hk) { t[0] = 0; t[1] = 0; t[2] = 0; }
  return t;
}
DEVI unsigned cs_pack(float cs) { unsigned h = pk2(cs, 0.f); return pk2(cs, cs - bflo(h)); }
DEVI u32x4 aug_w(unsigned csp, unsigned b) {
  u32x4 t;
  t[0] = __builtin_amdgcn_perm(csp, csp, 0x01000100u);
  t[1] = __builtin_amdgcn_perm(b, csp, 0x05040302u);
  t[2] = b & 0xffffu;
  t[3] = 0;
  return t;
}

struct Cx {
  rsrc_t rs; char* ws; char *A, *B, *C;
  int lane, w, tok, hk, g, c, cb, th, vo, gb;
  int fv16, fv8, fvz, f63, oo, ho, fo, xo, so, fxo;
  unsigned N, tk_kv, tk_oh, tk_x;
  int budget;
};

DEVI void put_flag(Cx& cx, int so, int i) { u32x2 v = {cx.N + i, cx.N + i}; stc2(cx.rs, cx.f63, so, v); }
DEVI void put_final(Cx& cx, int so, unsigned sum) { u32x2 v = {cx.N + 7, cx.N - wsum(sum)}; stc2(cx.rs, cx.f63, so, v); }
template <int NP> DEVI void poll_wait(Cx& cx, unsigned a, int so, unsigned tk, int i) {
  unsigned t; int s;
  asm volatile("v_sub_u32 %0, %3, %4\n\t"
               "v_cmp_ne_u32 vcc, %9, %0\n\t"
               "s_and_b32 %1, vcc_lo, %8\n\t"
               "s_cbranch_scc0 2f\n"
               "1:\n\t"
               "s_cmp_eq_u32 %2, 0\n\t"
               "s_cbranch_scc1 2f\n\t"
               "buffer_load_dword %0, %5, %6, %7 offen sc1\n\t"
               "s_sub_u32 %2, %2, 1\n\t"
               "s_waitcnt vmcnt(0)\n\t"
               "v_sub_u32 %0, %0, %4\n\t"
               "v_cmp_ne_u32 vcc, %9, %0\n\t"
               "s_and_b32 %1, vcc_lo, %8\n\t"
               "s_cbranch_scc1 1b\n"
               "2:"
               : "=&v"(t), "=&s"(s), "+s"(cx.budget)
               : "v"(a), "v"(tk), "v"(NP == 16 ? cx.fv16 : cx.fv8), "s"(cx.rs), "s"(so), "n"((1 << NP) - 1), "n"(i)
               : "vcc", "scc", "memory");
}
template <int NP> DEVI unsigned final_wait(Cx& cx, u32x2 fc, int so, unsigned tk) {
  int s; unsigned f = fc[0], k = fc[1];
  asm volatile("v_sub_u32 %0, %0, %4\n\t"
               "v_cmp_ne_u32 vcc, 7, %0\n\t"
               "s_and_b32 %2, vcc_lo, %8\n\t"
               "s_cbranch_scc0 2f\n"
               "1:\n\t"
               "s_cmp_eq_u32 %3, 0\n\t"
               "s_cbranch_scc1 2f\n\t"
               "buffer_load_dword %0, %5, %6, %7 offen sc1\n\t"
               "s_sub_u32 %3, %3, 1\n\t"
               "s_waitcnt vmcnt(0)\n\t"
               "v_sub_u32 %0, %0, %4\n\t"
               "v_cmp_ne_u32 vcc, 7, %0\n\t"
               "s_and_b32 %2, vcc_lo, %8\n\t"
               "s_cbranch_scc1 1b\n\t"
               "buffer_load_dword %1, %5, %6, %7 offen offset:4 sc1\n\t"
               "s_waitcnt vmcnt(0)\n"
               "2:"
               : "+v"(f), "+v"(k), "=&s"(s), "+s"(cx.budget)
               : "v"(tk), "v"(NP == 16 ? cx.fv16 : cx.fv8), "s"(cx.rs), "s"(so), "n"((1 << NP) - 1)
               : "vcc", "scc", "memory");
  return k - tk;
}
DEVI void kv_wait(Cx& cx, u32x4& q, int so) {
  int s; unsigned f = q[2];
  asm volatile("v_sub_u32 %0, %0, %4\n\t"
               "v_cmp_ne_u32 vcc, 7, %0\n\t"
               "s_and_b32 %2, vcc_hi, 0xffff\n\t"
               "s_cbranch_scc0 2f\n"
               "1:\n\t"
               "s_cmp_eq_u32 %3, 0\n\t"
               "s_cbranch_scc1 2f\n\t"
               "buffer_load_dword %0, %5, %6, %7 offen offset:8 sc1\n\t"
               "s_sub_u32 %3, %3, 1\n\t"
               "s_waitcnt vmcnt(0)\n\t"
               "v_sub_u32 %0, %0, %4\n\t"
               "v_cmp_ne_u32 vcc, 7, %0\n\t"
               "s_and_b32 %2, vcc_hi, 0xffff\n\t"
               "s_cbranch_scc1 1b\n\t"
               "buffer_load_dwordx4 %1, %5, %6, %7 offen sc1\n\t"
               "s_waitcnt vmcnt(0)\n"
               "2:"
               : "+v"(f), "+v"(q), "=&s"(s), "+s"(cx.budget)
               : "v"(cx.tk_kv), "v"(cx.fvz), "s"(cx.rs), "s"(so)
               : "vcc", "scc", "memory");
}
DEVI bool validate(Cx& cx, unsigned sum) {
  const unsigned r = __builtin_amdgcn_readlane(wsum(sum), 63);
  if (r == 0 || cx.budget < 64) return true;
  cx.budget -= 64;
  return false;
}
#define D_O 8
#define D_X1 6
#define D_X2 2
#define PRE 1
#define TOUCH 1
template <int N> DEVI void nap() { if (N > 0) { SB(); __builtin_amdgcn_s_sleep(N); SB(); } }
DEVI unsigned usel(unsigned long long m, unsigned t, unsigned f) { unsigned r; asm("v_cndmask_b32_e64 %0, %1, %2, %3" : "=v"(r) : "v"(f), "v"(t), "s"(m)); return r; }
DEVI void mail_wait(Cx& cx, unsigned a, int mvo, int so, unsigned v) {
  int s;
  asm volatile("v_cmp_ne_u32 vcc, %3, %0\n\t"
               "s_and_b32 %1, vcc_lo, 0xffff\n\t"
               "s_cbranch_scc0 2f\n"
               "1:\n\t"
               "s_cmp_eq_u32 %2, 0\n\t"
               "s_cbranch_scc1 2f\n\t"
               "buffer_load_dword %0, %4, %5, %6 offen sc1\n\t"
               "s_sub_u32 %2, %2, 1\n\t"
               "s_waitcnt vmcnt(0)\n\t"
               "v_cmp_ne_u32 vcc, %3, %0\n\t"
               "s_and_b32 %1, vcc_lo, 0xffff\n\t"
               "s_cbranch_scc1 1b\n"
               "2:"
               : "+v"(a), "=&s"(s), "+s"(cx.budget)
               : "s"(v), "v"(mvo), "s"(cx.rs), "s"(so)
               : "vcc", "scc", "memory");
}
DEVI void kv_write_v(Cx& cx, const f32x16& accv, float rstd) {
  float vn[16];
#pragma unroll
  for (int r = 0; r < 16; r++) vn[r] = accv[r] * rstd;
  u32x4 v0, v1; pack16(vn, v0, v1);
  char* p = cx.C + 2 * cx.hk * INBOX + C_V + (32 * cx.w + cx.tok) * 16;
  *(u32x4*)p = v0; *(u32x4*)(p + INBOX + 128) = v1;
}
template <int VMW>
DEVI void kv_tail(Cx& cx, const u32x4 (&phk)[4], int kvb) {
  char* p = cx.C + 2 * cx.hk * INBOX + (32 * cx.w + cx.tok) * 16;
  *(u32x4*)p = phk[0]; *(u32x4*)(p + 2176) = phk[1]; *(u32x4*)(p + INBOX) = phk[2]; *(u32x4*)(p + INBOX + 2176) = phk[3];
  const int G = cx.lane >> 4, d = cx.lane & 15, q4 = d >> 2, p4 = cx.lane & 3;
  const int ro = (4 * G + q4) * 16 + (p4 & 1) * 8;
  const char* pa = cx.C + cx.w * INBOX + (p4 >> 1) * 2176 + ro;
  const char* pb = cx.C + ((cx.w & 2) + (p4 >> 1)) * INBOX + C_V + (p4 >> 1) * 128 + ro;
  if (VMW >= 0) vm_wait<(VMW >= 0 ? VMW : 0)>();
  lds_barrier();
  u32x4 a[4], b[4];
#pragma unroll
  for (int t = 0; t < 4; t++) { a[t] = tr_readc<256>(pa + t * 512); b[t] = tr_readc<256>(pb + t * 512); }
  const u32x4 ones = {0x3f803f80u, 0x3f803f80u, 0x3f803f80u, 0x3f803f80u};
  f32x4 kv = {}, z = {};
#pragma unroll
  for (int t = 0; t < 4; t++) { kv = MFMA16(a[t], b[t], kv); z = MFMA16(a[t], ones, z); }
  const u32x2 k2 = {pk2(kv[0], kv[1]), pk2(kv[2], kv[3])}, z2 = {pk2(z[0], z[1]), pk2(z[2], z[3])};
  const int e = cx.w * 16 + cx.g;
  stc2(cx.rs, (G >> 1) * 256 + d * 16 + (G & 1) * 8, kvb + e * 512, k2);
  const unsigned cs = -7u - wsum(k2[0] + k2[1] + usel(0x1000900010001000ull, z2[0] + z2[1], 0u));
  const u32x2 zl = {usel(1ull << 63, cx.N + 7, z2[0]), usel(1ull << 63, cs, z2[1])};
  stc2(cx.rs, d >= 12 ? (d - 12) * 32 + G * 8 : OOB, kvb + KV_F + e * FS, zl);
}
struct KvIn { u32x4 pa[8], pz; };
DEVI u32x4 kv_poll(Cx& cx, int kvb) { return ldc(cx.rs, cx.fvz, kvb + KV_F + cx.w * 16 * FS); }
DEVI void kv_load(Cx& cx, KvIn& k, int kvb) {
#pragma unroll
  for (int m = 0; m < 8; m++) k.pa[m] = ldck(cx.rs, cx.vo, kvb + cx.w * 8192, m);
}

#define NW2_A 4

DEVI void stage1a(Cx& cx, const u16* __restrict__ x, const u16* __restrict__ wqkv, const u16* __restrict__ wphi,
                  const u16* __restrict__ wo, const u16* __restrict__ w1, const u16* __restrict__ w2,
                  u32x4 (&phq)[4], unsigned (&csw)[3], u32x4 (&aphi)[2], KvIn& kin, u32x4 (&w2f)[56]) {
  const int lane = cx.lane, w = cx.w, tok = cx.tok, hk = cx.hk, g = cx.g, c = cx.c;
  const int rot = (c + 2 * g) & 7;
  const rsrc_t rs = cx.rs;
  const unsigned lA = (unsigned)(size_t)cx.A, lB = (unsigned)(size_t)cx.B, lC = (unsigned)(size_t)cx.C;
  char* xs = cx.C + w * INBOX;
  char* stg = cx.B + 32768 + w * 4096;
  const unsigned lstg = lB + 32768 + w * 4096;
  unsigned vx[4];
#pragma unroll
  for (int i = 0; i < 4; i++) vx[i] = (8 * i + (lane >> 3)) * 1024 + (((lane & 7) ^ ((4 * i + (lane >> 4)) & 7)) * 16) - i * 1024;
  const char* xb = (const char*)(x + (size_t)(128 * g + 32 * w) * 512);
  const unsigned xl = lC + w * INBOX;
  const unsigned wvo = (lane >> 2) * 3072 + (lane & 3) * 16;
  const char* wb = (const char*)(wqkv + (size_t)(16 * w) * 1536 + 32 * c);
  auto cload = [&](int j, int i) LAMBDA {
    const int cj = (j + rot) & 7;
    if (i < 4) {
      const char* sb = xb + cj * 128;
      if (i == 0) gldso<0>(vx[0], sb, xl + (j % 3) * 4096);
      if (i == 1) gldso<1024>(vx[1], sb, xl + (j % 3) * 4096);
      if (i == 2) gldso<2048>(vx[2], sb, xl + (j % 3) * 4096);
      if (i == 3) gldso<3072>(vx[3], sb, xl + (j % 3) * 4096);
    } else {
      const char* sb = wb + cj * (64 * 3072);
      if (i == 4) gldso<0>(wvo + 1024, sb, lA + j * 8192 + w * 2048);
      if (i == 5) gldso<1024>(wvo + 1024, sb, lA + j * 8192 + w * 2048);
      if (i == 6) gldso<0>(wvo, sb, lB + j * 4096 + w * 1024);
    }
  };
#pragma unroll
  for (int i = 0; i < 6; i++) cload(0, i);
  const unsigned tvo = lane < 16 ? TOKR + g * 128 + lane * 4 : TOKC + cx.cb * 128 + ((lane - 16) & 31) * 4;
  const unsigned mvo = MBX + (g * 16 + c) * 512 + (lane & 31) * 16;
  gldsc1(tvo, cx.ws, lB + 5 * 4096 + w * 1024);
  gldsc4(mvo, cx.ws, lB + 6 * 4096 + w * 1024);
#pragma unroll
  for (int i = 0; i < 6; i++) cload(1, i);
  const int ch = (lane >> 4) & 1, q4 = (lane & 15) >> 2, p4 = lane & 3;
  const int cperm = (16 * (p4 & 1) + 4 * (2 * ch + (p4 >> 1))) * 2;
  int r_tr, r_a, r_b, r_c, r_dst, r_d0, r_d1, r_cs = OOB; unsigned r_dv = lane * 16; bool r_half = false;
  {
    const int lj = g * 4 + w, cb = cx.cb, th = cx.th;
    const u16* src; unsigned v0; int s0, s1;
    if (lj < 24) {
      const int bb = lj >> 3, m = lj & 7;
      src = w1 + (size_t)(64 * m) * 1792 + 112 * c + 32 * bb;
      v0 = (lane >> 2) * 3584 + (lane & 3) * 16; s0 = 16 * 3584; s1 = 32 * 3584;
      r_tr = (16 * hk + q4) * 64 + cperm; r_a = 512; r_b = 2048; r_c = 256;
      r_d0 = bb < 2 ? 2048 : 1536; r_d1 = 2 * r_d0;
      r_dst = WT_1 + c * W1_C + (bb < 2 ? bb * 1024 : 65536) + 4 * m * r_d0;
      r_cs = CS1 + (c * 4 + bb) * 1024 + m * 4;
    } else if (lj < 28) {
      const int m4 = lj - 24;
      src = w1 + (size_t)(128 * m4) * 1792 + 112 * c + 96;
      v0 = (lane >> 1) * 3584 + (lane & 1) * 16; s0 = 32 * 3584; s1 = 64 * 3584;
      r_tr = (8 * ch + 16 * hk + q4) * 32 + (8 * (p4 & 1) + 4 * (p4 >> 1)) * 2; r_a = 1024; r_b = 2048; r_c = 128;
      r_dst = WT_1 + c * W1_C + 65536 + 8 * m4 * 1536 + 1024; r_d0 = 3072; r_d1 = 6144;
      r_dv = ch * 1536 + (hk * 16 + (lane & 15)) * 16;
      r_cs = CS1 + (c * 4 + 3) * 1024 + m4 * 8; r_half = true;
    } else {
      v0 = (lane >> 3) * 1024 + (lane & 7) * 16; s0 = 8192; s1 = 16384;
      r_tr = (16 * hk + q4) * 128 + cperm; r_b = 1024; r_c = 512; r_d0 = 1024;
      int kq;
      if (lj < 36) {
        const int rg = 8 * th + lj - 28;
        kq = rg >> 2;
        src = wo + (size_t)(32 * rg) * 512 + 64 * cb;
        r_dst = WT_O + ((cb * 4 + kq) * 8 + ((2 * rg) & 7)) * 2048; r_d1 = 2048;
      } else if (lj < 60) {
        const int u = lj - 36, cc = 8 * th + (u & 7), blk = u >> 3;
        kq = cc >> 2;
        src = w2 + (size_t)(112 * cc + 32 * blk) * 512 + 64 * cb;
        r_dst = WT_2 + ((cb * 4 + kq) * 28 + 8 * blk + (cc & 3)) * 2048; r_d1 = 8192;
      } else {
        const int cc = 8 * th + 2 * (lj - 60);
        kq = cc >> 2;
        src = w2 + (size_t)(112 * cc + 96) * 512 + 64 * cb; s1 = 112 * 1024;
        r_tr = (8 * hk + q4) * 128 + cperm; r_b = 2048;
        r_dst = WT_2 + ((cb * 4 + kq) * 28 + 24 + (cc & 3)) * 2048; r_d1 = 2048;
      }
      r_tr += (kq & 1) * 64; r_a = (kq & 1) ? -64 : 64;
    }
    r_dst = RFL(r_dst); r_d0 = RFL(r_d0); r_d1 = RFL(r_d1); r_cs = RFL(r_cs);
    gldso<0>(v0, src, lstg); gldso<1024>(v0 + s0 - 1024, src, lstg); gldso<2048>(v0 + s1 - 2048, src, lstg); gldso<3072>(v0 + s0 + s1 - 3072, src, lstg);
  }
  gldso<0>(lane * 16, wphi + c * 512, lB + 7 * 4096 + w * 1024);
  const int xro = tok * 128 + (((2 * hk) ^ ((tok >> 1) & 7)) * 16);
  const char* wk = cx.A + hk * 2048 + q4 * 64 + cperm;
  const char* wq = cx.B + hk * 1024 + q4 * 64 + cperm;
  const unsigned on1 = (ch == ((lane >> 2) & 1)) ? 0x3f803f80u : 0u;
  const u32x4 sel = {on1, on1, on1, on1};
  const int mdst = w != 0 || lane >= 48 ? OOB
                 : lane < 16 ? MBX + (g * 16 + lane) * 512 + c * 8
                             : MBX + (((lane - 16) & 15) * 16 + ((c & ~4) | (((lane - 16) >> 4) << 2))) * 512 + (16 + g + 16 * cx.th) * 8;
  unsigned N = 0, sent = 0;
  u32x4 xf[33], ka[3], va[3], rf[4];
  f32x16 acck = {}, accv = {}, gram = {};
  f32x4 csk = {}, csv = {}, s1v = {};
  auto rdx = [&](int n) LAMBDA { xf[n] = *(const u32x4*)(xs + ((n >> 2) % 3) * 4096 + (xro ^ ((4 * ((n >> 1) & 1) + (n & 1)) * 16))); };
  auto rdk = [&](int n) LAMBDA { ka[n % 3] = tr_readc<256>(wk + (n >> 1) * 4096 + (n & 1) * 512); };
  auto rdv = [&](int n) LAMBDA { va[n % 3] = tr_readc<256>(wk + 1024 + (n >> 1) * 4096 + (n & 1) * 512); };
  auto send = [&](int slot, bool first) LAMBDA {
    const unsigned t = *(const unsigned*)(stg + slot * 256 + lane * 4);
    const u32x2 m = {t, N};
    stc2(rs, first || t != sent ? mdst : OOB, 0, m);
    sent = t;
  };
  vm_wait<13>(); bar(); STAMP(1); FST(1, 0);
  rdx(0); rdk(0); rdv(0); rdx(1); rdk(1); rdv(1);
  SB();
  float s2 = 0.f;
  sfor<32>([&](auto nn) LAMBDA {
    constexpr int n = nn, j = n >> 2, s = n & 3;
    if (s == 2 && j < 7) {
      constexpr int cnt[7] = {11, 7, 11, 6, 6, 7, 3};
      FST(1, 2 * j + 1); vm_wait<cnt[j < 7 ? j : 0]>(); bar(); FST(1, 2 * j + 2);
    }
    acck = MFMA(ka[n % 3], xf[n], acck); SB();
    if (n + 2 < 32) rdx(n + 2);
    if (s < 2 && j < 6) cload(j + 2, 3 * s);
    if (n == 14 || n == 18 || n == 22) gldsc1(tvo, cx.ws, lstg + (j & 1) * 256);
    if (n == 26) gldsc4(mvo, cx.ws, lstg + 1024);
    SB();
    csk = MFMA16(sel, ka[n % 3], csk); SB();
    if (n + 2 < 32) rdk(n + 2);
    if (n == 18 || n == 22 || n == 26) send((j - 1) & 1, n == 18);
    if (j >= 6) cload(n - 24, 6);
    SB();
    accv = MFMA(va[n % 3], xf[n], accv); SB();
    if (s < 2 && j < 6) cload(j + 2, 3 * s + 1);
    if (n == 2) {
      const unsigned t = *(const unsigned*)(cx.B + 5 * 4096 + w * 1024 + lane * 4);
      const unsigned m = *(const unsigned*)(cx.B + 6 * 4096 + w * 1024 + lane * 8);
      N = __builtin_amdgcn_readlane(t, c) + 64;
      while (BALLOT(t - N < 64u || m - N < 64u) != 0) N += 64;
      stc1(rs, w == 0 && (lane == c || lane == 16 + 16 * cx.th + g) ? (int)tvo : OOB, 0, N);
    }
    if (n == 6) {
      const char* st = stg + r_tr;
      rf[0] = tr_read2(st, st + r_c); rf[1] = tr_read2(st + r_a, st + r_a + r_c);
      rf[2] = tr_read2(st + r_b, st + r_b + r_c); rf[3] = tr_read2(st + r_a + r_b, st + r_a + r_b + r_c);
    }
    if (n == 7) {
      stc(rs, r_dv, r_dst, rf[0]); stc(rs, r_dv, r_dst + r_d0, rf[1]); stc(rs, r_dv, r_dst + r_d1, rf[2]); stc(rs, r_dv, r_dst + r_d0 + r_d1, rf[3]);
    }
    SB();
    csv = MFMA16(sel, va[n % 3], csv); SB();
    if (n + 2 < 32) rdv(n + 2);
    SB();
    if (n == 0) { gram = MFMA(xf[0], xf[0], gram); s1v = MFMA16(sel, xf[0], s1v); SB(); }
    if (n < 31) { gram = MFMA(xf[n + 1], xf[n + 1], gram); SB(); }
    if (s < 2 && j < 6) cload(j + 2, 3 * s + 2);
    if (n == 7) {
      const unsigned o2 = r_half ? 0x3f803f80u : on1;
      const u32x4 sl = {o2, o2, o2, o2};
      f32x4 ca = {}, cb2 = {};
      ca = MFMA16(sl, rf[0], ca); cb2 = MFMA16(sl, rf[2], cb2); ca = MFMA16(sl, rf[1], ca); cb2 = MFMA16(sl, rf[3], cb2);
      stc1(rs, r_half ? tok * 32 + hk * 4 : (hk ? OOB : tok * 32), r_cs, __float_as_uint(r_half ? (hk ? cb2[0] : ca[0]) : ca[0] + cb2[0]));
    }
    if (n == 8) {
      const char* ap = cx.B + 7 * 4096 + w * 1024 + hk * 512 + q4 * 32 + (2 * ch + (p4 >> 1)) * 8;
      aphi[0] = tr_readc<128>(ap); aphi[1] = tr_readc<128>(ap + 256);
    }
    if (n == 11) {
      const bool on = ((lane >> 2) & 1) == hk;
#pragma unroll
      for (int t = 0; t < 4; t++) { aphi[0][t] = on ? aphi[0][t] : 0u; aphi[1][t] = on ? aphi[1][t] : 0u; }
      asm volatile("" : "+v"(aphi[0]), "+v"(aphi[1]));
    }
    if (n == 31) {
      float t8[8], t4[4];
#pragma unroll
      for (int r = 0; r < 8; r++) t8[r] = lsel(0xFFFF0000FFFF0000ull, gram[8 + r], gram[r]);
#pragma unroll
      for (int r = 0; r < 4; r++) t4[r] = lsel(0xFF00FF00FF00FF00ull, t8[4 + r], t8[r]);
      const float u0 = lsel(0xCCCCCCCCCCCCCCCCull, t4[2], t4[0]), u1 = lsel(0xCCCCCCCCCCCCCCCCull, t4[3], t4[1]);
      s2 = lsel(0xAAAAAAAAAAAAAAAAull, u1, u0);
      s2 = xhalf_sum(((tok >> 2) & 1) == hk ? s2 : 0.f);
    }
    SB();
    if (n < 31) { s1v = MFMA16(sel, xf[n + 1], s1v); SB(); }
    if (n < 31) asm volatile("" : "+a"(acck), "+a"(accv), "+a"(csk), "+a"(csv), "+a"(s1v), "+a"(gram));
    SB();
  });
  STAMP(2); FST(1, 15);
  const float mean = s1v[0] * (1.f / 512.f);
  xf[32] = aug_tok(mean, 0.f, hk);
  csw[1] = cs_pack(csk[0]);
  acck = MFMA(aug_w(csw[1], 0u), xf[32], acck);
  csw[2] = cs_pack(csv[0]);
  accv = MFMA(aug_w(csw[2], 0u), xf[32], accv);
  const float rstd = __builtin_amdgcn_rsqf(fmaxf(s2 * (1.f / 512.f) - mean * mean, 0.f) + 1e-5f);
  const float sc = rstd * LOG2E;
  f32x16 p = {};
  { u32x4 b0, b1; pack16(acck, b0, b1); p = MFMA(aphi[0], b0, p); p = MFMA(aphi[1], b1, p); }
  SB();
  const int js = ((g >> 1) - rot) & 7;
  u32x4 wf0, wf1;
  { const char* wsrc = wk + (w & 1) * 1024 + js * 8192 + (g & 1) * 4096; wf0 = tr_readc<256>(wsrc); wf1 = tr_readc<256>(wsrc + 512); }
  char* tab = cx.B + 32768 + 2048;
  if (w == 0) {
    vm_wait<7>();
    u32x2 m = *(const u32x2*)(stg + 1024 + lane * 8);
    while (true) {
      const bool valid = m[0] == N;
      if (BALLOT(valid && m[1] != sent && lane < 48) != 0) {
        const u32x2 r = {m[1], N};
        stc2(rs, valid && m[1] != sent ? mdst : OOB, 0, r);
        sent = valid ? m[1] : sent;
        COUNT(40);
      }
      if (BALLOT(!valid && lane < 48) == 0) break;
      if (cx.budget == 0) break;
      cx.budget--;
      COUNT(41);
      asm volatile("" ::: "memory");
      m = ldc2(rs, lane * 8, MBX + (g * 16 + c) * 512);
    }
    *(unsigned*)(tab + lane * 4) = lane < 48 ? m[1] : 0u;
    if (lane == 0) *(int*)(tab + 256) = cx.budget;
  }
  lds_barrier();
  STAMP(3);
  {
    const int e = lane >> 1;
    cx.N = RFL(*(const unsigned*)(tab + c * 4));
    cx.budget = RFL(*(const int*)(tab + 256));
    cx.tk_kv = *(const unsigned*)(tab + ((lane >> 4) == 2 ? 16 * cx.th + lane - 16 : 63) * 4);
    cx.tk_oh = *(const unsigned*)(tab + (lane < 8 ? 4 * w + e : 63) * 4);
    cx.tk_x = *(const unsigned*)(tab + (lane < 16 ? 8 * (e >> 2) + 4 * (w >> 1) + (e & 3) : 63) * 4);
  }
  const int kvb = KVA + c * KV_C;
  if (TOUCH) {
    const int t = lane >> 3;
    const u32x4 v = {cx.N, cx.N, cx.N, cx.N};
    const int o = (t < 5 ? G_FO + t * FHL + cx.fo : t == 5 ? G_FX1 + cx.fxo : t == 6 ? G_FX2 + cx.fxo : kvb + KV_L + KV_F + (w * 16 + g) * FS - cx.gb) + (lane & 7) * 16 + cx.gb;
    stc(rs, o, 0, v);
    stc(rs, t < 6 ? o : OOB, G_L, v);
  }
  char* dA = cx.A + w * 16384;
  const int sA = WT_1 + c * W1_C + w * 16384;
  {
    Softmax sm; u32x4 phk[4];
    sfor<29>([&](auto ii) LAMBDA {
      constexpr int i = ii;
      sm.step(i, p, sc, phk);
      if (i < 10) dma<(i < 10 ? i : 0)>(rs, dA, cx.vo, sA);
      if (i == 2) kv_write_v(cx, accv, rstd);
      SB();
    });
    STAMP(4);
    kv_tail<(TOUCH ? 12 : 10)>(cx, phk, kvb);
  }
  STAMP(5);
  {
    f32x16 accq = {}; f32x4 csq = {};
    u32x4 qa[3];
    auto rdq = [&](int n) LAMBDA { qa[n % 3] = tr_readc<256>(wq + (n >> 1) * 2048 + (n & 1) * 512); };
    rdq(0); rdq(1); rdq(2);
    const int wd = WT_QKV + c * WQ_C;
    SB();
    sfor<32>([&](auto nn) LAMBDA {
      constexpr int n = nn;
      accq = MFMA(qa[n % 3], xf[n], accq); SB();
      if (n == 0 && w == 2) { const char* wsrc = wq + js * 4096 + (g & 1) * 2048; wf0 = tr_readc<256>(wsrc); wf1 = tr_readc<256>(wsrc + 512); }
      if (n == 3) {
        const int so = w < 2 ? wd + g * 4096 + w * 1024 : w == 2 ? wd + 65536 + g * 2048 : OOB;
        stc(rs, cx.vo, so, wf0); stc(rs, cx.vo, so + (w < 2 ? 2048 : 1024), wf1);
      }
      if (n < 6) dma<(n < 6 ? 10 + n : 0)>(rs, dA, cx.vo, sA);
      else if (n - 6 < NW2_A) w2f[n - 6] = ldck(rs, cx.vo, WT_2 + (cx.cb * 4 + w) * 57344, n - 6);
      if (n == 10) kin.pz = kv_poll(cx, kvb);
      if (n == 20) { kv_wait(cx, kin.pz, kvb + KV_F + w * 16 * FS); STAMP(6); kv_load(cx, kin, kvb); }
      SB();
      csq = MFMA16(sel, qa[n % 3], csq); SB();
      if (n + 3 < 32) rdq(n + 3);
      asm volatile("" : "+a"(accq), "+a"(csq));
      SB();
    });
    csw[0] = cs_pack(csq[0]);
    accq = MFMA(aug_w(csw[0], 0u), xf[32], accq);
    u32x4 b0, b1; pack16(accq, b0, b1);
    f32x16 pq = {}; pq = MFMA(aphi[0], b0, pq); pq = MFMA(aphi[1], b1, pq);
    Softmax sq;
#pragma unroll
    for (int i = 0; i < 29; i++) sq.step(i, pq, sc, phq);
  }
}

DEVI void red_send(Cx& cx, const f32x16& a, int j) {
  char* dst = cx.C + (cx.w ^ j) * INBOX + (j - 1) * 4096 + cx.vo;
#pragma unroll
  for (int m = 0; m < 4; m++) { f32x4 v = {a[4 * m], a[4 * m + 1], a[4 * m + 2], a[4 * m + 3]}; *(f32x4*)(dst + m * 1024) = v; }
}
DEVI void red_recv(Cx& cx, const f32x16& own, float (&xr)[16]) {
  f32x4 v[12];
#pragma unroll
  for (int k = 0; k < 12; k++) v[k] = *(const f32x4*)(cx.C + cx.w * INBOX + ((k % 3) * 4 + k / 3) * 1024 + cx.vo);
#pragma unroll
  for (int m = 0; m < 4; m++)
#pragma unroll
    for (int e = 0; e < 4; e++) xr[4 * m + e] = (own[4 * m + e] + v[3 * m][e]) + (v[3 * m + 1][e] + v[3 * m + 2][e]);
}
DEVI void unpack16(const u32x4& a, const u32x4& b, float (&x)[16]) {
#pragma unroll
  for (int t = 0; t < 4; t++) { x[2 * t] = bflo(a[t]); x[2 * t + 1] = bfhi(a[t]); x[8 + 2 * t] = bflo(b[t]); x[9 + 2 * t] = bfhi(b[t]); }
}
DEVI void publish_x(Cx& cx, float (&xr)[16], int xb, int sb, int fb) {
  u32x4 f0, f1; pack16(xr, f0, f1);
  stc(cx.rs, cx.vo, xb + cx.xo, f0);
  stc(cx.rs, cx.vo + 1024, xb + cx.xo, f1);
  SB();
  unpack16(f0, f1, xr);
  float s1 = xr[0] + xr[1], s2 = xr[0] * xr[0], t1 = xr[2] + xr[3], t2 = xr[1] * xr[1];
#pragma unroll
  for (int r = 2; r < 16; r += 2) { s2 = fmaf(xr[r], xr[r], s2); t2 = fmaf(xr[r + 1], xr[r + 1], t2); if (r >= 4) { s1 += xr[r]; t1 += xr[r + 1]; } }
  auto t = __builtin_amdgcn_permlane32_swap(__float_as_uint(s1 + t1), __float_as_uint(s2 + t2), false, false);
  const unsigned st = __float_as_uint(__uint_as_float(t[0]) + __uint_as_float(t[1]));
  stc1(cx.rs, (cx.lane & 15) * 16 + (cx.lane >> 4 & 1) * 8 + cx.hk * 4, sb + cx.so, st);
  put_final(cx, fb + cx.fxo, sum4(sum4(st, f0), f1));
}
DEVI void park(Cx& cx, const float (&xr)[16]) {
  char* p = cx.C + cx.w * INBOX + C_PARK + cx.vo;
#pragma unroll
  for (int m = 0; m < 4; m++) { f32x4 v = {xr[4 * m], xr[4 * m + 1], xr[4 * m + 2], xr[4 * m + 3]}; *(f32x4*)(p + m * 1024) = v; }
}
DEVI f32x16 unpark(Cx& cx) {
  const char* p = cx.C + cx.w * INBOX + C_PARK + cx.vo;
  f32x16 r;
#pragma unroll
  for (int m = 0; m < 4; m++) { f32x4 v = *(const f32x4*)(p + m * 1024); r[4 * m] = v[0]; r[4 * m + 1] = v[1]; r[4 * m + 2] = v[2]; r[4 * m + 3] = v[3]; }
  return r;
}
DEVI f32x16 tovec(const float (&x)[16]) {
  f32x16 r;
#pragma unroll
  for (int e = 0; e < 16; e++) r[e] = x[e];
  return r;
}
#define UNLIKELY(x) __builtin_expect(!!(x), 0)

template <class AUG, class WIN>
DEVI void xpass(Cx& cx, int xb, int sb, int fb, u32x4 (&xf)[33], f32x16& a0, f32x16& a1, int sti, AUG aug, WIN win) {
  const rsrc_t rs = cx.rs;
  xb += cx.w * 32768; sb += cx.w * 4096; fb += cx.w * 16 * FS;
  if (sti == 15) FST(7, 0);
  if (sti == 25) FST(8, 0);
  if (sti == 15) nap<D_X2>(); else nap<D_X1>();
  u32x2 pl0 = ldc2(rs, cx.fv16, fb);
  if (sti == 25 && FINE == 9) { FST(9, 8); vm_wait<0>(); FST(9, 9); }
  const unsigned sum0 = final_wait<16>(cx, pl0, fb, cx.tk_x);
  if (sti == 25) FST(9, 10);
  STAMP(sti); if (sti != 15) FST(sti == 11 ? 4 : 5, 0); else FST(7, 1);
  if (sti == 25) FST(8, 1);
  u32x4 st[4], ra[3], rb[3], w0, w1;
  auto ld = [&](int q) LAMBDA {
    if (q < 24) xf[q] = ldck(rs, cx.vo, xb, q);
    else if (q < 28) st[q - 24] = ldck(rs, cx.vo, sb, q - 24);
    else if (q < 36) xf[q - 4] = ldck(rs, cx.vo, xb, q - 4);
  };
  auto stats = [&]() LAMBDA {
    float v[4];
#pragma unroll
    for (int e = 0; e < 4; e++) v[e] = (__uint_as_float(st[0][e]) + __uint_as_float(st[1][e])) + (__uint_as_float(st[2][e]) + __uint_as_float(st[3][e]));
    auto p = __builtin_amdgcn_permlane32_swap(__float_as_uint(v[0]), __float_as_uint(v[1]), false, false);
    auto q = __builtin_amdgcn_permlane32_swap(__float_as_uint(v[2]), __float_as_uint(v[3]), false, false);
    auto r = __builtin_amdgcn_permlane16_swap(__float_as_uint(__uint_as_float(p[0]) + __uint_as_float(p[1])), __float_as_uint(__uint_as_float(q[0]) + __uint_as_float(q[1])), false, false);
    const unsigned t = __float_as_uint((__uint_as_float(r[0]) + __uint_as_float(r[1])) * (1.f / 512.f));
    auto u = __builtin_amdgcn_permlane32_swap(t, t, false, false);
    const float mean = __uint_as_float(u[0]);
    aug(mean, fmaxf(__uint_as_float(u[1]) - mean * mean, 0.f), w0, w1);
  };
  const char* pa = cx.A + cx.vo;
#pragma unroll
  for (int q = 0; q < 10; q++) ld(q);
  SB();
  if (sti == 15) FST(7, 2);
  if (sti == 25) FST(8, 2);
  bar();
  if (sti == 15) FST(7, 3);
  if (sti == 25) FST(8, 3);
#pragma unroll
  for (int n = 0; n < 3; n++) { ra[n] = *(const u32x4*)(pa + n * 2048); rb[n] = *(const u32x4*)(pa + n * 2048 + 1024); }
  SB();
  unsigned sum = sum0;
  const f32x16 zero = {};
  sfor<32>([&](auto nn) LAMBDA {
    constexpr int n = nn;
    if (n % 8 == 0 && sti != 15) FST(sti == 11 ? 4 : 5, 1 + n / 8);
    a0 = MFMA(ra[n % 3], xf[n], n ? a0 : zero); SB();
    if (n % 4 == 0 && sti == 15) FST(7, 4 + n / 4);
    if (n % 4 == 0 && sti == 25) FST(8, 4 + n / 4);
    if (n == 0 && sti == 25) FST(9, 11);
    if (n + 3 < 32) ra[n % 3] = *(const u32x4*)(pa + (n + 3) * 2048);
    if (n + 10 < 36) ld(n + 10); else if (n >= 27) win(ic<(n < 27 ? 0 : 2 * (n - 27))>());
    if (n == 25) sum = sum4(sum4(sum, st[0]), st[1]);
    if (n == 26) sum = sum4(sum4(sum, st[2]), st[3]);
    SB();
    a1 = MFMA(rb[n % 3], xf[n], n ? a1 : zero); SB();
    if (n + 3 < 32) rb[n % 3] = *(const u32x4*)(pa + (n + 3) * 2048 + 1024);
    sum = sum4(sum, xf[n]);
    if (n == 27) stats();
    if (n >= 27) win(ic<(n < 27 ? 0 : 2 * (n - 27) + 1)>());
    SB();
    if (n == 29) { a0 = MFMA(w0, xf[32], a0); a1 = MFMA(w1, xf[32], a1); SB(); }
  });
  if (sti != 15) FST(sti == 11 ? 4 : 5, 5); else FST(7, 12);
  if (sti == 25) FST(8, 12);
  asm volatile("" : "+a"(a0), "+a"(a1));
  if (UNLIKELY(!validate(cx, sum))) {
    do {
      asm volatile("" ::: "memory");
#pragma unroll
      for (int q = 0; q < 36; q++) ld(q);
      sum = sum0;
#pragma unroll
      for (int q = 0; q < 32; q++) sum = sum4(sum, xf[q]);
#pragma unroll
      for (int q = 0; q < 4; q++) sum = sum4(sum, st[q]);
    } while (!validate(cx, sum));
    stats();
    a0 = MFMA(w0, xf[32], zero); a1 = MFMA(w1, xf[32], zero);
#pragma unroll
    for (int n = 0; n < 32; n++) {
      a0 = MFMA(*(const u32x4*)(pa + n * 2048), xf[n], a0); a1 = MFMA(*(const u32x4*)(pa + n * 2048 + 1024), xf[n], a1);
      SB();
    }
  }
}

struct Gelu {
  float iA, c0, c1, a[4], u[2], e[2], r[2], g;
  u32x4 f; unsigned sum;
  template <int K0, int K1, int K, int H>
  DEVI void tick(Cx& cx, const f32x16& lo, const f32x16& hi, int hb) {
    if (H == 0) {
      if (K >= K0 && K < K1) { a[K & 3] = (K & 16) ? hi[K & 15] : lo[K & 15]; u[K & 1] = fmaf(a[K & 3] * a[K & 3], c1, c0); }
      if (K - 2 >= K0 && K - 2 < K1) r[K & 1] = __builtin_amdgcn_rcpf(fmaf(e[K & 1], iA, iA));
    } else {
      if (K - 1 >= K0 && K - 1 < K1) e[(K - 1) & 1] = __builtin_amdgcn_exp2f(a[(K - 1) & 3] * u[(K - 1) & 1]);
      if (K - 3 >= K0 && K - 3 < K1) {
        constexpr int v = K - 3;
        const float y = a[v & 3] * r[v & 1];
        if (v & 1) f[(v >> 1) & 3] = pk2(g, y); else { g = y; asm volatile("" : "+v"(g)); }
        if ((v & 7) == 7) { stc(cx.rs, cx.vo, hb + (v >> 3) * 8192, f); sum = sum4(sum, f); }
      }
    }
  }
};

#define W2KEEP 44
#define NW2_B 6
#define NW2_C 10
#define NW2_D 32

template <int L>
DEVI void layer(Cx& cx, const u16* __restrict__ x, const u16* __restrict__ b1, const u16* __restrict__ b2, u16* __restrict__ out,
                u32x4 (&phq)[4], const unsigned (&csw)[3], const u32x4 (&aphi)[2], KvIn& kin, u32x4 (&w2f)[56],
                unsigned (&csp)[4], unsigned (&b1p)[2]) {
  const int lane = cx.lane, w = cx.w, tok = cx.tok, hk = cx.hk, g = cx.g, c = cx.c;
  const rsrc_t rs = cx.rs;
  const int gl = cx.gb + L * G_L;
  const int kvb = KVA + L * KV_L + c * KV_C;
  const int w2b = WT_2 + (cx.cb * 4 + w) * 57344;
  const f32x16 zero = {};
  constexpr int SO = 14 * L;
  u32x4 wof[16], xa, xb2, ba, bb;
  const int col = 64 * cx.cb + 32 * (w & 1) + 16 * hk;
  const size_t xrow = (size_t)(128 * g + 64 * cx.th + 32 * (w >> 1) + tok) * 512 + col;
  if (L == 1) {
    u32x4 xf[33]; f32x16 acck, accv; float rstd;
    xpass(cx, cx.gb + G_X2, cx.gb + G_ST2, cx.gb + G_FX2, xf, acck, accv, 15,
          [&](float mean, float var, u32x4& w0, u32x4& w1) LAMBDA {
            rstd = __builtin_amdgcn_rsqf(var + 1e-5f);
            xf[32] = aug_tok(mean, 0.f, hk); w0 = aug_w(csw[1], 0u); w1 = aug_w(csw[2], 0u);
          },
          [&](auto) LAMBDA {});
    STAMP(16); FST(6, 0); FST(7, 13);
    const float sc = rstd * LOG2E;
    f32x16 p;
    { u32x4 b0, b1; pack16(acck, b0, b1); p = MFMA(aphi[0], b0, zero); p = MFMA(aphi[1], b1, p); }
    FST(6, 1);
    kv_write_v(cx, accv, rstd);
    SB();
    {
      Softmax sm; u32x4 phk[4];
      sfor<29>([&](auto ii) LAMBDA {
        constexpr int i = ii;
        if (i % 4 == 0) FST(6, 2 + i / 4);
        sm.step(i, p, sc, phk);
        SB();
      });
      STAMP(17); FST(6, 10);
      kv_tail<-1>(cx, phk, kvb);
    }
    STAMP(18); FST(6, 11);
    f32x16 accq = MFMA(aug_w(csw[0], 0u), xf[32], zero);
    u32x4 qa[3];
    const char* pq = cx.B + cx.vo;
#pragma unroll
    for (int n = 0; n < 3; n++) qa[n] = *(const u32x4*)(pq + n * 1024);
    char* dA = cx.A + w * 16384;
    const int sA = WT_1 + c * W1_C + w * 16384;
    SB();
    sfor<32>([&](auto nn) LAMBDA {
      constexpr int n = nn;
      accq = MFMA(qa[n % 3], xf[n], accq); SB();
      if (n + 3 < 32) qa[n % 3] = *(const u32x4*)(pq + (n + 3) * 1024);
      if ((n & 1) == 0) dma<n / 2>(rs, dA, cx.vo, sA);
      if (n % 8 == 7) FST(6, 12 + n / 8);
      SB();
    });
    kin.pz = kv_poll(cx, kvb);
    u32x4 b0, b1; pack16(accq, b0, b1);
    f32x16 pq2 = MFMA(aphi[0], b0, zero); pq2 = MFMA(aphi[1], b1, pq2);
    Softmax sq;
    sfor<29>([&](auto ii) LAMBDA {
      constexpr int i = ii;
      sq.step(i, pq2, sc, phq);
      if (i == 12) { SB(); kv_wait(cx, kin.pz, kvb + KV_F + w * 16 * FS); STAMP(19); kv_load(cx, kin, kvb); }
      SB();
    });
  }
  asm volatile("" : "+v"(phq[0]), "+v"(phq[1]), "+v"(phq[2]), "+v"(phq[3]));
#pragma unroll
  for (int s = 0; s < 16; s++) wof[s] = ldck(rs, cx.vo, WT_O + (cx.cb * 4 + w) * 16384, s);
  if (L == 0) { xa = *(const u32x4*)(x + xrow); xb2 = *(const u32x4*)(x + xrow + 8); }
  ba = *(const u32x4*)(b2 + col); bb = *(const u32x4*)(b2 + col + 8);
  u32x4 cs[4]; u16 b1v[4];
  if (L == 0) {
#pragma unroll
    for (int k = 0; k < 4; k++) cs[k] = ldc(rs, tok * 32 + hk * 16 + k * 1024, CS1 + c * 4096);
    const int pi = 16 * ((tok >> 2) & 1) + (tok & 3) + 4 * (tok >> 3), ph = 8 * ((tok >> 2) & 1) + (tok & 3) + 4 * ((tok >> 3) & 1);
    const u16* bp = b1 + 112 * c;
    b1v[0] = bp[pi]; b1v[1] = bp[32 + pi]; b1v[2] = bp[64 + pi]; b1v[3] = bp[96 + ph];
#pragma unroll
    for (int k = NW2_A; k < NW2_B; k++) w2f[k] = ldck(rs, cx.vo, w2b, k);
  }
  SB();
  STAMP(7 + SO);
  {
    const int G = lane >> 4, d = lane & 15;
    u32x4 idn;
#pragma unroll
    for (int t = 0; t < 4; t++) { const int k = 8 * (G & 1) + 2 * t; idn[t] = k == d ? 0x3f80u : k + 1 == d ? 0x3f800000u : 0u; }
    f32x4 kv; unsigned sum;
    auto red = [&]() LAMBDA {
      f32x4 k0 = {}, k1 = {}; sum = -cx.tk_kv;
#pragma unroll
      for (int m = 0; m < 8; m += 2) {
        k0 = MFMA16(idn, kin.pa[m], k0); k1 = MFMA16(idn, kin.pa[m + 1], k1);
        sum = sum4(sum4(sum, kin.pa[m]), kin.pa[m + 1]);
      }
      sum = sum4(sum, kin.pz); kv = k0 + k1;
    };
    red();
    asm volatile("" : "+v"(kv));
    if (UNLIKELY(!validate(cx, sum))) {
      do { asm volatile("" ::: "memory"); kin.pz = kv_poll(cx, kvb); kv_load(cx, kin, kvb); red(); } while (!validate(cx, sum));
    }
    char* img = cx.C + w * INBOX + C_IMG;
    { const u32x2 v = {pk2(kv[0], kv[1]), pk2(kv[2], kv[3])}; *(u32x2*)(img + d * 32 + G * 8) = v; }
    { const u32x4 z4 = {}; *(u32x4*)(img + 512 + cx.vo) = hk ? z4 : kin.pz; }
    lds_barrier();
    const int hp = (tok >> 2) & 1, dd = (tok & 3) + 4 * (tok >> 3);
    const char* ap = cx.C + (hp == hk ? 2 * hp * INBOX + C_IMG + dd * 32 : C_ZERO);
    const int zo = hp == hk ? 512 : 0;
    u32x4 a[4], az[4];
#pragma unroll
    for (int s4 = 0; s4 < 4; s4++) az[s4] = *(const u32x4*)(ap + zo + (s4 >> 1) * INBOX + (s4 & 1) * 16);
#pragma unroll
    for (int s4 = 0; s4 < 4; s4++) a[s4] = *(const u32x4*)(ap + (s4 >> 1) * INBOX + (s4 & 1) * 16);
    if (L == 0) stc1(rs, w == 0 && (lane >> 4) == 2 ? (lane - 32) * 8192 : OOB, MBX + c * 512 + (48 + g) * 8, cx.tk_kv + 1);
    SB();
    f32x16 num = {}, den = {};
#pragma unroll
    for (int s4 = 0; s4 < 4; s4++) den = MFMA(az[s4], phq[s4], den);
#pragma unroll
    for (int s4 = 0; s4 < 4; s4++) num = MFMA(a[s4], phq[s4], num);
    float ds[4];
#pragma unroll
    for (int r = 0; r < 4; r++) ds[r] = (den[r] + den[4 + r]) + (den[8 + r] + den[12 + r]);
    const float inv = __builtin_amdgcn_rcpf(((ds[0] + ds[1]) + (ds[2] + ds[3])) + 1e-6f);
    float ov[16];
#pragma unroll
    for (int r = 0; r < 16; r++) ov[r] = num[r] * inv;
    u32x4 f0, f1; pack16(ov, f0, f1);
    stc(rs, cx.vo, gl + G_O + cx.oo, f0);
    stc(rs, cx.vo + 1024, gl + G_O + cx.oo, f1);
    put_final(cx, gl + G_FO + cx.fo, sum4(sum4(0u, f0), f1));
    STAMP(8 + SO);
  }
  float xr[16];
  {
    const int ob = gl + G_O + (cx.th * 4 + w) * 16384, fb = gl + G_FO + (cx.th * 4 + w) * 8 * FS;
    f32x16 acc[4];
    if (L == 0) {
      unpack16(xa, xb2, xr); acc[0] = tovec(xr);
#pragma unroll
      for (int k = 0; k < 4; k++) {
        const f32x4 v = __builtin_bit_cast(f32x4, cs[k]);
        csp[k] = cs_pack(xhalf_sum((v[0] + v[1]) + (v[2] + v[3])));
      }
      b1p[0] = b1v[0] | ((unsigned)b1v[1] << 16); b1p[1] = b1v[2] | ((unsigned)b1v[3] << 16);
    } else acc[0] = unpark(cx);
    SB();
    nap<D_O>();
    const unsigned sum0 = final_wait<8>(cx, ldc2(rs, cx.fv8, fb), fb, cx.tk_oh);
    STAMP(9 + SO);
    u32x4 of[16];
#pragma unroll
    for (int k = 0; k < 10; k++) of[k ^ 8] = ldck(rs, cx.vo, ob, k ^ 8);
    SB();
    lds_barrier();
    char* dB = cx.B + w * 12288;
    const int sB = WT_1 + c * W1_C + 65536 + w * 12288;
    unsigned sum = sum0;
    sfor<32>([&](auto nn) LAMBDA {
      constexpr int n = nn, s = (n >> 1) & 7, j = (n < 16 ? 3 : 1) - (n & 1);
      acc[j] = MFMA(wof[2 * s + (j & 1)], of[8 * (j >> 1) + s], s == 0 && j ? zero : acc[j]); SB();
      if (n < 6) of[(n + 10) ^ 8] = ldck(rs, cx.vo, ob, (n + 10) ^ 8);
      if (n >= 7 && n < 19) dma<(n >= 7 && n < 19 ? n - 7 : 0)>(rs, dB, cx.vo, sB);
      if (L == 0 && n >= 19 && NW2_B + n - 19 < NW2_C) w2f[NW2_B + n - 19] = ldck(rs, cx.vo, w2b, NW2_B + n - 19);
      if (n & 1) sum = sum4(sum, of[8 * (j >> 1) + s]);
      if (n == 18) red_send(cx, acc[3], 3);
      if (n == 22) red_send(cx, acc[2], 2);
      SB();
    });
    red_send(cx, acc[1], 1);
    asm volatile("" : "+a"(acc[0]));
    if (UNLIKELY(!validate(cx, sum))) {
      do {
        asm volatile("" ::: "memory");
        sum = sum0;
#pragma unroll
        for (int k = 0; k < 16; k++) { of[k] = ldck(rs, cx.vo, ob, k); sum = sum4(sum, of[k]); }
      } while (!validate(cx, sum));
      if (L == 0) { xa = *(const u32x4*)(x + xrow); xb2 = *(const u32x4*)(x + xrow + 8); }
      else { xa = ldc(rs, cx.vo, cx.gb + G_X2 + cx.xo); xb2 = ldc(rs, cx.vo + 1024, cx.gb + G_X2 + cx.xo); }
      unpack16(xa, xb2, xr); acc[0] = tovec(xr); acc[1] = zero; acc[2] = zero; acc[3] = zero;
#pragma unroll
      for (int n = 0; n < 32; n++) acc[n >> 3] = MFMA(wof[2 * (n & 7) + ((n >> 3) & 1)], of[8 * (n >> 4) + (n & 7)], acc[n >> 3]);
      red_send(cx, acc[1], 1); red_send(cx, acc[2], 2); red_send(cx, acc[3], 3);
    }
    if (L == 1) FST(9, 0);
    lds_barrier();
    if (L == 1) FST(9, 1);
    red_recv(cx, acc[0], xr);
    if (L == 1 && FINE == 9) { FST(9, 2); vm_wait<0>(); FST(9, 3); }
    publish_x(cx, xr, gl + G_X1, gl + G_ST1, gl + G_FX1);
    if (L == 1 && FINE == 9) { FST(9, 4); vm_wait<3>(); FST(9, 5); vm_wait<1>(); FST(9, 6); vm_wait<0>(); FST(9, 7); }
    STAMP(10 + SO);
    float bv[16]; unpack16(ba, bb, bv);
#pragma unroll
    for (int r = 0; r < 16; r++) xr[r] += bv[r];
    park(cx, xr);
  }
  {
    u32x4 xf[33]; f32x16 h0, h1, h2, h3;
    Gelu ge;
    xpass(cx, gl + G_X1, gl + G_ST1, gl + G_FX1, xf, h0, h1, 11 + SO,
          [&](float mean, float var, u32x4& w0, u32x4& w1) LAMBDA {
            const float r1 = __builtin_amdgcn_rsqf(var + 1e-5f);
            const float A = r1 * __builtin_amdgcn_rsqf(var * r1 * r1 + 1e-5f);
            ge.iA = __builtin_amdgcn_rcpf(A);
            xf[32] = aug_tok(mean, ge.iA, hk);
            const float k0 = -2.f * LOG2E * 0.7978845608028654f, k1 = k0 * 0.044715f;
            ge.c0 = k0 * A; ge.c1 = k1 * A * A * A;
            w0 = aug_w(csp[0], b1p[0]); w1 = aug_w(csp[1], b1p[0] >> 16);
          },
          [&](auto ii) LAMBDA { constexpr int k = NW2_C + ii.v; if (L == 0 && k < NW2_D) w2f[k] = ldck(rs, cx.vo, w2b, k); });
    STAMP(12 + SO); if (L == 1) FST(8, 13);
    const int hs = gl + G_H + cx.ho, hfo = gl + G_FH + cx.fo;
    const int hb = gl + G_H + (cx.th * 4 + w) * 57344, fb = gl + G_FH + (cx.th * 4 + w) * 8 * FS;
    ge.sum = 0;
    h2 = MFMA(aug_w(csp[2], b1p[1]), xf[32], zero); h3 = MFMA(aug_w(csp[3], b1p[1] >> 16), xf[32], zero);
    const char* pb = cx.B + cx.vo;
    const char* ph = cx.B + 1024 + (hk * 16 + (lane & 15)) * 16;
    u32x4 ra[3], rb[3], hf[56]; u32x2 fc;
#pragma unroll
    for (int n = 0; n < 3; n++) { ra[n] = *(const u32x4*)(pb + n * 1536); rb[n] = *(const u32x4*)(ph + n * 1536); }
    SB();
    unsigned pl = 0, sum = 0;
    sfor<64>([&](auto gg) LAMBDA {
      constexpr int G = gg, n = G >> 1;
      if (G % 8 == 0) FST(4 + L, 6 + G / 8);
      if (G & 1) { h3 = MFMA(rb[n % 3], xf[n], h3); SB(); if (n + 3 < 32) rb[n % 3] = *(const u32x4*)(ph + (n + 3) * 1536); }
      else       { h2 = MFMA(ra[n % 3], xf[n], h2); SB(); if (n + 3 < 32) ra[n % 3] = *(const u32x4*)(pb + (n + 3) * 1536); }
      if (G < 35) { ge.tick<0, 32, G, 0>(cx, h0, h1, hs); ge.tick<0, 32, G, 1>(cx, h0, h1, hs); }
      if (L == 0 && G % 2 == 0 && NW2_C + 10 + G / 2 < NW2_D) w2f[NW2_C + 10 + G / 2] = ldck(rs, cx.vo, w2b, NW2_C + 10 + G / 2);
      if (G == 18) put_flag(cx, hfo + FHL, 1);
      if (G == 34) put_flag(cx, hfo + 2 * FHL, 3);
      if (G == 26) pl = ldc1(rs, cx.fv8, fb + FHL);
      if (G == 40) { SB(); poll_wait<8>(cx, pl, fb + FHL, cx.tk_oh, 1); STAMP(13 + SO); }
      if (G >= 40 && G < 56) { constexpr int k = 2 * ((G - 40) & 7) + (G >= 48); hf[k] = ldck(rs, cx.vo, hb, k); }
      if (G == 48) pl = ldc1(rs, cx.fv8, fb + 2 * FHL);
      SB();
    });
    f32x16 acc[4];
    FST(4 + L, 14);
    acc[0] = unpark(cx);
    poll_wait<8>(cx, pl, fb + 2 * FHL, cx.tk_oh, 3);
    lds_barrier();
    STAMP(14 + SO); FST(4 + L, 15);
    unsigned msg = 0;
    char* dA = cx.A + w * 16384; char* dB = cx.B + w * 8192;
    const int sA = WT_QKV + c * WQ_C + w * 16384, sB = WT_QKV + c * WQ_C + 65536 + w * 8192;
    sfor<112>([&](auto gg) LAMBDA {
      constexpr int G = gg;
      constexpr int q = G < 64 ? (G >> 1) & 15 : G < 96 ? G >> 2 : 24 + (G & 3);
      constexpr int j = G < 64 ? (G & 1) + 2 * (G >> 5) : G < 96 ? G & 3 : 3 - ((G >> 2) & 3);
      if (G % 8 == 0) FST(2 + L, G / 8);
      acc[j] = MFMA(w2f[2 * q + (j & 1)], hf[2 * q + (j >> 1)], q == 0 && j ? zero : acc[j]); SB();
      if (G < 27) { ge.tick<32, 56, 32 + G, 0>(cx, h2, h3, hs); ge.tick<32, 56, 32 + G, 1>(cx, h2, h3, hs); }
      if (G == 18) put_flag(cx, hfo + 3 * FHL, 5);
      if (G == 27) put_final(cx, hfo, ge.sum);
      if (G < 16) { constexpr int k = 16 + 2 * (G & 7) + (G >= 8); hf[k] = ldck(rs, cx.vo, hb, k); }
      if (L == 0 && G >= 16 && NW2_D + G - 16 < 56) w2f[NW2_D + G - 16] = ldck(rs, cx.vo, w2b, NW2_D + G - 16);
      if (L == 1 && G >= 69 && G < 69 + 56 - W2KEEP) w2f[G - 69 + W2KEEP] = ldck(rs, cx.vo, w2b, G - 69 + W2KEEP);
      if (G == 32) pl = ldc1(rs, cx.fv8, fb + 3 * FHL);
      if (G == 42) { SB(); poll_wait<8>(cx, pl, fb + 3 * FHL, cx.tk_oh, 5); }
      if (G >= 42 && G < 58) hf[G - 10] = ldck(rs, cx.vo, hb, G - 10);
      if (G == 48) fc = ldc2(rs, cx.fv8, fb);
      if (G == 60) { SB(); sum += final_wait<8>(cx, fc, fb, cx.tk_oh); }
      if (G >= 60 && G < 68) hf[G - 12] = ldck(rs, cx.vo, hb, G - 12);
      if (L == 0) {
        if (G == 59) msg = ldc1(rs, (lane & 15) * 8, MBX + (g * 16 + c) * 512 + 384);
        if (G == 72) { SB(); mail_wait(cx, msg, (lane & 15) * 8, MBX + (g * 16 + c) * 512 + 384, cx.N + 1); }
        if (G >= 72 && G < 88) dma<(G >= 72 && G < 88 ? G - 72 : 0)>(rs, dA, cx.vo, sA);
        if (G >= 88 && G < 96) dma<(G >= 88 && G < 96 ? G - 88 : 0)>(rs, dB, cx.vo, sB);
      }
      if (G < 96 ? (j & 1) : !(j & 1)) sum = sum4(sum, hf[2 * q + (j >> 1)]);
      if (G == 102) red_send(cx, acc[3], 3);
      if (G == 106) red_send(cx, acc[2], 2);
      if (G == 110) red_send(cx, acc[1], 1);
      SB();
    });
      FST(2 + L, 14);
    asm volatile("" : "+a"(acc[0]));
    if (UNLIKELY(!validate(cx, sum))) {
      do {
        asm volatile("" ::: "memory");
        xa = ldc(rs, cx.vo, gl + G_X1 + cx.xo); xb2 = ldc(rs, cx.vo + 1024, gl + G_X1 + cx.xo);
        float bv[16]; unpack16(*(const u32x4*)(b2 + col), *(const u32x4*)(b2 + col + 8), bv);
        unpack16(xa, xb2, xr);
#pragma unroll
        for (int r = 0; r < 16; r++) acc[0][r] = xr[r] + bv[r];
        acc[1] = zero; acc[2] = zero; acc[3] = zero; sum = ldc2(rs, cx.fv8, fb)[1] - cx.tk_oh;
#pragma unroll
        for (int k = 0; k < 56; k++) {
          const u32x4 f = ldck(rs, cx.vo, hb, k);
          sum = sum4(sum, f);
          acc[2 * (k & 1)] = MFMA(w2f[k & ~1], f, acc[2 * (k & 1)]); acc[2 * (k & 1) + 1] = MFMA(w2f[k | 1], f, acc[2 * (k & 1) + 1]);
          if ((k & 7) == 7) SB();
        }
      } while (!validate(cx, sum));
      red_send(cx, acc[1], 1); red_send(cx, acc[2], 2); red_send(cx, acc[3], 3);
    }
    lds_barrier();
    FST(2 + L, 15);
    if (L == 0) {
      red_recv(cx, acc[0], xr);
      publish_x(cx, xr, cx.gb + G_X2, cx.gb + G_ST2, cx.gb + G_FX2);
      STAMP(20);
      park(cx, xr);
    } else {
      red_recv(cx, acc[0], xr);
      u32x4 f0, f1; pack16(xr, f0, f1);
      *(u32x4*)(out + xrow) = f0; *(u32x4*)(out + xrow + 8) = f1;
      STAMP(29);
    }
  }
}

__global__ __launch_bounds__(256, 1) void fused(const u16* __restrict__ x, const u16* __restrict__ wqkv, const u16* __restrict__ wphi,
                                                const u16* __restrict__ wo, const u16* __restrict__ w1, const u16* __restrict__ b1,
                                                const u16* __restrict__ w2, const u16* __restrict__ b2, char* ws, u16* __restrict__ out) {
  __shared__ __attribute__((aligned(16))) char ldsA[65536];
  __shared__ __attribute__((aligned(16))) char ldsB[49152];
  __shared__ __attribute__((aligned(16))) char ldsC[49152];
  Cx cx;
  STAMP(0); CSTAMP(30);
  {
    unsigned long long pc; unsigned len;
    asm volatile("s_getpc_b64 %0\n.Lcb0:\n\ts_mov_b32 %1, .Lce0-.Lcb0-16" : "=s"(pc), "=s"(len));
    unsigned o = ((blockIdx.x >> 3) * 256 + threadIdx.x) * 16;
    gldso<0>(o < len ? o : 0u, (const void*)pc, (unsigned)(size_t)ldsC + 48128u);
  }
  cx.ws = ws; cx.A = ldsA; cx.B = ldsB; cx.C = ldsC;
  cx.lane = threadIdx.x & 63; cx.w = RFL(threadIdx.x >> 6);
  cx.tok = cx.lane & 31; cx.hk = cx.lane >> 5; cx.vo = cx.lane * 16; cx.budget = SPIN_MAX;
  { const int b = blockIdx.x, xx = b & 7, j = b >> 3; cx.g = 4 * (xx >> 1) + (j >> 3); cx.c = 8 * (xx & 1) + (j & 7); }
  cx.cb = 4 * (cx.c >> 3) + (cx.c & 3); cx.th = (cx.c >> 2) & 1;
  cx.rs = __builtin_amdgcn_make_buffer_rsrc(ws, 0, WS_USED, 0x00020000);
  cx.gb = GRP + cx.g * G_S;
  cx.fv16 = cx.lane < 16 ? cx.lane * FS + 120 : OOB; cx.fv8 = cx.lane < 8 ? cx.lane * FS + 120 : OOB; cx.f63 = cx.lane >= 48 ? (cx.lane - 48) * 8 : OOB;
  cx.fvz = cx.lane < 32 ? (cx.lane >> 1) * FS + (cx.lane & 1) * 16 : cx.lane < 48 ? (cx.lane - 32) * FS + 112 : OOB;
  {
    const int w = cx.w, kq = cx.c >> 2, tx = (w & 1) ^ (kq >> 1), ln = (w >> 1) * 4 + kq, tb = 2 * cx.th + (w >> 1), blk = 2 * cx.cb + (w & 1);
    cx.oo = (ln * 16 + tx * 8 + (cx.c & 3) * 2) * 1024;
    cx.ho = (ln * 56 + (cx.c & 3) * 2 + tx) * 1024;
    cx.fo = (ln * 8 + (cx.c & 3) * 2 + (w & 1)) * FS;
    cx.xo = (tb * 32 + 2 * blk) * 1024;
    cx.so = tb * 4096 + blk * 256;
    cx.fxo = (tb * 16 + blk) * FS;
  }
  u32x4 phq[4], aphi[2], w2f[56]; unsigned csw[3], csp[4], b1p[2]; KvIn kin;
  stage1a(cx, x, wqkv, wphi, wo, w1, w2, phq, csw, aphi, kin, w2f);
  layer<0>(cx, x, b1, b2, out, phq, csw, aphi, kin, w2f, csp, b1p);
  asm volatile("" : "+v"(cx.lane), "+v"(cx.tok), "+v"(cx.hk), "+v"(cx.vo));
  asm volatile("" : "+v"(csp[0]), "+v"(csp[1]), "+v"(csp[2]), "+v"(csp[3]), "+v"(b1p[0]), "+v"(b1p[1]), "+v"(csw[0]), "+v"(csw[1]), "+v"(csw[2]));
#pragma unroll
  for (int k = 0; k < W2KEEP; k++) asm volatile("" : "+a"(w2f[k]));
  layer<1>(cx, x, b1, b2, out, phq, csw, aphi, kin, w2f, csp, b1p);
  asm volatile(".Lce0:");
}

extern "C" void kernel_launch(void* const* d_in, const int* in_sizes, int n_in, void* d_out, int out_size, void* d_ws, size_t ws_size,
                              hipStream_t stream) {
  fused<<<256, 256, 0, stream>>>((const u16*)d_in[0], (const u16*)d_in[1], (const u16*)d_in[2], (const u16*)d_in[3], (const u16*)d_in[5],
                                 (const u16*)d_in[6], (const u16*)d_in[7], (const u16*)d_in[8], (char*)d_ws, (u16*)d_out);
}
```

```cpp
#include <hip/hip_runtime.h>
#include <stdint.h>

typedef unsigned short u16;
typedef __attribute__((ext_vector_type(8))) __bf16 bf16x8;
typedef __attribute__((ext_vector_type(2))) __bf16 bf16x2;
typedef __attribute__((ext_vector_type(16))) float f32x16;
typedef __attribute__((ext_vector_type(4))) float f32x4;
typedef __attribute__((ext_vector_type(2))) float f32x2;
typedef __attribute__((ext_vector_type(4))) unsigned u32x4;
typedef __attribute__((ext_vector_type(2))) unsigned u32x2;
typedef __attribute__((ext_vector_type(4))) short s16x4;
typedef __amdgpu_buffer_rsrc_t rsrc_t;
typedef __attribute__((address_space(3))) void* ldsp;

#define DEVI __device__ __forceinline__
#define LAMBDA __attribute__((always_inline))
#define MFMA(a, b, c) __builtin_amdgcn_mfma_f32_32x32x16_bf16(__builtin_bit_cast(bf16x8, a), __builtin_bit_cast(bf16x8, b), c, 0, 0, 0)
#define MFMA16(a, b, c) __builtin_amdgcn_mfma_f32_16x16x32_bf16(__builtin_bit_cast(bf16x8, a), __builtin_bit_cast(bf16x8, b), c, 0, 0, 0)
#define SB() __builtin_amdgcn_sched_barrier(0)
#define RFL(v) __builtin_amdgcn_readfirstlane(v)
#define BALLOT(p) __builtin_amdgcn_ballot_w64(p)

constexpr int WT_QKV = 0x0, WQ_C = 98304;
constexpr int WT_O   = 0x180000;
constexpr int WT_1   = 0x200000, W1_C = 114688;
constexpr int CS1    = 0x3C0000;
constexpr int WT_2   = 0x400000;
constexpr int MBX    = 0x5C0000;
constexpr int TOKR   = 0x5E0000;
constexpr int TOKC   = 0x5E1000;
constexpr int KVA    = 0x600000, KV_C = 0xC000, KV_L = 0xC0000, KV_F = 0x8000;
constexpr int GRP    = 0x800000, G_S = 0x200000, G_L = 0xC0000;
constexpr int G_O = 0, G_X1 = 0x20000, G_H = 0x40000, G_ST1 = 0xB0000, G_FO = 0xB4000, G_FH = 0xB6000, G_FX1 = 0xBE000;
constexpr int FS = 128, FHL = 0x2000;
constexpr int G_X2 = 0x180000, G_ST2 = 0x1A0000, G_FX2 = 0x1A4000;
constexpr int WS_USED = 0x2800000;
constexpr int OOB = 0x40000000;
#define STAMP_OFF 0x2800000
#define STAMP_N 64

constexpr int INBOX = 12288;
constexpr int C_V = 4352, C_IMG = 6656, C_ZERO = 7680, C_PARK = 8192;
constexpr float LOG2E = 1.4426950408889634f;
constexpr int SPIN_MAX = 1 << 16;

DEVI unsigned pk2(float a, float b) { f32x2 v = {a, b}; return __builtin_bit_cast(unsigned, __builtin_convertvector(v, bf16x2)); }
DEVI float bflo(unsigned u) { return __uint_as_float(u << 16); }
DEVI float bfhi(unsigned u) { return __uint_as_float(u & 0xffff0000u); }
DEVI void lds_barrier() { asm volatile("s_waitcnt lgkmcnt(0)\n\ts_barrier" ::: "memory"); }
DEVI void bar() { asm volatile("s_barrier" ::: "memory"); }
template <int N> DEVI void vm_wait() { asm volatile("s_waitcnt vmcnt(%0)" :: "n"(N) : "memory"); }
template <int OFF> DEVI void gldso(unsigned voff, const void* sbase, unsigned lds_base) {
  asm volatile("s_mov_b32 m0, %2\n\ts_nop 0\n\tglobal_load_lds_dwordx4 %0, %1 offset:%3" :: "v"(voff), "s"(sbase), "s"(lds_base), "n"(OFF) : "memory", "m0");
}
DEVI void gldsc4(unsigned voff, const void* sbase, unsigned lds_base) {
  asm volatile("s_mov_b32 m0, %2\n\ts_nop 0\n\tglobal_load_lds_dwordx4 %0, %1 sc1" :: "v"(voff), "s"(sbase), "s"(lds_base) : "memory", "m0");
}
DEVI void gldsc1(unsigned voff, const void* sbase, unsigned lds_base) {
  asm volatile("s_mov_b32 m0, %2\n\ts_nop 0\n\tglobal_load_lds_dword %0, %1 sc1" :: "v"(voff), "s"(sbase), "s"(lds_base) : "memory", "m0");
}
DEVI float xhalf_sum(float v) {
  auto t = __builtin_amdgcn_permlane32_swap(__float_as_uint(v), __float_as_uint(v), false, false);
  return __uint_as_float(t[0]) + __uint_as_float(t[1]);
}
DEVI u32x4 ldc(rsrc_t rs, int vo, int so) { return __builtin_amdgcn_raw_buffer_load_b128(rs, vo, so, 16); }
DEVI u32x2 ldc2(rsrc_t rs, int vo, int so) { return __builtin_amdgcn_raw_buffer_load_b64(rs, vo, so, 16); }
DEVI unsigned ldc1(rsrc_t rs, int vo, int so) { return __builtin_amdgcn_raw_buffer_load_b32(rs, vo, so, 16); }
DEVI void stc(rsrc_t rs, int vo, int so, u32x4 v) { __builtin_amdgcn_raw_buffer_store_b128(v, rs, vo, so, 16); }
DEVI void stc2(rsrc_t rs, int vo, int so, u32x2 v) { __builtin_amdgcn_raw_buffer_store_b64(v, rs, vo, so, 16); }
DEVI void stc1(rsrc_t rs, int vo, int so, unsigned v) { __builtin_amdgcn_raw_buffer_store_b32(v, rs, vo, so, 16); }
DEVI u32x4 ldck(rsrc_t rs, int vo, int so, int kb) { return ldc(rs, vo + (kb & 3) * 1024, so + (kb >> 2) * 4096); }
template <int KB> DEVI void dma(rsrc_t rs, char* dst, int vo, int so) {
  __builtin_amdgcn_raw_ptr_buffer_load_lds(rs, (ldsp)(dst + (KB >> 2) * 4096), 16, vo, so + (KB >> 2) * 4096, (KB & 3) * 1024, 16);
}
DEVI float lsel(unsigned long long m, float t, float f) { float r; asm("v_cndmask_b32_e64 %0, %1, %2, %3" : "=v"(r) : "v"(f), "v"(t), "s"(m)); return r; }
DEVI float fmax3(float a, float b, float c) { return fmaxf(fmaxf(a, b), c); }
DEVI float fmin3(float a, float b, float c) { return fminf(fminf(a, b), c); }
DEVI unsigned sum4(unsigned s, const u32x4& f) { s = (s + f[0] + f[1]) + f[2] + f[3]; asm volatile("" : "+v"(s)); return s; }
DEVI unsigned wsum(unsigned v) {
  v += __builtin_amdgcn_update_dpp(0u, v, 0x111, 0xf, 0xf, false);
  v += __builtin_amdgcn_update_dpp(0u, v, 0x112, 0xf, 0xf, false);
  v += __builtin_amdgcn_update_dpp(0u, v, 0x114, 0xf, 0xf, false);
  v += __builtin_amdgcn_update_dpp(0u, v, 0x118, 0xf, 0xf, false);
  v += __builtin_amdgcn_update_dpp(0u, v, 0x142, 0xa, 0xf, false);
  v += __builtin_amdgcn_update_dpp(0u, v, 0x143, 0xc, 0xf, false);
  return v;
}

#define FINE 0
#define FST(sel, i)
#define STAMP(i)
#define CSTAMP(i)
#define COUNT(i)
#define BUD(i)

template <class T>
DEVI void pack16(const T& v, u32x4& a, u32x4& b) {
#pragma unroll
  for (int i = 0; i < 4; i++) { a[i] = pk2(v[2 * i], v[2 * i + 1]); b[i] = pk2(v[8 + 2 * i], v[9 + 2 * i]); }
}
template <int I> struct ic { static constexpr int v = I; constexpr operator int() const { return I; } };
template <int... I> struct iseq {};
template <int N, int... I> struct mkseq : mkseq<N - 1, N - 1, I...> {};
template <int... I> struct mkseq<0, I...> { typedef iseq<I...> type; };
template <class F, int... I> DEVI void sfor_impl(F& f, iseq<I...>) { (f(ic<I>()), ...); }
template <int N, class F> DEVI void sfor(F f) { sfor_impl(f, typename mkseq<N>::type()); }

struct Softmax {
  float mx, mn, sp, sn, ep[16], en[16];
  DEVI void step(int i, const f32x16& p, float sc, u32x4 (&ph)[4]) {
    if (i == 0) { mx = fmax3(p[0], p[1], p[2]); mn = fmin3(p[0], p[1], p[2]); mx = fmax3(mx, p[3], p[4]); mn = fmin3(mn, p[3], p[4]); }
    else if (i < 3) { int b = 5 + 4 * (i - 1); mx = fmax3(mx, p[b], p[b + 1]); mn = fmin3(mn, p[b], p[b + 1]); mx = fmax3(mx, p[b + 2], p[b + 3]); mn = fmin3(mn, p[b + 2], p[b + 3]); }
    else if (i == 3) { mx = fmax3(mx, p[13], p[14]); mn = fmin3(mn, p[13], p[14]); mx = fmaxf(mx, p[15]) * -sc; mn = fminf(mn, p[15]) * sc; }
    else if (i < 20) {
      int r = i - 4;
      ep[r] = __builtin_amdgcn_exp2f(fmaf(p[r], sc, mx)); en[r] = __builtin_amdgcn_exp2f(fmaf(p[r], -sc, mn));
      if (r == 2) { sp = ep[0] + ep[1]; sn = en[0] + en[1]; } else if (r > 2) { sp += ep[r - 1]; sn += en[r - 1]; }
    } else if (i == 20) { sp = __builtin_amdgcn_rcpf(sp + ep[15]); sn = __builtin_amdgcn_rcpf(sn + en[15]); }
    else if (i < 29) {
      int t = i - 21;
      ph[t >> 2][t & 3] = pk2(ep[2 * t] * sp, ep[2 * t + 1] * sp);
      ph[2 + (t >> 2)][t & 3] = pk2(en[2 * t] * sn, en[2 * t + 1] * sn);
    }
  }
};

template <int C> DEVI u32x4 tr_readc(const char* p) {
  typedef __attribute__((address_space(3))) s16x4* lp;
  s16x4 a = __builtin_amdgcn_ds_read_tr16_b64_v4i16((lp)(p));
  s16x4 b = __builtin_amdgcn_ds_read_tr16_b64_v4i16((lp)(p + C));
  u32x2 ua = __builtin_bit_cast(u32x2, a), ub = __builtin_bit_cast(u32x2, b);
  u32x4 r = {ua[0], ua[1], ub[0], ub[1]};
  return r;
}
DEVI u32x4 tr_read2(const char* p, const char* p2) {
  typedef __attribute__((address_space(3))) s16x4* lp;
  s16x4 a = __builtin_amdgcn_ds_read_tr16_b64_v4i16((lp)(p));
  s16x4 b = __builtin_amdgcn_ds_read_tr16_b64_v4i16((lp)(p2));
  u32x2 ua = __builtin_bit_cast(u32x2, a), ub = __builtin_bit_cast(u32x2, b);
  u32x4 r = {ua[0], ua[1], ub[0], ub[1]};
  return r;
}

DEVI u32x4 aug_tok(float mean, float ia, int hk) {
  unsigned mh = pk2(-mean, ia);
  unsigned ml = pk2(-mean - bflo(mh), ia - bfhi(mh));
  u32x4 t;
  t[0] = __builtin_amdgcn_perm(ml, mh, 0x05040100u);
  t[1] = mh;
  t[2] = ml >> 16;
  t[3] = 0;
  if (hk) { t[0] = 0; t[1] = 0; t[2] = 0; }
  return t;
}
DEVI unsigned cs_pack(float cs) { unsigned h = pk2(cs, 0.f); return pk2(cs, cs - bflo(h)); }
DEVI u32x4 aug_w(unsigned csp, unsigned b) {
  u32x4 t;
  t[0] = __builtin_amdgcn_perm(csp, csp, 0x01000100u);
  t[1] = __builtin_amdgcn_perm(b, csp, 0x05040302u);
  t[2] = b & 0xffffu;
  t[3] = 0;
  return t;
}

struct Cx {
  rsrc_t rs; char* ws; char *A, *B, *C;
  int lane, w, tok, hk, g, c, cb, th, vo, gb;
  int fv16, fv8, fvz, f63, oo, ho, fo, xo, so, fxo;
  unsigned N, tk_kv, tk_oh, tk_x;
  int budget;
};

DEVI void put_flag(Cx& cx, int so, int i) { u32x2 v = {cx.N + i, cx.N + i}; stc2(cx.rs, cx.f63, so, v); }
DEVI void put_final(Cx& cx, int so, unsigned sum) { u32x2 v = {cx.N + 7, cx.N - wsum(sum)}; stc2(cx.rs, cx.f63, so, v); }
template <int NP> DEVI void poll_wait(Cx& cx, unsigned a, int so, unsigned tk, int i) {
  unsigned t; int s;
  asm volatile("v_sub_u32 %0, %3, %4\n\t"
               "v_cmp_ne_u32 vcc, %9, %0\n\t"
               "s_and_b32 %1, vcc_lo, %8\n\t"
               "s_cbranch_scc0 2f\n"
               "1:\n\t"
               "s_cmp_eq_u32 %2, 0\n\t"
               "s_cbranch_scc1 2f\n\t"
               "buffer_load_dword %0, %5, %6, %7 offen sc1\n\t"
               "s_sub_u32 %2, %2, 1\n\t"
               "s_waitcnt vmcnt(0)\n\t"
               "v_sub_u32 %0, %0, %4\n\t"
               "v_cmp_ne_u32 vcc, %9, %0\n\t"
               "s_and_b32 %1, vcc_lo, %8\n\t"
               "s_cbranch_scc1 1b\n"
               "2:"
               : "=&v"(t), "=&s"(s), "+s"(cx.budget)
               : "v"(a), "v"(tk), "v"(NP == 16 ? cx.fv16 : cx.fv8), "s"(cx.rs), "s"(so), "n"((1 << NP) - 1), "n"(i)
               : "vcc", "scc", "memory");
}
template <int NP> DEVI unsigned final_wait(Cx& cx, u32x2 fc, int so, unsigned tk) {
  int s; unsigned f = fc[0], k = fc[1];
  asm volatile("v_sub_u32 %0, %0, %4\n\t"
               "v_cmp_ne_u32 vcc, 7, %0\n\t"
               "s_and_b32 %2, vcc_lo, %8\n\t"
               "s_cbranch_scc0 2f\n"
               "1:\n\t"
               "s_cmp_eq_u32 %3, 0\n\t"
               "s_cbranch_scc1 2f\n\t"
               "buffer_load_dword %0, %5, %6, %7 offen sc1\n\t"
               "s_sub_u32 %3, %3, 1\n\t"
               "s_waitcnt vmcnt(0)\n\t"
               "v_sub_u32 %0, %0, %4\n\t"
               "v_cmp_ne_u32 vcc, 7, %0\n\t"
               "s_and_b32 %2, vcc_lo, %8\n\t"
               "s_cbranch_scc1 1b\n\t"
               "buffer_load_dword %1, %5, %6, %7 offen offset:4 sc1\n\t"
               "s_waitcnt vmcnt(0)\n"
               "2:"
               : "+v"(f), "+v"(k), "=&s"(s), "+s"(cx.budget)
               : "v"(tk), "v"(NP == 16 ? cx.fv16 : cx.fv8), "s"(cx.rs), "s"(so), "n"((1 << NP) - 1)
               : "vcc", "scc", "memory");
  return k - tk;
}
DEVI void kv_wait(Cx& cx, u32x4& q, int so) {
  int s; unsigned f = q[2];
  asm volatile("v_sub_u32 %0, %0, %4\n\t"
               "v_cmp_ne_u32 vcc, 7, %0\n\t"
               "s_and_b32 %2, vcc_hi, 0xffff\n\t"
               "s_cbranch_scc0 2f\n"
               "1:\n\t"
               "s_cmp_eq_u32 %3, 0\n\t"
               "s_cbranch_scc1 2f\n\t"
               "buffer_load_dword %0, %5, %6, %7 offen offset:8 sc1\n\t"
               "s_sub_u32 %3, %3, 1\n\t"
               "s_waitcnt vmcnt(0)\n\t"
               "v_sub_u32 %0, %0, %4\n\t"
               "v_cmp_ne_u32 vcc, 7, %0\n\t"
               "s_and_b32 %2, vcc_hi, 0xffff\n\t"
               "s_cbranch_scc1 1b\n\t"
               "buffer_load_dwordx4 %1, %5, %6, %7 offen sc1\n\t"
               "s_waitcnt vmcnt(0)\n"
               "2:"
               : "+v"(f), "+v"(q), "=&s"(s), "+s"(cx.budget)
               : "v"(cx.tk_kv), "v"(cx.fvz), "s"(cx.rs), "s"(so)
               : "vcc", "scc", "memory");
}
DEVI bool invalid(Cx& cx, unsigned sum) {
  const unsigned r = __builtin_amdgcn_readlane(wsum(sum), 63);
  return (r != 0) & (cx.budget >= 64);
}
#define D_O 8
#define D_X1 6
#define D_X2 2
#define PRE 1
#define TOUCH 1
template <int N> DEVI void nap() { if (N > 0) { SB(); __builtin_amdgcn_s_sleep(N); SB(); } }
DEVI unsigned usel(unsigned long long m, unsigned t, unsigned f) { unsigned r; asm("v_cndmask_b32_e64 %0, %1, %2, %3" : "=v"(r) : "v"(f), "v"(t), "s"(m)); return r; }
DEVI void mail_wait(Cx& cx, unsigned a, int mvo, int so, unsigned v) {
  int s;
  asm volatile("v_cmp_ne_u32 vcc, %3, %0\n\t"
               "s_and_b32 %1, vcc_lo, 0xffff\n\t"
               "s_cbranch_scc0 2f\n"
               "1:\n\t"
               "s_cmp_eq_u32 %2, 0\n\t"
               "s_cbranch_scc1 2f\n\t"
               "buffer_load_dword %0, %4, %5, %6 offen sc1\n\t"
               "s_sub_u32 %2, %2, 1\n\t"
               "s_waitcnt vmcnt(0)\n\t"
               "v_cmp_ne_u32 vcc, %3, %0\n\t"
               "s_and_b32 %1, vcc_lo, 0xffff\n\t"
               "s_cbranch_scc1 1b\n"
               "2:"
               : "+v"(a), "=&s"(s), "+s"(cx.budget)
               : "s"(v), "v"(mvo), "s"(cx.rs), "s"(so)
               : "vcc", "scc", "memory");
}
DEVI void kv_write_v(Cx& cx, const f32x16& accv, float rstd) {
  float vn[16];
#pragma unroll
  for (int r = 0; r < 16; r++) vn[r] = accv[r] * rstd;
  u32x4 v0, v1; pack16(vn, v0, v1);
  char* p = cx.C + 2 * cx.hk * INBOX + C_V + (32 * cx.w + cx.tok) * 16;
  *(u32x4*)p = v0; *(u32x4*)(p + INBOX + 128) = v1;
}
template <int VMW>
DEVI void kv_tail(Cx& cx, const u32x4 (&phk)[4], int kvb) {
  char* p = cx.C + 2 * cx.hk * INBOX + (32 * cx.w + cx.tok) * 16;
  *(u32x4*)p = phk[0]; *(u32x4*)(p + 2176) = phk[1]; *(u32x4*)(p + INBOX) = phk[2]; *(u32x4*)(p + INBOX + 2176) = phk[3];
  const int G = cx.lane >> 4, d = cx.lane & 15, q4 = d >> 2, p4 = cx.lane & 3;
  const int ro = (4 * G + q4) * 16 + (p4 & 1) * 8;
  const char* pa = cx.C + cx.w * INBOX + (p4 >> 1) * 2176 + ro;
  const char* pb = cx.C + ((cx.w & 2) + (p4 >> 1)) * INBOX + C_V + (p4 >> 1) * 128 + ro;
  if (VMW >= 0) vm_wait<(VMW >= 0 ? VMW : 0)>();
  lds_barrier();
  u32x4 a[4], b[4];
#pragma unroll
  for (int t = 0; t < 4; t++) { a[t] = tr_readc<256>(pa + t * 512); b[t] = tr_readc<256>(pb + t * 512); }
  const u32x4 ones = {0x3f803f80u, 0x3f803f80u, 0x3f803f80u, 0x3f803f80u};
  f32x4 kv = {}, z = {};
#pragma unroll
  for (int t = 0; t < 4; t++) { kv = MFMA16(a[t], b[t], kv); z = MFMA16(a[t], ones, z); }
  const u32x2 k2 = {pk2(kv[0], kv[1]), pk2(kv[2], kv[3])}, z2 = {pk2(z[0], z[1]), pk2(z[2], z[3])};
  const int e = cx.w * 16 + cx.g;
  stc2(cx.rs, (G >> 1) * 256 + d * 16 + (G & 1) * 8, kvb + e * 512, k2);
  const unsigned cs = -7u - wsum(k2[0] + k2[1] + usel(0x1000900010001000ull, z2[0] + z2[1], 0u));
  const u32x2 zl = {usel(1ull << 63, cx.N + 7, z2[0]), usel(1ull << 63, cs, z2[1])};
  stc2(cx.rs, d >= 12 ? (d - 12) * 32 + G * 8 : OOB, kvb + KV_F + e * FS, zl);
}
struct KvIn { u32x4 pa[8], pz; };
struct Misc { u32x4 xa, xb, ba, bb, cs[4]; u16 b1v[4]; };
DEVI u32x4 kv_poll(Cx& cx, int kvb) { return ldc(cx.rs, cx.fvz, kvb + KV_F + cx.w * 16 * FS); }
DEVI void kv_load(Cx& cx, KvIn& k, int kvb) {
#pragma unroll
  for (int m = 0; m < 8; m++) k.pa[m] = ldck(cx.rs, cx.vo, kvb + cx.w * 8192, m);
}

#define NW2_A 10
#define KQP0 31
#define KQW0 12

DEVI void stage1a(Cx& cx, const u16* __restrict__ x, const u16* __restrict__ wqkv, const u16* __restrict__ wphi,
                  const u16* __restrict__ wo, const u16* __restrict__ w1, const u16* __restrict__ b1, const u16* __restrict__ w2,
                  const u16* __restrict__ b2, u32x4 (&phq)[4], unsigned (&csw)[3], u32x4 (&aphi)[2], KvIn& kin, u32x4 (&w2f)[56],
                  u32x4 (&wof)[16], Misc& mi) {
  const int lane = cx.lane, w = cx.w, tok = cx.tok, hk = cx.hk, g = cx.g, c = cx.c;
  const int rot = (c + 2 * g) & 7;
  const rsrc_t rs = cx.rs;
  const unsigned lA = (unsigned)(size_t)cx.A, lB = (unsigned)(size_t)cx.B, lC = (unsigned)(size_t)cx.C;
  char* xs = cx.C + w * INBOX;
  char* stg = cx.B + 32768 + w * 4096;
  const unsigned lstg = lB + 32768 + w * 4096;
  unsigned vx[4];
#pragma unroll
  for (int i = 0; i < 4; i++) vx[i] = (8 * i + (lane >> 3)) * 1024 + (((lane & 7) ^ ((4 * i + (lane >> 4)) & 7)) * 16) - i * 1024;
  const char* xb = (const char*)(x + (size_t)(128 * g + 32 * w) * 512);
  const unsigned xl = lC + w * INBOX;
  const unsigned wvo = (lane >> 2) * 3072 + (lane & 3) * 16;
  const char* wb = (const char*)(wqkv + (size_t)(16 * w) * 1536 + 32 * c);
  auto cload = [&](int j, int i) LAMBDA {
    const int cj = (j + rot) & 7;
    if (i < 4) {
      const char* sb = xb + cj * 128;
      if (i == 0) gldso<0>(vx[0], sb, xl + (j % 3) * 4096);
      if (i == 1) gldso<1024>(vx[1], sb, xl + (j % 3) * 4096);
      if (i == 2) gldso<2048>(vx[2], sb, xl + (j % 3) * 4096);
      if (i == 3) gldso<3072>(vx[3], sb, xl + (j % 3) * 4096);
    } else {
      const char* sb = wb + cj * (64 * 3072);
      if (i == 4) gldso<0>(wvo + 1024, sb, lA + j * 8192 + w * 2048);
      if (i == 5) gldso<1024>(wvo + 1024, sb, lA + j * 8192 + w * 2048);
      if (i == 6) gldso<0>(wvo, sb, lB + j * 4096 + w * 1024);
    }
  };
#pragma unroll
  for (int i = 0; i < 6; i++) cload(0, i);
  const unsigned tvo = lane < 16 ? TOKR + g * 128 + lane * 4 : TOKC + cx.cb * 128 + ((lane - 16) & 31) * 4;
  const unsigned mvo = MBX + (g * 16 + c) * 512 + (lane & 31) * 16;
  gldsc1(tvo, cx.ws, lB + 5 * 4096 + w * 1024);
  gldsc4(mvo, cx.ws, lB + 6 * 4096 + w * 1024);
#pragma unroll
  for (int i = 0; i < 6; i++) cload(1, i);
  const int ch = (lane >> 4) & 1, q4 = (lane & 15) >> 2, p4 = lane & 3;
  const int cperm = (16 * (p4 & 1) + 4 * (2 * ch + (p4 >> 1))) * 2;
  int r_tr, r_a, r_b, r_c, r_dst, r_d0, r_d1, r_cs = OOB; unsigned r_dv = lane * 16; bool r_half = false;
  {
    const int lj = g * 4 + w, cb = cx.cb, th = cx.th;
    const u16* src; unsigned v0; int s0, s1;
    if (lj < 24) {
      const int bb = lj >> 3, m = lj & 7;
      src = w1 + (size_t)(64 * m) * 1792 + 112 * c + 32 * bb;
      v0 = (lane >> 2) * 3584 + (lane & 3) * 16; s0 = 16 * 3584; s1 = 32 * 3584;
      r_tr = (16 * hk + q4) * 64 + cperm; r_a = 512; r_b = 2048; r_c = 256;
      r_d0 = bb < 2 ? 2048 : 1536; r_d1 = 2 * r_d0;
      r_dst = WT_1 + c * W1_C + (bb < 2 ? bb * 1024 : 65536) + 4 * m * r_d0;
      r_cs = CS1 + (c * 4 + bb) * 1024 + m * 4;
    } else if (lj < 28) {
      const int m4 = lj - 24;
      src = w1 + (size_t)(128 * m4) * 1792 + 112 * c + 96;
      v0 = (lane >> 1) * 3584 + (lane & 1) * 16; s0 = 32 * 3584; s1 = 64 * 3584;
      r_tr = (8 * ch + 16 * hk + q4) * 32 + (8 * (p4 & 1) + 4 * (p4 >> 1)) * 2; r_a = 1024; r_b = 2048; r_c = 128;
      r_dst = WT_1 + c * W1_C + 65536 + 8 * m4 * 1536 + 1024; r_d0 = 3072; r_d1 = 6144;
      r_dv = ch * 1536 + (hk * 16 + (lane & 15)) * 16;
      r_cs = CS1 + (c * 4 + 3) * 1024 + m4 * 8; r_half = true;
    } else {
      v0 = (lane >> 3) * 1024 + (lane & 7) * 16; s0 = 8192; s1 = 16384;
      r_tr = (16 * hk + q4) * 128 + cperm; r_b = 1024; r_c = 512; r_d0 = 1024;
      int kq;
      if (lj < 36) {
        const int rg = 8 * th + lj - 28;
        kq = rg >> 2;
        src = wo + (size_t)(32 * rg) * 512 + 64 * cb;
        r_dst = WT_O + ((cb * 4 + kq) * 8 + ((2 * rg) & 7)) * 2048; r_d1 = 2048;
      } else if (lj < 60) {
        const int u = lj - 36, cc = 8 * th + (u & 7), blk = u >> 3;
        kq = cc >> 2;
        src = w2 + (size_t)(112 * cc + 32 * blk) * 512 + 64 * cb;
        r_dst = WT_2 + ((cb * 4 + kq) * 28 + 8 * blk + (cc & 3)) * 2048; r_d1 = 8192;
      } else {
        const int cc = 8 * th + 2 * (lj - 60);
        kq = cc >> 2;
        src = w2 + (size_t)(112 * cc + 96) * 512 + 64 * cb; s1 = 112 * 1024;
        r_tr = (8 * hk + q4) * 128 + cperm; r_b = 2048;
        r_dst = WT_2 + ((cb * 4 + kq) * 28 + 24 + (cc & 3)) * 2048; r_d1 = 2048;
      }
      r_tr += (kq & 1) * 64; r_a = (kq & 1) ? -64 : 64;
    }
    r_dst = RFL(r_dst); r_d0 = RFL(r_d0); r_d1 = RFL(r_d1); r_cs = RFL(r_cs);
    gldso<0>(v0, src, lstg); gldso<1024>(v0 + s0 - 1024, src, lstg); gldso<2048>(v0 + s1 - 2048, src, lstg); gldso<3072>(v0 + s0 + s1 - 3072, src, lstg);
  }
  gldso<0>(lane * 16, wphi + c * 512, lB + 7 * 4096 + w * 1024);
  const int xro = tok * 128 + (((2 * hk) ^ ((tok >> 1) & 7)) * 16);
  const char* wk = cx.A + hk * 2048 + q4 * 64 + cperm;
  const char* wq = cx.B + hk * 1024 + q4 * 64 + cperm;
  const unsigned on1 = (ch == ((lane >> 2) & 1)) ? 0x3f803f80u : 0u;
  const u32x4 sel = {on1, on1, on1, on1};
  const int mdst = w != 0 || lane >= 48 ? OOB
                 : lane < 16 ? MBX + (g * 16 + lane) * 512 + c * 8
                             : MBX + (((lane - 16) & 15) * 16 + ((c & ~4) | (((lane - 16) >> 4) << 2))) * 512 + (16 + g + 16 * cx.th) * 8;
  unsigned N = 0, sent = 0;
  u32x4 xf[33], ka[3], va[3], rf[4];
  f32x16 acck = {}, accv = {}, gram = {};
  f32x4 csk = {}, csv = {}, s1v = {};
  auto rdx = [&](int n) LAMBDA { xf[n] = *(const u32x4*)(xs + ((n >> 2) % 3) * 4096 + (xro ^ ((4 * ((n >> 1) & 1) + (n & 1)) * 16))); };
  auto rdk = [&](int n) LAMBDA { ka[n % 3] = tr_readc<256>(wk + (n >> 1) * 4096 + (n & 1) * 512); };
  auto rdv = [&](int n) LAMBDA { va[n % 3] = tr_readc<256>(wk + 1024 + (n >> 1) * 4096 + (n & 1) * 512); };
  auto send = [&](int slot, bool first) LAMBDA {
    const unsigned t = *(const unsigned*)(stg + slot * 256 + lane * 4);
    const u32x2 m = {t, N};
    stc2(rs, first || t != sent ? mdst : OOB, 0, m);
    sent = t;
  };
  vm_wait<13>(); bar(); STAMP(1); FST(1, 0);
  rdx(0); rdk(0); rdv(0); rdx(1); rdk(1); rdv(1);
  SB();
  float s2 = 0.f;
  sfor<32>([&](auto nn) LAMBDA {
    constexpr int n = nn, j = n >> 2, s = n & 3;
    if (s == 2 && j < 7) {
      constexpr int cnt[7] = {11, 7, 11, 6, 6, 7, 3};
      FST(1, 2 * j + 1); vm_wait<cnt[j < 7 ? j : 0]>(); bar(); FST(1, 2 * j + 2);
    }
    acck = MFMA(ka[n % 3], xf[n], acck); SB();
    if (n + 2 < 32) rdx(n + 2);
    if (s < 2 && j < 6) cload(j + 2, 3 * s);
    if (n == 14 || n == 18 || n == 22) gldsc1(tvo, cx.ws, lstg + (j & 1) * 256);
    if (n == 26) gldsc4(mvo, cx.ws, lstg + 1024);
    SB();
    csk = MFMA16(sel, ka[n % 3], csk); SB();
    if (n + 2 < 32) rdk(n + 2);
    if (n == 18 || n == 22 || n == 26) send((j - 1) & 1, n == 18);
    if (j >= 6) cload(n - 24, 6);
    SB();
    accv = MFMA(va[n % 3], xf[n], accv); SB();
    if (s < 2 && j < 6) cload(j + 2, 3 * s + 1);
    if (n == 2) {
      const unsigned t = *(const unsigned*)(cx.B + 5 * 4096 + w * 1024 + lane * 4);
      const unsigned m = *(const unsigned*)(cx.B + 6 * 4096 + w * 1024 + lane * 8);
      N = __builtin_amdgcn_readlane(t, c) + 64;
      while (BALLOT(t - N < 64u || m - N < 64u) != 0) N += 64;
      stc1(rs, w == 0 && (lane == c || lane == 16 + 16 * cx.th + g) ? (int)tvo : OOB, 0, N);
    }
    if (n == 6) {
      const char* st = stg + r_tr;
      rf[0] = tr_read2(st, st + r_c); rf[1] = tr_read2(st + r_a, st + r_a + r_c);
      rf[2] = tr_read2(st + r_b, st + r_b + r_c); rf[3] = tr_read2(st + r_a + r_b, st + r_a + r_b + r_c);
    }
    if (n == 7) {
      stc(rs, r_dv, r_dst, rf[0]); stc(rs, r_dv, r_dst + r_d0, rf[1]); stc(rs, r_dv, r_dst + r_d1, rf[2]); stc(rs, r_dv, r_dst + r_d0 + r_d1, rf[3]);
    }
    SB();
    csv = MFMA16(sel, va[n % 3], csv); SB();
    if (n + 2 < 32) rdv(n + 2);
    SB();
    if (n == 0) { gram = MFMA(xf[0], xf[0], gram); s1v = MFMA16(sel, xf[0], s1v); SB(); }
    if (n < 31) { gram = MFMA(xf[n + 1], xf[n + 1], gram); SB(); }
    if (s < 2 && j < 6) cload(j + 2, 3 * s + 2);
    if (n == 7) {
      const unsigned o2 = r_half ? 0x3f803f80u : on1;
      const u32x4 sl = {o2, o2, o2, o2};
      f32x4 ca = {}, cb2 = {};
      ca = MFMA16(sl, rf[0], ca); cb2 = MFMA16(sl, rf[2], cb2); ca = MFMA16(sl, rf[1], ca); cb2 = MFMA16(sl, rf[3], cb2);
      stc1(rs, r_half ? tok * 32 + hk * 4 : (hk ? OOB : tok * 32), r_cs, __float_as_uint(r_half ? (hk ? cb2[0] : ca[0]) : ca[0] + cb2[0]));
    }
    if (n == 8) {
      const char* ap = cx.B + 7 * 4096 + w * 1024 + hk * 512 + q4 * 32 + (2 * ch + (p4 >> 1)) * 8;
      aphi[0] = tr_readc<128>(ap); aphi[1] = tr_readc<128>(ap + 256);
    }
    if (n == 11) {
      const bool on = ((lane >> 2) & 1) == hk;
#pragma unroll
      for (int t = 0; t < 4; t++) { aphi[0][t] = on ? aphi[0][t] : 0u; aphi[1][t] = on ? aphi[1][t] : 0u; }
      asm volatile("" : "+v"(aphi[0]), "+v"(aphi[1]));
    }
    if (n == 31) {
      float t8[8], t4[4];
#pragma unroll
      for (int r = 0; r < 8; r++) t8[r] = lsel(0xFFFF0000FFFF0000ull, gram[8 + r], gram[r]);
#pragma unroll
      for (int r = 0; r < 4; r++) t4[r] = lsel(0xFF00FF00FF00FF00ull, t8[4 + r], t8[r]);
      const float u0 = lsel(0xCCCCCCCCCCCCCCCCull, t4[2], t4[0]), u1 = lsel(0xCCCCCCCCCCCCCCCCull, t4[3], t4[1]);
      s2 = lsel(0xAAAAAAAAAAAAAAAAull, u1, u0);
      s2 = xhalf_sum(((tok >> 2) & 1) == hk ? s2 : 0.f);
    }
    SB();
    if (n < 31) { s1v = MFMA16(sel, xf[n + 1], s1v); SB(); }
    if (n < 31) asm volatile("" : "+a"(acck), "+a"(accv), "+a"(csk), "+a"(csv), "+a"(s1v), "+a"(gram));
    SB();
  });
  STAMP(2); FST(1, 15);
  const float mean = s1v[0] * (1.f / 512.f);
  xf[32] = aug_tok(mean, 0.f, hk);
  csw[1] = cs_pack(csk[0]);
  acck = MFMA(aug_w(csw[1], 0u), xf[32], acck);
  csw[2] = cs_pack(csv[0]);
  accv = MFMA(aug_w(csw[2], 0u), xf[32], accv);
  const float rstd = __builtin_amdgcn_rsqf(fmaxf(s2 * (1.f / 512.f) - mean * mean, 0.f) + 1e-5f);
  const float sc = rstd * LOG2E;
  f32x16 p = {};
  { u32x4 b0, b1; pack16(acck, b0, b1); p = MFMA(aphi[0], b0, p); p = MFMA(aphi[1], b1, p); }
  SB();
  const int js = ((g >> 1) - rot) & 7;
  u32x4 wf0, wf1;
  { const char* wsrc = wk + (w & 1) * 1024 + js * 8192 + (g & 1) * 4096; wf0 = tr_readc<256>(wsrc); wf1 = tr_readc<256>(wsrc + 512); }
  char* tab = cx.B + 32768 + 2048;
  if (w == 0) {
    vm_wait<7>();
    u32x2 m = *(const u32x2*)(stg + 1024 + lane * 8);
    while (true) {
      const bool valid = m[0] == N;
      if (BALLOT(valid && m[1] != sent && lane < 48) != 0) {
        const u32x2 r = {m[1], N};
        stc2(rs, valid && m[1] != sent ? mdst : OOB, 0, r);
        sent = valid ? m[1] : sent;
        COUNT(40);
      }
      if (BALLOT(!valid && lane < 48) == 0) break;
      if (cx.budget == 0) break;
      cx.budget--;
      COUNT(41);
      asm volatile("" ::: "memory");
      m = ldc2(rs, lane * 8, MBX + (g * 16 + c) * 512);
    }
    *(unsigned*)(tab + lane * 4) = lane < 48 ? m[1] : 0u;
    if (lane == 0) *(int*)(tab + 256) = cx.budget;
  }
  lds_barrier();
  STAMP(3);
  {
    const int e = lane >> 1;
    cx.N = RFL(*(const unsigned*)(tab + c * 4));
    cx.budget = RFL(*(const int*)(tab + 256));
    cx.tk_kv = *(const unsigned*)(tab + ((lane >> 4) == 2 ? 16 * cx.th + lane - 16 : 63) * 4);
    cx.tk_oh = *(const unsigned*)(tab + (lane < 8 ? 4 * w + e : 63) * 4);
    cx.tk_x = *(const unsigned*)(tab + (lane < 16 ? 8 * (e >> 2) + 4 * (w >> 1) + (e & 3) : 63) * 4);
  }
  const int kvb = KVA + c * KV_C;
  if (TOUCH) {
    const int t = lane >> 3;
    const u32x4 v = {cx.N, cx.N, cx.N, cx.N};
    const int o = (t < 5 ? G_FO + t * FHL + cx.fo : t == 5 ? G_FX1 + cx.fxo : t == 6 ? G_FX2 + cx.fxo : kvb + KV_L + KV_F + (w * 16 + g) * FS - cx.gb) + (lane & 7) * 16 + cx.gb;
    stc(rs, o, 0, v);
    stc(rs, t < 6 ? o : OOB, G_L, v);
  }
  char* dA = cx.A + w * 16384;
  const int sA = WT_1 + c * W1_C + w * 16384;
  {
    Softmax sm; u32x4 phk[4];
    sfor<29>([&](auto ii) LAMBDA {
      constexpr int i = ii;
      sm.step(i, p, sc, phk);
      if (i < 10) dma<(i < 10 ? i : 0)>(rs, dA, cx.vo, sA);
      if (i == 2) kv_write_v(cx, accv, rstd);
      SB();
    });
    STAMP(4);
    kv_tail<(TOUCH ? 12 : 10)>(cx, phk, kvb);
  }
  STAMP(5);
  {
    f32x16 accq = {}; f32x4 csq = {};
    u32x4 qa[3];
    auto rdq = [&](int n) LAMBDA { qa[n % 3] = tr_readc<256>(wq + (n >> 1) * 2048 + (n & 1) * 512); };
    rdq(0); rdq(1); rdq(2);
    const int wd = WT_QKV + c * WQ_C;
    SB();
    sfor<32>([&](auto nn) LAMBDA {
      constexpr int n = nn;
      accq = MFMA(qa[n % 3], xf[n], accq); SB();
      if (n == 0 && w == 2) { const char* wsrc = wq + js * 4096 + (g & 1) * 2048; wf0 = tr_readc<256>(wsrc); wf1 = tr_readc<256>(wsrc + 512); }
      if (n == 3) {
        const int so = w < 2 ? wd + g * 4096 + w * 1024 : w == 2 ? wd + 65536 + g * 2048 : OOB;
        stc(rs, cx.vo, so, wf0); stc(rs, cx.vo, so + (w < 2 ? 2048 : 1024), wf1);
      }
      if (n < 6) dma<(n < 6 ? 10 + n : 0)>(rs, dA, cx.vo, sA);
      else if (n < 22) wof[n - 6] = ldck(rs, cx.vo, WT_O + (cx.cb * 4 + w) * 16384, n - 6);
      else if (n - 22 < NW2_A) w2f[n - 22] = ldck(rs, cx.vo, WT_2 + (cx.cb * 4 + w) * 57344, n - 22);
      if (n == KQP0) kin.pz = kv_poll(cx, kvb);
      SB();
      csq = MFMA16(sel, qa[n % 3], csq); SB();
      if (n + 3 < 32) rdq(n + 3);
      asm volatile("" : "+a"(accq), "+a"(csq));
      SB();
    });
    csw[0] = cs_pack(csq[0]);
    accq = MFMA(aug_w(csw[0], 0u), xf[32], accq);
    u32x4 q0, q1; pack16(accq, q0, q1);
    f32x16 pq = {}; pq = MFMA(aphi[0], q0, pq); pq = MFMA(aphi[1], q1, pq);
    Softmax sq;
    const int col = 64 * cx.cb + 32 * (w & 1) + 16 * hk;
    const u16* xr = x + (size_t)(128 * g + 64 * cx.th + 32 * (w >> 1) + tok) * 512 + col;
    const int pi = 16 * ((tok >> 2) & 1) + (tok & 3) + 4 * (tok >> 3), ph = 8 * ((tok >> 2) & 1) + (tok & 3) + 4 * ((tok >> 3) & 1);
    const u16* bp = b1 + 112 * c;
    SB();
    sfor<29>([&](auto ii) LAMBDA {
      constexpr int i = ii;
      sq.step(i, pq, sc, phq);
      if (i + 32 == KQP0) kin.pz = kv_poll(cx, kvb);
      if (i == KQW0) { SB(); kv_wait(cx, kin.pz, kvb + KV_F + w * 16 * FS); STAMP(6); kv_load(cx, kin, kvb); }
      if (i == 17) mi.xa = *(const u32x4*)xr;
      if (i == 18) mi.xb = *(const u32x4*)(xr + 8);
      if (i >= 19 && i < 23) mi.cs[(i - 19) & 3] = ldc(rs, tok * 32 + hk * 16 + ((i - 19) & 3) * 1024, CS1 + c * 4096);
      if (i >= 23 && i < 27) mi.b1v[(i - 23) & 3] = bp[i == 26 ? 96 + ph : 32 * (i - 23) + pi];
      if (i == 27) mi.ba = *(const u32x4*)(b2 + col);
      if (i == 28) mi.bb = *(const u32x4*)(b2 + col + 8);
      SB();
    });
  }
}

DEVI void red_send(Cx& cx, const f32x16& a, int j) {
  char* dst = cx.C + (cx.w ^ j) * INBOX + (j - 1) * 4096 + cx.vo;
#pragma unroll
  for (int m = 0; m < 4; m++) { f32x4 v = {a[4 * m], a[4 * m + 1], a[4 * m + 2], a[4 * m + 3]}; *(f32x4*)(dst + m * 1024) = v; }
}
DEVI void red_recv(Cx& cx, const f32x16& own, float (&xr)[16]) {
  f32x4 v[12];
#pragma unroll
  for (int k = 0; k < 12; k++) v[k] = *(const f32x4*)(cx.C + cx.w * INBOX + ((k % 3) * 4 + k / 3) * 1024 + cx.vo);
#pragma unroll
  for (int m = 0; m < 4; m++)
#pragma unroll
    for (int e = 0; e < 4; e++) xr[4 * m + e] = (own[4 * m + e] + v[3 * m][e]) + (v[3 * m + 1][e] + v[3 * m + 2][e]);
}
DEVI void unpack16(const u32x4& a, const u32x4& b, float (&x)[16]) {
#pragma unroll
  for (int t = 0; t < 4; t++) { x[2 * t] = bflo(a[t]); x[2 * t + 1] = bfhi(a[t]); x[8 + 2 * t] = bflo(b[t]); x[9 + 2 * t] = bfhi(b[t]); }
}
DEVI void publish_x(Cx& cx, float (&xr)[16], int xb, int sb, int fb) {
  u32x4 f0, f1; pack16(xr, f0, f1);
  stc(cx.rs, cx.vo, xb + cx.xo, f0);
  stc(cx.rs, cx.vo + 1024, xb + cx.xo, f1);
  SB();
  unpack16(f0, f1, xr);
  float s1 = xr[0] + xr[1], s2 = xr[0] * xr[0], t1 = xr[2] + xr[3], t2 = xr[1] * xr[1];
#pragma unroll
  for (int r = 2; r < 16; r += 2) { s2 = fmaf(xr[r], xr[r], s2); t2 = fmaf(xr[r + 1], xr[r + 1], t2); if (r >= 4) { s1 += xr[r]; t1 += xr[r + 1]; } }
  auto t = __builtin_amdgcn_permlane32_swap(__float_as_uint(s1 + t1), __float_as_uint(s2 + t2), false, false);
  const unsigned st = __float_as_uint(__uint_as_float(t[0]) + __uint_as_float(t[1]));
  stc1(cx.rs, (cx.lane & 15) * 16 + (cx.lane >> 4 & 1) * 8 + cx.hk * 4, sb + cx.so, st);
  put_final(cx, fb + cx.fxo, sum4(sum4(st, f0), f1));
}
DEVI void park(Cx& cx, const float (&xr)[16]) {
  char* p = cx.C + cx.w * INBOX + C_PARK + cx.vo;
#pragma unroll
  for (int m = 0; m < 4; m++) { f32x4 v = {xr[4 * m], xr[4 * m + 1], xr[4 * m + 2], xr[4 * m + 3]}; *(f32x4*)(p + m * 1024) = v; }
}
DEVI f32x16 unpark(Cx& cx) {
  const char* p = cx.C + cx.w * INBOX + C_PARK + cx.vo;
  f32x16 r;
#pragma unroll
  for (int m = 0; m < 4; m++) { f32x4 v = *(const f32x4*)(p + m * 1024); r[4 * m] = v[0]; r[4 * m + 1] = v[1]; r[4 * m + 2] = v[2]; r[4 * m + 3] = v[3]; }
  return r;
}
DEVI f32x16 tovec(const float (&x)[16]) {
  f32x16 r;
#pragma unroll
  for (int e = 0; e < 16; e++) r[e] = x[e];
  return r;
}
#define UNLIKELY(x) __builtin_expect(!!(x), 0)

template <class AUG, class WIN>
DEVI void xpass(Cx& cx, int xb, int sb, int fb, u32x4 (&xf)[33], f32x16& a0, f32x16& a1, int sti, AUG aug, WIN win) {
  const rsrc_t rs = cx.rs;
  xb += cx.w * 32768; sb += cx.w * 4096; fb += cx.w * 16 * FS;
  if (sti == 15) FST(7, 0);
  if (sti == 25) FST(8, 0);
  if (sti == 15) nap<D_X2>(); else nap<D_X1>();
  u32x2 pl0 = ldc2(rs, cx.fv16, fb);
  if (sti == 25 && FINE == 9) { FST(9, 8); vm_wait<0>(); FST(9, 9); }
  const unsigned sum0 = final_wait<16>(cx, pl0, fb, cx.tk_x);
  if (sti == 25) FST(9, 10);
  STAMP(sti); if (sti != 15) FST(sti == 11 ? 4 : 5, 0); else FST(7, 1);
  if (sti == 25) FST(8, 1);
  u32x4 st[4], ra[3], rb[3], w0, w1;
  auto ld = [&](int q) LAMBDA {
    if (q < 4) st[q] = ldck(rs, cx.vo, sb, q);
    else if (q < 36) xf[q - 4] = ldck(rs, cx.vo, xb, q - 4);
  };
  auto stats = [&]() LAMBDA {
    float v[4];
#pragma unroll
    for (int e = 0; e < 4; e++) v[e] = (__uint_as_float(st[0][e]) + __uint_as_float(st[1][e])) + (__uint_as_float(st[2][e]) + __uint_as_float(st[3][e]));
    auto p = __builtin_amdgcn_permlane32_swap(__float_as_uint(v[0]), __float_as_uint(v[1]), false, false);
    auto q = __builtin_amdgcn_permlane32_swap(__float_as_uint(v[2]), __float_as_uint(v[3]), false, false);
    auto r = __builtin_amdgcn_permlane16_swap(__float_as_uint(__uint_as_float(p[0]) + __uint_as_float(p[1])), __float_as_uint(__uint_as_float(q[0]) + __uint_as_float(q[1])), false, false);
    const unsigned t = __float_as_uint((__uint_as_float(r[0]) + __uint_as_float(r[1])) * (1.f / 512.f));
    auto u = __builtin_amdgcn_permlane32_swap(t, t, false, false);
    const float mean = __uint_as_float(u[0]);
    aug(mean, fmaxf(__uint_as_float(u[1]) - mean * mean, 0.f), w0, w1);
  };
  const char* pa = cx.A + cx.vo;
#pragma unroll
  for (int q = 0; q < 10; q++) ld(q);
  SB();
  if (sti == 15) FST(7, 2);
  if (sti == 25) FST(8, 2);
  bar();
  if (sti == 15) FST(7, 3);
  if (sti == 25) FST(8, 3);
#pragma unroll
  for (int n = 0; n < 3; n++) { ra[n] = *(const u32x4*)(pa + n * 2048); rb[n] = *(const u32x4*)(pa + n * 2048 + 1024); }
  SB();
  unsigned sum = sum0;
  const f32x16 zero = {};
#pragma unroll
  for (int q = 0; q < 4; q++) { sum = sum4(sum, st[q]); SB(); ld(10 + q); SB(); }
  stats();
  SB();
  a0 = MFMA(w0, xf[32], zero); a1 = MFMA(w1, xf[32], zero);
  SB();
  sfor<32>([&](auto nn) LAMBDA {
    constexpr int n = nn;
    if (n % 8 == 0 && sti != 15) FST(sti == 11 ? 4 : 5, 1 + n / 8);
    a0 = MFMA(ra[n % 3], xf[n], a0); SB();
    if (n % 4 == 0 && sti == 15) FST(7, 4 + n / 4);
    if (n % 4 == 0 && sti == 25) FST(8, 4 + n / 4);
    if (n == 0 && sti == 25) FST(9, 11);
    if (n + 3 < 32) ra[n % 3] = *(const u32x4*)(pa + (n + 3) * 2048);
    if (n + 14 < 36) ld(n + 14); else win(ic<(n < 22 ? 0 : n - 22)>());
    SB();
    a1 = MFMA(rb[n % 3], xf[n], a1); SB();
    if (n + 3 < 32) rb[n % 3] = *(const u32x4*)(pa + (n + 3) * 2048 + 1024);
    sum = sum4(sum, xf[n]);
    SB();
  });
  if (sti != 15) FST(sti == 11 ? 4 : 5, 5); else FST(7, 12);
  if (sti == 25) FST(8, 12);
  asm volatile("" : "+a"(a0), "+a"(a1));
  if (UNLIKELY(invalid(cx, sum))) {
    do {
      asm volatile("" ::: "memory"); cx.budget -= 64;
#pragma unroll
      for (int q = 0; q < 36; q++) ld(q);
      sum = sum0;
#pragma unroll
      for (int q = 0; q < 32; q++) sum = sum4(sum, xf[q]);
#pragma unroll
      for (int q = 0; q < 4; q++) sum = sum4(sum, st[q]);
    } while (invalid(cx, sum));
    stats();
    a0 = MFMA(w0, xf[32], zero); a1 = MFMA(w1, xf[32], zero);
#pragma unroll
    for (int n = 0; n < 32; n++) {
      a0 = MFMA(*(const u32x4*)(pa + n * 2048), xf[n], a0); a1 = MFMA(*(const u32x4*)(pa + n * 2048 + 1024), xf[n], a1);
      SB();
    }
  }
}

template <bool P> DEVI f32x2 mul2(f32x2 a, f32x2 b) {
  f32x2 r;
  if (P) asm volatile("v_pk_mul_f32 %0, %1, %2" : "=v"(r) : "v"(a), "v"(b));
  else {
    asm volatile("v_mul_f32 %0, %1, %2" : "=v"(r[0]) : "v"(a[0]), "v"(b[0]));
    asm volatile("v_mul_f32 %0, %1, %2" : "=v"(r[1]) : "v"(a[1]), "v"(b[1]));
  }
  return r;
}
template <bool P> DEVI f32x2 fma2(f32x2 a, f32x2 c) {
  f32x2 r;
  if (P) asm volatile("v_pk_fma_f32 %0, %1, %2, %2 op_sel:[0,1,0] op_sel_hi:[1,1,0]" : "=v"(r) : "v"(a), "v"(c));
  else {
    asm volatile("v_fma_f32 %0, %1, %2, %3" : "=v"(r[0]) : "v"(a[0]), "v"(c[1]), "v"(c[0]));
    asm volatile("v_fma_f32 %0, %1, %2, %3" : "=v"(r[1]) : "v"(a[1]), "v"(c[1]), "v"(c[0]));
  }
  return r;
}
struct Gelu {
  f32x2 cc, ia;
  f32x2 a[4], t, d, e, r, u;
  u32x4 f; unsigned sum;
  template <int K0, int K1, int K, int H, class MID>
  DEVI void step(Cx& cx, const f32x16& lo, const f32x16& hi, int hb, MID mid) {
    constexpr bool P = H == 2, p0 = K >= K0 && K < K1, p1 = K - 1 >= K0 && K - 1 < K1, p2 = K - 2 >= K0 && K - 2 < K1;
    constexpr int v = K - 2;
    f32x2 s, y;
    auto rc = [&]() LAMBDA { if (p2) { r[0] = __builtin_amdgcn_rcpf(d[0]); r[1] = __builtin_amdgcn_rcpf(d[1]); SB(); } };
    auto ex = [&]() LAMBDA { if (p1) { e[0] = __builtin_amdgcn_exp2f(t[0]); e[1] = __builtin_amdgcn_exp2f(t[1]); SB(); } };
    auto rd = [&]() LAMBDA {
      if (p0) {
        const f32x16& h = (K & 8) ? hi : lo;
        float x0, x1;
        asm volatile("v_accvgpr_read_b32 %0, %2\n\tv_accvgpr_read_b32 %1, %3" : "=v"(x0), "=v"(x1) : "a"(h[(2 * K) & 15]), "a"(h[(2 * K + 1) & 15]));
        a[K & 3][0] = x0; a[K & 3][1] = x1;
        SB();
      }
    };
    auto fin = [&]() LAMBDA { if (p2 && (v & 3) == 3) { stc(cx.rs, cx.vo, hb + (v >> 2) * 8192, f); sum = sum4(sum, f); } };
    if (H != 1) rd();
    if (H != 0) rc();
    if (H != 1) ex();
    mid(); SB();
    if (H != 0 && p0 && p2) {
      float y0, y1;
      asm volatile("v_mul_f32 %2, %6, %8\n\t"
                   "v_mul_f32 %3, %7, %9\n\t"
                   "v_pk_mul_f32 %0, %5, %5\n\t"
                   "v_cvt_pk_bf16_f32 %4, %2, %3\n\t"
                   "v_pk_fma_f32 %0, %0, %11, %11 op_sel:[0,1,0] op_sel_hi:[1,1,0]\n\t"
                   "v_pk_fma_f32 %1, %10, %12, %12\n\t"
                   "v_pk_mul_f32 %0, %5, %0"
                   : "=&v"(t), "=&v"(d), "=&v"(y0), "=&v"(y1), "=&v"(f[v & 3])
                   : "v"(a[K & 3]), "v"(a[v & 3][0]), "v"(a[v & 3][1]), "v"(r[0]), "v"(r[1]), "v"(e), "v"(cc), "v"(ia));
    } else if (H != 0) {
      if (p0) s = mul2<0>(a[K & 3], a[K & 3]);
      if (p2) y = mul2<0>(a[v & 3], r);
      if (p0) u = fma2<0>(s, cc);
      if (p1) d = fma2<0>(e, ia);
      if (p0) t = mul2<0>(a[K & 3], u);
      if (p2) f[v & 3] = pk2(y[0], y[1]);
    }
    if (H != 0) fin();
  }
};

#define GB2 34
#define GP1 26
#define GW1 40
#define GP3 23
#define GP4 55
#define W2KEEP 46
#define NW2_C 22
#define NW2_H 28
#define NB1H 6
#define NQK 10
#define KQP1 31
#define KQW1 12

template <int L>
DEVI void layer(Cx& cx, const u16* __restrict__ x, const u16* __restrict__ b1, const u16* __restrict__ b2, u16* __restrict__ out,
                u32x4 (&phq)[4], const unsigned (&csw)[3], const u32x4 (&aphi)[2], KvIn& kin, u32x4 (&w2f)[56], u32x4 (&wof)[16],
                Misc& mi, unsigned (&csp)[4], unsigned (&b1p)[2]) {
  const int lane = cx.lane, w = cx.w, tok = cx.tok, hk = cx.hk, g = cx.g, c = cx.c;
  const rsrc_t rs = cx.rs;
  const int gl = cx.gb + L * G_L;
  const int kvb = KVA + L * KV_L + c * KV_C;
  const int w2b = WT_2 + (cx.cb * 4 + w) * 57344;
  const f32x16 zero = {};
  constexpr int SO = 14 * L;
  u32x4 xa, xb2;
  const int col = 64 * cx.cb + 32 * (w & 1) + 16 * hk;
  const size_t xrow = (size_t)(128 * g + 64 * cx.th + 32 * (w >> 1) + tok) * 512 + col;
  if (L == 1) {
    u32x4 xf[33]; f32x16 acck, accv; float rstd;
    xpass(cx, cx.gb + G_X2, cx.gb + G_ST2, cx.gb + G_FX2, xf, acck, accv, 15,
          [&](float mean, float var, u32x4& w0, u32x4& w1) LAMBDA {
            rstd = __builtin_amdgcn_rsqf(var + 1e-5f);
            xf[32] = aug_tok(mean, 0.f, hk); w0 = aug_w(csw[1], 0u); w1 = aug_w(csw[2], 0u);
          },
          [&](auto ii) LAMBDA { if (ii.v < 8) dma<(ii.v & 7)>(rs, cx.B + w * 8192, cx.vo, WT_QKV + c * WQ_C + 65536 + w * 8192); });
    STAMP(16); FST(6, 0); FST(7, 13);
    const float sc = rstd * LOG2E;
    f32x16 p;
    { u32x4 b0, b1; pack16(acck, b0, b1); p = MFMA(aphi[0], b0, zero); p = MFMA(aphi[1], b1, p); }
    FST(6, 1);
    kv_write_v(cx, accv, rstd);
    SB();
    {
      Softmax sm; u32x4 phk[4];
      sfor<29>([&](auto ii) LAMBDA {
        constexpr int i = ii;
        if (i % 4 == 0) FST(6, 2 + i / 4);
        sm.step(i, p, sc, phk);
        SB();
      });
      STAMP(17); FST(6, 10);
      kv_tail<0>(cx, phk, kvb);
    }
    STAMP(18); FST(6, 11);
    f32x16 accq = MFMA(aug_w(csw[0], 0u), xf[32], zero);
    u32x4 qa[3];
    const char* pq = cx.B + cx.vo;
#pragma unroll
    for (int n = 0; n < 3; n++) qa[n] = *(const u32x4*)(pq + n * 1024);
    char* dA = cx.A + w * 16384;
    const int sA = WT_1 + c * W1_C + w * 16384;
    SB();
    sfor<32>([&](auto nn) LAMBDA {
      constexpr int n = nn;
      accq = MFMA(qa[n % 3], xf[n], accq); SB();
      if (n + 3 < 32) qa[n % 3] = *(const u32x4*)(pq + (n + 3) * 1024);
      if (n >= 1 && n < 17) wof[n - 1] = ldck(rs, cx.vo, WT_O + (cx.cb * 4 + w) * 16384, n - 1);
      if (n >= 17 && (n & 1)) dma<(n >= 17 ? (n - 17) / 2 : 0)>(rs, dA, cx.vo, sA);
      if (n == KQP1) kin.pz = kv_poll(cx, kvb);
      if (n % 8 == 7) FST(6, 12 + n / 8);
      SB();
    });
    u32x4 b0, b1; pack16(accq, b0, b1);
    f32x16 pq2 = MFMA(aphi[0], b0, zero); pq2 = MFMA(aphi[1], b1, pq2);
    Softmax sq;
    sfor<29>([&](auto ii) LAMBDA {
      constexpr int i = ii;
      sq.step(i, pq2, sc, phq);
      if (i + 32 == KQP1) kin.pz = kv_poll(cx, kvb);
      if (i == KQW1) { SB(); kv_wait(cx, kin.pz, kvb + KV_F + w * 16 * FS); STAMP(19); kv_load(cx, kin, kvb); }
      if (i >= 19 && i < 27) dma<(i >= 19 && i < 27 ? i - 11 : 0)>(rs, dA, cx.vo, sA);
      if (i == 27) mi.ba = *(const u32x4*)(b2 + col);
      if (i == 28) mi.bb = *(const u32x4*)(b2 + col + 8);
      SB();
    });
  }
  asm volatile("" : "+v"(phq[0]), "+v"(phq[1]), "+v"(phq[2]), "+v"(phq[3]));
  SB();
  STAMP(7 + SO);
  {
    const int G = lane >> 4, d = lane & 15;
    u32x4 idn;
#pragma unroll
    for (int t = 0; t < 4; t++) { const int k = 8 * (G & 1) + 2 * t; idn[t] = k == d ? 0x3f80u : k + 1 == d ? 0x3f800000u : 0u; }
    f32x4 kv; unsigned sum;
    auto red = [&]() LAMBDA {
      f32x4 k0 = {}, k1 = {}; sum = -cx.tk_kv;
#pragma unroll
      for (int m = 0; m < 8; m += 2) {
        k0 = MFMA16(idn, kin.pa[m], k0); k1 = MFMA16(idn, kin.pa[m + 1], k1);
        sum = sum4(sum4(sum, kin.pa[m]), kin.pa[m + 1]);
      }
      sum = sum4(sum, kin.pz); kv = k0 + k1;
    };
    red();
    asm volatile("" : "+v"(kv));
    if (UNLIKELY(invalid(cx, sum))) {
      do { asm volatile("" ::: "memory"); cx.budget -= 64; kin.pz = kv_poll(cx, kvb); kv_load(cx, kin, kvb); red(); } while (invalid(cx, sum));
    }
    char* img = cx.C + w * INBOX + C_IMG;
    { const u32x2 v = {pk2(kv[0], kv[1]), pk2(kv[2], kv[3])}; *(u32x2*)(img + d * 32 + G * 8) = v; }
    { const u32x4 z4 = {}; *(u32x4*)(img + 512 + cx.vo) = hk ? z4 : kin.pz; }
    lds_barrier();
    const int hp = (tok >> 2) & 1, dd = (tok & 3) + 4 * (tok >> 3);
    const char* ap = cx.C + (hp == hk ? 2 * hp * INBOX + C_IMG + dd * 32 : C_ZERO);
    const int zo = hp == hk ? 512 : 0;
    u32x4 a[4], az[4];
#pragma unroll
    for (int s4 = 0; s4 < 4; s4++) az[s4] = *(const u32x4*)(ap + zo + (s4 >> 1) * INBOX + (s4 & 1) * 16);
#pragma unroll
    for (int s4 = 0; s4 < 4; s4++) a[s4] = *(const u32x4*)(ap + (s4 >> 1) * INBOX + (s4 & 1) * 16);
    if (L == 0) stc1(rs, w == 0 && (lane >> 4) == 2 ? (lane - 32) * 8192 : OOB, MBX + c * 512 + (48 + g) * 8, cx.tk_kv + 1);
    SB();
    f32x16 num = {}, den = {};
#pragma unroll
    for (int s4 = 0; s4 < 4; s4++) den = MFMA(az[s4], phq[s4], den);
#pragma unroll
    for (int s4 = 0; s4 < 4; s4++) num = MFMA(a[s4], phq[s4], num);
    float ds[4];
#pragma unroll
    for (int r = 0; r < 4; r++) ds[r] = (den[r] + den[4 + r]) + (den[8 + r] + den[12 + r]);
    const float inv = __builtin_amdgcn_rcpf(((ds[0] + ds[1]) + (ds[2] + ds[3])) + 1e-6f);
    float ov[16];
#pragma unroll
    for (int r = 0; r < 16; r++) ov[r] = num[r] * inv;
    u32x4 f0, f1; pack16(ov, f0, f1);
    stc(rs, cx.vo, gl + G_O + cx.oo, f0);
    stc(rs, cx.vo + 1024, gl + G_O + cx.oo, f1);
    put_final(cx, gl + G_FO + cx.fo, sum4(sum4(0u, f0), f1));
    STAMP(8 + SO);
  }
  float xr[16];
  {
    const int ob = gl + G_O + (cx.th * 4 + w) * 16384, fb = gl + G_FO + (cx.th * 4 + w) * 8 * FS;
    f32x16 acc[4];
    if (L == 0) {
      unpack16(mi.xa, mi.xb, xr); acc[0] = tovec(xr);
#pragma unroll
      for (int k = 0; k < 4; k++) {
        const f32x4 v = __builtin_bit_cast(f32x4, mi.cs[k]);
        csp[k] = cs_pack(xhalf_sum((v[0] + v[1]) + (v[2] + v[3])));
      }
      b1p[0] = mi.b1v[0] | ((unsigned)mi.b1v[1] << 16); b1p[1] = mi.b1v[2] | ((unsigned)mi.b1v[3] << 16);
    } else acc[0] = unpark(cx);
    SB();
    char* dB = cx.B + w * 12288;
    const int sB = WT_1 + c * W1_C + 65536 + w * 12288;
    sfor<NB1H>([&](auto ii) LAMBDA { dma<ii.v>(rs, dB, cx.vo, sB); });
    nap<D_O>();
    const unsigned sum0 = final_wait<8>(cx, ldc2(rs, cx.fv8, fb), fb, cx.tk_oh);
    STAMP(9 + SO);
    u32x4 of[16];
#pragma unroll
    for (int k = 0; k < 10; k++) of[k ^ 8] = ldck(rs, cx.vo, ob, k ^ 8);
    SB();
    lds_barrier();
    unsigned sum = sum0;
    sfor<32>([&](auto nn) LAMBDA {
      constexpr int n = nn, s = (n >> 1) & 7, j = (n < 16 ? 3 : 1) - (n & 1);
      acc[j] = MFMA(wof[2 * s + (j & 1)], of[8 * (j >> 1) + s], s == 0 && j ? zero : acc[j]); SB();
      if (n < 6) of[(n + 10) ^ 8] = ldck(rs, cx.vo, ob, (n + 10) ^ 8);
      if (n >= 7 && n < 19 - NB1H) dma<(n >= 7 && n < 19 - NB1H ? n - 7 + NB1H : 0)>(rs, dB, cx.vo, sB);
      if (L == 0 && n >= 19 - NB1H && NW2_A + n - (19 - NB1H) < NW2_C) w2f[NW2_A + n - (19 - NB1H)] = ldck(rs, cx.vo, w2b, NW2_A + n - (19 - NB1H));
      if (n & 1) sum = sum4(sum, of[8 * (j >> 1) + s]);
      if (n == 18) red_send(cx, acc[3], 3);
      if (n == 22) red_send(cx, acc[2], 2);
      SB();
    });
    red_send(cx, acc[1], 1);
    asm volatile("" : "+a"(acc[0]));
    if (UNLIKELY(invalid(cx, sum))) {
      do {
        asm volatile("" ::: "memory"); cx.budget -= 64;
        sum = sum0;
#pragma unroll
        for (int k = 0; k < 16; k++) { of[k] = ldck(rs, cx.vo, ob, k); sum = sum4(sum, of[k]); }
      } while (invalid(cx, sum));
      if (L == 0) { xa = *(const u32x4*)(x + xrow); xb2 = *(const u32x4*)(x + xrow + 8); }
      else { xa = ldc(rs, cx.vo, cx.gb + G_X2 + cx.xo); xb2 = ldc(rs, cx.vo + 1024, cx.gb + G_X2 + cx.xo); }
      unpack16(xa, xb2, xr); acc[0] = tovec(xr); acc[1] = zero; acc[2] = zero; acc[3] = zero;
#pragma unroll
      for (int n = 0; n < 32; n++) acc[n >> 3] = MFMA(wof[2 * (n & 7) + ((n >> 3) & 1)], of[8 * (n >> 4) + (n & 7)], acc[n >> 3]);
      red_send(cx, acc[1], 1); red_send(cx, acc[2], 2); red_send(cx, acc[3], 3);
    }
    if (L == 1) FST(9, 0);
    lds_barrier();
    if (L == 1) FST(9, 1);
    red_recv(cx, acc[0], xr);
    if (L == 1 && FINE == 9) { FST(9, 2); vm_wait<0>(); FST(9, 3); }
    publish_x(cx, xr, gl + G_X1, gl + G_ST1, gl + G_FX1);
    if (L == 1 && FINE == 9) { FST(9, 4); vm_wait<3>(); FST(9, 5); vm_wait<1>(); FST(9, 6); vm_wait<0>(); FST(9, 7); }
    STAMP(10 + SO);
    if (L == 0) {
#pragma unroll
      for (int k = NW2_C; k < NW2_H; k++) w2f[k] = ldck(rs, cx.vo, w2b, k);
    }
    float bv[16]; unpack16(mi.ba, mi.bb, bv);
#pragma unroll
    for (int r = 0; r < 16; r++) xr[r] += bv[r];
    park(cx, xr);
  }
  {
    u32x4 xf[33]; f32x16 h0, h1, h2, h3;
    Gelu ge;
    xpass(cx, gl + G_X1, gl + G_ST1, gl + G_FX1, xf, h0, h1, 11 + SO,
          [&](float mean, float var, u32x4& w0, u32x4& w1) LAMBDA {
            const float r1 = __builtin_amdgcn_rsqf(var + 1e-5f);
            const float A = r1 * __builtin_amdgcn_rsqf(var * r1 * r1 + 1e-5f);
            const float iA = __builtin_amdgcn_rcpf(A);
            ge.ia[0] = iA; ge.ia[1] = iA;
            xf[32] = aug_tok(mean, iA, hk);
            const float k0 = -2.f * LOG2E * 0.7978845608028654f, k1 = k0 * 0.044715f;
            ge.cc[0] = k0 * A; ge.cc[1] = k1 * A * A * A;
            w0 = aug_w(csp[0], b1p[0]); w1 = aug_w(csp[1], b1p[0] >> 16);
          },
          [&](auto ii) LAMBDA {
            constexpr int k = NW2_H + ii.v - 8;
            if (ii.v < 8) dma<(ii.v & 7)>(rs, cx.C + w * INBOX, cx.vo, w2b + 48 * 1024);
            else if (L == 0 && k < W2KEEP) w2f[k] = ldck(rs, cx.vo, w2b, k);
          });
    STAMP(12 + SO); if (L == 1) FST(8, 13);
    const int hs = gl + G_H + cx.ho, hfo = gl + G_FH + cx.fo;
    const int hb = gl + G_H + (cx.th * 4 + w) * 57344, fb = gl + G_FH + (cx.th * 4 + w) * 8 * FS;
    ge.sum = 0;
    asm volatile("" : "+a"(h0), "+a"(h1));
    h2 = MFMA(aug_w(csp[2], b1p[1]), xf[32], zero); h3 = MFMA(aug_w(csp[3], b1p[1] >> 16), xf[32], zero);
    const char* pb = cx.B + cx.vo;
    const char* ph = cx.B + 1024 + (hk * 16 + (lane & 15)) * 16;
    u32x4 ra[4], hf[56]; u32x2 fc;
    auto rdw = [&](int G) LAMBDA { ra[G & 3] = *(const u32x4*)((G < 32 ? pb : ph) + (G & 31) * 1536); };
    auto hk_ = [](int k) LAMBDA { return k < 48 ? k : k < 52 ? 49 + 2 * (k - 48) : 48 + 2 * (k - 52); };
    auto ldh = [&](int k) LAMBDA { hf[hk_(k)] = ldck(rs, cx.vo, hb, hk_(k)); };
#pragma unroll
    for (int n = 0; n < 4; n++) rdw(n);
    SB();
    unsigned pl = 0, sum = 0;
    f32x16 acc[4];
    sfor<64>([&](auto gg) LAMBDA {
      constexpr int G = gg, n = G & 31;
      if (G % 8 == 0) FST(4 + L, 6 + G / 8);
      if (G < 32) h2 = MFMA(ra[G & 3], xf[n], h2); else h3 = MFMA(ra[G & 3], xf[n], h3);
      SB();
      auto mid = [&]() LAMBDA { if (G + 4 < 64) rdw(G + 4); };
      if (G < 10) ge.step<0, 16, G, 2>(cx, h0, h1, hs, mid);
      else if (G < 26) ge.step<0, 16, 10 + (G - 10) / 2, G & 1>(cx, h0, h1, hs, mid);
      else if (G >= GB2 && G < GB2 + 20) ge.step<16, 24, 16 + (G - GB2) / 2, (G - GB2) & 1>(cx, h2, h2, hs, mid);
      else mid();
      if (L == 0 && NW2_H + 2 + G < W2KEEP) w2f[NW2_H + 2 + G] = ldck(rs, cx.vo, w2b, NW2_H + 2 + G);
      if (G == 9) put_flag(cx, hfo + FHL, 1);
      if (G == 25) put_flag(cx, hfo + 2 * FHL, 3);
      if (G == GB2 + 19) put_flag(cx, hfo + 3 * FHL, 5);
      if (G == GP1) pl = ldc1(rs, cx.fv8, fb + FHL);
      if (G == GW1) { SB(); poll_wait<8>(cx, pl, fb + FHL, cx.tk_oh, 1); STAMP(13 + SO); }
      if (G >= GW1 && G < GW1 + 10) ldh(G - GW1);
      if (G == GW1 + 7) pl = ldc1(rs, cx.fv8, fb + 2 * FHL);
      if (G == 60) acc[0] = unpark(cx);
      SB();
    });
    FST(4 + L, 14);
    STAMP(14 + SO);
    unsigned msg = 0;
    char* dA = cx.A + w * 16384;
    const int sA = WT_QKV + c * WQ_C + w * 16384;
    sfor<112>([&](auto gg) LAMBDA {
      constexpr int G = gg, k = G >> 1, f = k < 48 ? k : k < 52 ? 49 + 2 * (k - 48) : 48 + 2 * (k - 52);
      constexpr int q = f >> 1, j = 2 * (f & 1) + 1 - (G & 1);
      if (G % 8 == 0) FST(2 + L, G / 8);
      acc[j] = MFMA(w2f[2 * q + (j & 1)], hf[f], q == 0 && j ? zero : acc[j]); SB();
      if (G >= 2 && G < 14) ge.step<24, 28, 24 + (G - 2) / 2, G & 1>(cx, h3, h3, hs, []() LAMBDA {});
      if (G == 14) put_final(cx, hfo, ge.sum);
      if (G == 12) { SB(); poll_wait<8>(cx, pl, fb + 2 * FHL, cx.tk_oh, 3); }
      if (G == 44) { SB(); poll_wait<8>(cx, pl, fb + 3 * FHL, cx.tk_oh, 5); }
      if (G == 76) { SB(); sum += final_wait<8>(cx, fc, fb, cx.tk_oh); }
      if (!(G & 1) && k + 10 < 56) ldh(k + 10);
      if (G == GP3) pl = ldc1(rs, cx.fv8, fb + 3 * FHL);
      if (G == GP4) fc = ldc2(rs, cx.fv8, fb);
      if (G >= 14 && G < 22) w2f[G + 34] = *(const u32x4*)(cx.C + w * INBOX + (G - 14) * 1024 + cx.vo);
      if (G == 24) lds_barrier();
      if ((G & 1) && G >= 19 && (G - 19) / 2 + W2KEEP < 48) w2f[(G - 19) / 2 + W2KEEP] = ldck(rs, cx.vo, w2b, (G - 19) / 2 + W2KEEP);
      if (L == 0) {
        if (G == 61) msg = ldc1(rs, (lane & 15) * 8, MBX + (g * 16 + c) * 512 + 384);
        if (G == 91) { SB(); mail_wait(cx, msg, (lane & 15) * 8, MBX + (g * 16 + c) * 512 + 384, cx.N + 1); }
        if (G >= 92 && !(G & 1) && (G - 92) / 2 < NQK) dma<(G >= 92 ? (G - 92) / 2 : 0)>(rs, dA, cx.vo, sA);
      }
      if (G & 1) sum = sum4(sum, hf[f]);
      if (G == 105) red_send(cx, acc[3], 3);
      if (G == 107) red_send(cx, acc[2], 2);
      SB();
    });
    red_send(cx, acc[1], 1);
      FST(2 + L, 14);
    asm volatile("" : "+a"(acc[0]));
    if (UNLIKELY(invalid(cx, sum))) {
      do {
        asm volatile("" ::: "memory"); cx.budget -= 64;
        xa = ldc(rs, cx.vo, gl + G_X1 + cx.xo); xb2 = ldc(rs, cx.vo + 1024, gl + G_X1 + cx.xo);
        float bv[16]; unpack16(*(const u32x4*)(b2 + col), *(const u32x4*)(b2 + col + 8), bv);
        unpack16(xa, xb2, xr);
#pragma unroll
        for (int r = 0; r < 16; r++) acc[0][r] = xr[r] + bv[r];
        acc[1] = zero; acc[2] = zero; acc[3] = zero; sum = ldc2(rs, cx.fv8, fb)[1] - cx.tk_oh;
#pragma unroll
        for (int k = 0; k < 56; k++) {
          const u32x4 f = ldck(rs, cx.vo, hb, k);
          sum = sum4(sum, f);
          acc[2 * (k & 1)] = MFMA(w2f[k & ~1], f, acc[2 * (k & 1)]); acc[2 * (k & 1) + 1] = MFMA(w2f[k | 1], f, acc[2 * (k & 1) + 1]);
          if ((k & 7) == 7) SB();
        }
      } while (invalid(cx, sum));
      red_send(cx, acc[1], 1); red_send(cx, acc[2], 2); red_send(cx, acc[3], 3);
    }
    lds_barrier();
    FST(2 + L, 15);
    if (L == 0) {
      red_recv(cx, acc[0], xr);
      publish_x(cx, xr, cx.gb + G_X2, cx.gb + G_ST2, cx.gb + G_FX2);
      STAMP(20);
      sfor<16 - NQK>([&](auto ii) LAMBDA { dma<NQK + ii.v>(rs, dA, cx.vo, sA); });
      park(cx, xr);
    } else {
      red_recv(cx, acc[0], xr);
      u32x4 f0, f1; pack16(xr, f0, f1);
      *(u32x4*)(out + xrow) = f0; *(u32x4*)(out + xrow + 8) = f1;
      STAMP(29);
    }
  }
}

__global__ __launch_bounds__(256, 1) void fused(const u16* __restrict__ x, const u16* __restrict__ wqkv, const u16* __restrict__ wphi,
                                                const u16* __restrict__ wo, const u16* __restrict__ w1, const u16* __restrict__ b1,
                                                const u16* __restrict__ w2, const u16* __restrict__ b2, char* ws, u16* __restrict__ out) {
  __shared__ __attribute__((aligned(16))) char ldsA[65536];
  __shared__ __attribute__((aligned(16))) char ldsB[49152];
  __shared__ __attribute__((aligned(16))) char ldsC[49152];
  Cx cx;
  STAMP(0); CSTAMP(30);
  {
    unsigned long long pc; unsigned len;
    asm volatile("s_getpc_b64 %0\n.Lcb0:\n\ts_mov_b32 %1, .Lce0-.Lcb0-16" : "=s"(pc), "=s"(len));
    unsigned o = ((blockIdx.x >> 3) * 256 + threadIdx.x) * 16;
    gldso<0>(o < len ? o : 0u, (const void*)pc, (unsigned)(size_t)ldsC + 48128u);
  }
  cx.ws = ws; cx.A = ldsA; cx.B = ldsB; cx.C = ldsC;
  cx.lane = threadIdx.x & 63; cx.w = RFL(threadIdx.x >> 6);
  cx.tok = cx.lane & 31; cx.hk = cx.lane >> 5; cx.vo = cx.lane * 16; cx.budget = SPIN_MAX;
  { const int b = blockIdx.x, xx = b & 7, j = b >> 3; cx.g = 4 * (xx >> 1) + (j >> 3); cx.c = 8 * (xx & 1) + (j & 7); }
  cx.cb = 4 * (cx.c >> 3) + (cx.c & 3); cx.th = (cx.c >> 2) & 1;
  cx.rs = __builtin_amdgcn_make_buffer_rsrc(ws, 0, WS_USED, 0x00020000);
  cx.gb = GRP + cx.g * G_S;
  cx.fv16 = cx.lane < 16 ? cx.lane * FS + 120 : OOB; cx.fv8 = cx.lane < 8 ? cx.lane * FS + 120 : OOB; cx.f63 = cx.lane >= 48 ? (cx.lane - 48) * 8 : OOB;
  cx.fvz = cx.lane < 32 ? (cx.lane >> 1) * FS + (cx.lane & 1) * 16 : cx.lane < 48 ? (cx.lane - 32) * FS + 112 : OOB;
  {
    const int w = cx.w, kq = cx.c >> 2, tx = (w & 1) ^ (kq >> 1), ln = (w >> 1) * 4 + kq, tb = 2 * cx.th + (w >> 1), blk = 2 * cx.cb + (w & 1);
    cx.oo = (ln * 16 + tx * 8 + (cx.c & 3) * 2) * 1024;
    cx.ho = (ln * 56 + (cx.c & 3) * 2 + tx) * 1024;
    cx.fo = (ln * 8 + (cx.c & 3) * 2 + (w & 1)) * FS;
    cx.xo = (tb * 32 + 2 * blk) * 1024;
    cx.so = tb * 4096 + blk * 256;
    cx.fxo = (tb * 16 + blk) * FS;
  }
  u32x4 phq[4], aphi[2], w2f[56], wof[16]; unsigned csw[3], csp[4], b1p[2]; KvIn kin; Misc mi;
  stage1a(cx, x, wqkv, wphi, wo, w1, b1, w2, b2, phq, csw, aphi, kin, w2f, wof, mi);
  layer<0>(cx, x, b1, b2, out, phq, csw, aphi, kin, w2f, wof, mi, csp, b1p);
  asm volatile("" : "+v"(cx.lane), "+v"(cx.tok), "+v"(cx.hk), "+v"(cx.vo));
  asm volatile("" : "+v"(csp[0]), "+v"(csp[1]), "+v"(csp[2]), "+v"(csp[3]), "+v"(b1p[0]), "+v"(b1p[1]), "+v"(csw[0]), "+v"(csw[1]), "+v"(csw[2]));
#pragma unroll
  for (int k = 0; k < W2KEEP; k++) asm volatile("" : "+a"(w2f[k]));
  layer<1>(cx, x, b1, b2, out, phq, csw, aphi, kin, w2f, wof, mi, csp, b1p);
  asm volatile(".Lce0:");
}

extern "C" void kernel_launch(void* const* d_in, const int* in_sizes, int n_in, void* d_out, int out_size, void* d_ws, size_t ws_size,
                              hipStream_t stream) {
  fused<<<256, 256, 0, stream>>>((const u16*)d_in[0], (const u16*)d_in[1], (const u16*)d_in[2], (const u16*)d_in[3], (const u16*)d_in[5],
                                 (const u16*)d_in[6], (const u16*)d_in[7], (const u16*)d_in[8], (char*)d_ws, (u16*)d_out);
}
```

```cpp
#include <hip/hip_runtime.h>
#include <stdint.h>

typedef unsigned short u16;
typedef __attribute__((ext_vector_type(8))) __bf16 bf16x8;
typedef __attribute__((ext_vector_type(2))) __bf16 bf16x2;
typedef __attribute__((ext_vector_type(16))) float f32x16;
typedef __attribute__((ext_vector_type(4))) float f32x4;
typedef __attribute__((ext_vector_type(2))) float f32x2;
typedef __attribute__((ext_vector_type(4))) unsigned u32x4;
typedef __attribute__((ext_vector_type(2))) unsigned u32x2;
typedef __attribute__((ext_vector_type(4))) short s16x4;
typedef __amdgpu_buffer_rsrc_t rsrc_t;
typedef __attribute__((address_space(3))) void* ldsp;

#define DEVI __device__ __forceinline__
#define LAMBDA __attribute__((always_inline))
#define MFMA(a, b, c) __builtin_amdgcn_mfma_f32_32x32x16_bf16(__builtin_bit_cast(bf16x8, a), __builtin_bit_cast(bf16x8, b), c, 0, 0, 0)
#define MFMA16(a, b, c) __builtin_amdgcn_mfma_f32_16x16x32_bf16(__builtin_bit_cast(bf16x8, a), __builtin_bit_cast(bf16x8, b), c, 0, 0, 0)
#define SB() __builtin_amdgcn_sched_barrier(0)
#define RFL(v) __builtin_amdgcn_readfirstlane(v)
#define BALLOT(p) __builtin_amdgcn_ballot_w64(p)

constexpr int WT_QKV = 0x0, WQ_C = 98304;
constexpr int WT_O   = 0x180000;
constexpr int WT_1   = 0x200000, W1_C = 114688;
constexpr int CS1    = 0x3C0000;
constexpr int WT_2   = 0x400000;
constexpr int MBX    = 0x5C0000;
constexpr int TOKR   = 0x5E0000;
constexpr int TOKC   = 0x5E1000;
constexpr int KVA    = 0x600000, KV_C = 0xC000, KV_L = 0xC0000, KV_F = 0x8000;
constexpr int GRP    = 0x800000, G_S = 0x200000, G_L = 0xC6000;
constexpr int G_O = 0, G_X1 = 0x20000, G_H = 0x40000, G_ST1 = 0xB0000, G_FO = 0xB4000, G_FH = 0xB6000, G_FX1 = 0xBE000;
constexpr int FS = 128, FHL = 0x2000, CKO = 0xC000, CKX = 0x4000, CKH = 0xE000;
constexpr int G_X2 = 0x18C000, G_ST2 = 0x1AC000, G_FX2 = 0x1B0000;
constexpr int WS_USED = 0x2800000;
constexpr int OOB = 0x40000000;
#define STAMP_OFF 0x2800000
#define STAMP_N 64

constexpr int INBOX = 12288;
constexpr int C_V = 4352, C_IMG = 6656, C_ZERO = 7680, C_PARK = 8192;
constexpr float LOG2E = 1.4426950408889634f;
constexpr int SPIN_MAX = 1 << 16;

DEVI unsigned pk2(float a, float b) { f32x2 v = {a, b}; return __builtin_bit_cast(unsigned, __builtin_convertvector(v, bf16x2)); }
DEVI float bflo(unsigned u) { return __uint_as_float(u << 16); }
DEVI float bfhi(unsigned u) { return __uint_as_float(u & 0xffff0000u); }
DEVI void lds_barrier() { asm volatile("s_waitcnt lgkmcnt(0)\n\ts_barrier" ::: "memory"); }
template <int N> DEVI void lds_barrier_n() { asm volatile("s_waitcnt lgkmcnt(%0)\n\ts_barrier" :: "n"(N) : "memory"); }
DEVI void bar() { asm volatile("s_barrier" ::: "memory"); }
template <int N> DEVI void vm_wait() { asm volatile("s_waitcnt vmcnt(%0)" :: "n"(N) : "memory"); }
template <int OFF> DEVI void gldso(unsigned voff, const void* sbase, unsigned lds_base) {
  asm volatile("s_mov_b32 m0, %2\n\ts_nop 0\n\tglobal_load_lds_dwordx4 %0, %1 offset:%3" :: "v"(voff), "s"(sbase), "s"(lds_base), "n"(OFF) : "memory", "m0");
}
DEVI void gldsc4(unsigned voff, const void* sbase, unsigned lds_base) {
  asm volatile("s_mov_b32 m0, %2\n\ts_nop 0\n\tglobal_load_lds_dwordx4 %0, %1 sc1" :: "v"(voff), "s"(sbase), "s"(lds_base) : "memory", "m0");
}
DEVI void gldsc1(unsigned voff, const void* sbase, unsigned lds_base) {
  asm volatile("s_mov_b32 m0, %2\n\ts_nop 0\n\tglobal_load_lds_dword %0, %1 sc1" :: "v"(voff), "s"(sbase), "s"(lds_base) : "memory", "m0");
}
DEVI float xhalf_sum(float v) {
  auto t = __builtin_amdgcn_permlane32_swap(__float_as_uint(v), __float_as_uint(v), false, false);
  return __uint_as_float(t[0]) + __uint_as_float(t[1]);
}
DEVI u32x4 ldc(rsrc_t rs, int vo, int so) { return __builtin_amdgcn_raw_buffer_load_b128(rs, vo, so, 16); }
DEVI u32x2 ldc2(rsrc_t rs, int vo, int so) { return __builtin_amdgcn_raw_buffer_load_b64(rs, vo, so, 16); }
DEVI unsigned ldc1(rsrc_t rs, int vo, int so) { return __builtin_amdgcn_raw_buffer_load_b32(rs, vo, so, 16); }
DEVI void stc(rsrc_t rs, int vo, int so, u32x4 v) { __builtin_amdgcn_raw_buffer_store_b128(v, rs, vo, so, 16); }
DEVI void stc2(rsrc_t rs, int vo, int so, u32x2 v) { __builtin_amdgcn_raw_buffer_store_b64(v, rs, vo, so, 16); }
DEVI void stc1(rsrc_t rs, int vo, int so, unsigned v) { __builtin_amdgcn_raw_buffer_store_b32(v, rs, vo, so, 16); }
DEVI u32x4 ldck(rsrc_t rs, int vo, int so, int kb) { return ldc(rs, vo + (kb & 3) * 1024, so + (kb >> 2) * 4096); }
template <int KB> DEVI void dma(rsrc_t rs, char* dst, int vo, int so) {
  __builtin_amdgcn_raw_ptr_buffer_load_lds(rs, (ldsp)(dst + (KB >> 2) * 4096), 16, vo, so + (KB >> 2) * 4096, (KB & 3) * 1024, 16);
}
DEVI float lsel(unsigned long long m, float t, float f) { float r; asm("v_cndmask_b32_e64 %0, %1, %2, %3" : "=v"(r) : "v"(f), "v"(t), "s"(m)); return r; }
DEVI float fmax3(float a, float b, float c) { return fmaxf(fmaxf(a, b), c); }
DEVI float fmin3(float a, float b, float c) { return fminf(fminf(a, b), c); }
DEVI unsigned sum4(unsigned s, const u32x4& f) { s = (s + f[0] + f[1]) + f[2] + f[3]; asm volatile("" : "+v"(s)); return s; }
DEVI unsigned wsum(unsigned v) {
  v += __builtin_amdgcn_update_dpp(0u, v, 0x111, 0xf, 0xf, false);
  v += __builtin_amdgcn_update_dpp(0u, v, 0x112, 0xf, 0xf, false);
  v += __builtin_amdgcn_update_dpp(0u, v, 0x114, 0xf, 0xf, false);
  v += __builtin_amdgcn_update_dpp(0u, v, 0x118, 0xf, 0xf, false);
  v += __builtin_amdgcn_update_dpp(0u, v, 0x142, 0xa, 0xf, false);
  v += __builtin_amdgcn_update_dpp(0u, v, 0x143, 0xc, 0xf, false);
  return v;
}

template <int I> DEVI void wsum_step(unsigned& v) {
  constexpr int ctl[6] = {0x111, 0x112, 0x114, 0x118, 0x142, 0x143}, rm[6] = {0xf, 0xf, 0xf, 0xf, 0xa, 0xc};
  v += __builtin_amdgcn_update_dpp(0u, v, ctl[I], rm[I], 0xf, false);
}

#define FINE 0
#define FST(sel, i)
#define STAMP(i)
#define CSTAMP(i)
#define COUNT(i)
#define BUD(i)

template <class T>
DEVI void pack16(const T& v, u32x4& a, u32x4& b) {
#pragma unroll
  for (int i = 0; i < 4; i++) { a[i] = pk2(v[2 * i], v[2 * i + 1]); b[i] = pk2(v[8 + 2 * i], v[9 + 2 * i]); }
}
template <int I> struct ic { static constexpr int v = I; constexpr operator int() const { return I; } };
template <int... I> struct iseq {};
template <int N, int... I> struct mkseq : mkseq<N - 1, N - 1, I...> {};
template <int... I> struct mkseq<0, I...> { typedef iseq<I...> type; };
template <class F, int... I> DEVI void sfor_impl(F& f, iseq<I...>) { (f(ic<I>()), ...); }
template <int N, class F> DEVI void sfor(F f) { sfor_impl(f, typename mkseq<N>::type()); }

struct Softmax {
  float mx, mn, sp, sn, ep[16], en[16];
  DEVI void step(int i, const f32x16& p, float sc, u32x4 (&ph)[4]) {
    if (i == 0) { mx = fmax3(p[0], p[1], p[2]); mn = fmin3(p[0], p[1], p[2]); mx = fmax3(mx, p[3], p[4]); mn = fmin3(mn, p[3], p[4]); }
    else if (i < 3) { int b = 5 + 4 * (i - 1); mx = fmax3(mx, p[b], p[b + 1]); mn = fmin3(mn, p[b], p[b + 1]); mx = fmax3(mx, p[b + 2], p[b + 3]); mn = fmin3(mn, p[b + 2], p[b + 3]); }
    else if (i == 3) { mx = fmax3(mx, p[13], p[14]); mn = fmin3(mn, p[13], p[14]); mx = fmaxf(mx, p[15]) * -sc; mn = fminf(mn, p[15]) * sc; }
    else if (i < 20) {
      int r = i - 4;
      ep[r] = __builtin_amdgcn_exp2f(fmaf(p[r], sc, mx)); en[r] = __builtin_amdgcn_exp2f(fmaf(p[r], -sc, mn));
      if (r == 2) { sp = ep[0] + ep[1]; sn = en[0] + en[1]; } else if (r > 2) { sp += ep[r - 1]; sn += en[r - 1]; }
    } else if (i == 20) { sp = __builtin_amdgcn_rcpf(sp + ep[15]); sn = __builtin_amdgcn_rcpf(sn + en[15]); }
    else if (i < 29) {
      int t = i - 21;
      ph[t >> 2][t & 3] = pk2(ep[2 * t] * sp, ep[2 * t + 1] * sp);
      ph[2 + (t >> 2)][t & 3] = pk2(en[2 * t] * sn, en[2 * t + 1] * sn);
    }
  }
};

template <int C> DEVI u32x4 tr_readc(const char* p) {
  typedef __attribute__((address_space(3))) s16x4* lp;
  s16x4 a = __builtin_amdgcn_ds_read_tr16_b64_v4i16((lp)(p));
  s16x4 b = __builtin_amdgcn_ds_read_tr16_b64_v4i16((lp)(p + C));
  u32x2 ua = __builtin_bit_cast(u32x2, a), ub = __builtin_bit_cast(u32x2, b);
  u32x4 r = {ua[0], ua[1], ub[0], ub[1]};
  return r;
}
DEVI u32x4 tr_read2(const char* p, const char* p2) {
  typedef __attribute__((address_space(3))) s16x4* lp;
  s16x4 a = __builtin_amdgcn_ds_read_tr16_b64_v4i16((lp)(p));
  s16x4 b = __builtin_amdgcn_ds_read_tr16_b64_v4i16((lp)(p2));
  u32x2 ua = __builtin_bit_cast(u32x2, a), ub = __builtin_bit_cast(u32x2, b);
  u32x4 r = {ua[0], ua[1], ub[0], ub[1]};
  return r;
}

DEVI u32x4 aug_tok(float mean, float ia, int hk) {
  unsigned mh = pk2(-mean, ia);
  unsigned ml = pk2(-mean - bflo(mh), ia - bfhi(mh));
  u32x4 t;
  t[0] = __builtin_amdgcn_perm(ml, mh, 0x05040100u);
  t[1] = mh;
  t[2] = ml >> 16;
  t[3] = 0;
  if (hk) { t[0] = 0; t[1] = 0; t[2] = 0; }
  return t;
}
DEVI unsigned cs_pack(float cs) { unsigned h = pk2(cs, 0.f); return pk2(cs, cs - bflo(h)); }
DEVI u32x4 aug_w(unsigned csp, unsigned b) {
  u32x4 t;
  t[0] = __builtin_amdgcn_perm(csp, csp, 0x01000100u);
  t[1] = __builtin_amdgcn_perm(b, csp, 0x05040302u);
  t[2] = b & 0xffffu;
  t[3] = 0;
  return t;
}

struct Cx {
  rsrc_t rs; char* ws; char *A, *B, *C;
  int lane, w, tok, hk, g, c, cb, th, vo, gb;
  int fv16, fv8, fvz, f63, oo, ho, fo, xo, so, fxo;
  unsigned N, tk_kv, tk_oh, tk_x;
  int budget;
};

DEVI void put_flag(Cx& cx, int so, int i) { u32x2 v = {cx.N + i, cx.N + i}; stc2(cx.rs, cx.f63, so, v); }
DEVI void put_final(Cx& cx, int so, unsigned sum) { u32x2 v = {cx.N + 7, cx.N - wsum(sum)}; stc2(cx.rs, cx.f63, so, v); }
template <int NP> DEVI void poll_wait(Cx& cx, unsigned a, int so, unsigned tk, int i) {
  unsigned t; int s;
  asm volatile("v_sub_u32 %0, %3, %4\n\t"
               "v_cmp_ne_u32 vcc, %9, %0\n\t"
               "s_and_b32 %1, vcc_lo, %8\n\t"
               "s_cbranch_scc0 2f\n"
               "1:\n\t"
               "s_cmp_eq_u32 %2, 0\n\t"
               "s_cbranch_scc1 2f\n\t"
               "buffer_load_dword %0, %5, %6, %7 offen sc1\n\t"
               "s_sub_u32 %2, %2, 1\n\t"
               "s_waitcnt vmcnt(0)\n\t"
               "v_sub_u32 %0, %0, %4\n\t"
               "v_cmp_ne_u32 vcc, %9, %0\n\t"
               "s_and_b32 %1, vcc_lo, %8\n\t"
               "s_cbranch_scc1 1b\n"
               "2:"
               : "=&v"(t), "=&s"(s), "+s"(cx.budget)
               : "v"(a), "v"(tk), "v"(NP == 16 ? cx.fv16 : cx.fv8), "s"(cx.rs), "s"(so), "n"((1 << NP) - 1), "n"(i)
               : "vcc", "scc", "memory");
}
DEVI void kv_wait(Cx& cx, u32x4& q, int so) {
  int s; unsigned f = q[2];
  asm volatile("v_sub_u32 %0, %0, %4\n\t"
               "v_cmp_ne_u32 vcc, 7, %0\n\t"
               "s_and_b32 %2, vcc_hi, 0xffff\n\t"
               "s_cbranch_scc0 2f\n"
               "1:\n\t"
               "s_cmp_eq_u32 %3, 0\n\t"
               "s_cbranch_scc1 2f\n\t"
               "buffer_load_dword %0, %5, %6, %7 offen offset:8 sc1\n\t"
               "s_sub_u32 %3, %3, 1\n\t"
               "s_waitcnt vmcnt(0)\n\t"
               "v_sub_u32 %0, %0, %4\n\t"
               "v_cmp_ne_u32 vcc, 7, %0\n\t"
               "s_and_b32 %2, vcc_hi, 0xffff\n\t"
               "s_cbranch_scc1 1b\n\t"
               "buffer_load_dwordx4 %1, %5, %6, %7 offen sc1\n\t"
               "s_waitcnt vmcnt(0)\n"
               "2:"
               : "+v"(f), "+v"(q), "=&s"(s), "+s"(cx.budget)
               : "v"(cx.tk_kv), "v"(cx.fvz), "s"(cx.rs), "s"(so)
               : "vcc", "scc", "memory");
}
#define FORCE_BAD 0
DEVI bool bad(Cx& cx, unsigned sum) { return __builtin_amdgcn_readlane(wsum(sum), 63) + (FORCE_BAD ? cx.w & 1 : 0) != 0; }
DEVI bool again(Cx& cx, unsigned sum) {
  const unsigned r = __builtin_amdgcn_readlane(wsum(sum), 63);
  return (r != 0) & (cx.budget >= 64);
}
DEVI void spend(Cx& cx, int id) {
  asm volatile("" ::: "memory"); cx.budget = cx.budget >= 64 ? cx.budget - 64 : 0; }
#define D_O 8
#define D_X1 6
#define D_X2 2
#define PRE 1
#define TOUCH 1
template <int N> DEVI void nap() { if (N > 0) { SB(); __builtin_amdgcn_s_sleep(N); SB(); } }
DEVI unsigned usel(unsigned long long m, unsigned t, unsigned f) { unsigned r; asm("v_cndmask_b32_e64 %0, %1, %2, %3" : "=v"(r) : "v"(f), "v"(t), "s"(m)); return r; }
DEVI void mail_wait(Cx& cx, unsigned a, int mvo, int so, unsigned v) {
  int s;
  asm volatile("v_cmp_ne_u32 vcc, %3, %0\n\t"
               "s_and_b32 %1, vcc_lo, 0xffff\n\t"
               "s_cbranch_scc0 2f\n"
               "1:\n\t"
               "s_cmp_eq_u32 %2, 0\n\t"
               "s_cbranch_scc1 2f\n\t"
               "buffer_load_dword %0, %4, %5, %6 offen sc1\n\t"
               "s_sub_u32 %2, %2, 1\n\t"
               "s_waitcnt vmcnt(0)\n\t"
               "v_cmp_ne_u32 vcc, %3, %0\n\t"
               "s_and_b32 %1, vcc_lo, 0xffff\n\t"
               "s_cbranch_scc1 1b\n"
               "2:"
               : "+v"(a), "=&s"(s), "+s"(cx.budget)
               : "s"(v), "v"(mvo), "s"(cx.rs), "s"(so)
               : "vcc", "scc", "memory");
}
DEVI void kv_write_v(Cx& cx, const f32x16& accv, float rstd) {
  float vn[16];
#pragma unroll
  for (int r = 0; r < 16; r++) vn[r] = accv[r] * rstd;
  u32x4 v0, v1; pack16(vn, v0, v1);
  char* p = cx.C + 2 * cx.hk * INBOX + C_V + (32 * cx.w + cx.tok) * 16;
  *(u32x4*)p = v0; *(u32x4*)(p + INBOX + 128) = v1;
}
template <int VMW>
DEVI void kv_tail(Cx& cx, const u32x4 (&phk)[4], int kvb) {
  char* p = cx.C + 2 * cx.hk * INBOX + (32 * cx.w + cx.tok) * 16;
  *(u32x4*)p = phk[0]; *(u32x4*)(p + 2176) = phk[1]; *(u32x4*)(p + INBOX) = phk[2]; *(u32x4*)(p + INBOX + 2176) = phk[3];
  const int G = cx.lane >> 4, d = cx.lane & 15, q4 = d >> 2, p4 = cx.lane & 3;
  const int ro = (4 * G + q4) * 16 + (p4 & 1) * 8;
  const char* pa = cx.C + cx.w * INBOX + (p4 >> 1) * 2176 + ro;
  const char* pb = cx.C + ((cx.w & 2) + (p4 >> 1)) * INBOX + C_V + (p4 >> 1) * 128 + ro;
  if (VMW >= 0) vm_wait<(VMW >= 0 ? VMW : 0)>();
  lds_barrier();
  u32x4 a[4], b[4];
#pragma unroll
  for (int t = 0; t < 4; t++) { a[t] = tr_readc<256>(pa + t * 512); b[t] = tr_readc<256>(pb + t * 512); }
  const u32x4 ones = {0x3f803f80u, 0x3f803f80u, 0x3f803f80u, 0x3f803f80u};
  f32x4 kv = {}, z = {};
#pragma unroll
  for (int t = 0; t < 4; t++) { kv = MFMA16(a[t], b[t], kv); z = MFMA16(a[t], ones, z); }
  const u32x2 k2 = {pk2(kv[0], kv[1]), pk2(kv[2], kv[3])}, z2 = {pk2(z[0], z[1]), pk2(z[2], z[3])};
  const int e = cx.w * 16 + cx.g;
  stc2(cx.rs, (G >> 1) * 256 + d * 16 + (G & 1) * 8, kvb + e * 512, k2);
  const unsigned cs = -7u - wsum(k2[0] + k2[1] + usel(0x1000900010001000ull, z2[0] + z2[1], 0u));
  const u32x2 zl = {usel(1ull << 63, cx.N + 7, z2[0]), usel(1ull << 63, cs, z2[1])};
  stc2(cx.rs, d >= 12 ? (d - 12) * 32 + G * 8 : OOB, kvb + KV_F + e * FS, zl);
}
struct KvIn { u32x4 pa[8], pz; };
struct Misc { u32x4 xa, xb, ba, bb, cs[4]; u16 b1v[4]; };
DEVI u32x4 kv_poll(Cx& cx, int kvb) { return ldc(cx.rs, cx.fvz, kvb + KV_F + cx.w * 16 * FS); }
DEVI void kv_load(Cx& cx, KvIn& k, int kvb) {
#pragma unroll
  for (int m = 0; m < 8; m++) k.pa[m] = ldck(cx.rs, cx.vo, kvb + cx.w * 8192, m);
}

#define NW2_A 10
#define KQP0 31
#define KQW0 12

DEVI void stage1a(Cx& cx, const u16* __restrict__ x, const u16* __restrict__ wqkv, const u16* __restrict__ wphi,
                  const u16* __restrict__ wo, const u16* __restrict__ w1, const u16* __restrict__ b1, const u16* __restrict__ w2,
                  const u16* __restrict__ b2, u32x4 (&phq)[4], unsigned (&csw)[3], u32x4 (&aphi)[2], KvIn& kin, u32x4 (&w2f)[56],
                  u32x4 (&wof)[16], Misc& mi) {
  const int lane = cx.lane, w = cx.w, tok = cx.tok, hk = cx.hk, g = cx.g, c = cx.c;
  const int rot = (c + 2 * g) & 7;
  const rsrc_t rs = cx.rs;
  const unsigned lA = (unsigned)(size_t)cx.A, lB = (unsigned)(size_t)cx.B, lC = (unsigned)(size_t)cx.C;
  char* xs = cx.C + w * INBOX;
  char* stg = cx.B + 32768 + w * 4096;
  const unsigned lstg = lB + 32768 + w * 4096;
  unsigned vx[4];
#pragma unroll
  for (int i = 0; i < 4; i++) vx[i] = (8 * i + (lane >> 3)) * 1024 + (((lane & 7) ^ ((4 * i + (lane >> 4)) & 7)) * 16) - i * 1024;
  const char* xb = (const char*)(x + (size_t)(128 * g + 32 * w) * 512);
  const unsigned xl = lC + w * INBOX;
  const unsigned wvo = (lane >> 2) * 3072 + (lane & 3) * 16;
  const char* wb = (const char*)(wqkv + (size_t)(16 * w) * 1536 + 32 * c);
  auto cload = [&](int j, int i) LAMBDA {
    const int cj = (j + rot) & 7;
    if (i < 4) {
      const char* sb = xb + cj * 128;
      if (i == 0) gldso<0>(vx[0], sb, xl + (j % 3) * 4096);
      if (i == 1) gldso<1024>(vx[1], sb, xl + (j % 3) * 4096);
      if (i == 2) gldso<2048>(vx[2], sb, xl + (j % 3) * 4096);
      if (i == 3) gldso<3072>(vx[3], sb, xl + (j % 3) * 4096);
    } else {
      const char* sb = wb + cj * (64 * 3072);
      if (i == 4) gldso<0>(wvo + 1024, sb, lA + j * 8192 + w * 2048);
      if (i == 5) gldso<1024>(wvo + 1024, sb, lA + j * 8192 + w * 2048);
      if (i == 6) gldso<0>(wvo, sb, lB + j * 4096 + w * 1024);
    }
  };
#pragma unroll
  for (int i = 0; i < 6; i++) cload(0, i);
  const unsigned tvo = lane < 16 ? TOKR + g * 128 + lane * 4 : TOKC + cx.cb * 128 + ((lane - 16) & 31) * 4;
  const unsigned mvo = MBX + (g * 16 + c) * 512 + (lane & 31) * 16;
  gldsc1(tvo, cx.ws, lB + 5 * 4096 + w * 1024);
  gldsc4(mvo, cx.ws, lB + 6 * 4096 + w * 1024);
#pragma unroll
  for (int i = 0; i < 6; i++) cload(1, i);
  const int ch = (lane >> 4) & 1, q4 = (lane & 15) >> 2, p4 = lane & 3;
  const int cperm = (16 * (p4 & 1) + 4 * (2 * ch + (p4 >> 1))) * 2;
  int r_tr, r_a, r_b, r_c, r_dst, r_d0, r_d1, r_cs = OOB; unsigned r_dv = lane * 16; bool r_half = false;
  {
    const int lj = g * 4 + w, cb = cx.cb, th = cx.th;
    const u16* src; unsigned v0; int s0, s1;
    if (lj < 24) {
      const int bb = lj >> 3, m = lj & 7;
      src = w1 + (size_t)(64 * m) * 1792 + 112 * c + 32 * bb;
      v0 = (lane >> 2) * 3584 + (lane & 3) * 16; s0 = 16 * 3584; s1 = 32 * 3584;
      r_tr = (16 * hk + q4) * 64 + cperm; r_a = 512; r_b = 2048; r_c = 256;
      r_d0 = bb < 2 ? 2048 : 1536; r_d1 = 2 * r_d0;
      r_dst = WT_1 + c * W1_C + (bb < 2 ? bb * 1024 : 65536) + 4 * m * r_d0;
      r_cs = CS1 + (c * 4 + bb) * 1024 + m * 4;
    } else if (lj < 28) {
      const int m4 = lj - 24;
      src = w1 + (size_t)(128 * m4) * 1792 + 112 * c + 96;
      v0 = (lane >> 1) * 3584 + (lane & 1) * 16; s0 = 32 * 3584; s1 = 64 * 3584;
      r_tr = (8 * ch + 16 * hk + q4) * 32 + (8 * (p4 & 1) + 4 * (p4 >> 1)) * 2; r_a = 1024; r_b = 2048; r_c = 128;
      r_dst = WT_1 + c * W1_C + 65536 + 8 * m4 * 1536 + 1024; r_d0 = 3072; r_d1 = 6144;
      r_dv = ch * 1536 + (hk * 16 + (lane & 15)) * 16;
      r_cs = CS1 + (c * 4 + 3) * 1024 + m4 * 8; r_half = true;
    } else {
      v0 = (lane >> 3) * 1024 + (lane & 7) * 16; s0 = 8192; s1 = 16384;
      r_tr = (16 * hk + q4) * 128 + cperm; r_b = 1024; r_c = 512; r_d0 = 1024;
      int kq;
      if (lj < 36) {
        const int rg = 8 * th + lj - 28;
        kq = rg >> 2;
        src = wo + (size_t)(32 * rg) * 512 + 64 * cb;
        r_dst = WT_O + ((cb * 4 + kq) * 8 + ((2 * rg) & 7)) * 2048; r_d1 = 2048;
      } else if (lj < 60) {
        const int u = lj - 36, cc = 8 * th + (u & 7), blk = u >> 3;
        kq = cc >> 2;
        src = w2 + (size_t)(112 * cc + 32 * blk) * 512 + 64 * cb;
        r_dst = WT_2 + ((cb * 4 + kq) * 28 + 8 * blk + (cc & 3)) * 2048; r_d1 = 8192;
      } else {
        const int cc = 8 * th + 2 * (lj - 60);
        kq = cc >> 2;
        src = w2 + (size_t)(112 * cc + 96) * 512 + 64 * cb; s1 = 112 * 1024;
        r_tr = (8 * hk + q4) * 128 + cperm; r_b = 2048;
        r_dst = WT_2 + ((cb * 4 + kq) * 28 + 24 + (cc & 3)) * 2048; r_d1 = 2048;
      }
      r_tr += (kq & 1) * 64; r_a = (kq & 1) ? -64 : 64;
    }
    r_dst = RFL(r_dst); r_d0 = RFL(r_d0); r_d1 = RFL(r_d1); r_cs = RFL(r_cs);
    gldso<0>(v0, src, lstg); gldso<1024>(v0 + s0 - 1024, src, lstg); gldso<2048>(v0 + s1 - 2048, src, lstg); gldso<3072>(v0 + s0 + s1 - 3072, src, lstg);
  }
  gldso<0>(lane * 16, wphi + c * 512, lB + 7 * 4096 + w * 1024);
  const int xro = tok * 128 + (((2 * hk) ^ ((tok >> 1) & 7)) * 16);
  const char* wk = cx.A + hk * 2048 + q4 * 64 + cperm;
  const char* wq = cx.B + hk * 1024 + q4 * 64 + cperm;
  const unsigned on1 = (ch == ((lane >> 2) & 1)) ? 0x3f803f80u : 0u;
  const u32x4 sel = {on1, on1, on1, on1};
  const int mdst = w != 0 || lane >= 48 ? OOB
                 : lane < 16 ? MBX + (g * 16 + lane) * 512 + c * 8
                             : MBX + (((lane - 16) & 15) * 16 + ((c & ~4) | (((lane - 16) >> 4) << 2))) * 512 + (16 + g + 16 * cx.th) * 8;
  unsigned N = 0, sent = 0;
  u32x4 xf[33], ka[3], va[3], rf[4];
  f32x16 acck = {}, accv = {}, gram = {};
  f32x4 csk = {}, csv = {}, s1v = {};
  auto rdx = [&](int n) LAMBDA { xf[n] = *(const u32x4*)(xs + ((n >> 2) % 3) * 4096 + (xro ^ ((4 * ((n >> 1) & 1) + (n & 1)) * 16))); };
  auto rdk = [&](int n) LAMBDA { ka[n % 3] = tr_readc<256>(wk + (n >> 1) * 4096 + (n & 1) * 512); };
  auto rdv = [&](int n) LAMBDA { va[n % 3] = tr_readc<256>(wk + 1024 + (n >> 1) * 4096 + (n & 1) * 512); };
  auto send = [&](int slot, bool first) LAMBDA {
    const unsigned t = *(const unsigned*)(stg + slot * 256 + lane * 4);
    const u32x2 m = {t, N};
    stc2(rs, first || t != sent ? mdst : OOB, 0, m);
    sent = t;
  };
  vm_wait<13>(); bar(); STAMP(1); FST(1, 0);
  rdx(0); rdk(0); rdv(0); rdx(1); rdk(1); rdv(1);
  SB();
  float s2 = 0.f;
  sfor<32>([&](auto nn) LAMBDA {
    constexpr int n = nn, j = n >> 2, s = n & 3;
    if (s == 2 && j < 7) {
      constexpr int cnt[7] = {11, 7, 11, 6, 6, 7, 3};
      FST(1, 2 * j + 1); vm_wait<cnt[j < 7 ? j : 0]>(); bar(); FST(1, 2 * j + 2);
    }
    acck = MFMA(ka[n % 3], xf[n], acck); SB();
    if (n + 2 < 32) rdx(n + 2);
    if (s < 2 && j < 6) cload(j + 2, 3 * s);
    if (n == 14 || n == 18 || n == 22) gldsc1(tvo, cx.ws, lstg + (j & 1) * 256);
    if (n == 26) gldsc4(mvo, cx.ws, lstg + 1024);
    SB();
    csk = MFMA16(sel, ka[n % 3], csk); SB();
    if (n + 2 < 32) rdk(n + 2);
    if (n == 18 || n == 22 || n == 26) send((j - 1) & 1, n == 18);
    if (j >= 6) cload(n - 24, 6);
    SB();
    accv = MFMA(va[n % 3], xf[n], accv); SB();
    if (s < 2 && j < 6) cload(j + 2, 3 * s + 1);
    if (n == 2) {
      const unsigned t = *(const unsigned*)(cx.B + 5 * 4096 + w * 1024 + lane * 4);
      const unsigned m = *(const unsigned*)(cx.B + 6 * 4096 + w * 1024 + lane * 8);
      N = __builtin_amdgcn_readlane(t, c) + 64;
      while (BALLOT(t - N < 64u || m - N < 64u) != 0) N += 64;
      stc1(rs, w == 0 && (lane == c || lane == 16 + 16 * cx.th + g) ? (int)tvo : OOB, 0, N);
    }
    if (n == 6) {
      const char* st = stg + r_tr;
      rf[0] = tr_read2(st, st + r_c); rf[1] = tr_read2(st + r_a, st + r_a + r_c);
      rf[2] = tr_read2(st + r_b, st + r_b + r_c); rf[3] = tr_read2(st + r_a + r_b, st + r_a + r_b + r_c);
    }
    if (n == 7) {
      stc(rs, r_dv, r_dst, rf[0]); stc(rs, r_dv, r_dst + r_d0, rf[1]); stc(rs, r_dv, r_dst + r_d1, rf[2]); stc(rs, r_dv, r_dst + r_d0 + r_d1, rf[3]);
    }
    SB();
    csv = MFMA16(sel, va[n % 3], csv); SB();
    if (n + 2 < 32) rdv(n + 2);
    SB();
    if (n == 0) { gram = MFMA(xf[0], xf[0], gram); s1v = MFMA16(sel, xf[0], s1v); SB(); }
    if (n < 31) { gram = MFMA(xf[n + 1], xf[n + 1], gram); SB(); }
    if (s < 2 && j < 6) cload(j + 2, 3 * s + 2);
    if (n == 7) {
      const unsigned o2 = r_half ? 0x3f803f80u : on1;
      const u32x4 sl = {o2, o2, o2, o2};
      f32x4 ca = {}, cb2 = {};
      ca = MFMA16(sl, rf[0], ca); cb2 = MFMA16(sl, rf[2], cb2); ca = MFMA16(sl, rf[1], ca); cb2 = MFMA16(sl, rf[3], cb2);
      stc1(rs, r_half ? tok * 32 + hk * 4 : (hk ? OOB : tok * 32), r_cs, __float_as_uint(r_half ? (hk ? cb2[0] : ca[0]) : ca[0] + cb2[0]));
    }
    if (n == 8) {
      const char* ap = cx.B + 7 * 4096 + w * 1024 + hk * 512 + q4 * 32 + (2 * ch + (p4 >> 1)) * 8;
      aphi[0] = tr_readc<128>(ap); aphi[1] = tr_readc<128>(ap + 256);
    }
    if (n == 11) {
      const bool on = ((lane >> 2) & 1) == hk;
#pragma unroll
      for (int t = 0; t < 4; t++) { aphi[0][t] = on ? aphi[0][t] : 0u; aphi[1][t] = on ? aphi[1][t] : 0u; }
      asm volatile("" : "+v"(aphi[0]), "+v"(aphi[1]));
    }
    if (n == 31) {
      float t8[8], t4[4];
#pragma unroll
      for (int r = 0; r < 8; r++) t8[r] = lsel(0xFFFF0000FFFF0000ull, gram[8 + r], gram[r]);
#pragma unroll
      for (int r = 0; r < 4; r++) t4[r] = lsel(0xFF00FF00FF00FF00ull, t8[4 + r], t8[r]);
      const float u0 = lsel(0xCCCCCCCCCCCCCCCCull, t4[2], t4[0]), u1 = lsel(0xCCCCCCCCCCCCCCCCull, t4[3], t4[1]);
      s2 = lsel(0xAAAAAAAAAAAAAAAAull, u1, u0);
      s2 = xhalf_sum(((tok >> 2) & 1) == hk ? s2 : 0.f);
    }
    SB();
    if (n < 31) { s1v = MFMA16(sel, xf[n + 1], s1v); SB(); }
    if (n < 31) asm volatile("" : "+a"(acck), "+a"(accv), "+a"(csk), "+a"(csv), "+a"(s1v), "+a"(gram));
    SB();
  });
  STAMP(2); FST(1, 15);
  const float mean = s1v[0] * (1.f / 512.f);
  xf[32] = aug_tok(mean, 0.f, hk);
  csw[1] = cs_pack(csk[0]);
  acck = MFMA(aug_w(csw[1], 0u), xf[32], acck);
  csw[2] = cs_pack(csv[0]);
  accv = MFMA(aug_w(csw[2], 0u), xf[32], accv);
  const float rstd = __builtin_amdgcn_rsqf(fmaxf(s2 * (1.f / 512.f) - mean * mean, 0.f) + 1e-5f);
  const float sc = rstd * LOG2E;
  f32x16 p = {};
  { u32x4 b0, b1; pack16(acck, b0, b1); p = MFMA(aphi[0], b0, p); p = MFMA(aphi[1], b1, p); }
  SB();
  const int js = ((g >> 1) - rot) & 7;
  u32x4 wf0, wf1;
  { const char* wsrc = wk + (w & 1) * 1024 + js * 8192 + (g & 1) * 4096; wf0 = tr_readc<256>(wsrc); wf1 = tr_readc<256>(wsrc + 512); }
  char* tab = cx.B + 32768 + 2048;
  if (w == 0) {
    vm_wait<7>();
    u32x2 m = *(const u32x2*)(stg + 1024 + lane * 8);
    while (true) {
      const bool valid = m[0] == N;
      if (BALLOT(valid && m[1] != sent && lane < 48) != 0) {
        const u32x2 r = {m[1], N};
        stc2(rs, valid && m[1] != sent ? mdst : OOB, 0, r);
        sent = valid ? m[1] : sent;
        COUNT(40);
      }
      if (BALLOT(!valid && lane < 48) == 0) break;
      if (cx.budget == 0) break;
      cx.budget--;
      COUNT(41);
      asm volatile("" ::: "memory");
      m = ldc2(rs, lane * 8, MBX + (g * 16 + c) * 512);
    }
    *(unsigned*)(tab + lane * 4) = lane < 48 ? m[1] : 0u;
    if (lane == 0) *(int*)(tab + 256) = cx.budget;
  }
  lds_barrier();
  STAMP(3);
  {
    const int e = lane >> 1;
    cx.N = RFL(*(const unsigned*)(tab + c * 4));
    cx.budget = RFL(*(const int*)(tab + 256));
    cx.tk_kv = *(const unsigned*)(tab + ((lane >> 4) == 2 ? 16 * cx.th + lane - 16 : 63) * 4);
    cx.tk_oh = *(const unsigned*)(tab + (lane < 8 ? 4 * w + e : 63) * 4);
    cx.tk_x = *(const unsigned*)(tab + (lane < 16 ? 8 * (e >> 2) + 4 * (w >> 1) + (e & 3) : 63) * 4);
  }
  const int kvb = KVA + c * KV_C;
  if (TOUCH) {
    const int t = lane >> 3;
    const u32x4 v = {cx.N, cx.N, cx.N, cx.N};
    const int o = (t < 5 ? G_FO + t * FHL + cx.fo : t == 5 ? G_FX1 + cx.fxo : t == 6 ? G_FX2 + cx.fxo : kvb + KV_L + KV_F + (w * 16 + g) * FS - cx.gb) + (lane & 7) * 16 + cx.gb;
    stc(rs, o, 0, v);
    stc(rs, t < 6 ? o : OOB, G_L, v);
  }
  char* dA = cx.A + w * 16384;
  const int sA = WT_1 + c * W1_C + w * 16384;
  {
    Softmax sm; u32x4 phk[4];
    sfor<29>([&](auto ii) LAMBDA {
      constexpr int i = ii;
      sm.step(i, p, sc, phk);
      if (i < 10) dma<(i < 10 ? i : 0)>(rs, dA, cx.vo, sA);
      if (i == 2) kv_write_v(cx, accv, rstd);
      SB();
    });
    STAMP(4);
    kv_tail<(TOUCH ? 12 : 10)>(cx, phk, kvb);
  }
  STAMP(5);
  {
    f32x16 accq = {}; f32x4 csq = {};
    u32x4 qa[3];
    auto rdq = [&](int n) LAMBDA { qa[n % 3] = tr_readc<256>(wq + (n >> 1) * 2048 + (n & 1) * 512); };
    rdq(0); rdq(1); rdq(2);
    const int wd = WT_QKV + c * WQ_C;
    SB();
    sfor<32>([&](auto nn) LAMBDA {
      constexpr int n = nn;
      accq = MFMA(qa[n % 3], xf[n], accq); SB();
      if (n == 0 && w == 2) { const char* wsrc = wq + js * 4096 + (g & 1) * 2048; wf0 = tr_readc<256>(wsrc); wf1 = tr_readc<256>(wsrc + 512); }
      if (n == 3) {
        const int so = w < 2 ? wd + g * 4096 + w * 1024 : w == 2 ? wd + 65536 + g * 2048 : OOB;
        stc(rs, cx.vo, so, wf0); stc(rs, cx.vo, so + (w < 2 ? 2048 : 1024), wf1);
      }
      if (n < 6) dma<(n < 6 ? 10 + n : 0)>(rs, dA, cx.vo, sA);
      else if (n < 22) wof[n - 6] = ldck(rs, cx.vo, WT_O + (cx.cb * 4 + w) * 16384, n - 6);
      else if (n - 22 < NW2_A) w2f[n - 22] = ldck(rs, cx.vo, WT_2 + (cx.cb * 4 + w) * 57344, n - 22);
      if (n == KQP0) kin.pz = kv_poll(cx, kvb);
      SB();
      csq = MFMA16(sel, qa[n % 3], csq); SB();
      if (n + 3 < 32) rdq(n + 3);
      asm volatile("" : "+a"(accq), "+a"(csq));
      SB();
    });
    csw[0] = cs_pack(csq[0]);
    accq = MFMA(aug_w(csw[0], 0u), xf[32], accq);
    u32x4 q0, q1; pack16(accq, q0, q1);
    f32x16 pq = {}; pq = MFMA(aphi[0], q0, pq); pq = MFMA(aphi[1], q1, pq);
    Softmax sq;
    const int col = 64 * cx.cb + 32 * (w & 1) + 16 * hk;
    const u16* xr = x + (size_t)(128 * g + 64 * cx.th + 32 * (w >> 1) + tok) * 512 + col;
    const int pi = 16 * ((tok >> 2) & 1) + (tok & 3) + 4 * (tok >> 3), ph = 8 * ((tok >> 2) & 1) + (tok & 3) + 4 * ((tok >> 3) & 1);
    const u16* bp = b1 + 112 * c;
    SB();
    sfor<29>([&](auto ii) LAMBDA {
      constexpr int i = ii;
      sq.step(i, pq, sc, phq);
      if (i + 32 == KQP0) kin.pz = kv_poll(cx, kvb);
      if (i == KQW0) { SB(); kv_wait(cx, kin.pz, kvb + KV_F + w * 16 * FS); STAMP(6); kv_load(cx, kin, kvb); }
      if (i == 17) mi.xa = *(const u32x4*)xr;
      if (i == 18) mi.xb = *(const u32x4*)(xr + 8);
      if (i >= 19 && i < 23) mi.cs[(i - 19) & 3] = ldc(rs, tok * 32 + hk * 16 + ((i - 19) & 3) * 1024, CS1 + c * 4096);
      if (i >= 23 && i < 27) mi.b1v[(i - 23) & 3] = bp[i == 26 ? 96 + ph : 32 * (i - 23) + pi];
      if (i == 27) mi.ba = *(const u32x4*)(b2 + col);
      if (i == 28) mi.bb = *(const u32x4*)(b2 + col + 8);
      SB();
    });
  }
}

DEVI void red_send(Cx& cx, const f32x16& a, int j) {
  char* dst = cx.C + (cx.w ^ j) * INBOX + (j - 1) * 4096 + cx.vo;
#pragma unroll
  for (int m = 0; m < 4; m++) { f32x4 v = {a[4 * m], a[4 * m + 1], a[4 * m + 2], a[4 * m + 3]}; *(f32x4*)(dst + m * 1024) = v; }
}
DEVI void red_send1(Cx& cx, const f32x16& a, int j, int m) {
  const f32x4 v = {a[4 * m], a[4 * m + 1], a[4 * m + 2], a[4 * m + 3]};
  *(f32x4*)(cx.C + (cx.w ^ j) * INBOX + (j - 1) * 4096 + cx.vo + m * 1024) = v;
}
struct Red {
  typedef const __attribute__((address_space(3))) unsigned* lp;
  f32x4 r1[4], r2[4], r3[4]; unsigned chk; lp cp;
  DEVI void rd32(Cx& cx, int m) {
    const char* p = cx.C + cx.w * INBOX + m * 1024 + cx.vo;
    r2[m] = *(const f32x4*)(p + 4096); r3[m] = *(const f32x4*)(p + 8192);
  }
  DEVI void put(Cx& cx, unsigned tot) {
    *(unsigned*)(cx.C + cx.w * INBOX + 4096 + cx.lane * 4) = tot + (FORCE_BAD ? cx.w & 1 : 0);
    cp = (lp)(cx.C + (cx.lane & 3) * INBOX + 4096 + 252);
    asm volatile("" : "+v"(cp));
  }
  DEVI void rd1(Cx& cx) {
    chk = *cp;
    SB();
#pragma unroll
    for (int m = 0; m < 4; m++) r1[m] = *(const f32x4*)(cx.C + cx.w * INBOX + m * 1024 + cx.vo);
  }
  DEVI void add32(int m) {
#pragma unroll
    for (int e = 0; e < 4; e++) asm volatile("v_add_f32 %0, %0, %1" : "+v"(r2[m][e]) : "v"(r3[m][e]));
  }
  DEVI bool fin(const f32x16& own, float (&xr)[16]) {
#pragma unroll
    for (int m = 0; m < 4; m++) { const f32x4 o = {own[4 * m], own[4 * m + 1], own[4 * m + 2], own[4 * m + 3]}; r2[m] += o; }
    asm volatile("" : "+v"(r2[0]), "+v"(r2[1]), "+v"(r2[2]), "+v"(r2[3]));
#pragma unroll
    for (int r = 0; r < 16; r++) xr[r] = r2[r >> 2][r & 3] + r1[r >> 2][r & 3];
    return BALLOT(chk != 0) != 0;
  }
};
DEVI void red_recv(Cx& cx, const f32x16& own, float (&xr)[16]) {
  f32x4 v[12];
#pragma unroll
  for (int k = 0; k < 12; k++) v[k] = *(const f32x4*)(cx.C + cx.w * INBOX + ((k % 3) * 4 + k / 3) * 1024 + cx.vo);
#pragma unroll
  for (int m = 0; m < 4; m++)
#pragma unroll
    for (int e = 0; e < 4; e++) xr[4 * m + e] = (own[4 * m + e] + v[3 * m][e]) + (v[3 * m + 1][e] + v[3 * m + 2][e]);
}
DEVI void unpack16(const u32x4& a, const u32x4& b, float (&x)[16]) {
#pragma unroll
  for (int t = 0; t < 4; t++) { x[2 * t] = bflo(a[t]); x[2 * t + 1] = bfhi(a[t]); x[8 + 2 * t] = bflo(b[t]); x[9 + 2 * t] = bfhi(b[t]); }
}
#define EFX 3
#define EFO 0
DEVI void publish_x(Cx& cx, float (&xr)[16], int xb, int sb, int fb) {
  u32x4 f0, f1; pack16(xr, f0, f1);
  stc(cx.rs, cx.vo, xb + cx.xo, f0);
  stc(cx.rs, cx.vo + 1024, xb + cx.xo, f1);
  if (EFX == 0) put_flag(cx, fb + cx.fxo, 7);
  SB();
  f32x2 s1 = {xr[0], xr[1]}, s2 = s1 * s1;
#pragma unroll
  for (int r = 2; r < 16; r += 2) { const f32x2 v = {xr[r], xr[r + 1]}; s1 += v; s2 = __builtin_elementwise_fma(v, v, s2); }
  auto t = __builtin_amdgcn_permlane32_swap(__float_as_uint(s1[0] + s1[1]), __float_as_uint(s2[0] + s2[1]), false, false);
  const unsigned st = __float_as_uint(__uint_as_float(t[0]) + __uint_as_float(t[1]));
  stc1(cx.rs, (cx.lane & 15) * 16 + (cx.lane >> 4 & 1) * 8 + cx.hk * 4, sb + cx.so, st);
  if (EFX == 3) put_flag(cx, fb + cx.fxo, 7);
  SB();
  put_final(cx, fb + CKX + cx.fxo, sum4(sum4(st, f0), f1));
  unpack16(f0, f1, xr);
}
DEVI void park(Cx& cx, const float (&xr)[16]) {
  char* p = cx.C + cx.w * INBOX + C_PARK + cx.vo;
#pragma unroll
  for (int m = 0; m < 4; m++) { f32x4 v = {xr[4 * m], xr[4 * m + 1], xr[4 * m + 2], xr[4 * m + 3]}; *(f32x4*)(p + m * 1024) = v; }
}
DEVI f32x16 unpark(Cx& cx) {
  const char* p = cx.C + cx.w * INBOX + C_PARK + cx.vo;
  f32x16 r;
#pragma unroll
  for (int m = 0; m < 4; m++) { f32x4 v = *(const f32x4*)(p + m * 1024); r[4 * m] = v[0]; r[4 * m + 1] = v[1]; r[4 * m + 2] = v[2]; r[4 * m + 3] = v[3]; }
  return r;
}
DEVI f32x16 tovec(const float (&x)[16]) {
  f32x16 r;
#pragma unroll
  for (int e = 0; e < 16; e++) r[e] = x[e];
  return r;
}
#define UNLIKELY(x) __builtin_expect(!!(x), 0)

template <class AUG, class WIN>
DEVI void xpass(Cx& cx, int xb, int sb, int fb, u32x4 (&xf)[33], f32x16& a0, f32x16& a1, int sti, AUG aug, WIN win) {
  const rsrc_t rs = cx.rs;
  xb += cx.w * 32768; sb += cx.w * 4096; fb += cx.w * 16 * FS;
  if (sti == 15) FST(7, 0);
  if (sti == 25) FST(8, 0);
  if (sti == 15) nap<D_X2>(); else nap<D_X1>();
  poll_wait<16>(cx, ldc1(rs, cx.fv16, fb), fb, cx.tk_x, 7);
  STAMP(sti); if (sti != 15) FST(sti == 11 ? 4 : 5, 0); else FST(7, 1);
  if (sti == 25) FST(8, 1);
  u32x4 st[4], ra[3], rb[3], w0, w1; unsigned ck;
  auto ld = [&](int q) LAMBDA {
    if (q < 4) st[q] = ldck(rs, cx.vo, sb, q);
    else if (q < 36) xf[q - 4] = ldck(rs, cx.vo, xb, q - 4);
    if (q == 30) ck = ldc1(rs, cx.fv16, fb + CKX + 4);
  };
  auto stats = [&]() LAMBDA {
    float v[4];
#pragma unroll
    for (int e = 0; e < 4; e++) v[e] = (__uint_as_float(st[0][e]) + __uint_as_float(st[1][e])) + (__uint_as_float(st[2][e]) + __uint_as_float(st[3][e]));
    auto p = __builtin_amdgcn_permlane32_swap(__float_as_uint(v[0]), __float_as_uint(v[1]), false, false);
    auto q = __builtin_amdgcn_permlane32_swap(__float_as_uint(v[2]), __float_as_uint(v[3]), false, false);
    auto r = __builtin_amdgcn_permlane16_swap(__float_as_uint(__uint_as_float(p[0]) + __uint_as_float(p[1])), __float_as_uint(__uint_as_float(q[0]) + __uint_as_float(q[1])), false, false);
    const unsigned t = __float_as_uint((__uint_as_float(r[0]) + __uint_as_float(r[1])) * (1.f / 512.f));
    auto u = __builtin_amdgcn_permlane32_swap(t, t, false, false);
    const float mean = __uint_as_float(u[0]);
    aug(mean, fmaxf(__uint_as_float(u[1]) - mean * mean, 0.f), w0, w1);
  };
  const char* pa = cx.A + cx.vo;
#pragma unroll
  for (int q = 0; q < 10; q++) { ld(q); SB(); }
  if (sti == 15) FST(7, 2);
  if (sti == 25) FST(8, 2);
  bar();
  if (sti == 15) FST(7, 3);
  if (sti == 25) FST(8, 3);
#pragma unroll
  for (int n = 0; n < 3; n++) { ra[n] = *(const u32x4*)(pa + n * 2048); rb[n] = *(const u32x4*)(pa + n * 2048 + 1024); }
  SB();
  unsigned sum = -cx.tk_x;
  const f32x16 zero = {};
#pragma unroll
  for (int q = 0; q < 4; q++) { sum = sum4(sum, st[q]); SB(); ld(10 + q); SB(); }
  stats();
  SB();
  a0 = MFMA(w0, xf[32], zero); a1 = MFMA(w1, xf[32], zero);
  SB();
  sfor<32>([&](auto nn) LAMBDA {
    constexpr int n = nn;
    if (n % 8 == 0 && sti != 15) FST(sti == 11 ? 4 : 5, 1 + n / 8);
    a0 = MFMA(ra[n % 3], xf[n], a0); SB();
    if (n % 4 == 0 && sti == 15) FST(7, 4 + n / 4);
    if (n % 4 == 0 && sti == 25) FST(8, 4 + n / 4);
    if (n + 3 < 32) ra[n % 3] = *(const u32x4*)(pa + (n + 3) * 2048);
    if (n + 14 < 36) ld(n + 14); else win(ic<(n < 22 ? 0 : n - 22)>());
    SB();
    a1 = MFMA(rb[n % 3], xf[n], a1); SB();
    if (n + 3 < 32) rb[n % 3] = *(const u32x4*)(pa + (n + 3) * 2048 + 1024);
    sum = sum4(sum, xf[n]);
    if (n == 29) sum += ck;
    SB();
  });
  if (sti != 15) FST(sti == 11 ? 4 : 5, 5); else FST(7, 12);
  if (sti == 25) FST(8, 12);
  asm volatile("" : "+a"(a0), "+a"(a1));
  if (UNLIKELY(bad(cx, sum))) {
    do {
      spend(cx, sti == 11 ? 2 : sti == 15 ? 4 : 7);
#pragma unroll
      for (int q = 0; q < 36; q++) ld(q);
      sum = ck - cx.tk_x;
#pragma unroll
      for (int q = 0; q < 32; q++) sum = sum4(sum, xf[q]);
#pragma unroll
      for (int q = 0; q < 4; q++) sum = sum4(sum, st[q]);
    } while (again(cx, sum));
    stats();
    a0 = MFMA(w0, xf[32], zero); a1 = MFMA(w1, xf[32], zero);
#pragma unroll
    for (int n = 0; n < 32; n++) {
      a0 = MFMA(*(const u32x4*)(pa + n * 2048), xf[n], a0); a1 = MFMA(*(const u32x4*)(pa + n * 2048 + 1024), xf[n], a1);
      SB();
    }
  }
}

template <bool P> DEVI f32x2 mul2(f32x2 a, f32x2 b) {
  f32x2 r;
  if (P) asm volatile("v_pk_mul_f32 %0, %1, %2" : "=v"(r) : "v"(a), "v"(b));
  else {
    asm volatile("v_mul_f32 %0, %1, %2" : "=v"(r[0]) : "v"(a[0]), "v"(b[0]));
    asm volatile("v_mul_f32 %0, %1, %2" : "=v"(r[1]) : "v"(a[1]), "v"(b[1]));
  }
  return r;
}
template <bool P> DEVI f32x2 fma2(f32x2 a, f32x2 c) {
  f32x2 r;
  if (P) asm volatile("v_pk_fma_f32 %0, %1, %2, %2 op_sel:[0,1,0] op_sel_hi:[1,1,0]" : "=v"(r) : "v"(a), "v"(c));
  else {
    asm volatile("v_fma_f32 %0, %1, %2, %3" : "=v"(r[0]) : "v"(a[0]), "v"(c[1]), "v"(c[0]));
    asm volatile("v_fma_f32 %0, %1, %2, %3" : "=v"(r[1]) : "v"(a[1]), "v"(c[1]), "v"(c[0]));
  }
  return r;
}
struct Gelu {
  f32x2 cc, ia;
  f32x2 a[4], t, d, e, r, u;
  u32x4 f; unsigned sum;
  template <int K0, int K1, int K, int H, class MID>
  DEVI void step(Cx& cx, const f32x16& lo, const f32x16& hi, int hb, MID mid) {
    constexpr bool P = H == 2, p0 = K >= K0 && K < K1, p1 = K - 1 >= K0 && K - 1 < K1, p2 = K - 2 >= K0 && K - 2 < K1;
    constexpr int v = K - 2;
    f32x2 s, y;
    auto rc = [&]() LAMBDA { if (p2) { r[0] = __builtin_amdgcn_rcpf(d[0]); r[1] = __builtin_amdgcn_rcpf(d[1]); SB(); } };
    auto ex = [&]() LAMBDA { if (p1) { e[0] = __builtin_amdgcn_exp2f(t[0]); e[1] = __builtin_amdgcn_exp2f(t[1]); SB(); } };
    auto rd = [&]() LAMBDA {
      if (p0) {
        const f32x16& h = (K & 8) ? hi : lo;
        float x0, x1;
        asm volatile("v_accvgpr_read_b32 %0, %2\n\tv_accvgpr_read_b32 %1, %3" : "=v"(x0), "=v"(x1) : "a"(h[(2 * K) & 15]), "a"(h[(2 * K + 1) & 15]));
        a[K & 3][0] = x0; a[K & 3][1] = x1;
        SB();
      }
    };
    auto fin = [&]() LAMBDA { if (p2 && (v & 3) == 3) { stc(cx.rs, cx.vo, hb + (v >> 2) * 8192, f); sum = sum4(sum, f); } };
    if (H != 1) rd();
    if (H != 0) rc();
    if (H != 1) ex();
    mid(); SB();
    if (H != 0 && p0 && p2) {
      float y0, y1;
      asm volatile("v_mul_f32 %2, %6, %8\n\t"
                   "v_mul_f32 %3, %7, %9\n\t"
                   "v_pk_mul_f32 %0, %5, %5\n\t"
                   "v_cvt_pk_bf16_f32 %4, %2, %3\n\t"
                   "v_pk_fma_f32 %0, %0, %11, %11 op_sel:[0,1,0] op_sel_hi:[1,1,0]\n\t"
                   "v_pk_fma_f32 %1, %10, %12, %12\n\t"
                   "v_pk_mul_f32 %0, %5, %0"
                   : "=&v"(t), "=&v"(d), "=&v"(y0), "=&v"(y1), "=&v"(f[v & 3])
                   : "v"(a[K & 3]), "v"(a[v & 3][0]), "v"(a[v & 3][1]), "v"(r[0]), "v"(r[1]), "v"(e), "v"(cc), "v"(ia));
    } else if (H != 0) {
      if (p0) s = mul2<0>(a[K & 3], a[K & 3]);
      if (p2) y = mul2<0>(a[v & 3], r);
      if (p0) u = fma2<0>(s, cc);
      if (p1) d = fma2<0>(e, ia);
      if (p0) t = mul2<0>(a[K & 3], u);
      if (p2) f[v & 3] = pk2(y[0], y[1]);
    }
    if (H != 0) fin();
  }
};

#define GB2 34
#define GP1 26
#define GW1 40
#define GP3 9
#define GP4 37
#define GP2 42
#define GW3 26
#define H3F 1
#define GBR0 78
#define GBR1 90
#define W2KEEP 45
#define NW2_C 22
#define NW2_H 28
#define NB1H 6
#define NQK 8
#define KQP1 31
#define KQW1 12

constexpr int h_iss(int i) { return i < 32 ? i : i < 40 ? 33 + 2 * (i - 32) : i < 48 ? 32 + 2 * (i - 40) : i < 52 ? 49 + 2 * (i - 48) : 48 + 2 * (i - 52); }
constexpr int s4_f(int G) {
  return G < 64 ? G >> 1 : G < 80 ? 33 + 2 * ((G - 64) >> 1) : G < 84 ? 32 + 2 * (G - 80) : G < 92 ? 49 + 2 * ((G - 84) >> 1) : G < 100 ? 40 + 2 * (G - 92) : 32 + 2 * (G - 100);
}
constexpr int s4_j(int G) { return G < 64 ? (G & 2) + 1 - (G & 1) : G < 80 || (G >= 84 && G < 92) ? 3 - (G & 1) : G < 100 ? 1 : 0; }

#define BL 1
#define BUDL(i) do { if (L == BL) BUD(53 + (i)); } while (0)
template <int L>
DEVI void layer(Cx& cx, const u16* __restrict__ x, const u16* __restrict__ b1, const u16* __restrict__ b2, u16* __restrict__ out,
                u32x4 (&phq)[4], const unsigned (&csw)[3], const u32x4 (&aphi)[2], KvIn& kin, u32x4 (&w2f)[56], u32x4 (&wof)[16],
                Misc& mi, unsigned (&csp)[4], unsigned (&b1p)[2]) {
  const int lane = cx.lane, w = cx.w, tok = cx.tok, hk = cx.hk, g = cx.g, c = cx.c;
  const rsrc_t rs = cx.rs;
  const int gl = cx.gb + L * G_L;
  const int kvb = KVA + L * KV_L + c * KV_C;
  const int w2b = WT_2 + (cx.cb * 4 + w) * 57344;
  const f32x16 zero = {};
  constexpr int SO = 14 * L;
  u32x4 xa, xb2;
  const int col = 64 * cx.cb + 32 * (w & 1) + 16 * hk;
  const size_t xrow = (size_t)(128 * g + 64 * cx.th + 32 * (w >> 1) + tok) * 512 + col;
  BUDL(-1);
  if (L == 1) {
    u32x4 xf[33]; f32x16 acck, accv; float rstd;
    xpass(cx, cx.gb + G_X2, cx.gb + G_ST2, cx.gb + G_FX2, xf, acck, accv, 15,
          [&](float mean, float var, u32x4& w0, u32x4& w1) LAMBDA {
            rstd = __builtin_amdgcn_rsqf(var + 1e-5f);
            xf[32] = aug_tok(mean, 0.f, hk); w0 = aug_w(csw[1], 0u); w1 = aug_w(csw[2], 0u);
          },
          [&](auto ii) LAMBDA { if (ii.v < 8) dma<(ii.v & 7)>(rs, cx.B + w * 8192, cx.vo, WT_QKV + c * WQ_C + 65536 + w * 8192); });
    STAMP(16); FST(6, 0); FST(7, 13); BUDL(0);
    const float sc = rstd * LOG2E;
    f32x16 p;
    { u32x4 b0, b1; pack16(acck, b0, b1); p = MFMA(aphi[0], b0, zero); p = MFMA(aphi[1], b1, p); }
    FST(6, 1);
    kv_write_v(cx, accv, rstd);
    SB();
    {
      Softmax sm; u32x4 phk[4];
      sfor<29>([&](auto ii) LAMBDA {
        constexpr int i = ii;
        if (i % 4 == 0) FST(6, 2 + i / 4);
        sm.step(i, p, sc, phk);
        SB();
      });
      STAMP(17); FST(6, 10);
      kv_tail<0>(cx, phk, kvb);
    }
    STAMP(18); FST(6, 11);
    f32x16 accq = MFMA(aug_w(csw[0], 0u), xf[32], zero);
    u32x4 qa[3];
    const char* pq = cx.B + cx.vo;
#pragma unroll
    for (int n = 0; n < 3; n++) qa[n] = *(const u32x4*)(pq + n * 1024);
    char* dA = cx.A + w * 16384;
    const int sA = WT_1 + c * W1_C + w * 16384;
    SB();
    sfor<32>([&](auto nn) LAMBDA {
      constexpr int n = nn;
      accq = MFMA(qa[n % 3], xf[n], accq); SB();
      if (n + 3 < 32) qa[n % 3] = *(const u32x4*)(pq + (n + 3) * 1024);
      if (n >= 1 && n < 17) wof[n - 1] = ldck(rs, cx.vo, WT_O + (cx.cb * 4 + w) * 16384, n - 1);
      if (n >= 17 && (n & 1)) dma<(n >= 17 ? (n - 17) / 2 : 0)>(rs, dA, cx.vo, sA);
      if (n == KQP1) kin.pz = kv_poll(cx, kvb);
      if (n % 8 == 7) FST(6, 12 + n / 8);
      SB();
    });
    u32x4 b0, b1; pack16(accq, b0, b1);
    f32x16 pq2 = MFMA(aphi[0], b0, zero); pq2 = MFMA(aphi[1], b1, pq2);
    Softmax sq;
    sfor<29>([&](auto ii) LAMBDA {
      constexpr int i = ii;
      sq.step(i, pq2, sc, phq);
      if (i + 32 == KQP1) kin.pz = kv_poll(cx, kvb);
      if (i == KQW1) { SB(); kv_wait(cx, kin.pz, kvb + KV_F + w * 16 * FS); STAMP(19); kv_load(cx, kin, kvb); }
      if (i >= 19 && i < 27) dma<(i >= 19 && i < 27 ? i - 11 : 0)>(rs, dA, cx.vo, sA);
      if (i == 27) mi.ba = *(const u32x4*)(b2 + col);
      if (i == 28) mi.bb = *(const u32x4*)(b2 + col + 8);
      SB();
    });
  }
  asm volatile("" : "+v"(phq[0]), "+v"(phq[1]), "+v"(phq[2]), "+v"(phq[3]));
  SB();
  STAMP(7 + SO); BUDL(1);
  {
    const int G = lane >> 4, d = lane & 15;
    u32x4 idn;
#pragma unroll
    for (int t = 0; t < 4; t++) { const int k = 8 * (G & 1) + 2 * t; idn[t] = k == d ? 0x3f80u : k + 1 == d ? 0x3f800000u : 0u; }
    f32x4 kv; unsigned sum;
    auto red = [&]() LAMBDA {
      f32x4 k0 = {}, k1 = {}; sum = -cx.tk_kv;
#pragma unroll
      for (int m = 0; m < 8; m += 2) {
        k0 = MFMA16(idn, kin.pa[m], k0); k1 = MFMA16(idn, kin.pa[m + 1], k1);
        sum = sum4(sum4(sum, kin.pa[m]), kin.pa[m + 1]);
      }
      sum = sum4(sum, kin.pz); kv = k0 + k1;
    };
    red();
    asm volatile("" : "+v"(kv));
    if (UNLIKELY(bad(cx, sum))) {
      do { spend(cx, 5 * L); kin.pz = kv_poll(cx, kvb); kv_load(cx, kin, kvb); red(); } while (again(cx, sum));
    }
    char* img = cx.C + w * INBOX + C_IMG;
    { const u32x2 v = {pk2(kv[0], kv[1]), pk2(kv[2], kv[3])}; *(u32x2*)(img + d * 32 + G * 8) = v; }
    { const u32x4 z4 = {}; *(u32x4*)(img + 512 + cx.vo) = hk ? z4 : kin.pz; }
    lds_barrier();
    const int hp = (tok >> 2) & 1, dd = (tok & 3) + 4 * (tok >> 3);
    const char* ap = cx.C + (hp == hk ? 2 * hp * INBOX + C_IMG + dd * 32 : C_ZERO);
    const int zo = hp == hk ? 512 : 0;
    u32x4 a[4], az[4];
#pragma unroll
    for (int s4 = 0; s4 < 4; s4++) az[s4] = *(const u32x4*)(ap + zo + (s4 >> 1) * INBOX + (s4 & 1) * 16);
#pragma unroll
    for (int s4 = 0; s4 < 4; s4++) a[s4] = *(const u32x4*)(ap + (s4 >> 1) * INBOX + (s4 & 1) * 16);
    if (L == 0) stc1(rs, w == 0 && (lane >> 4) == 2 ? (lane - 32) * 8192 : OOB, MBX + c * 512 + (48 + g) * 8, cx.tk_kv + 1);
    if (EFO == 2) put_flag(cx, gl + G_FO + cx.fo, 7);
    SB();
    f32x16 num = {}, den = {};
#pragma unroll
    for (int s4 = 0; s4 < 4; s4++) den = MFMA(az[s4], phq[s4], den);
    if (EFO == 1) { SB(); put_flag(cx, gl + G_FO + cx.fo, 7); SB(); }
#pragma unroll
    for (int s4 = 0; s4 < 4; s4++) num = MFMA(a[s4], phq[s4], num);
    float ds[4];
#pragma unroll
    for (int r = 0; r < 4; r++) ds[r] = (den[r] + den[4 + r]) + (den[8 + r] + den[12 + r]);
    const float inv = __builtin_amdgcn_rcpf(((ds[0] + ds[1]) + (ds[2] + ds[3])) + 1e-6f);
    float ov[16];
#pragma unroll
    for (int r = 0; r < 16; r++) ov[r] = num[r] * inv;
    u32x4 f0, f1; pack16(ov, f0, f1);
    stc(rs, cx.vo, gl + G_O + cx.oo, f0);
    stc(rs, cx.vo + 1024, gl + G_O + cx.oo, f1);
    if (EFO == 0) put_flag(cx, gl + G_FO + cx.fo, 7);
    put_final(cx, gl + G_FO + CKO + cx.fo, sum4(sum4(0u, f0), f1));
    STAMP(8 + SO);
  }
  float xr[16];
  {
    const int ob = gl + G_O + (cx.th * 4 + w) * 16384, fb = gl + G_FO + (cx.th * 4 + w) * 8 * FS;
    f32x16 acc[4];
    if (L == 0) {
      unpack16(mi.xa, mi.xb, xr); acc[0] = tovec(xr);
#pragma unroll
      for (int k = 0; k < 4; k++) {
        const f32x4 v = __builtin_bit_cast(f32x4, mi.cs[k]);
        csp[k] = cs_pack(xhalf_sum((v[0] + v[1]) + (v[2] + v[3])));
      }
      b1p[0] = mi.b1v[0] | ((unsigned)mi.b1v[1] << 16); b1p[1] = mi.b1v[2] | ((unsigned)mi.b1v[3] << 16);
    } else acc[0] = unpark(cx);
    SB();
    char* dB = cx.B + w * 12288;
    const int sB = WT_1 + c * W1_C + 65536 + w * 12288;
    sfor<NB1H>([&](auto ii) LAMBDA { dma<ii.v>(rs, dB, cx.vo, sB); });
    nap<D_O>();
    poll_wait<8>(cx, ldc1(rs, cx.fv8, fb), fb, cx.tk_oh, 7);
    STAMP(9 + SO); if (L == 1) FST(9, 0);
    BUDL(2);
    u32x4 of[16];
#pragma unroll
    for (int k = 0; k < 10; k++) { of[k ^ 8] = ldck(rs, cx.vo, ob, k ^ 8); SB(); }
    lds_barrier();
    unsigned sum = -cx.tk_oh, ck;
    Red rd;
    sfor<32>([&](auto nn) LAMBDA {
      constexpr int n = nn, j = n < 12 ? 3 - (n % 3 == 2) : 3 - n / 8, s = n < 12 ? (n % 3 == 2 ? n / 3 : n - n / 3) : n & 7;
      constexpr int i = n >= 13 && n % 3 != 0 ? n - 13 - (n - 13) / 3 : -1;
      if (L == 1 && n % 4 == 0) FST(9, 1 + n / 4);
      acc[j] = MFMA(wof[2 * s + (j & 1)], of[8 * (j >> 1) + s], s == 0 && j ? zero : acc[j]); SB();
      if (n < 6) of[(n + 10) ^ 8] = ldck(rs, cx.vo, ob, (n + 10) ^ 8);
      if (n == 6) ck = ldc1(rs, cx.fv8, fb + CKO + 4);
      if (n >= 7 && n < 19 - NB1H) dma<(n >= 7 && n < 19 - NB1H ? n - 7 + NB1H : 0)>(rs, dB, cx.vo, sB);
      if (L == 0 && n >= 19 - NB1H && NW2_A + n - (19 - NB1H) < NW2_C) w2f[NW2_A + n - (19 - NB1H)] = ldck(rs, cx.vo, w2b, NW2_A + n - (19 - NB1H));
      if (j & 1) sum = sum4(sum, of[8 * (j >> 1) + s]);
      if (i >= 0 && i < 12) red_send1(cx, acc[3 - (i / 4 & 3)], 3 - (i / 4 & 3), i & 3);
      if (n == 24) sum += ck;
      if (n >= 24 && n < 27) { wsum_step<(n >= 24 && n < 27 ? 2 * (n - 24) : 0)>(sum); SB(); wsum_step<(n >= 24 && n < 27 ? 2 * (n - 24) + 1 : 0)>(sum); }
      if (n == 27) { lds_barrier_n<2>(); rd.rd32(cx, 0); rd.rd32(cx, 1); rd.rd32(cx, 2); rd.rd32(cx, 3); rd.put(cx, sum); }
      if (n >= 30) { rd.add32(2 * (n & 1)); rd.add32(2 * (n & 1) + 1); }
      SB();
    });
    if (L == 1) FST(9, 9);
    lds_barrier();
    if (L == 1) FST(9, 10);
    rd.rd1(cx);
    SB();
    if (UNLIKELY(rd.fin(acc[0], xr))) {
      lds_barrier();
      do {
        spend(cx, 1 + 5 * L);
        sum = ldc1(rs, cx.fv8, fb + CKO + 4) - cx.tk_oh;
#pragma unroll
        for (int k = 0; k < 16; k++) { of[k] = ldck(rs, cx.vo, ob, k); sum = sum4(sum, of[k]); }
      } while (again(cx, sum));
      if (L == 0) { xa = *(const u32x4*)(x + xrow); xb2 = *(const u32x4*)(x + xrow + 8); }
      else { xa = ldc(rs, cx.vo, cx.gb + G_X2 + cx.xo); xb2 = ldc(rs, cx.vo + 1024, cx.gb + G_X2 + cx.xo); }
      unpack16(xa, xb2, xr); acc[0] = tovec(xr); acc[1] = zero; acc[2] = zero; acc[3] = zero;
#pragma unroll
      for (int n = 0; n < 32; n++) acc[n >> 3] = MFMA(wof[2 * (n & 7) + ((n >> 3) & 1)], of[8 * (n >> 4) + (n & 7)], acc[n >> 3]);
      red_send(cx, acc[1], 1); red_send(cx, acc[2], 2); red_send(cx, acc[3], 3);
      lds_barrier();
      red_recv(cx, acc[0], xr);
    }
    if (L == 1) FST(9, 11);
    publish_x(cx, xr, gl + G_X1, gl + G_ST1, gl + G_FX1);
    if (L == 1) FST(9, 12);
    STAMP(10 + SO);
    if (L == 0) {
#pragma unroll
      for (int k = NW2_C; k < NW2_H; k++) w2f[k] = ldck(rs, cx.vo, w2b, k);
    }
    float bv[16]; unpack16(mi.ba, mi.bb, bv);
#pragma unroll
    for (int r = 0; r < 16; r++) xr[r] += bv[r];
    park(cx, xr);
  }
  {
    u32x4 xf[33]; f32x16 h0, h1, h2, h3;
    Gelu ge;
    xpass(cx, gl + G_X1, gl + G_ST1, gl + G_FX1, xf, h0, h1, 11 + SO,
          [&](float mean, float var, u32x4& w0, u32x4& w1) LAMBDA {
            const float r1 = __builtin_amdgcn_rsqf(var + 1e-5f);
            const float A = r1 * __builtin_amdgcn_rsqf(var * r1 * r1 + 1e-5f);
            const float iA = __builtin_amdgcn_rcpf(A);
            ge.ia[0] = iA; ge.ia[1] = iA;
            xf[32] = aug_tok(mean, iA, hk);
            const float k0 = -2.f * LOG2E * 0.7978845608028654f, k1 = k0 * 0.044715f;
            ge.cc[0] = k0 * A; ge.cc[1] = k1 * A * A * A;
            w0 = aug_w(csp[0], b1p[0]); w1 = aug_w(csp[1], b1p[0] >> 16);
          },
          [&](auto ii) LAMBDA {
            constexpr int k = NW2_H + ii.v - 8;
            if (ii.v < 8) dma<(ii.v & 7)>(rs, cx.C + w * INBOX, cx.vo, w2b + 48 * 1024);
            else if (L == 0 && k < W2KEEP) w2f[k] = ldck(rs, cx.vo, w2b, k);
          });
    STAMP(12 + SO); if (L == 1) FST(8, 13);
    BUDL(3);
    const int hs = gl + G_H + cx.ho, hfo = gl + G_FH + cx.fo;
    const int hb = gl + G_H + (cx.th * 4 + w) * 57344, fb = gl + G_FH + (cx.th * 4 + w) * 8 * FS;
    ge.sum = 0;
    asm volatile("" : "+a"(h0), "+a"(h1));
    h2 = MFMA(aug_w(csp[2], b1p[1]), xf[32], zero); h3 = MFMA(aug_w(csp[3], b1p[1] >> 16), xf[32], zero);
    const char* pb = cx.B + cx.vo;
    const char* ph = cx.B + 1024 + (hk * 16 + (lane & 15)) * 16;
    u32x4 ra[4], hf[56];
    auto rdw = [&](int G) LAMBDA { ra[G & 3] = *(const u32x4*)((G < 32 ? pb : ph) + (G & 31) * 1536); };
    auto ldh = [&](int k) LAMBDA { hf[h_iss(k)] = ldck(rs, cx.vo, hb, h_iss(k)); };
#pragma unroll
    for (int n = 0; n < 4; n++) rdw(n);
    SB();
    unsigned pl = 0, sum = -cx.tk_oh, ck;
    f32x16 acc[4];
    sfor<64>([&](auto gg) LAMBDA {
      constexpr int G = gg, n = G & 31;
      if (G % 8 == 0) FST(4 + L, 6 + G / 8);
      if (G < 32) h2 = MFMA(ra[G & 3], xf[n], h2); else h3 = MFMA(ra[G & 3], xf[n], h3);
      SB();
      auto mid = [&]() LAMBDA { if (G + 4 < 64) rdw(G + 4); };
      if (G < 10) ge.step<0, 16, G, 2>(cx, h0, h1, hs, mid);
      else if (G < 26) ge.step<0, 16, 10 + (G - 10) / 2, G & 1>(cx, h0, h1, hs, mid);
      else if (G >= GB2 && G < GB2 + 20) ge.step<16, 24, 16 + (G - GB2) / 2, (G - GB2) & 1>(cx, h2, h2, hs, mid);
      else mid();
      if (L == 0 && NW2_H + 2 + G < W2KEEP) w2f[NW2_H + 2 + G] = ldck(rs, cx.vo, w2b, NW2_H + 2 + G);
      if (G == 9) put_flag(cx, hfo + FHL, 1);
      if (G == 25) put_flag(cx, hfo + 2 * FHL, 3);
      if (G == GB2 + 19) put_flag(cx, hfo + 3 * FHL, 5);
      if (G == GP1) pl = ldc1(rs, cx.fv8, fb + FHL);
      if (G == GW1) { SB(); poll_wait<8>(cx, pl, fb + FHL, cx.tk_oh, 1); STAMP(13 + SO); BUDL(4); }
      if (G >= GW1 && G < GW1 + 16) ldh(G - GW1);
      if (G == GP2) pl = ldc1(rs, cx.fv8, fb + 2 * FHL);
      if (G == 56) { SB(); poll_wait<8>(cx, pl, fb + 2 * FHL, cx.tk_oh, 3); BUDL(5); }
      if (G >= 56 && !(G & 1)) ldh(G / 2 - 12);
      if (G == 60) acc[0] = unpark(cx);
      SB();
    });
    FST(4 + L, 14);
    STAMP(14 + SO);
    unsigned msg = 0;
    char* dA = cx.A + w * 16384;
    const int sA = WT_QKV + c * WQ_C + w * 16384;
    Red rd;
    sfor<112>([&](auto gg) LAMBDA {
      constexpr int G = gg, f = s4_f(G), q = f >> 1, j = s4_j(G);
      constexpr int i = G >= 92 && G % 3 != 1 ? G - 92 - (G - 92) / 3 : -1;
      if (G % 8 == 0) FST(2 + L, G / 8);
      acc[j] = MFMA(w2f[2 * q + (j & 1)], hf[f], q == 0 && j ? zero : acc[j]); SB();
      if (H3F) {
        if (G >= 2 && G < 8) ge.step<24, 28, 22 + G, 2>(cx, h3, h3, hs, []() LAMBDA {});
        if (G == 7) put_flag(cx, hfo, 7);
      } else {
        if (G >= 2 && G < 14) ge.step<24, 28, 24 + (G - 2) / 2, G & 1>(cx, h3, h3, hs, []() LAMBDA {});
        if (G == 13) put_flag(cx, hfo, 7);
      }
      if (G == 15) put_final(cx, hfo + CKH, ge.sum);
      if (G == GW3) { SB(); poll_wait<8>(cx, pl, fb + 3 * FHL, cx.tk_oh, 5); BUDL(6); }
      if (G == GW3 + 32) { SB(); poll_wait<8>(cx, pl, fb, cx.tk_oh, 7); BUDL(7); }
      if (G < 8) ldh(20 + G);
      else if (G < 16) { if (!(G & 1)) ldh(24 + G / 2); }
      else if (G >= GW3 && G < GW3 + 48 && !((G - GW3) & 1)) ldh(32 + (G - GW3) / 2);
      if (G == GP3) pl = ldc1(rs, cx.fv8, fb + 3 * FHL);
      if (G == GP4) pl = ldc1(rs, cx.fv8, fb);
      if (G == GW3 + 45) ck = ldc1(rs, cx.fv8, fb + CKH + 4);
      if (G >= 16 && G < 24) w2f[G + 32] = *(const u32x4*)(cx.C + w * INBOX + (G - 16) * 1024 + cx.vo);
      if (G == (L ? GBR1 : GBR0)) lds_barrier();
      if (!(G & 1) && G >= 16 && (G - 16) / 2 + W2KEEP < 48) w2f[(G - 16) / 2 + W2KEEP] = ldck(rs, cx.vo, w2b, (G - 16) / 2 + W2KEEP);
      if (L == 0) {
        if (G == 51) msg = ldc1(rs, (lane & 15) * 8, MBX + (g * 16 + c) * 512 + 384);
        if (G == 79) { SB(); mail_wait(cx, msg, (lane & 15) * 8, MBX + (g * 16 + c) * 512 + 384, cx.N + 1); }
        if (G >= 80 && !(G & 1) && (G - 80) / 2 < NQK) dma<(G >= 80 ? (G - 80) / 2 : 0)>(rs, dA, cx.vo, sA);
      }
      if (j & 1) sum = sum4(sum, hf[f]);
      if (i >= 0 && i < 12) red_send1(cx, acc[3 - (i / 4 & 3)], 3 - (i / 4 & 3), i & 3);
      if (G == 100) sum += ck;
      if (G >= 100 && G < 106) wsum_step<(G >= 100 && G < 106 ? G - 100 : 0)>(sum);
      if (G == 106) { lds_barrier_n<2>(); rd.rd32(cx, 0); rd.rd32(cx, 1); rd.rd32(cx, 2); rd.rd32(cx, 3); rd.put(cx, sum); }
      if (G >= 109) rd.add32(G & 3);
      if (G == 111) rd.add32(0);
      SB();
    });
    lds_barrier();
    rd.rd1(cx);
    SB();
    FST(2 + L, 14);
    if (UNLIKELY(rd.fin(acc[0], xr))) {
      lds_barrier();
      do {
        spend(cx, 3 + 5 * L);
        xa = ldc(rs, cx.vo, gl + G_X1 + cx.xo); xb2 = ldc(rs, cx.vo + 1024, gl + G_X1 + cx.xo);
        float bv[16]; unpack16(*(const u32x4*)(b2 + col), *(const u32x4*)(b2 + col + 8), bv);
        unpack16(xa, xb2, xr);
#pragma unroll
        for (int r = 0; r < 16; r++) acc[0][r] = xr[r] + bv[r];
        acc[1] = zero; acc[2] = zero; acc[3] = zero; sum = ldc1(rs, cx.fv8, fb + CKH + 4) - cx.tk_oh;
#pragma unroll
        for (int k = 0; k < 56; k++) {
          const u32x4 f = ldck(rs, cx.vo, hb, k);
          sum = sum4(sum, f);
          acc[2 * (k & 1)] = MFMA(w2f[k & ~1], f, acc[2 * (k & 1)]); acc[2 * (k & 1) + 1] = MFMA(w2f[k | 1], f, acc[2 * (k & 1) + 1]);
          if ((k & 7) == 7) SB();
        }
      } while (again(cx, sum));
      red_send(cx, acc[1], 1); red_send(cx, acc[2], 2); red_send(cx, acc[3], 3);
      lds_barrier();
      red_recv(cx, acc[0], xr);
    }
    FST(2 + L, 15);
    if (L == 0) {
      publish_x(cx, xr, cx.gb + G_X2, cx.gb + G_ST2, cx.gb + G_FX2);
      STAMP(20);
      sfor<16 - NQK>([&](auto ii) LAMBDA { dma<NQK + ii.v>(rs, dA, cx.vo, sA); });
      park(cx, xr);
    } else {
      u32x4 f0, f1; pack16(xr, f0, f1);
      *(u32x4*)(out + xrow) = f0; *(u32x4*)(out + xrow + 8) = f1;
      STAMP(29);
    }
  }
}

__global__ __launch_bounds__(256, 1) void fused(const u16* __restrict__ x, const u16* __restrict__ wqkv, const u16* __restrict__ wphi,
                                                const u16* __restrict__ wo, const u16* __restrict__ w1, const u16* __restrict__ b1,
                                                const u16* __restrict__ w2, const u16* __restrict__ b2, char* ws, u16* __restrict__ out) {
  __shared__ __attribute__((aligned(16))) char ldsA[65536];
  __shared__ __attribute__((aligned(16))) char ldsB[49152];
  __shared__ __attribute__((aligned(16))) char ldsC[49152];
  Cx cx;
  STAMP(0); CSTAMP(30);
  {
    unsigned long long pc; unsigned len;
    asm volatile("s_getpc_b64 %0\n.Lcb0:\n\ts_mov_b32 %1, .Lce0-.Lcb0-16" : "=s"(pc), "=s"(len));
    unsigned o = ((blockIdx.x >> 3) * 256 + threadIdx.x) * 16;
    gldso<0>(o < len ? o : 0u, (const void*)pc, (unsigned)(size_t)ldsC + 48128u);
  }
  cx.ws = ws; cx.A = ldsA; cx.B = ldsB; cx.C = ldsC;
  cx.lane = threadIdx.x & 63; cx.w = RFL(threadIdx.x >> 6);
  cx.tok = cx.lane & 31; cx.hk = cx.lane >> 5; cx.vo = cx.lane * 16; cx.budget = SPIN_MAX;
  { const int b = blockIdx.x, xx = b & 7, j = b >> 3; cx.g = 4 * (xx >> 1) + (j >> 3); cx.c = 8 * (xx & 1) + (j & 7); }
  cx.cb = 4 * (cx.c >> 3) + (cx.c & 3); cx.th = (cx.c >> 2) & 1;
  cx.rs = __builtin_amdgcn_make_buffer_rsrc(ws, 0, WS_USED, 0x00020000);
  cx.gb = GRP + cx.g * G_S;
  cx.fv16 = cx.lane < 16 ? cx.lane * FS + 120 : OOB; cx.fv8 = cx.lane < 8 ? cx.lane * FS + 120 : OOB; cx.f63 = cx.lane >= 48 ? (cx.lane - 48) * 8 : OOB;
  cx.fvz = cx.lane < 32 ? (cx.lane >> 1) * FS + (cx.lane & 1) * 16 : cx.lane < 48 ? (cx.lane - 32) * FS + 112 : OOB;
  {
    const int w = cx.w, kq = cx.c >> 2, tx = (w & 1) ^ (kq >> 1), ln = (w >> 1) * 4 + kq, tb = 2 * cx.th + (w >> 1), blk = 2 * cx.cb + (w & 1);
    cx.oo = (ln * 16 + tx * 8 + (cx.c & 3) * 2) * 1024;
    cx.ho = (ln * 56 + (cx.c & 3) * 2 + tx) * 1024;
    cx.fo = (ln * 8 + (cx.c & 3) * 2 + (w & 1)) * FS;
    cx.xo = (tb * 32 + 2 * blk) * 1024;
    cx.so = tb * 4096 + blk * 256;
    cx.fxo = (tb * 16 + blk) * FS;
  }
  u32x4 phq[4], aphi[2], w2f[56], wof[16]; unsigned csw[3], csp[4], b1p[2]; KvIn kin; Misc mi;
  stage1a(cx, x, wqkv, wphi, wo, w1, b1, w2, b2, phq, csw, aphi, kin, w2f, wof, mi);
  layer<0>(cx, x, b1, b2, out, phq, csw, aphi, kin, w2f, wof, mi, csp, b1p);
  asm volatile("" : "+v"(cx.lane), "+v"(cx.tok), "+v"(cx.hk), "+v"(cx.vo));
  asm volatile("" : "+v"(csp[0]), "+v"(csp[1]), "+v"(csp[2]), "+v"(csp[3]), "+v"(b1p[0]), "+v"(b1p[1]), "+v"(csw[0]), "+v"(csw[1]), "+v"(csw[2]));
#pragma unroll
  for (int k = 0; k < W2KEEP; k++) asm volatile("" : "+a"(w2f[k]));
  layer<1>(cx, x, b1, b2, out, phq, csw, aphi, kin, w2f, wof, mi, csp, b1p);
  asm volatile(".Lce0:");
}

extern "C" void kernel_launch(void* const* d_in, const int* in_sizes, int n_in, void* d_out, int out_size, void* d_ws, size_t ws_size,
                              hipStream_t stream) {
  fused<<<256, 256, 0, stream>>>((const u16*)d_in[0], (const u16*)d_in[1], (const u16*)d_in[2], (const u16*)d_in[3], (const u16*)d_in[5],
                                 (const u16*)d_in[6], (const u16*)d_in[7], (const u16*)d_in[8], (char*)d_ws, (u16*)d_out);
}
```

```cpp
#include <hip/hip_runtime.h>
#include <stdint.h>

typedef unsigned short u16;
typedef __attribute__((ext_vector_type(8))) __bf16 bf16x8;
typedef __attribute__((ext_vector_type(2))) __bf16 bf16x2;
typedef __attribute__((ext_vector_type(16))) float f32x16;
typedef __attribute__((ext_vector_type(4))) float f32x4;
typedef __attribute__((ext_vector_type(2))) float f32x2;
typedef __attribute__((ext_vector_type(4))) unsigned u32x4;
typedef __attribute__((ext_vector_type(2))) unsigned u32x2;
typedef __attribute__((ext_vector_type(4))) short s16x4;
typedef __amdgpu_buffer_rsrc_t rsrc_t;
typedef __attribute__((address_space(3))) void* ldsp;

#define DEVI __device__ __forceinline__
#define LAMBDA __attribute__((always_inline))
#define MFMA(a, b, c) __builtin_amdgcn_mfma_f32_32x32x16_bf16(__builtin_bit_cast(bf16x8, a), __builtin_bit_cast(bf16x8, b), c, 0, 0, 0)
#define MFMA16(a, b, c) __builtin_amdgcn_mfma_f32_16x16x32_bf16(__builtin_bit_cast(bf16x8, a), __builtin_bit_cast(bf16x8, b), c, 0, 0, 0)
#define SB() __builtin_amdgcn_sched_barrier(0)
#define RFL(v) __builtin_amdgcn_readfirstlane(v)
#define UNLIKELY(x) __builtin_expect(!!(x), 0)
#define BALLOT(p) __builtin_amdgcn_ballot_w64(p)

constexpr int WT_QKV = 0x0, WQ_C = 98304;
constexpr int WT_O   = 0x180000;
constexpr int WT_1   = 0x200000, W1_C = 114688;
constexpr int CS1    = 0x3C0000;
constexpr int WT_2   = 0x400000;
constexpr int MBX    = 0x5C0000;
constexpr int TOKR   = 0x5E0000;
constexpr int TOKC   = 0x5E1000;
constexpr int KVA    = 0x600000, KV_C = 0xC000, KV_L = 0xC0000, KV_F = 0x8000;
constexpr int GRP    = 0x800000, G_S = 0x200000, G_L = 0xC6000;
constexpr int G_O = 0, G_X1 = 0x20000, G_H = 0x40000, G_ST1 = 0xB0000, G_FO = 0xB4000, G_FH = 0xB6000, G_FX1 = 0xBE000;
constexpr int FS = 128, FHL = 0x2000, CKO = 0xC000, CKX = 0x4000, CKH = 0xE000;
constexpr int G_X2 = 0x18C000, G_ST2 = 0x1AC000, G_FX2 = 0x1B0000;
constexpr int WS_USED = 0x2800000;
constexpr int OOB = 0x40000000;
#define STAMP_OFF 0x2800000
#define STAMP_N 64

constexpr int INBOX = 12288;
constexpr int C_V = 4352, C_IMG = 6656, C_ZERO = 7680, C_PARK = 8192;
constexpr float LOG2E = 1.4426950408889634f;
constexpr int SPIN_MAX = 1 << 16;

DEVI unsigned pk2(float a, float b) { f32x2 v = {a, b}; return __builtin_bit_cast(unsigned, __builtin_convertvector(v, bf16x2)); }
DEVI float bflo(unsigned u) { return __uint_as_float(u << 16); }
DEVI float bfhi(unsigned u) { return __uint_as_float(u & 0xffff0000u); }
DEVI void lds_barrier() { asm volatile("s_waitcnt lgkmcnt(0)\n\ts_barrier" ::: "memory"); }
template <int N> DEVI void lds_barrier_n() { asm volatile("s_waitcnt lgkmcnt(%0)\n\ts_barrier" :: "n"(N) : "memory"); }
DEVI void bar() { asm volatile("s_barrier" ::: "memory"); }
template <int N> DEVI void vm_wait() { asm volatile("s_waitcnt vmcnt(%0)" :: "n"(N) : "memory"); }
template <int OFF> DEVI void gldso(unsigned voff, const void* sbase, unsigned lds_base) {
  asm volatile("s_mov_b32 m0, %2\n\ts_nop 0\n\tglobal_load_lds_dwordx4 %0, %1 offset:%3" :: "v"(voff), "s"(sbase), "s"(lds_base), "n"(OFF) : "memory", "m0");
}
DEVI void gldsv(const void* p, unsigned lds_base) {
  asm volatile("s_mov_b32 m0, %1\n\ts_nop 0\n\tglobal_load_lds_dwordx4 %0, off" :: "v"(p), "s"(lds_base) : "memory", "m0");
}
DEVI void gldsc4(unsigned voff, const void* sbase, unsigned lds_base) {
  asm volatile("s_mov_b32 m0, %2\n\ts_nop 0\n\tglobal_load_lds_dwordx4 %0, %1 sc1" :: "v"(voff), "s"(sbase), "s"(lds_base) : "memory", "m0");
}
DEVI void gldsc1(unsigned voff, const void* sbase, unsigned lds_base) {
  asm volatile("s_mov_b32 m0, %2\n\ts_nop 0\n\tglobal_load_lds_dword %0, %1 sc1" :: "v"(voff), "s"(sbase), "s"(lds_base) : "memory", "m0");
}
DEVI float xhalf_sum(float v) {
  auto t = __builtin_amdgcn_permlane32_swap(__float_as_uint(v), __float_as_uint(v), false, false);
  return __uint_as_float(t[0]) + __uint_as_float(t[1]);
}
DEVI u32x4 ldc(rsrc_t rs, int vo, int so) { return __builtin_amdgcn_raw_buffer_load_b128(rs, vo, so, 16); }
DEVI u32x2 ldc2(rsrc_t rs, int vo, int so) { return __builtin_amdgcn_raw_buffer_load_b64(rs, vo, so, 16); }
DEVI unsigned ldc1(rsrc_t rs, int vo, int so) { return __builtin_amdgcn_raw_buffer_load_b32(rs, vo, so, 16); }
DEVI void stc(rsrc_t rs, int vo, int so, u32x4 v) { __builtin_amdgcn_raw_buffer_store_b128(v, rs, vo, so, 16); }
DEVI void stc2(rsrc_t rs, int vo, int so, u32x2 v) { __builtin_amdgcn_raw_buffer_store_b64(v, rs, vo, so, 16); }
DEVI void stc1(rsrc_t rs, int vo, int so, unsigned v) { __builtin_amdgcn_raw_buffer_store_b32(v, rs, vo, so, 16); }
DEVI u32x4 ldck(rsrc_t rs, int vo, int so, int kb) { return ldc(rs, vo + (kb & 3) * 1024, so + (kb >> 2) * 4096); }
template <int KB> DEVI void dma(rsrc_t rs, char* dst, int vo, int so) {
  __builtin_amdgcn_raw_ptr_buffer_load_lds(rs, (ldsp)(dst + (KB >> 2) * 4096), 16, vo, so + (KB >> 2) * 4096, (KB & 3) * 1024, 16);
}
DEVI float lsel(unsigned long long m, float t, float f) { float r; asm("v_cndmask_b32_e64 %0, %1, %2, %3" : "=v"(r) : "v"(f), "v"(t), "s"(m)); return r; }
DEVI float fmax3(float a, float b, float c) { return fmaxf(fmaxf(a, b), c); }
DEVI float fmin3(float a, float b, float c) { return fminf(fminf(a, b), c); }
DEVI unsigned sum4(unsigned s, const u32x4& f) { s = (s + f[0] + f[1]) + f[2] + f[3]; asm volatile("" : "+v"(s)); return s; }
DEVI unsigned wsum(unsigned v) {
  v += __builtin_amdgcn_update_dpp(0u, v, 0x111, 0xf, 0xf, false);
  v += __builtin_amdgcn_update_dpp(0u, v, 0x112, 0xf, 0xf, false);
  v += __builtin_amdgcn_update_dpp(0u, v, 0x114, 0xf, 0xf, false);
  v += __builtin_amdgcn_update_dpp(0u, v, 0x118, 0xf, 0xf, false);
  v += __builtin_amdgcn_update_dpp(0u, v, 0x142, 0xa, 0xf, false);
  v += __builtin_amdgcn_update_dpp(0u, v, 0x143, 0xc, 0xf, false);
  return v;
}

template <int I> DEVI void wsum_step(unsigned& v) {
  constexpr int ctl[6] = {0x111, 0x112, 0x114, 0x118, 0x142, 0x143}, rm[6] = {0xf, 0xf, 0xf, 0xf, 0xa, 0xc};
  v += __builtin_amdgcn_update_dpp(0u, v, ctl[I], rm[I], 0xf, false);
}

#define FINE 0
#define FST(sel, i)
#define STAMP(i)
#define CSTAMP(i)
#define COUNT(i)
#define BUD(i)

template <class T>
DEVI void pack16(const T& v, u32x4& a, u32x4& b) {
#pragma unroll
  for (int i = 0; i < 4; i++) { a[i] = pk2(v[2 * i], v[2 * i + 1]); b[i] = pk2(v[8 + 2 * i], v[9 + 2 * i]); }
}
template <int I> struct ic { static constexpr int v = I; constexpr operator int() const { return I; } };
template <int... I> struct iseq {};
template <int N, int... I> struct mkseq : mkseq<N - 1, N - 1, I...> {};
template <int... I> struct mkseq<0, I...> { typedef iseq<I...> type; };
template <class F, int... I> DEVI void sfor_impl(F& f, iseq<I...>) { (f(ic<I>()), ...); }
template <int N, class F> DEVI void sfor(F f) { sfor_impl(f, typename mkseq<N>::type()); }

struct Softmax {
  float mx, mn, sp, sn, ep[16], en[16];
  DEVI void step(int i, const f32x16& p, float sc, u32x4 (&ph)[4]) {
    if (i == 0) { mx = fmax3(p[0], p[1], p[2]); mn = fmin3(p[0], p[1], p[2]); mx = fmax3(mx, p[3], p[4]); mn = fmin3(mn, p[3], p[4]); }
    else if (i < 3) { int b = 5 + 4 * (i - 1); mx = fmax3(mx, p[b], p[b + 1]); mn = fmin3(mn, p[b], p[b + 1]); mx = fmax3(mx, p[b + 2], p[b + 3]); mn = fmin3(mn, p[b + 2], p[b + 3]); }
    else if (i == 3) { mx = fmax3(mx, p[13], p[14]); mn = fmin3(mn, p[13], p[14]); mx = fmaxf(mx, p[15]) * -sc; mn = fminf(mn, p[15]) * sc; }
    else if (i < 20) {
      int r = i - 4;
      ep[r] = __builtin_amdgcn_exp2f(fmaf(p[r], sc, mx)); en[r] = __builtin_amdgcn_exp2f(fmaf(p[r], -sc, mn));
      if (r == 2) { sp = ep[0] + ep[1]; sn = en[0] + en[1]; } else if (r > 2) { sp += ep[r - 1]; sn += en[r - 1]; }
    } else if (i == 20) { sp = __builtin_amdgcn_rcpf(sp + ep[15]); sn = __builtin_amdgcn_rcpf(sn + en[15]); }
    else if (i < 29) {
      int t = i - 21;
      ph[t >> 2][t & 3] = pk2(ep[2 * t] * sp, ep[2 * t + 1] * sp);
      ph[2 + (t >> 2)][t & 3] = pk2(en[2 * t] * sn, en[2 * t + 1] * sn);
    }
  }
};

template <int C> DEVI u32x4 tr_readc(const char* p) {
  typedef __attribute__((address_space(3))) s16x4* lp;
  s16x4 a = __builtin_amdgcn_ds_read_tr16_b64_v4i16((lp)(p));
  s16x4 b = __builtin_amdgcn_ds_read_tr16_b64_v4i16((lp)(p + C));
  u32x2 ua = __builtin_bit_cast(u32x2, a), ub = __builtin_bit_cast(u32x2, b);
  u32x4 r = {ua[0], ua[1], ub[0], ub[1]};
  return r;
}
DEVI u32x4 tr_read2(const char* p, const char* p2) {
  typedef __attribute__((address_space(3))) s16x4* lp;
  s16x4 a = __builtin_amdgcn_ds_read_tr16_b64_v4i16((lp)(p));
  s16x4 b = __builtin_amdgcn_ds_read_tr16_b64_v4i16((lp)(p2));
  u32x2 ua = __builtin_bit_cast(u32x2, a), ub = __builtin_bit_cast(u32x2, b);
  u32x4 r = {ua[0], ua[1], ub[0], ub[1]};
  return r;
}

DEVI u32x4 aug_tok(float mean, float ia, int hk) {
  unsigned mh = pk2(-mean, ia);
  unsigned ml = pk2(-mean - bflo(mh), ia - bfhi(mh));
  u32x4 t;
  t[0] = __builtin_amdgcn_perm(ml, mh, 0x05040100u);
  t[1] = mh;
  t[2] = ml >> 16;
  t[3] = 0;
  if (hk) { t[0] = 0; t[1] = 0; t[2] = 0; }
  return t;
}
DEVI unsigned cs_pack(float cs) { unsigned h = pk2(cs, 0.f); return pk2(cs, cs - bflo(h)); }
DEVI u32x4 aug_w(unsigned csp, unsigned b) {
  u32x4 t;
  t[0] = __builtin_amdgcn_perm(csp, csp, 0x01000100u);
  t[1] = __builtin_amdgcn_perm(b, csp, 0x05040302u);
  t[2] = b & 0xffffu;
  t[3] = 0;
  return t;
}

struct Cx {
  rsrc_t rs; char* ws; char *A, *B, *C;
  int lane, w, tok, hk, g, c, cb, th, vo, gb;
  int fv16, fv8, fvz, f63, oo, ho, fo, xo, so, fxo;
  unsigned N, tk_kv, tk_oh, tk_x;
  int budget;
};

DEVI void put_flag(Cx& cx, int so, int i) { u32x2 v = {cx.N + i, cx.N + i}; stc2(cx.rs, cx.f63, so, v); }
DEVI void put_final(Cx& cx, int so, unsigned sum) { u32x2 v = {cx.N + 7, cx.N - wsum(sum)}; stc2(cx.rs, cx.f63, so, v); }
template <int NP> DEVI void poll_wait(Cx& cx, unsigned a, int so, unsigned tk, int i) {
  unsigned t; int s;
  asm volatile("v_sub_u32 %0, %3, %4\n\t"
               "v_cmp_ne_u32 vcc, %9, %0\n\t"
               "s_and_b32 %1, vcc_lo, %8\n\t"
               "s_cbranch_scc0 2f\n"
               "1:\n\t"
               "s_cmp_eq_u32 %2, 0\n\t"
               "s_cbranch_scc1 2f\n\t"
               "buffer_load_dword %0, %5, %6, %7 offen sc1\n\t"
               "s_sub_u32 %2, %2, 1\n\t"
               "s_waitcnt vmcnt(0)\n\t"
               "v_sub_u32 %0, %0, %4\n\t"
               "v_cmp_ne_u32 vcc, %9, %0\n\t"
               "s_and_b32 %1, vcc_lo, %8\n\t"
               "s_cbranch_scc1 1b\n"
               "2:"
               : "=&v"(t), "=&s"(s), "+s"(cx.budget)
               : "v"(a), "v"(tk), "v"(NP == 16 ? cx.fv16 : cx.fv8), "s"(cx.rs), "s"(so), "n"((1 << NP) - 1), "n"(i)
               : "vcc", "scc", "memory");
}
DEVI void kv_wait(Cx& cx, u32x4& q, int so) {
  int s; unsigned f = q[2];
  asm volatile("v_sub_u32 %0, %0, %4\n\t"
               "v_cmp_ne_u32 vcc, 7, %0\n\t"
               "s_and_b32 %2, vcc_hi, 0xffff\n\t"
               "s_cbranch_scc0 2f\n"
               "1:\n\t"
               "s_cmp_eq_u32 %3, 0\n\t"
               "s_cbranch_scc1 2f\n\t"
               "buffer_load_dword %0, %5, %6, %7 offen offset:8 sc1\n\t"
               "s_sub_u32 %3, %3, 1\n\t"
               "s_waitcnt vmcnt(0)\n\t"
               "v_sub_u32 %0, %0, %4\n\t"
               "v_cmp_ne_u32 vcc, 7, %0\n\t"
               "s_and_b32 %2, vcc_hi, 0xffff\n\t"
               "s_cbranch_scc1 1b\n\t"
               "buffer_load_dwordx4 %1, %5, %6, %7 offen sc1\n\t"
               "s_waitcnt vmcnt(0)\n"
               "2:"
               : "+v"(f), "+v"(q), "=&s"(s), "+s"(cx.budget)
               : "v"(cx.tk_kv), "v"(cx.fvz), "s"(cx.rs), "s"(so)
               : "vcc", "scc", "memory");
}
#define FORCE_BAD 0
DEVI bool bad(Cx& cx, unsigned sum) { return __builtin_amdgcn_readlane(wsum(sum), 63) + (FORCE_BAD ? cx.w & 1 : 0) != 0; }
DEVI bool again(Cx& cx, unsigned sum) {
  const unsigned r = __builtin_amdgcn_readlane(wsum(sum), 63);
  return (r != 0) & (cx.budget >= 64);
}
DEVI void spend(Cx& cx, int id) {
  asm volatile("" ::: "memory"); cx.budget = cx.budget >= 64 ? cx.budget - 64 : 0; }
#define D_O 8
#define D_X1 6
#define D_X2 2
#define CROT 0
#define PRE 1
#define TOUCH 1
template <int N> DEVI void nap() { if (N > 0) { SB(); __builtin_amdgcn_s_sleep(N); SB(); } }
DEVI unsigned usel(unsigned long long m, unsigned t, unsigned f) { unsigned r; asm("v_cndmask_b32_e64 %0, %1, %2, %3" : "=v"(r) : "v"(f), "v"(t), "s"(m)); return r; }
DEVI void mail_wait(Cx& cx, unsigned a, int mvo, int so, unsigned v) {
  int s;
  asm volatile("v_cmp_ne_u32 vcc, %3, %0\n\t"
               "s_and_b32 %1, vcc_lo, 0xffff\n\t"
               "s_cbranch_scc0 2f\n"
               "1:\n\t"
               "s_cmp_eq_u32 %2, 0\n\t"
               "s_cbranch_scc1 2f\n\t"
               "buffer_load_dword %0, %4, %5, %6 offen sc1\n\t"
               "s_sub_u32 %2, %2, 1\n\t"
               "s_waitcnt vmcnt(0)\n\t"
               "v_cmp_ne_u32 vcc, %3, %0\n\t"
               "s_and_b32 %1, vcc_lo, 0xffff\n\t"
               "s_cbranch_scc1 1b\n"
               "2:"
               : "+v"(a), "=&s"(s), "+s"(cx.budget)
               : "s"(v), "v"(mvo), "s"(cx.rs), "s"(so)
               : "vcc", "scc", "memory");
}
#define S1PRE 10, 11, 12, 13
#define S1NT 10
#define S1SCH \
  120, 121, 122,  123, 124, 125,    0,   0,   0,  130, 131,  30,  132, 133,   0,  134, 135,   0,  144, 145,   0,  140, 141,   0, \
  142, 143,   0,  154, 155,   0,  164, 165,   0,  150, 151,  20,  152, 153,  21,   22,  23,  24,  174, 175,   0,  160, 161,   0, \
  162, 163,   0,    0,   0,   0,   40,   0,   0,  170, 171,   0,  172, 173,   0,  106, 116,   0,  126, 136,   0,  146, 166,   0, \
  176,   0,   0,    0,   0,   0,    0,   0,   0,    0,   0,  50,  156,   0,   0,    0,   0,   0,    0,   0,   0,    0,   0,   0
DEVI void kv_write_v(Cx& cx, const f32x16& accv, float rstd) {
  float vn[16];
#pragma unroll
  for (int r = 0; r < 16; r++) vn[r] = accv[r] * rstd;
  u32x4 v0, v1; pack16(vn, v0, v1);
  char* p = cx.C + 2 * cx.hk * INBOX + C_V + (32 * cx.w + cx.tok) * 16;
  *(u32x4*)p = v0; *(u32x4*)(p + INBOX + 128) = v1;
}
template <int VMW>
DEVI void kv_tail(Cx& cx, const u32x4 (&phk)[4], int kvb) {
  char* p = cx.C + 2 * cx.hk * INBOX + (32 * cx.w + cx.tok) * 16;
  *(u32x4*)p = phk[0]; *(u32x4*)(p + 2176) = phk[1]; *(u32x4*)(p + INBOX) = phk[2]; *(u32x4*)(p + INBOX + 2176) = phk[3];
  const int G = cx.lane >> 4, d = cx.lane & 15, q4 = d >> 2, p4 = cx.lane & 3;
  const int ro = (4 * G + q4) * 16 + (p4 & 1) * 8;
  const char* pa = cx.C + cx.w * INBOX + (p4 >> 1) * 2176 + ro;
  const char* pb = cx.C + ((cx.w & 2) + (p4 >> 1)) * INBOX + C_V + (p4 >> 1) * 128 + ro;
  if (VMW >= 0) vm_wait<(VMW >= 0 ? VMW : 0)>();
  lds_barrier();
  u32x4 a[4], b[4];
#pragma unroll
  for (int t = 0; t < 4; t++) { a[t] = tr_readc<256>(pa + t * 512); b[t] = tr_readc<256>(pb + t * 512); }
  const u32x4 ones = {0x3f803f80u, 0x3f803f80u, 0x3f803f80u, 0x3f803f80u};
  f32x4 kv = {}, z = {};
#pragma unroll
  for (int t = 0; t < 4; t++) { kv = MFMA16(a[t], b[t], kv); z = MFMA16(a[t], ones, z); }
  const u32x2 k2 = {pk2(kv[0], kv[1]), pk2(kv[2], kv[3])}, z2 = {pk2(z[0], z[1]), pk2(z[2], z[3])};
  const int e = cx.w * 16 + cx.g;
  stc2(cx.rs, (G >> 1) * 256 + d * 16 + (G & 1) * 8, kvb + e * 512, k2);
  const unsigned cs = -7u - wsum(k2[0] + k2[1] + usel(0x1000900010001000ull, z2[0] + z2[1], 0u));
  const u32x2 zl = {usel(1ull << 63, cx.N + 7, z2[0]), usel(1ull << 63, cs, z2[1])};
  stc2(cx.rs, d >= 12 ? (d - 12) * 32 + G * 8 : OOB, kvb + KV_F + e * FS, zl);
}
struct KvIn { u32x4 pa[8], pz; };
struct Misc { u32x4 xa, xb, ba, bb, cs[4]; u16 b1v[4]; };
DEVI u32x4 kv_poll(Cx& cx, int kvb) { return ldc(cx.rs, cx.fvz, kvb + KV_F + cx.w * 16 * FS); }
DEVI void kv_load(Cx& cx, KvIn& k, int kvb) {
#pragma unroll
  for (int m = 0; m < 8; m++) k.pa[m] = ldck(cx.rs, cx.vo, kvb + cx.w * 8192, m);
}

#define NW2_A 0
#define SND 26
#define MBI 8
#define CSL 2
#define CSR 22
DEVI f32x4 sq4(const u32x4& f, int h, f32x4 c) {
  const u32x2 t = {f[2 * h], f[2 * h + 1]};
  return __builtin_amdgcn_mfma_f32_4x4x4bf16_1k(__builtin_bit_cast(s16x4, t), __builtin_bit_cast(s16x4, t), c, 0, 0, 0);
}
#define KQP0 31
#define KQW0 12

constexpr int S1P = 19;
constexpr int s1_op[S1P + 96] = {100, 101, 102, 103, 104, 105, 2, 3, 110, 111, 112, 113, 114, 115, 4, S1PRE, S1SCH};
constexpr int s1_key(int o) { return o >= 100 ? o / 10 * 10 + (o % 10 >= 4) + (o % 10 == 6) : o >= 10 && o < 20 ? 10 : (o >= 20 && o < 40) || o >= 50 ? 0 : o; }
constexpr bool s1_m0(int i) {
  for (int k = i - 1; k >= 0; k--) if (s1_key(s1_op[k])) return s1_key(s1_op[k]) != s1_key(s1_op[i]);
  return true;
}
constexpr int s1_pos(int o) { for (int k = 0; k < S1P + 96; k++) if (s1_op[k] == o) return k; return -1; }
constexpr int s1_step(int o) { return (s1_pos(o) - S1P) / 3; }
constexpr int s1_cnt(int n, int lo, int hi, int e = 0) {
  int last = e ? s1_pos(e) : -1, r = 0;
  for (int o = lo; o <= hi; o++) if (s1_pos(o) > last) last = s1_pos(o);
  for (int k = last + 1; k < S1P + 3 * n; k++) r += s1_op[k] != 0;
  return r;
}
template <int OFF, bool M> DEVI void gldm(unsigned voff, const void* sbase, unsigned lds_base) {
  if (M) gldso<OFF>(voff, sbase, lds_base);
  else asm volatile("global_load_lds_dwordx4 %0, %1 offset:%2" :: "v"(voff), "s"(sbase), "n"(OFF) : "memory");
}

DEVI void stage1a(Cx& cx, const u16* __restrict__ x, const u16* __restrict__ wqkv, const u16* __restrict__ wphi,
                  const u16* __restrict__ wo, const u16* __restrict__ w1, const u16* __restrict__ b1, const u16* __restrict__ w2,
                  const u16* __restrict__ b2, u32x4 (&phq)[4], unsigned (&csw)[3], u32x4 (&aphi)[2], KvIn& kin, u32x4 (&w2f)[56],
                  u32x4 (&wof)[16], Misc& mi) {
  const int lane = cx.lane, w = cx.w, tok = cx.tok, hk = cx.hk, g = cx.g, c = cx.c;
  const int rot = (c + 2 * g) & 7;
  const rsrc_t rs = cx.rs;
  const unsigned lA = (unsigned)(size_t)cx.A, lB = (unsigned)(size_t)cx.B, lC = (unsigned)(size_t)cx.C;
  char* xs = cx.C + w * INBOX;
  char* stg = cx.B + 32768 + w * 4096;
  const unsigned lstg = lB + 32768 + w * 4096;
  unsigned vx[4];
#pragma unroll
  for (int i = 0; i < 4; i++) vx[i] = (8 * i + (lane >> 3)) * 1024 + (((lane & 7) ^ ((4 * i + (lane >> 4)) & 7)) * 16) - i * 1024;
  const char* xb = (const char*)(x + (size_t)(128 * g + 32 * w) * 512);
  const unsigned xl = lC + w * INBOX;
  const unsigned wvo = (lane >> 2) * 3072 + (lane & 3) * 16;
  const char* wb = (const char*)(wqkv + (size_t)(16 * w) * 1536 + 32 * c);
  auto cload = [&](auto kk) LAMBDA {
    constexpr int j = s1_op[kk.v] / 10 - 10, i = s1_op[kk.v] % 10; constexpr bool M = s1_m0(kk.v);
    const int cj = (j + rot) & 7;
    if (i < 4) gldm<(i & 3) * 1024, M>(vx[i & 3], xb + cj * 128, xl + (j % 3) * 4096);
    else if (i < 6) gldm<(i & 1) * 1024, M>(wvo + 1024, wb + cj * (64 * 3072), lA + j * 8192 + w * 2048);
    else gldm<0, M>(wvo, wb + cj * (64 * 3072), lB + j * 4096 + w * 1024);
  };
  sfor<6>([&](auto kk) LAMBDA { cload(kk); });
  const unsigned tvo = lane < 16 ? TOKR + g * 128 + lane * 4 : TOKC + cx.cb * 128 + ((lane - 16) & 31) * 4;
  const unsigned mvo = MBX + (g * 16 + c) * 512 + (lane & 31) * 16;
  gldsc1(tvo, cx.ws, lB + 5 * 4096 + w * 1024);
  gldsc4(mvo, cx.ws, lB + 6 * 4096 + w * 1024);
  sfor<6>([&](auto kk) LAMBDA { cload(ic<8 + kk.v>()); });
  const int ch = (lane >> 4) & 1, q4 = (lane & 15) >> 2, p4 = lane & 3;
  const int cperm = (16 * (p4 & 1) + 4 * (2 * ch + (p4 >> 1))) * 2;
  int r_tr, r_a, r_b, r_c, r_t3, r_c3, r_dst, r_d1, r_cs = OOB; unsigned r_d3;
  const char *src, *src2, *src3; unsigned v0, v1, v2;
  gldso<0>(lane * 16, wphi + c * 512, lB + 7 * 4096 + w * 1024);
  {
    const int cb = cx.cb, hf = g & 1, cc = 8 * cx.th + (g >> 1), kq = cc >> 2;
    const char* s2 = (const char*)(w2 + (size_t)(112 * cc) * 512 + 64 * cb);
    const int d2 = WT_2 + ((cb * 4 + kq) * 28 + (cc & 3)) * 2048;
    if (w < 2) {
      const int n = 2 * g + w;
      src = src2 = (const char*)(w1 + (size_t)(32 * g + 8 * w) * 1792 + 112 * c);
      auto vo = [&](int t) LAMBDA { const int i = (t * 4682) >> 16; return (i + (i & 8)) * 3584 + (t - 14 * i) * 16; };
      v0 = vo(lane); v1 = vo(lane + 64); v2 = vo(lane + 128);
      src3 = hk ? s2 + (96 + 8 * w + (tok >> 2)) * 1024 + hf * 64 + p4 * 16 : src + vo(lane + 192);
      r_tr = (8 * hk + q4) * 224 + cperm; r_a = 64; r_b = 128; r_c = 896;
      r_t3 = hk ? 3584 + q4 * 64 + cperm : (8 * ch + q4) * 224 + 192 + (8 * (p4 & 1) + 4 * (p4 >> 1)) * 2; r_c3 = hk ? 256 : 896;
      r_dst = WT_1 + c * W1_C + n * 2048; r_d1 = 65536 - n * 512;
      r_d3 = hk ? d2 + 24 * 2048 + (hf ^ (kq & 1)) * 1024 + (32 * w + tok) * 16 : r_dst + r_d1 + 1024 + lane * 16;
      r_cs = CS1 + c * 16384 + n * 512;
    } else {
      v0 = (lane >> 3) * 1024 + (lane & 7) * 16; r_c = r_c3 = 512;
      if (w == 2) {
        src = src2 = s2 + hf * 65536; v1 = v0 + 8192; v2 = v0 + 16384; src3 = src + v0 + 24576;
        r_tr = (16 * hk + q4) * 128 + cperm; r_b = 1024;
        r_dst = d2 + hf * 32768; r_d1 = 8192;
      } else {
        src = s2 + (32 + 8 * hf) * 1024; src2 = (const char*)(wo + (size_t)(32 * cc + 8 * hf) * 512 + 64 * cb);
        v1 = v0 + 16384; v2 = v0; src3 = src2 + v1;
        r_tr = (8 * hk + q4) * 128 + cperm; r_b = 2048;
        r_dst = d2 + (8 + 4 * hf) * 2048; r_d1 = WT_O + ((cb * 4 + kq) * 8 + ((2 * cc) & 7) + hf) * 2048 - r_dst;
      }
      r_tr += (kq & 1) * 64; r_a = (kq & 1) ? -64 : 64;
      r_t3 = r_tr + r_a + r_b; r_d3 = r_dst + r_d1 + 1024 + lane * 16;
    }
    r_dst = RFL(r_dst); r_d1 = RFL(r_d1); r_cs = RFL(r_cs); src2 -= 2048; src3 -= 3072; v1 -= 1024;
  }
  const int xro = tok * 128 + (((2 * hk) ^ ((tok >> 1) & 7) ^ (4 * (w >> 1) + (w & 1))) * 16);
  const char* wk = cx.A + hk * 2048 + q4 * 64 + cperm;
  const char* wq = cx.B + hk * 1024 + q4 * 64 + cperm;
  const char* wks[4];
#pragma unroll
  for (int s = 0; s < 4; s++) { const int t = s ^ w; wks[s] = wk + (t >> 1) * 4096 + (t & 1) * 512; }
  const unsigned on1 = (ch == ((lane >> 2) & 1)) ? 0x3f803f80u : 0u;
  const u32x4 sel = {on1, on1, on1, on1};
  const int mdst = w != 0 || lane >= 48 ? OOB
                 : lane < 16 ? MBX + (g * 16 + lane) * 512 + c * 8
                             : MBX + (((lane - 16) & 15) * 16 + ((c & ~4) | (((lane - 16) >> 4) << 2))) * 512 + (16 + g + 16 * cx.th) * 8;
  unsigned N = 0, sent = 0;
  u32x4 xf[33], ka[3], va[3], rf[4];
  f32x16 acck = {}, accv = {};
  f32x4 csk = {}, csv = {}, s1v = {}, q0 = {}, q1 = {}, cj[4];
  f32x2 cso[3] = {};
  auto rdx = [&](int n) LAMBDA { xf[n] = *(const u32x4*)(xs + ((n >> 2) % 3) * 4096 + (xro ^ ((4 * ((n >> 1) & 1) + (n & 1)) * 16))); };
  auto rdk = [&](int n) LAMBDA { ka[n % 3] = tr_readc<256>(wks[n & 3] + (n >> 2) * 8192); };
  auto rdv = [&](int n) LAMBDA { va[n % 3] = tr_readc<256>(wks[n & 3] + 1024 + (n >> 2) * 8192); };
  unsigned tk0, tk1;
  auto issue = [&](auto kk) LAMBDA {
    constexpr int o = s1_op[kk.v]; constexpr bool M = s1_m0(kk.v);
    if (o >= 100) cload(kk);
    if (o == 10) gldm<0, M>(v0, src, lstg);
    if (o == 11) gldm<1024, M>(v1, src, lstg);
    if (o == 12) gldm<2048, M>(v2, src2, lstg);
    if (o == 13) {
      if (M) asm volatile("s_mov_b32 m0, %0\n\ts_nop 0" :: "s"(lstg) : "memory", "m0");
      asm volatile("global_load_lds_dwordx4 %0, off offset:3072" :: "v"(src3) : "memory");
    }
    if (o == 20) stc(rs, cx.vo, r_dst, rf[0]);
    if (o == 21) stc(rs, cx.vo + 1024, r_dst, rf[1]);
    if (o == 22) stc(rs, cx.vo, r_dst + r_d1, rf[2]);
    if (o == 23) stc(rs, r_d3, 0, rf[3]);
    if (o == 24) { const u32x2 cv = {__float_as_uint(hk ? cj[2][0] : cj[0][0]), __float_as_uint(hk ? cj[3][0] : cj[1][0])}; stc2(rs, lane * 8, r_cs, cv); }
    if (o == 30) {
      N = __builtin_amdgcn_readlane(tk0, c) + 64;
      while (BALLOT(tk0 - N < 64u || tk1 - N < 64u) != 0) N += 64;
      stc1(rs, w == 0 && (lane == c || lane == 16 + 16 * cx.th + g) ? (int)tvo : OOB, 0, N);
      *(unsigned*)(cx.B + 5 * 4096 + w * 1024 + 768) = N;
    }
    if (o >= 40 && o < 45) gldsc1(tvo, cx.ws, lB + 5 * 4096 + w * 1024 + (o - 39) * 256);
    if (o >= 50 && o < 60) {
      const u32x2 m = {tk0, N};
      stc2(rs, o == 50 || tk0 != sent ? mdst : OOB, 0, m);
      sent = tk0;
    }
    SB();
  };
  constexpr int NT = S1NT;
  static_assert(s1_step(20) > NT && s1_step(24) > NT + 1 && s1_pos(156) > s1_pos(50) && s1_step(50) > SND && SND % 4 == 2 && NT % 4 == 2 && SND > NT && s1_step(30) < 5 && s1_step(30) > 2, "");
  sfor<4>([&](auto kk) LAMBDA { issue(ic<15 + kk.v>()); });
  vm_wait<s1_cnt(0, 100, 105)>(); bar(); STAMP(1); FST(1, 0);
  rdx(0); rdk(0); rdv(0); rdx(1); rdk(1); rdv(1);
  SB();
  float s2 = 0.f;
  sfor<32>([&](auto nn) LAMBDA {
    constexpr int n = nn, j = n >> 2, s = n & 3;
    if (s == 2 && j < 7) {
      constexpr int e = n == NT ? 13 : n == SND ? 40 : j == 1 ? 4 : 0;
      constexpr int cnt = s1_cnt(n, 110 + 10 * j, 115 + 10 * j, e);
      static_assert(n != SND || (cnt <= s1_cnt(n, 20, 24) && s1_step(24) < n && s1_step(40) < n), "");
      static_assert(n != NT || (cnt <= s1_cnt(n, 10, 13) && (s1_pos(13) < S1P || s1_step(13) < n)), "");
      FST(1, 2 * j + 1); vm_wait<cnt>();
      if (j == 6) lds_barrier_n<5>(); else bar();
      FST(1, 2 * j + 2);
    }
    constexpr bool cs = s == 0 || j == 7;
    acck = MFMA(ka[n % 3], xf[n], acck); SB();
    if (n == 25) { const f32x2 v = {csk[0], csv[0]}; *(f32x2*)(stg + 3072 + lane * 8) = v; SB(); }
    issue(ic<S1P + 3 * n>());
    if (n + 2 < 32) rdx(n + 2);
    if (n == 27) {
#pragma unroll
      for (int t = 0; t < 3; t++) cso[t] = *(const f32x2*)(cx.B + 32768 + (w ^ (t + 1)) * 4096 + 3072 + lane * 8);
    }
    if (n == 29) { cso[0] += cso[1] + cso[2]; asm volatile("" : "+v"(cso[0])); }
    if (n == 2) { tk0 = *(const unsigned*)(cx.B + 5 * 4096 + w * 1024 + lane * 4); tk1 = *(const unsigned*)(cx.B + 6 * 4096 + w * 1024 + lane * 8); }
    if (n == SND) tk0 = *(const unsigned*)(cx.B + 5 * 4096 + w * 1024 + 256 + lane * 4);
    if (n == 7) tk1 = *(const unsigned*)(cx.B + 5 * 4096 + 768);
    if (n == 9) cx.N = RFL(tk1);
    SB();
    if (cs) { csk = MFMA16(sel, ka[n % 3], csk); SB(); }
    if (n + 2 < 32) rdk(n + 2);
    SB();
    accv = MFMA(va[n % 3], xf[n], accv); SB();
    issue(ic<S1P + 3 * n + 1>());
    if (n == NT) {
      const char* st = stg + r_tr;
      rf[0] = tr_read2(st, st + r_c); rf[1] = tr_read2(st + r_a, st + r_a + r_c);
      rf[2] = tr_read2(st + r_b, st + r_b + r_c); rf[3] = tr_read2(stg + r_t3, stg + r_t3 + r_c3);
    }
    SB();
    if (cs) { csv = MFMA16(sel, va[n % 3], csv); SB(); }
    if (n + 2 < 32) rdv(n + 2);
    SB();
    if (n == 0) { q0 = sq4(xf[0], 0, q0); q1 = sq4(xf[0], 1, q1); s1v = MFMA16(sel, xf[0], s1v); SB(); }
    if (n < 31) { q0 = sq4(xf[n + 1], 0, q0); q1 = sq4(xf[n + 1], 1, q1); SB(); }
    if (n == NT + 1) {
      const unsigned o2 = hk ? 0u : 0x3f803f80u;
      const u32x4 sl = {o2, o2, o2, o2};
      const f32x4 z4 = {};
      cj[0] = MFMA16(sel, rf[0], z4); cj[1] = MFMA16(sel, rf[1], z4); cj[2] = MFMA16(sel, rf[2], z4); cj[3] = MFMA16(sl, rf[3], z4);
    }
    if (n == 10) {
      const char* ap = cx.B + 7 * 4096 + w * 1024 + hk * 512 + q4 * 32 + (2 * ch + (p4 >> 1)) * 8;
      aphi[0] = tr_readc<128>(ap); aphi[1] = tr_readc<128>(ap + 256);
    }
    if (n == 24) {
      const bool on = ((lane >> 2) & 1) == hk;
#pragma unroll
      for (int t = 0; t < 4; t++) { aphi[0][t] = on ? aphi[0][t] : 0u; aphi[1][t] = on ? aphi[1][t] : 0u; }
      asm volatile("" : "+v"(aphi[0]), "+v"(aphi[1]));
    }
    if (n == 31) {
      q0 += q1;
      const float u0 = lsel(0xCCCCCCCCCCCCCCCCull, q0[2], q0[0]), u1 = lsel(0xCCCCCCCCCCCCCCCCull, q0[3], q0[1]);
      s2 = xhalf_sum(lsel(0xAAAAAAAAAAAAAAAAull, u1, u0));
    }
    SB();
    if (n < 31) { s1v = MFMA16(sel, xf[n + 1], s1v); SB(); }
    issue(ic<S1P + 3 * n + 2>());
    if (n < 31) asm volatile("" : "+a"(acck), "+a"(accv), "+a"(csk), "+a"(csv), "+a"(s1v), "+a"(q0), "+a"(q1));
    SB();
  });
  STAMP(2); FST(1, 15);
  const float mean = s1v[0] * (1.f / 512.f);
  xf[32] = aug_tok(mean, 0.f, hk);
  csw[1] = cs_pack(csk[0] + cso[0][0]);
  acck = MFMA(aug_w(csw[1], 0u), xf[32], acck);
  csw[2] = cs_pack(csv[0] + cso[0][1]);
  accv = MFMA(aug_w(csw[2], 0u), xf[32], accv);
  const float rstd = __builtin_amdgcn_rsqf(fmaxf(s2 * (1.f / 512.f) - mean * mean, 0.f) + 1e-5f);
  const float sc = rstd * LOG2E;
  f32x16 p = {};
  { u32x4 b0, b1; pack16(acck, b0, b1); p = MFMA(aphi[0], b0, p); p = MFMA(aphi[1], b1, p); }
  SB();
  const int js = ((g >> 1) - rot) & 7;
  u32x4 wf0, wf1;
  { const char* wsrc = wk + (w & 1) * 1024 + js * 8192 + (g & 1) * 4096; wf0 = tr_readc<256>(wsrc); wf1 = tr_readc<256>(wsrc + 512); }
  char* tab = cx.B + 32768 + 2048;
  lds_barrier();
  STAMP(3);
  const int kvb = KVA + c * KV_C;
  char* dA = cx.A + w * 16384;
  const int sA = WT_1 + c * W1_C + w * 16384;
  {
    Softmax sm; u32x4 phk[4];
    sfor<29>([&](auto ii) LAMBDA {
      constexpr int i = ii;
      sm.step(i, p, sc, phk);
      if (i == 2) kv_write_v(cx, accv, rstd);
      if (i == MBI) gldsc4(mvo, cx.ws, lstg + 1024);
      if (TOUCH && i == MBI + 2) {
        const int t = lane >> 3;
        const u32x4 v = {cx.N, cx.N, cx.N, cx.N};
        const int o = (t < 5 ? G_FO + t * FHL + cx.fo : t == 5 ? G_FX1 + cx.fxo : t == 6 ? G_FX2 + cx.fxo : kvb + KV_L + KV_F + (w * 16 + g) * FS - cx.gb) + (lane & 7) * 16 + cx.gb;
        stc(rs, o, 0, v);
        stc(rs, t < 6 ? o : OOB, G_L, v);
      }
      SB();
    });
    if (w == 0) {
      vm_wait<(TOUCH ? 2 : 0)>();
      u32x2 m = *(const u32x2*)(stg + 1024 + lane * 8);
      if (UNLIKELY(BALLOT((m[0] != N || m[1] != sent) && lane < 48) != 0)) {
        while (true) {
          const bool valid = m[0] == N;
          if (BALLOT(valid && m[1] != sent && lane < 48) != 0) {
            const u32x2 r = {m[1], N};
            stc2(rs, valid && m[1] != sent ? mdst : OOB, 0, r);
            sent = valid ? m[1] : sent;
          }
          if (BALLOT(!valid && lane < 48) == 0) break;
          if (cx.budget == 0) break;
          cx.budget--;
          asm volatile("" ::: "memory");
          m = ldc2(rs, lane * 8, MBX + (g * 16 + c) * 512);
        }
      }
      *(unsigned*)(tab + lane * 4) = lane < 48 ? m[1] : lane == 48 ? (unsigned)cx.budget : 0u;
    }
    STAMP(4);
    kv_tail<(TOUCH ? 2 : 0)>(cx, phk, kvb);
  }
  STAMP(5);
  {
    const int e = lane >> 1;
    cx.budget = RFL(*(const int*)(tab + 192));
    cx.tk_kv = *(const unsigned*)(tab + ((lane >> 4) == 2 ? 16 * cx.th + lane - 16 : 63) * 4);
    cx.tk_oh = *(const unsigned*)(tab + (lane < 8 ? 4 * w + e : 63) * 4);
    cx.tk_x = *(const unsigned*)(tab + (lane < 16 ? 8 * (e >> 2) + 4 * (w >> 1) + (e & 3) : 63) * 4);
  }
  {
    f32x16 accq = {}; f32x4 csq = {};
    u32x4 qa[3];
    int wqs[4];
#pragma unroll
    for (int s = 0; s < 4; s++) {
      const int t = s ^ w;
      wqs[s] = hk * 1024 + q4 * 64 + cperm + (t >> 1) * 2048 + (t & 1) * 512;
      asm volatile("" : "+v"(wqs[s]));
    }
    auto rdq = [&](int n) LAMBDA { qa[n % 3] = tr_readc<256>(cx.B + wqs[n & 3] + (n >> 2) * 4096); };
    rdq(0); rdq(1); rdq(2);
    const int wd = WT_QKV + c * WQ_C;
    SB();
    sfor<32>([&](auto nn) LAMBDA {
      constexpr int n = nn;
      accq = MFMA(qa[n % 3], xf[n], accq); SB();
      if (n == 0 && w == 2) { const char* wsrc = wq + js * 4096 + (g & 1) * 2048; wf0 = tr_readc<256>(wsrc); wf1 = tr_readc<256>(wsrc + 512); }
      if (n == 3) {
        const int so = w < 2 ? wd + g * 4096 + w * 1024 : w == 2 ? wd + 65536 + g * 2048 : OOB;
        stc(rs, cx.vo, so, wf0); stc(rs, cx.vo, so + (w < 2 ? 2048 : 1024), wf1);
      }
      if (n < 16) dma<(n & 15)>(rs, dA, cx.vo, sA);
      else wof[n - 16] = ldck(rs, cx.vo, WT_O + (cx.cb * 4 + w) * 16384, n - 16);
      if (n == KQP0) kin.pz = kv_poll(cx, kvb);
      SB();
      csq = MFMA16(sel, qa[n % 3], csq); SB();
      if (n + 3 < 32) rdq(n + 3);
      asm volatile("" : "+a"(accq), "+a"(csq));
      SB();
    });
    csw[0] = cs_pack(csq[0]);
    accq = MFMA(aug_w(csw[0], 0u), xf[32], accq);
    u32x4 q0, q1; pack16(accq, q0, q1);
    f32x16 pq = {}; pq = MFMA(aphi[0], q0, pq); pq = MFMA(aphi[1], q1, pq);
    Softmax sq;
    const int col = 64 * cx.cb + 32 * (w & 1) + 16 * hk;
    const u16* xr = x + (size_t)(128 * g + 64 * cx.th + 32 * (w >> 1) + tok) * 512 + col;
    const int pi = 16 * ((tok >> 2) & 1) + (tok & 3) + 4 * (tok >> 3), ph = 8 * ((tok >> 2) & 1) + (tok & 3) + 4 * ((tok >> 3) & 1);
    const u16* bp = b1 + 112 * c;
    SB();
    sfor<29>([&](auto ii) LAMBDA {
      constexpr int i = ii;
      sq.step(i, pq, sc, phq);
      if (i + 32 == KQP0) kin.pz = kv_poll(cx, kvb);
      if (i == KQW0) { SB(); kv_wait(cx, kin.pz, kvb + KV_F + w * 16 * FS); STAMP(6); kv_load(cx, kin, kvb); }
      if (i == 17) mi.xa = *(const u32x4*)xr;
      if (i == 18) mi.xb = *(const u32x4*)(xr + 8);
      if (i >= CSL && i < CSL + 4) mi.cs[(i - CSL) & 3] = ldck(rs, cx.vo, CS1 + c * 16384 + w * 4096, (i - CSL) & 3);
      if (i == CSR) {
        f32x4 v = (__builtin_bit_cast(f32x4, mi.cs[0]) + __builtin_bit_cast(f32x4, mi.cs[1])) + (__builtin_bit_cast(f32x4, mi.cs[2]) + __builtin_bit_cast(f32x4, mi.cs[3]));
        *(f32x4*)(cx.C + w * INBOX + C_PARK + cx.vo) = v;
      }
      if (i >= 23 && i < 27) mi.b1v[(i - 23) & 3] = bp[i == 26 ? 96 + ph : 32 * (i - 23) + pi];
      if (i == 27) mi.ba = *(const u32x4*)(b2 + col);
      if (i == 28) mi.bb = *(const u32x4*)(b2 + col + 8);
      SB();
    });
  }
}

DEVI void red_send(Cx& cx, const f32x16& a, int j) {
  char* dst = cx.C + (cx.w ^ j) * INBOX + (j - 1) * 4096 + cx.vo;
#pragma unroll
  for (int m = 0; m < 4; m++) { f32x4 v = {a[4 * m], a[4 * m + 1], a[4 * m + 2], a[4 * m + 3]}; *(f32x4*)(dst + m * 1024) = v; }
}
DEVI void red_send1(Cx& cx, const f32x16& a, int j, int m) {
  const f32x4 v = {a[4 * m], a[4 * m + 1], a[4 * m + 2], a[4 * m + 3]};
  *(f32x4*)(cx.C + (cx.w ^ j) * INBOX + (j - 1) * 4096 + cx.vo + m * 1024) = v;
}
struct Red {
  typedef const __attribute__((address_space(3))) unsigned* lp;
  f32x4 r1[4], r2[4], r3[4]; unsigned chk; lp cp;
  DEVI void rd32(Cx& cx, int m) {
    const char* p = cx.C + cx.w * INBOX + m * 1024 + cx.vo;
    r2[m] = *(const f32x4*)(p + 4096); r3[m] = *(const f32x4*)(p + 8192);
  }
  DEVI void put(Cx& cx, unsigned tot) {
    *(unsigned*)(cx.C + cx.w * INBOX + 4096 + cx.lane * 4) = tot + (FORCE_BAD ? cx.w & 1 : 0);
    cp = (lp)(cx.C + (cx.lane & 3) * INBOX + 4096 + 252);
    asm volatile("" : "+v"(cp));
  }
  DEVI void rd1(Cx& cx) {
    chk = *cp;
    SB();
#pragma unroll
    for (int m = 0; m < 4; m++) r1[m] = *(const f32x4*)(cx.C + cx.w * INBOX + m * 1024 + cx.vo);
  }
  DEVI void add32(int m) {
#pragma unroll
    for (int e = 0; e < 4; e++) asm volatile("v_add_f32 %0, %0, %1" : "+v"(r2[m][e]) : "v"(r3[m][e]));
  }
  DEVI bool fin(const f32x16& own, float (&xr)[16]) {
#pragma unroll
    for (int m = 0; m < 4; m++) { const f32x4 o = {own[4 * m], own[4 * m + 1], own[4 * m + 2], own[4 * m + 3]}; r2[m] += o; }
    asm volatile("" : "+v"(r2[0]), "+v"(r2[1]), "+v"(r2[2]), "+v"(r2[3]));
#pragma unroll
    for (int r = 0; r < 16; r++) xr[r] = r2[r >> 2][r & 3] + r1[r >> 2][r & 3];
    return BALLOT(chk != 0) != 0;
  }
};
DEVI void red_recv(Cx& cx, const f32x16& own, float (&xr)[16]) {
  f32x4 v[12];
#pragma unroll
  for (int k = 0; k < 12; k++) v[k] = *(const f32x4*)(cx.C + cx.w * INBOX + ((k % 3) * 4 + k / 3) * 1024 + cx.vo);
#pragma unroll
  for (int m = 0; m < 4; m++)
#pragma unroll
    for (int e = 0; e < 4; e++) xr[4 * m + e] = (own[4 * m + e] + v[3 * m][e]) + (v[3 * m + 1][e] + v[3 * m + 2][e]);
}
DEVI void unpack16(const u32x4& a, const u32x4& b, float (&x)[16]) {
#pragma unroll
  for (int t = 0; t < 4; t++) { x[2 * t] = bflo(a[t]); x[2 * t + 1] = bfhi(a[t]); x[8 + 2 * t] = bflo(b[t]); x[9 + 2 * t] = bfhi(b[t]); }
}
#define EFX 3
#define EFO 0
DEVI void publish_x(Cx& cx, float (&xr)[16], int xb, int sb, int fb) {
  u32x4 f0, f1; pack16(xr, f0, f1);
  stc(cx.rs, cx.vo, xb + cx.xo, f0);
  stc(cx.rs, cx.vo + 1024, xb + cx.xo, f1);
  if (EFX == 0) put_flag(cx, fb + cx.fxo, 7);
  SB();
  f32x2 s1 = {xr[0], xr[1]}, s2 = s1 * s1;
#pragma unroll
  for (int r = 2; r < 16; r += 2) { const f32x2 v = {xr[r], xr[r + 1]}; s1 += v; s2 = __builtin_elementwise_fma(v, v, s2); }
  auto t = __builtin_amdgcn_permlane32_swap(__float_as_uint(s1[0] + s1[1]), __float_as_uint(s2[0] + s2[1]), false, false);
  const unsigned st = __float_as_uint(__uint_as_float(t[0]) + __uint_as_float(t[1]));
  stc1(cx.rs, (cx.lane & 15) * 16 + (cx.lane >> 4 & 1) * 8 + cx.hk * 4, sb + cx.so, st);
  if (EFX == 3) put_flag(cx, fb + cx.fxo, 7);
  SB();
  put_final(cx, fb + CKX + cx.fxo, sum4(sum4(st, f0), f1));
  unpack16(f0, f1, xr);
}
DEVI void park(Cx& cx, const float (&xr)[16]) {
  char* p = cx.C + cx.w * INBOX + C_PARK + cx.vo;
#pragma unroll
  for (int m = 0; m < 4; m++) { f32x4 v = {xr[4 * m], xr[4 * m + 1], xr[4 * m + 2], xr[4 * m + 3]}; *(f32x4*)(p + m * 1024) = v; }
}
DEVI f32x16 unpark(Cx& cx) {
  const char* p = cx.C + cx.w * INBOX + C_PARK + cx.vo;
  f32x16 r;
#pragma unroll
  for (int m = 0; m < 4; m++) { f32x4 v = *(const f32x4*)(p + m * 1024); r[4 * m] = v[0]; r[4 * m + 1] = v[1]; r[4 * m + 2] = v[2]; r[4 * m + 3] = v[3]; }
  return r;
}
DEVI f32x16 tovec(const float (&x)[16]) {
  f32x16 r;
#pragma unroll
  for (int e = 0; e < 16; e++) r[e] = x[e];
  return r;
}

template <class AUG, class WIN>
DEVI void xpass(Cx& cx, int xb, int sb, int fb, u32x4 (&xf)[33], f32x16& a0, f32x16& a1, int sti, AUG aug, WIN win) {
  const rsrc_t rs = cx.rs;
  xb += cx.w * 32768; sb += cx.w * 4096; fb += cx.w * 16 * FS;
  if (sti == 15) FST(7, 0);
  if (sti == 25) FST(8, 0);
  if (sti == 15) nap<D_X2>(); else nap<D_X1>();
  poll_wait<16>(cx, ldc1(rs, cx.fv16, fb), fb, cx.tk_x, 7);
  STAMP(sti); if (sti != 15) FST(sti == 11 ? 4 : 5, 0); else FST(7, 1);
  if (sti == 25) FST(8, 1);
  u32x4 st[4], ra[3], rb[3], w0, w1; unsigned ck;
  auto ld = [&](int q) LAMBDA {
    if (q < 4) st[q] = ldck(rs, cx.vo, sb, q);
    else if (q < 36) xf[q - 4] = ldck(rs, cx.vo, xb, q - 4);
    if (q == 30) ck = ldc1(rs, cx.fv16, fb + CKX + 4);
  };
  auto stats = [&]() LAMBDA {
    float v[4];
#pragma unroll
    for (int e = 0; e < 4; e++) v[e] = (__uint_as_float(st[0][e]) + __uint_as_float(st[1][e])) + (__uint_as_float(st[2][e]) + __uint_as_float(st[3][e]));
    auto p = __builtin_amdgcn_permlane32_swap(__float_as_uint(v[0]), __float_as_uint(v[1]), false, false);
    auto q = __builtin_amdgcn_permlane32_swap(__float_as_uint(v[2]), __float_as_uint(v[3]), false, false);
    auto r = __builtin_amdgcn_permlane16_swap(__float_as_uint(__uint_as_float(p[0]) + __uint_as_float(p[1])), __float_as_uint(__uint_as_float(q[0]) + __uint_as_float(q[1])), false, false);
    const unsigned t = __float_as_uint((__uint_as_float(r[0]) + __uint_as_float(r[1])) * (1.f / 512.f));
    auto u = __builtin_amdgcn_permlane32_swap(t, t, false, false);
    const float mean = __uint_as_float(u[0]);
    aug(mean, fmaxf(__uint_as_float(u[1]) - mean * mean, 0.f), w0, w1);
  };
  const char* pa = cx.A + cx.vo;
#pragma unroll
  for (int q = 0; q < 10; q++) { ld(q); SB(); }
  if (sti == 15) FST(7, 2);
  if (sti == 25) FST(8, 2);
  bar();
  if (sti == 15) FST(7, 3);
  if (sti == 25) FST(8, 3);
#pragma unroll
  for (int n = 0; n < 3; n++) { ra[n] = *(const u32x4*)(pa + n * 2048); rb[n] = *(const u32x4*)(pa + n * 2048 + 1024); }
  SB();
  unsigned sum = -cx.tk_x;
  const f32x16 zero = {};
#pragma unroll
  for (int q = 0; q < 4; q++) { sum = sum4(sum, st[q]); SB(); ld(10 + q); SB(); }
  stats();
  SB();
  a0 = MFMA(w0, xf[32], zero); a1 = MFMA(w1, xf[32], zero);
  SB();
  sfor<32>([&](auto nn) LAMBDA {
    constexpr int n = nn;
    if (n % 8 == 0 && sti != 15) FST(sti == 11 ? 4 : 5, 1 + n / 8);
    a0 = MFMA(ra[n % 3], xf[n], a0); SB();
    if (n % 4 == 0 && sti == 15) FST(7, 4 + n / 4);
    if (n % 4 == 0 && sti == 25) FST(8, 4 + n / 4);
    if (n + 3 < 32) ra[n % 3] = *(const u32x4*)(pa + (n + 3) * 2048);
    if (n + 14 < 36) ld(n + 14); else win(ic<(n < 22 ? 0 : n - 22)>());
    SB();
    a1 = MFMA(rb[n % 3], xf[n], a1); SB();
    if (n + 3 < 32) rb[n % 3] = *(const u32x4*)(pa + (n + 3) * 2048 + 1024);
    sum = sum4(sum, xf[n]);
    if (n == 29) sum += ck;
    SB();
  });
  if (sti != 15) FST(sti == 11 ? 4 : 5, 5); else FST(7, 12);
  if (sti == 25) FST(8, 12);
  asm volatile("" : "+a"(a0), "+a"(a1));
  if (UNLIKELY(bad(cx, sum))) {
    do {
      spend(cx, sti == 11 ? 2 : sti == 15 ? 4 : 7);
#pragma unroll
      for (int q = 0; q < 36; q++) ld(q);
      sum = ck - cx.tk_x;
#pragma unroll
      for (int q = 0; q < 32; q++) sum = sum4(sum, xf[q]);
#pragma unroll
      for (int q = 0; q < 4; q++) sum = sum4(sum, st[q]);
    } while (again(cx, sum));
    stats();
    a0 = MFMA(w0, xf[32], zero); a1 = MFMA(w1, xf[32], zero);
#pragma unroll
    for (int n = 0; n < 32; n++) {
      a0 = MFMA(*(const u32x4*)(pa + n * 2048), xf[n], a0); a1 = MFMA(*(const u32x4*)(pa + n * 2048 + 1024), xf[n], a1);
      SB();
    }
  }
}

template <bool P> DEVI f32x2 mul2(f32x2 a, f32x2 b) {
  f32x2 r;
  if (P) asm volatile("v_pk_mul_f32 %0, %1, %2" : "=v"(r) : "v"(a), "v"(b));
  else {
    asm volatile("v_mul_f32 %0, %1, %2" : "=v"(r[0]) : "v"(a[0]), "v"(b[0]));
    asm volatile("v_mul_f32 %0, %1, %2" : "=v"(r[1]) : "v"(a[1]), "v"(b[1]));
  }
  return r;
}
template <bool P> DEVI f32x2 fma2(f32x2 a, f32x2 c) {
  f32x2 r;
  if (P) asm volatile("v_pk_fma_f32 %0, %1, %2, %2 op_sel:[0,1,0] op_sel_hi:[1,1,0]" : "=v"(r) : "v"(a), "v"(c));
  else {
    asm volatile("v_fma_f32 %0, %1, %2, %3" : "=v"(r[0]) : "v"(a[0]), "v"(c[1]), "v"(c[0]));
    asm volatile("v_fma_f32 %0, %1, %2, %3" : "=v"(r[1]) : "v"(a[1]), "v"(c[1]), "v"(c[0]));
  }
  return r;
}
struct Gelu {
  f32x2 cc, ia;
  f32x2 a[4], t, d, e, r, u;
  u32x4 f; unsigned sum;
  template <int K0, int K1, int K, int H, class MID>
  DEVI void step(Cx& cx, const f32x16& lo, const f32x16& hi, int hb, MID mid) {
    constexpr bool P = H == 2, p0 = K >= K0 && K < K1, p1 = K - 1 >= K0 && K - 1 < K1, p2 = K - 2 >= K0 && K - 2 < K1;
    constexpr int v = K - 2;
    f32x2 s, y;
    auto rc = [&]() LAMBDA { if (p2) { r[0] = __builtin_amdgcn_rcpf(d[0]); r[1] = __builtin_amdgcn_rcpf(d[1]); SB(); } };
    auto ex = [&]() LAMBDA { if (p1) { e[0] = __builtin_amdgcn_exp2f(t[0]); e[1] = __builtin_amdgcn_exp2f(t[1]); SB(); } };
    auto rd = [&]() LAMBDA {
      if (p0) {
        const f32x16& h = (K & 8) ? hi : lo;
        float x0, x1;
        asm volatile("v_accvgpr_read_b32 %0, %2\n\tv_accvgpr_read_b32 %1, %3" : "=v"(x0), "=v"(x1) : "a"(h[(2 * K) & 15]), "a"(h[(2 * K + 1) & 15]));
        a[K & 3][0] = x0; a[K & 3][1] = x1;
        SB();
      }
    };
    auto fin = [&]() LAMBDA { if (p2 && (v & 3) == 3) { stc(cx.rs, cx.vo, hb + (v >> 2) * 8192, f); sum = sum4(sum, f); } };
    if (H != 1) rd();
    if (H != 0) rc();
    if (H != 1) ex();
    mid(); SB();
    if (H != 0 && p0 && p2) {
      float y0, y1;
      asm volatile("v_mul_f32 %2, %6, %8\n\t"
                   "v_mul_f32 %3, %7, %9\n\t"
                   "v_pk_mul_f32 %0, %5, %5\n\t"
                   "v_cvt_pk_bf16_f32 %4, %2, %3\n\t"
                   "v_pk_fma_f32 %0, %0, %11, %11 op_sel:[0,1,0] op_sel_hi:[1,1,0]\n\t"
                   "v_pk_fma_f32 %1, %10, %12, %12\n\t"
                   "v_pk_mul_f32 %0, %5, %0"
                   : "=&v"(t), "=&v"(d), "=&v"(y0), "=&v"(y1), "=&v"(f[v & 3])
                   : "v"(a[K & 3]), "v"(a[v & 3][0]), "v"(a[v & 3][1]), "v"(r[0]), "v"(r[1]), "v"(e), "v"(cc), "v"(ia));
    } else if (H != 0) {
      if (p0) s = mul2<0>(a[K & 3], a[K & 3]);
      if (p2) y = mul2<0>(a[v & 3], r);
      if (p0) u = fma2<0>(s, cc);
      if (p1) d = fma2<0>(e, ia);
      if (p0) t = mul2<0>(a[K & 3], u);
      if (p2) f[v & 3] = pk2(y[0], y[1]);
    }
    if (H != 0) fin();
  }
};

#define GB2 34
#define GP1 26
#define GW1 40
#define GP3 9
#define GP4 37
#define GP2 42
#define GW3 26
#define H3F 1
#define GBR0 78
#define GBR1 90
#define W2KEEP 45
#define NW2_C 12
#define NW2_H 18
#define NB1H 6
#define NQK 8
#define KQP1 31
#define KQW1 12

constexpr int h_iss(int i) { return i < 32 ? i : i < 40 ? 33 + 2 * (i - 32) : i < 48 ? 32 + 2 * (i - 40) : i < 52 ? 49 + 2 * (i - 48) : 48 + 2 * (i - 52); }
constexpr int s4_f(int G) {
  return G < 64 ? G >> 1 : G < 80 ? 33 + 2 * ((G - 64) >> 1) : G < 84 ? 32 + 2 * (G - 80) : G < 92 ? 49 + 2 * ((G - 84) >> 1) : G < 100 ? 40 + 2 * (G - 92) : 32 + 2 * (G - 100);
}
constexpr int s4_j(int G) { return G < 64 ? (G & 2) + 1 - (G & 1) : G < 80 || (G >= 84 && G < 92) ? 3 - (G & 1) : G < 100 ? 1 : 0; }

#define BL 1
#define BUDL(i) do { if (L == BL) BUD(53 + (i)); } while (0)
template <int L>
DEVI void layer(Cx& cx, const u16* __restrict__ x, const u16* __restrict__ b1, const u16* __restrict__ b2, u16* __restrict__ out,
                u32x4 (&phq)[4], const unsigned (&csw)[3], const u32x4 (&aphi)[2], KvIn& kin, u32x4 (&w2f)[56], u32x4 (&wof)[16],
                Misc& mi, unsigned (&csp)[4], unsigned (&b1p)[2]) {
  const int lane = cx.lane, w = cx.w, tok = cx.tok, hk = cx.hk, g = cx.g, c = cx.c;
  const rsrc_t rs = cx.rs;
  const int gl = cx.gb + L * G_L;
  const int kvb = KVA + L * KV_L + c * KV_C;
  const int w2b = WT_2 + (cx.cb * 4 + w) * 57344;
  const f32x16 zero = {};
  constexpr int SO = 14 * L;
  u32x4 xa, xb2;
  const int col = 64 * cx.cb + 32 * (w & 1) + 16 * hk;
  const size_t xrow = (size_t)(128 * g + 64 * cx.th + 32 * (w >> 1) + tok) * 512 + col;
  BUDL(-1);
  if (L == 1) {
    u32x4 xf[33]; f32x16 acck, accv; float rstd;
    xpass(cx, cx.gb + G_X2, cx.gb + G_ST2, cx.gb + G_FX2, xf, acck, accv, 15,
          [&](float mean, float var, u32x4& w0, u32x4& w1) LAMBDA {
            rstd = __builtin_amdgcn_rsqf(var + 1e-5f);
            xf[32] = aug_tok(mean, 0.f, hk); w0 = aug_w(csw[1], 0u); w1 = aug_w(csw[2], 0u);
          },
          [&](auto ii) LAMBDA { if (ii.v < 8) dma<(ii.v & 7)>(rs, cx.B + w * 8192, cx.vo, WT_QKV + c * WQ_C + 65536 + w * 8192); });
    STAMP(16); FST(6, 0); FST(7, 13); BUDL(0);
    const float sc = rstd * LOG2E;
    f32x16 p;
    { u32x4 b0, b1; pack16(acck, b0, b1); p = MFMA(aphi[0], b0, zero); p = MFMA(aphi[1], b1, p); }
    FST(6, 1);
    kv_write_v(cx, accv, rstd);
    SB();
    {
      Softmax sm; u32x4 phk[4];
      sfor<29>([&](auto ii) LAMBDA {
        constexpr int i = ii;
        if (i % 4 == 0) FST(6, 2 + i / 4);
        sm.step(i, p, sc, phk);
        SB();
      });
      STAMP(17); FST(6, 10);
      kv_tail<0>(cx, phk, kvb);
    }
    STAMP(18); FST(6, 11);
    f32x16 accq = MFMA(aug_w(csw[0], 0u), xf[32], zero);
    u32x4 qa[3];
    const char* pq = cx.B + cx.vo;
#pragma unroll
    for (int n = 0; n < 3; n++) qa[n] = *(const u32x4*)(pq + n * 1024);
    char* dA = cx.A + w * 16384;
    const int sA = WT_1 + c * W1_C + w * 16384;
    SB();
    sfor<32>([&](auto nn) LAMBDA {
      constexpr int n = nn;
      accq = MFMA(qa[n % 3], xf[n], accq); SB();
      if (n + 3 < 32) qa[n % 3] = *(const u32x4*)(pq + (n + 3) * 1024);
      if (n >= 1 && n < 17) wof[n - 1] = ldck(rs, cx.vo, WT_O + (cx.cb * 4 + w) * 16384, n - 1);
      if (n >= 17 && (n & 1)) dma<(n >= 17 ? (n - 17) / 2 : 0)>(rs, dA, cx.vo, sA);
      if (n == KQP1) kin.pz = kv_poll(cx, kvb);
      if (n % 8 == 7) FST(6, 12 + n / 8);
      SB();
    });
    u32x4 b0, b1; pack16(accq, b0, b1);
    f32x16 pq2 = MFMA(aphi[0], b0, zero); pq2 = MFMA(aphi[1], b1, pq2);
    Softmax sq;
    sfor<29>([&](auto ii) LAMBDA {
      constexpr int i = ii;
      sq.step(i, pq2, sc, phq);
      if (i + 32 == KQP1) kin.pz = kv_poll(cx, kvb);
      if (i == KQW1) { SB(); kv_wait(cx, kin.pz, kvb + KV_F + w * 16 * FS); STAMP(19); kv_load(cx, kin, kvb); }
      if (i >= 19 && i < 27) dma<(i >= 19 && i < 27 ? i - 11 : 0)>(rs, dA, cx.vo, sA);
      if (i == 27) mi.ba = *(const u32x4*)(b2 + col);
      if (i == 28) mi.bb = *(const u32x4*)(b2 + col + 8);
      SB();
    });
  }
  asm volatile("" : "+v"(phq[0]), "+v"(phq[1]), "+v"(phq[2]), "+v"(phq[3]));
  SB();
  STAMP(7 + SO); BUDL(1);
  {
    const int G = lane >> 4, d = lane & 15;
    u32x4 idn;
#pragma unroll
    for (int t = 0; t < 4; t++) { const int k = 8 * (G & 1) + 2 * t; idn[t] = k == d ? 0x3f80u : k + 1 == d ? 0x3f800000u : 0u; }
    f32x4 kv; unsigned sum;
    auto red = [&]() LAMBDA {
      f32x4 k0 = {}, k1 = {}; sum = -cx.tk_kv;
#pragma unroll
      for (int m = 0; m < 8; m += 2) {
        k0 = MFMA16(idn, kin.pa[m], k0); k1 = MFMA16(idn, kin.pa[m + 1], k1);
        sum = sum4(sum4(sum, kin.pa[m]), kin.pa[m + 1]);
      }
      sum = sum4(sum, kin.pz); kv = k0 + k1;
    };
    red();
    asm volatile("" : "+v"(kv));
    if (UNLIKELY(bad(cx, sum))) {
      do { spend(cx, 5 * L); kin.pz = kv_poll(cx, kvb); kv_load(cx, kin, kvb); red(); } while (again(cx, sum));
    }
    char* img = cx.C + w * INBOX + C_IMG;
    { const u32x2 v = {pk2(kv[0], kv[1]), pk2(kv[2], kv[3])}; *(u32x2*)(img + d * 32 + G * 8) = v; }
    { const u32x4 z4 = {}; *(u32x4*)(img + 512 + cx.vo) = hk ? z4 : kin.pz; }
    lds_barrier();
    const int hp = (tok >> 2) & 1, dd = (tok & 3) + 4 * (tok >> 3);
    const char* ap = cx.C + (hp == hk ? 2 * hp * INBOX + C_IMG + dd * 32 : C_ZERO);
    const int zo = hp == hk ? 512 : 0;
    u32x4 a[4], az[4];
#pragma unroll
    for (int s4 = 0; s4 < 4; s4++) az[s4] = *(const u32x4*)(ap + zo + (s4 >> 1) * INBOX + (s4 & 1) * 16);
#pragma unroll
    for (int s4 = 0; s4 < 4; s4++) a[s4] = *(const u32x4*)(ap + (s4 >> 1) * INBOX + (s4 & 1) * 16);
    if (L == 0) stc1(rs, w == 0 && (lane >> 4) == 2 ? (lane - 32) * 8192 : OOB, MBX + c * 512 + (48 + g) * 8, cx.tk_kv + 1);
    if (EFO == 2) put_flag(cx, gl + G_FO + cx.fo, 7);
    SB();
    f32x16 num = {}, den = {};
#pragma unroll
    for (int s4 = 0; s4 < 4; s4++) den = MFMA(az[s4], phq[s4], den);
    if (EFO == 1) { SB(); put_flag(cx, gl + G_FO + cx.fo, 7); SB(); }
#pragma unroll
    for (int s4 = 0; s4 < 4; s4++) num = MFMA(a[s4], phq[s4], num);
    float ds[4];
#pragma unroll
    for (int r = 0; r < 4; r++) ds[r] = (den[r] + den[4 + r]) + (den[8 + r] + den[12 + r]);
    const float inv = __builtin_amdgcn_rcpf(((ds[0] + ds[1]) + (ds[2] + ds[3])) + 1e-6f);
    float ov[16];
#pragma unroll
    for (int r = 0; r < 16; r++) ov[r] = num[r] * inv;
    u32x4 f0, f1; pack16(ov, f0, f1);
    stc(rs, cx.vo, gl + G_O + cx.oo, f0);
    stc(rs, cx.vo + 1024, gl + G_O + cx.oo, f1);
    if (EFO == 0) put_flag(cx, gl + G_FO + cx.fo, 7);
    put_final(cx, gl + G_FO + CKO + cx.fo, sum4(sum4(0u, f0), f1));
    STAMP(8 + SO);
  }
  float xr[16];
  {
    const int ob = gl + G_O + (cx.th * 4 + w) * 16384, fb = gl + G_FO + (cx.th * 4 + w) * 8 * FS;
    f32x16 acc[4];
    if (L == 0) {
      unpack16(mi.xa, mi.xb, xr); acc[0] = tovec(xr);
#pragma unroll
      for (int k = 0; k < 2; k++) {
        const char* p = cx.C + hk * 2 * INBOX + C_PARK + k * 256 + tok * 8;
        const f32x2 v = (*(const f32x2*)p + *(const f32x2*)(p + 512)) + (*(const f32x2*)(p + INBOX) + *(const f32x2*)(p + INBOX + 512));
        csp[2 * k] = cs_pack(xhalf_sum(v[0])); csp[2 * k + 1] = cs_pack(xhalf_sum(v[1]));
      }
      b1p[0] = mi.b1v[0] | ((unsigned)mi.b1v[1] << 16); b1p[1] = mi.b1v[2] | ((unsigned)mi.b1v[3] << 16);
    } else acc[0] = unpark(cx);
    SB();
    char* dB = cx.B + w * 12288;
    const int sB = WT_1 + c * W1_C + 65536 + w * 12288;
    sfor<NB1H>([&](auto ii) LAMBDA { dma<ii.v>(rs, dB, cx.vo, sB); });
    nap<D_O>();
    poll_wait<8>(cx, ldc1(rs, cx.fv8, fb), fb, cx.tk_oh, 7);
    STAMP(9 + SO); if (L == 1) FST(9, 0);
    BUDL(2);
    u32x4 of[16];
#pragma unroll
    for (int k = 0; k < 10; k++) { of[k ^ 8] = ldck(rs, cx.vo, ob, k ^ 8); SB(); }
    lds_barrier();
    unsigned sum = -cx.tk_oh, ck;
    Red rd;
    sfor<32>([&](auto nn) LAMBDA {
      constexpr int n = nn, j = n < 12 ? 3 - (n % 3 == 2) : 3 - n / 8, s = n < 12 ? (n % 3 == 2 ? n / 3 : n - n / 3) : n & 7;
      constexpr int i = n >= 13 && n % 3 != 0 ? n - 13 - (n - 13) / 3 : -1;
      if (L == 1 && n % 4 == 0) FST(9, 1 + n / 4);
      acc[j] = MFMA(wof[2 * s + (j & 1)], of[8 * (j >> 1) + s], s == 0 && j ? zero : acc[j]); SB();
      if (n < 6) of[(n + 10) ^ 8] = ldck(rs, cx.vo, ob, (n + 10) ^ 8);
      if (n == 6) ck = ldc1(rs, cx.fv8, fb + CKO + 4);
      if (n >= 7 && n < 19 - NB1H) dma<(n >= 7 && n < 19 - NB1H ? n - 7 + NB1H : 0)>(rs, dB, cx.vo, sB);
      if (L == 0 && n >= 19 - NB1H && NW2_A + n - (19 - NB1H) < NW2_C) w2f[NW2_A + n - (19 - NB1H)] = ldck(rs, cx.vo, w2b, NW2_A + n - (19 - NB1H));
      if (j & 1) sum = sum4(sum, of[8 * (j >> 1) + s]);
      if (i >= 0 && i < 12) red_send1(cx, acc[3 - (i / 4 & 3)], 3 - (i / 4 & 3), i & 3);
      if (n == 24) sum += ck;
      if (n >= 24 && n < 27) { wsum_step<(n >= 24 && n < 27 ? 2 * (n - 24) : 0)>(sum); SB(); wsum_step<(n >= 24 && n < 27 ? 2 * (n - 24) + 1 : 0)>(sum); }
      if (n == 27) { lds_barrier_n<2>(); rd.rd32(cx, 0); rd.rd32(cx, 1); rd.rd32(cx, 2); rd.rd32(cx, 3); rd.put(cx, sum); }
      if (n >= 30) { rd.add32(2 * (n & 1)); rd.add32(2 * (n & 1) + 1); }
      SB();
    });
    if (L == 1) FST(9, 9);
    lds_barrier();
    if (L == 1) FST(9, 10);
    rd.rd1(cx);
    SB();
    if (UNLIKELY(rd.fin(acc[0], xr))) {
      lds_barrier();
      do {
        spend(cx, 1 + 5 * L);
        sum = ldc1(rs, cx.fv8, fb + CKO + 4) - cx.tk_oh;
#pragma unroll
        for (int k = 0; k < 16; k++) { of[k] = ldck(rs, cx.vo, ob, k); sum = sum4(sum, of[k]); }
      } while (again(cx, sum));
      if (L == 0) { xa = *(const u32x4*)(x + xrow); xb2 = *(const u32x4*)(x + xrow + 8); }
      else { xa = ldc(rs, cx.vo, cx.gb + G_X2 + cx.xo); xb2 = ldc(rs, cx.vo + 1024, cx.gb + G_X2 + cx.xo); }
      unpack16(xa, xb2, xr); acc[0] = tovec(xr); acc[1] = zero; acc[2] = zero; acc[3] = zero;
#pragma unroll
      for (int n = 0; n < 32; n++) acc[n >> 3] = MFMA(wof[2 * (n & 7) + ((n >> 3) & 1)], of[8 * (n >> 4) + (n & 7)], acc[n >> 3]);
      red_send(cx, acc[1], 1); red_send(cx, acc[2], 2); red_send(cx, acc[3], 3);
      lds_barrier();
      red_recv(cx, acc[0], xr);
    }
    if (L == 1) FST(9, 11);
    publish_x(cx, xr, gl + G_X1, gl + G_ST1, gl + G_FX1);
    if (L == 1) FST(9, 12);
    STAMP(10 + SO);
    if (L == 0) {
#pragma unroll
      for (int k = NW2_C; k < NW2_H; k++) w2f[k] = ldck(rs, cx.vo, w2b, k);
    }
    float bv[16]; unpack16(mi.ba, mi.bb, bv);
#pragma unroll
    for (int r = 0; r < 16; r++) xr[r] += bv[r];
    park(cx, xr);
  }
  {
    u32x4 xf[33]; f32x16 h0, h1, h2, h3;
    Gelu ge;
    xpass(cx, gl + G_X1, gl + G_ST1, gl + G_FX1, xf, h0, h1, 11 + SO,
          [&](float mean, float var, u32x4& w0, u32x4& w1) LAMBDA {
            const float r1 = __builtin_amdgcn_rsqf(var + 1e-5f);
            const float A = r1 * __builtin_amdgcn_rsqf(var * r1 * r1 + 1e-5f);
            const float iA = __builtin_amdgcn_rcpf(A);
            ge.ia[0] = iA; ge.ia[1] = iA;
            xf[32] = aug_tok(mean, iA, hk);
            const float k0 = -2.f * LOG2E * 0.7978845608028654f, k1 = k0 * 0.044715f;
            ge.cc[0] = k0 * A; ge.cc[1] = k1 * A * A * A;
            w0 = aug_w(csp[0], b1p[0]); w1 = aug_w(csp[1], b1p[0] >> 16);
          },
          [&](auto ii) LAMBDA {
            constexpr int k = NW2_H + ii.v - 8;
            if (ii.v < 8) dma<(ii.v & 7)>(rs, cx.C + w * INBOX, cx.vo, w2b + 48 * 1024);
            else if (L == 0 && k < W2KEEP) w2f[k] = ldck(rs, cx.vo, w2b, k);
          });
    STAMP(12 + SO); if (L == 1) FST(8, 13);
    BUDL(3);
    const int hs = gl + G_H + cx.ho, hfo = gl + G_FH + cx.fo;
    const int hb = gl + G_H + (cx.th * 4 + w) * 57344, fb = gl + G_FH + (cx.th * 4 + w) * 8 * FS;
    ge.sum = 0;
    asm volatile("" : "+a"(h0), "+a"(h1));
    h2 = MFMA(aug_w(csp[2], b1p[1]), xf[32], zero); h3 = MFMA(aug_w(csp[3], b1p[1] >> 16), xf[32], zero);
    const char* pb = cx.B + cx.vo;
    const char* ph = cx.B + 1024 + (hk * 16 + (lane & 15)) * 16;
    u32x4 ra[4], hf[56];
    auto rdw = [&](int G) LAMBDA { ra[G & 3] = *(const u32x4*)((G < 32 ? pb : ph) + (G & 31) * 1536); };
    auto ldh = [&](int k) LAMBDA { hf[h_iss(k)] = ldck(rs, cx.vo, hb, h_iss(k)); };
#pragma unroll
    for (int n = 0; n < 4; n++) rdw(n);
    SB();
    unsigned pl = 0, sum = -cx.tk_oh, ck;
    f32x16 acc[4];
    sfor<64>([&](auto gg) LAMBDA {
      constexpr int G = gg, n = G & 31;
      if (G % 8 == 0) FST(4 + L, 6 + G / 8);
      if (G < 32) h2 = MFMA(ra[G & 3], xf[n], h2); else h3 = MFMA(ra[G & 3], xf[n], h3);
      SB();
      auto mid = [&]() LAMBDA { if (G + 4 < 64) rdw(G + 4); };
      if (G < 10) ge.step<0, 16, G, 2>(cx, h0, h1, hs, mid);
      else if (G < 26) ge.step<0, 16, 10 + (G - 10) / 2, G & 1>(cx, h0, h1, hs, mid);
      else if (G >= GB2 && G < GB2 + 20) ge.step<16, 24, 16 + (G - GB2) / 2, (G - GB2) & 1>(cx, h2, h2, hs, mid);
      else mid();
      if (L == 0 && NW2_H + 2 + G < W2KEEP) w2f[NW2_H + 2 + G] = ldck(rs, cx.vo, w2b, NW2_H + 2 + G);
      if (G == 9) put_flag(cx, hfo + FHL, 1);
      if (G == 25) put_flag(cx, hfo + 2 * FHL, 3);
      if (G == GB2 + 19) put_flag(cx, hfo + 3 * FHL, 5);
      if (G == GP1) pl = ldc1(rs, cx.fv8, fb + FHL);
      if (G == GW1) { SB(); poll_wait<8>(cx, pl, fb + FHL, cx.tk_oh, 1); STAMP(13 + SO); BUDL(4); }
      if (G >= GW1 && G < GW1 + 16) ldh(G - GW1);
      if (G == GP2) pl = ldc1(rs, cx.fv8, fb + 2 * FHL);
      if (G == 56) { SB(); poll_wait<8>(cx, pl, fb + 2 * FHL, cx.tk_oh, 3); BUDL(5); }
      if (G >= 56 && !(G & 1)) ldh(G / 2 - 12);
      if (G == 60) acc[0] = unpark(cx);
      SB();
    });
    FST(4 + L, 14);
    STAMP(14 + SO);
    unsigned msg = 0;
    char* dA = cx.A + w * 16384;
    const int sA = WT_QKV + c * WQ_C + w * 16384;
    Red rd;
    sfor<112>([&](auto gg) LAMBDA {
      constexpr int G = gg, f = s4_f(G), q = f >> 1, j = s4_j(G);
      constexpr int i = G >= 92 && G % 3 != 1 ? G - 92 - (G - 92) / 3 : -1;
      if (G % 8 == 0) FST(2 + L, G / 8);
      acc[j] = MFMA(w2f[2 * q + (j & 1)], hf[f], q == 0 && j ? zero : acc[j]); SB();
      if (H3F) {
        if (G >= 2 && G < 8) ge.step<24, 28, 22 + G, 2>(cx, h3, h3, hs, []() LAMBDA {});
        if (G == 7) put_flag(cx, hfo, 7);
      } else {
        if (G >= 2 && G < 14) ge.step<24, 28, 24 + (G - 2) / 2, G & 1>(cx, h3, h3, hs, []() LAMBDA {});
        if (G == 13) put_flag(cx, hfo, 7);
      }
      if (G == 15) put_final(cx, hfo + CKH, ge.sum);
      if (G == GW3) { SB(); poll_wait<8>(cx, pl, fb + 3 * FHL, cx.tk_oh, 5); BUDL(6); }
      if (G == GW3 + 32) { SB(); poll_wait<8>(cx, pl, fb, cx.tk_oh, 7); BUDL(7); }
      if (G < 8) ldh(20 + G);
      else if (G < 16) { if (!(G & 1)) ldh(24 + G / 2); }
      else if (G >= GW3 && G < GW3 + 48 && !((G - GW3) & 1)) ldh(32 + (G - GW3) / 2);
      if (G == GP3) pl = ldc1(rs, cx.fv8, fb + 3 * FHL);
      if (G == GP4) pl = ldc1(rs, cx.fv8, fb);
      if (G == GW3 + 45) ck = ldc1(rs, cx.fv8, fb + CKH + 4);
      if (G >= 16 && G < 24) w2f[G + 32] = *(const u32x4*)(cx.C + w * INBOX + (G - 16) * 1024 + cx.vo);
      if (G == (L ? GBR1 : GBR0)) lds_barrier();
      if (!(G & 1) && G >= 16 && (G - 16) / 2 + W2KEEP < 48) w2f[(G - 16) / 2 + W2KEEP] = ldck(rs, cx.vo, w2b, (G - 16) / 2 + W2KEEP);
      if (L == 0) {
        if (G == 51) msg = ldc1(rs, (lane & 15) * 8, MBX + (g * 16 + c) * 512 + 384);
        if (G == 79) { SB(); mail_wait(cx, msg, (lane & 15) * 8, MBX + (g * 16 + c) * 512 + 384, cx.N + 1); }
        if (G >= 80 && !(G & 1) && (G - 80) / 2 < NQK) dma<(G >= 80 ? (G - 80) / 2 : 0)>(rs, dA, cx.vo, sA);
      }
      if (j & 1) sum = sum4(sum, hf[f]);
      if (i >= 0 && i < 12) red_send1(cx, acc[3 - (i / 4 & 3)], 3 - (i / 4 & 3), i & 3);
      if (G == 100) sum += ck;
      if (G >= 100 && G < 106) wsum_step<(G >= 100 && G < 106 ? G - 100 : 0)>(sum);
      if (G == 106) { lds_barrier_n<2>(); rd.rd32(cx, 0); rd.rd32(cx, 1); rd.rd32(cx, 2); rd.rd32(cx, 3); rd.put(cx, sum); }
      if (G >= 109) rd.add32(G & 3);
      if (G == 111) rd.add32(0);
      SB();
    });
    lds_barrier();
    rd.rd1(cx);
    SB();
    FST(2 + L, 14);
    if (UNLIKELY(rd.fin(acc[0], xr))) {
      lds_barrier();
      do {
        spend(cx, 3 + 5 * L);
        xa = ldc(rs, cx.vo, gl + G_X1 + cx.xo); xb2 = ldc(rs, cx.vo + 1024, gl + G_X1 + cx.xo);
        float bv[16]; unpack16(*(const u32x4*)(b2 + col), *(const u32x4*)(b2 + col + 8), bv);
        unpack16(xa, xb2, xr);
#pragma unroll
        for (int r = 0; r < 16; r++) acc[0][r] = xr[r] + bv[r];
        acc[1] = zero; acc[2] = zero; acc[3] = zero; sum = ldc1(rs, cx.fv8, fb + CKH + 4) - cx.tk_oh;
#pragma unroll
        for (int k = 0; k < 56; k++) {
          const u32x4 f = ldck(rs, cx.vo, hb, k);
          sum = sum4(sum, f);
          acc[2 * (k & 1)] = MFMA(w2f[k & ~1], f, acc[2 * (k & 1)]); acc[2 * (k & 1) + 1] = MFMA(w2f[k | 1], f, acc[2 * (k & 1) + 1]);
          if ((k & 7) == 7) SB();
        }
      } while (again(cx, sum));
      red_send(cx, acc[1], 1); red_send(cx, acc[2], 2); red_send(cx, acc[3], 3);
      lds_barrier();
      red_recv(cx, acc[0], xr);
    }
    FST(2 + L, 15);
    if (L == 0) {
      publish_x(cx, xr, cx.gb + G_X2, cx.gb + G_ST2, cx.gb + G_FX2);
      STAMP(20);
      sfor<16 - NQK>([&](auto ii) LAMBDA { dma<NQK + ii.v>(rs, dA, cx.vo, sA); });
      park(cx, xr);
    } else {
      u32x4 f0, f1; pack16(xr, f0, f1);
      *(u32x4*)(out + xrow) = f0; *(u32x4*)(out + xrow + 8) = f1;
      STAMP(29);
    }
  }
}

__global__ __launch_bounds__(256, 1) void fused(const u16* __restrict__ x, const u16* __restrict__ wqkv, const u16* __restrict__ wphi,
                                                const u16* __restrict__ wo, const u16* __restrict__ w1, const u16* __restrict__ b1,
                                                const u16* __restrict__ w2, const u16* __restrict__ b2, char* ws, u16* __restrict__ out) {
  __shared__ __attribute__((aligned(16))) char ldsA[65536];
  __shared__ __attribute__((aligned(16))) char ldsB[49152];
  __shared__ __attribute__((aligned(16))) char ldsC[49152];
  Cx cx;
  STAMP(0); CSTAMP(30);
  {
    unsigned long long pc; unsigned len;
    asm volatile("s_getpc_b64 %0\n.Lcb0:\n\ts_mov_b32 %1, .Lce0-.Lcb0-16" : "=s"(pc), "=s"(len));
    unsigned o = ((blockIdx.x >> 3) * 256 + threadIdx.x) * 16;
    gldso<0>(o < len ? o : 0u, (const void*)pc, (unsigned)(size_t)ldsC + 48128u);
  }
  cx.ws = ws; cx.A = ldsA; cx.B = ldsB; cx.C = ldsC;
  cx.lane = threadIdx.x & 63; cx.w = RFL(threadIdx.x >> 6);
  cx.tok = cx.lane & 31; cx.hk = cx.lane >> 5; cx.vo = cx.lane * 16; cx.budget = SPIN_MAX;
  { const int b = blockIdx.x, xx = b & 7, j = b >> 3; cx.g = 4 * (xx >> 1) + (j >> 3); cx.c = 8 * (xx & 1) + ((j + CROT) & 7); }
  cx.cb = 4 * (cx.c >> 3) + (cx.c & 3); cx.th = (cx.c >> 2) & 1;
  cx.rs = __builtin_amdgcn_make_buffer_rsrc(ws, 0, WS_USED, 0x00020000);
  cx.gb = GRP + cx.g * G_S;
  cx.fv16 = cx.lane < 16 ? cx.lane * FS + 120 : OOB; cx.fv8 = cx.lane < 8 ? cx.lane * FS + 120 : OOB; cx.f63 = cx.lane >= 48 ? (cx.lane - 48) * 8 : OOB;
  cx.fvz = cx.lane < 32 ? (cx.lane >> 1) * FS + (cx.lane & 1) * 16 : cx.lane < 48 ? (cx.lane - 32) * FS + 112 : OOB;
  {
    const int w = cx.w, kq = cx.c >> 2, tx = (w & 1) ^ (kq >> 1), ln = (w >> 1) * 4 + kq, tb = 2 * cx.th + (w >> 1), blk = 2 * cx.cb + (w & 1);
    cx.oo = (ln * 16 + tx * 8 + (cx.c & 3) * 2) * 1024;
    cx.ho = (ln * 56 + (cx.c & 3) * 2 + tx) * 1024;
    cx.fo = (ln * 8 + (cx.c & 3) * 2 + (w & 1)) * FS;
    cx.xo = (tb * 32 + 2 * blk) * 1024;
    cx.so = tb * 4096 + blk * 256;
    cx.fxo = (tb * 16 + blk) * FS;
  }
  u32x4 phq[4], aphi[2], w2f[56], wof[16]; unsigned csw[3], csp[4], b1p[2]; KvIn kin; Misc mi;
  stage1a(cx, x, wqkv, wphi, wo, w1, b1, w2, b2, phq, csw, aphi, kin, w2f, wof, mi);
  layer<0>(cx, x, b1, b2, out, phq, csw, aphi, kin, w2f, wof, mi, csp, b1p);
  asm volatile("" : "+v"(cx.lane), "+v"(cx.tok), "+v"(cx.hk), "+v"(cx.vo));
  asm volatile("" : "+v"(csp[0]), "+v"(csp[1]), "+v"(csp[2]), "+v"(csp[3]), "+v"(b1p[0]), "+v"(b1p[1]), "+v"(csw[0]), "+v"(csw[1]), "+v"(csw[2]));
#pragma unroll
  for (int k = 0; k < W2KEEP; k++) asm volatile("" : "+a"(w2f[k]));
  layer<1>(cx, x, b1, b2, out, phq, csw, aphi, kin, w2f, wof, mi, csp, b1p);
  asm volatile(".Lce0:");
}

extern "C" void kernel_launch(void* const* d_in, const int* in_sizes, int n_in, void* d_out, int out_size, void* d_ws, size_t ws_size,
                              hipStream_t stream) {
  fused<<<256, 256, 0, stream>>>((const u16*)d_in[0], (const u16*)d_in[1], (const u16*)d_in[2], (const u16*)d_in[3], (const u16*)d_in[5],
                                 (const u16*)d_in[6], (const u16*)d_in[7], (const u16*)d_in[8], (char*)d_ws, (u16*)d_out);
}
```

```cpp
#include <hip/hip_runtime.h>
#include <stdint.h>

typedef unsigned short u16;
typedef __attribute__((ext_vector_type(8))) __bf16 bf16x8;
typedef __attribute__((ext_vector_type(2))) __bf16 bf16x2;
typedef __attribute__((ext_vector_type(16))) float f32x16;
typedef __attribute__((ext_vector_type(4))) float f32x4;
typedef __attribute__((ext_vector_type(2))) float f32x2;
typedef __attribute__((ext_vector_type(4))) unsigned u32x4;
typedef __attribute__((ext_vector_type(2))) unsigned u32x2;
typedef __attribute__((ext_vector_type(4))) short s16x4;
typedef __amdgpu_buffer_rsrc_t rsrc_t;
typedef __attribute__((address_space(3))) void* ldsp;

#define DEVI __device__ __forceinline__
#define LAMBDA __attribute__((always_inline))
#define MFMA(a, b, c) __builtin_amdgcn_mfma_f32_32x32x16_bf16(__builtin_bit_cast(bf16x8, a), __builtin_bit_cast(bf16x8, b), c, 0, 0, 0)
#define MFMA16(a, b, c) __builtin_amdgcn_mfma_f32_16x16x32_bf16(__builtin_bit_cast(bf16x8, a), __builtin_bit_cast(bf16x8, b), c, 0, 0, 0)
#define SB() __builtin_amdgcn_sched_barrier(0)
#define RFL(v) __builtin_amdgcn_readfirstlane(v)
#define UNLIKELY(x) __builtin_expect(!!(x), 0)
#define BALLOT(p) __builtin_amdgcn_ballot_w64(p)

constexpr int WT_QKV = 0x0, WQ_C = 98304;
constexpr int WT_O   = 0x180000;
constexpr int WT_1   = 0x200000, W1_C = 114688;
constexpr int CS1    = 0x3C0000;
constexpr int WT_2   = 0x400000;
constexpr int MBX    = 0x5C0000;
constexpr int TOKR   = 0x5E0000;
constexpr int TOKC   = 0x5E1000;
constexpr int KVA    = 0x600000, KV_C = 0xC000, KV_L = 0xC0000, KV_F = 0x8000;
constexpr int MB2    = 0x780000;
constexpr int GRP    = 0x800000, G_S = 0x200000, G_L = 0xC6000;
constexpr int G_O = 0, G_X1 = 0x20000, G_H = 0x40000, G_ST1 = 0xB0000, G_FO = 0xB4000, G_FH = 0xB6000, G_FX1 = 0xBE000;
constexpr int FS = 128, FHL = 0x2000, CKO = 0xC000, CKX = 0x4000, CKH = 0xE000;
constexpr int G_X2 = 0x18C000, G_ST2 = 0x1AC000, G_FX2 = 0x1B0000;
constexpr int WS_USED = 0x2800000;
constexpr int OOB = 0x40000000;
#define STAMP_OFF 0x2800000
#define STAMP_N 64

constexpr int INBOX = 12288;
constexpr int C_V = 4352, C_IMG = 6656, C_ZERO = 7680, C_PARK = 8192;
constexpr float LOG2E = 1.4426950408889634f;
constexpr int SPIN_MAX = 1 << 16;

DEVI unsigned pk2(float a, float b) { f32x2 v = {a, b}; return __builtin_bit_cast(unsigned, __builtin_convertvector(v, bf16x2)); }
DEVI float bflo(unsigned u) { return __uint_as_float(u << 16); }
DEVI float bfhi(unsigned u) { return __uint_as_float(u & 0xffff0000u); }
DEVI void lds_barrier() { asm volatile("s_waitcnt lgkmcnt(0)\n\ts_barrier" ::: "memory"); }
template <int N> DEVI void lds_barrier_n() { asm volatile("s_waitcnt lgkmcnt(%0)\n\ts_barrier" :: "n"(N) : "memory"); }
DEVI void bar() { asm volatile("s_barrier" ::: "memory"); }
template <int N> DEVI void vm_wait() { asm volatile("s_waitcnt vmcnt(%0)" :: "n"(N) : "memory"); }
template <int OFF> DEVI void gldso(unsigned voff, const void* sbase, unsigned lds_base) {
  asm volatile("s_mov_b32 m0, %2\n\ts_nop 0\n\tglobal_load_lds_dwordx4 %0, %1 offset:%3" :: "v"(voff), "s"(sbase), "s"(lds_base), "n"(OFF) : "memory", "m0");
}
DEVI void gldsv(const void* p, unsigned lds_base) {
  asm volatile("s_mov_b32 m0, %1\n\ts_nop 0\n\tglobal_load_lds_dwordx4 %0, off" :: "v"(p), "s"(lds_base) : "memory", "m0");
}
DEVI void gldsc4(unsigned voff, const void* sbase, unsigned lds_base) {
  asm volatile("s_mov_b32 m0, %2\n\ts_nop 0\n\tglobal_load_lds_dwordx4 %0, %1 sc1" :: "v"(voff), "s"(sbase), "s"(lds_base) : "memory", "m0");
}
DEVI void gldsc1(unsigned voff, const void* sbase, unsigned lds_base) {
  asm volatile("s_mov_b32 m0, %2\n\ts_nop 0\n\tglobal_load_lds_dword %0, %1 sc1" :: "v"(voff), "s"(sbase), "s"(lds_base) : "memory", "m0");
}
DEVI float xhalf_sum(float v) {
  auto t = __builtin_amdgcn_permlane32_swap(__float_as_uint(v), __float_as_uint(v), false, false);
  return __uint_as_float(t[0]) + __uint_as_float(t[1]);
}
DEVI u32x4 ldc(rsrc_t rs, int vo, int so) { return __builtin_amdgcn_raw_buffer_load_b128(rs, vo, so, 16); }
DEVI u32x2 ldc2(rsrc_t rs, int vo, int so) { return __builtin_amdgcn_raw_buffer_load_b64(rs, vo, so, 16); }
DEVI unsigned ldc1(rsrc_t rs, int vo, int so) { return __builtin_amdgcn_raw_buffer_load_b32(rs, vo, so, 16); }
DEVI void stc(rsrc_t rs, int vo, int so, u32x4 v) { __builtin_amdgcn_raw_buffer_store_b128(v, rs, vo, so, 16); }
DEVI void stc2(rsrc_t rs, int vo, int so, u32x2 v) { __builtin_amdgcn_raw_buffer_store_b64(v, rs, vo, so, 16); }
DEVI void stc1(rsrc_t rs, int vo, int so, unsigned v) { __builtin_amdgcn_raw_buffer_store_b32(v, rs, vo, so, 16); }
DEVI u32x4 ldck(rsrc_t rs, int vo, int so, int kb) { return ldc(rs, vo + (kb & 3) * 1024, so + (kb >> 2) * 4096); }
template <int KB> DEVI void dma(rsrc_t rs, char* dst, int vo, int so) {
  __builtin_amdgcn_raw_ptr_buffer_load_lds(rs, (ldsp)(dst + (KB >> 2) * 4096), 16, vo, so + (KB >> 2) * 4096, (KB & 3) * 1024, 16);
}
DEVI float lsel(unsigned long long m, float t, float f) { float r; asm("v_cndmask_b32_e64 %0, %1, %2, %3" : "=v"(r) : "v"(f), "v"(t), "s"(m)); return r; }
DEVI float fmax3(float a, float b, float c) { return fmaxf(fmaxf(a, b), c); }
DEVI float fmin3(float a, float b, float c) { return fminf(fminf(a, b), c); }
DEVI unsigned sum4(unsigned s, const u32x4& f) { s = (s + f[0] + f[1]) + f[2] + f[3]; asm volatile("" : "+v"(s)); return s; }
DEVI unsigned wsum(unsigned v) {
  v += __builtin_amdgcn_update_dpp(0u, v, 0x111, 0xf, 0xf, false);
  v += __builtin_amdgcn_update_dpp(0u, v, 0x112, 0xf, 0xf, false);
  v += __builtin_amdgcn_update_dpp(0u, v, 0x114, 0xf, 0xf, false);
  v += __builtin_amdgcn_update_dpp(0u, v, 0x118, 0xf, 0xf, false);
  v += __builtin_amdgcn_update_dpp(0u, v, 0x142, 0xa, 0xf, false);
  v += __builtin_amdgcn_update_dpp(0u, v, 0x143, 0xc, 0xf, false);
  return v;
}

template <int I> DEVI void wsum_step(unsigned& v) {
  constexpr int ctl[6] = {0x111, 0x112, 0x114, 0x118, 0x142, 0x143}, rm[6] = {0xf, 0xf, 0xf, 0xf, 0xa, 0xc};
  v += __builtin_amdgcn_update_dpp(0u, v, ctl[I], rm[I], 0xf, false);
}

#define FINE 0
#define FST(sel, i)
#define RAW(i, v)
#define STAMP(i)
#define CSTAMP(i)
#define COUNT(i)
#define BUD(i)

template <class T>
DEVI void pack16(const T& v, u32x4& a, u32x4& b) {
#pragma unroll
  for (int i = 0; i < 4; i++) { a[i] = pk2(v[2 * i], v[2 * i + 1]); b[i] = pk2(v[8 + 2 * i], v[9 + 2 * i]); }
}
template <int I> struct ic { static constexpr int v = I; constexpr operator int() const { return I; } };
template <int... I> struct iseq {};
template <int N, int... I> struct mkseq : mkseq<N - 1, N - 1, I...> {};
template <int... I> struct mkseq<0, I...> { typedef iseq<I...> type; };
template <class F, int... I> DEVI void sfor_impl(F& f, iseq<I...>) { (f(ic<I>()), ...); }
template <int N, class F> DEVI void sfor(F f) { sfor_impl(f, typename mkseq<N>::type()); }

struct Softmax {
  float mx, mn; f32x2 mm, sv, ss, a[2], e[16];
  DEVI f32x2 arg(float v) { const f32x2 t = {v, v}; return __builtin_elementwise_fma(t, sv, mm); }
  DEVI void step(int i, const f32x16& p, float sc, u32x4 (&ph)[4]) {
    if (i == 0) { mx = fmax3(p[0], p[1], p[2]); mn = fmin3(p[0], p[1], p[2]); mx = fmax3(mx, p[3], p[4]); mn = fmin3(mn, p[3], p[4]); sv[0] = sc; sv[1] = -sc; }
    else if (i < 3) { int b = 5 + 4 * (i - 1); mx = fmax3(mx, p[b], p[b + 1]); mn = fmin3(mn, p[b], p[b + 1]); mx = fmax3(mx, p[b + 2], p[b + 3]); mn = fmin3(mn, p[b + 2], p[b + 3]); }
    else if (i == 3) { mx = fmax3(mx, p[13], p[14]); mn = fmin3(mn, p[13], p[14]); mm[0] = fmaxf(mx, p[15]) * -sc; mm[1] = fminf(mn, p[15]) * sc; a[0] = arg(p[0]); }
    else if (i < 20) {
      int r = i - 4;
      if (r < 15) { a[(r + 1) & 1] = arg(p[r + 1]); SB(); }
      e[r][0] = __builtin_amdgcn_exp2f(a[r & 1][0]); e[r][1] = __builtin_amdgcn_exp2f(a[r & 1][1]);
      SB();
      if (r == 2) ss = e[0] + e[1]; else if (r > 2) ss += e[r - 1];
    } else if (i == 20) { ss += e[15]; ss[0] = __builtin_amdgcn_rcpf(ss[0]); ss[1] = __builtin_amdgcn_rcpf(ss[1]); }
    else if (i < 29) {
      int t = i - 21;
      const f32x2 u = e[2 * t] * ss, v = e[2 * t + 1] * ss;
      ph[t >> 2][t & 3] = pk2(u[0], v[0]);
      ph[2 + (t >> 2)][t & 3] = pk2(u[1], v[1]);
    }
  }
};

template <int C> DEVI u32x4 tr_readc(const char* p) {
  typedef __attribute__((address_space(3))) s16x4* lp;
  s16x4 a = __builtin_amdgcn_ds_read_tr16_b64_v4i16((lp)(p));
  s16x4 b = __builtin_amdgcn_ds_read_tr16_b64_v4i16((lp)(p + C));
  u32x2 ua = __builtin_bit_cast(u32x2, a), ub = __builtin_bit_cast(u32x2, b);
  u32x4 r = {ua[0], ua[1], ub[0], ub[1]};
  return r;
}
DEVI u32x4 tr_read2(const char* p, const char* p2) {
  typedef __attribute__((address_space(3))) s16x4* lp;
  s16x4 a = __builtin_amdgcn_ds_read_tr16_b64_v4i16((lp)(p));
  s16x4 b = __builtin_amdgcn_ds_read_tr16_b64_v4i16((lp)(p2));
  u32x2 ua = __builtin_bit_cast(u32x2, a), ub = __builtin_bit_cast(u32x2, b);
  u32x4 r = {ua[0], ua[1], ub[0], ub[1]};
  return r;
}

DEVI u32x4 aug_tok(float mean, float ia, int hk) {
  unsigned mh = pk2(-mean, ia);
  unsigned ml = pk2(-mean - bflo(mh), ia - bfhi(mh));
  u32x4 t;
  t[0] = __builtin_amdgcn_perm(ml, mh, 0x05040100u);
  t[1] = mh;
  t[2] = ml >> 16;
  t[3] = 0;
  if (hk) { t[0] = 0; t[1] = 0; t[2] = 0; }
  return t;
}
DEVI unsigned cs_pack(float cs) { unsigned h = pk2(cs, 0.f); return pk2(cs, cs - bflo(h)); }
DEVI u32x4 aug_w(unsigned csp, unsigned b) {
  u32x4 t;
  t[0] = __builtin_amdgcn_perm(csp, csp, 0x01000100u);
  t[1] = __builtin_amdgcn_perm(b, csp, 0x05040302u);
  t[2] = b & 0xffffu;
  t[3] = 0;
  return t;
}

struct Cx {
  rsrc_t rs; char* ws; char *A, *B, *C;
  int lane, w, tok, hk, g, c, cb, th, vo, gb;
  int fv16, fv8, fvz, f63, oo, ho, fo, xo, so, fxo;
  unsigned N, tk_kv, tk_oh, tk_x;
  int budget;
};

DEVI void put_flag(Cx& cx, int so, int i) { u32x2 v = {cx.N + i, cx.N + i}; stc2(cx.rs, cx.f63, so, v); }
DEVI void put_final(Cx& cx, int so, unsigned sum) { u32x2 v = {cx.N + 7, cx.N - wsum(sum)}; stc2(cx.rs, cx.f63, so, v); }
template <int NP> DEVI void poll_wait(Cx& cx, unsigned a, int so, unsigned tk, int i) {
  unsigned t; int s;
  asm volatile("v_sub_u32 %0, %3, %4\n\t"
               "v_cmp_ne_u32 vcc, %9, %0\n\t"
               "s_and_b32 %1, vcc_lo, %8\n\t"
               "s_cbranch_scc0 2f\n"
               "1:\n\t"
               "s_cmp_eq_u32 %2, 0\n\t"
               "s_cbranch_scc1 2f\n\t"
               "buffer_load_dword %0, %5, %6, %7 offen sc1\n\t"
               "s_sub_u32 %2, %2, 1\n\t"
               "s_waitcnt vmcnt(0)\n\t"
               "v_sub_u32 %0, %0, %4\n\t"
               "v_cmp_ne_u32 vcc, %9, %0\n\t"
               "s_and_b32 %1, vcc_lo, %8\n\t"
               "s_cbranch_scc1 1b\n"
               "2:"
               : "=&v"(t), "=&s"(s), "+s"(cx.budget)
               : "v"(a), "v"(tk), "v"(NP == 16 ? cx.fv16 : cx.fv8), "s"(cx.rs), "s"(so), "n"((1 << NP) - 1), "n"(i)
               : "vcc", "scc", "memory");
}
DEVI void kv_wait(Cx& cx, u32x4& q, int so) {
  int s; unsigned f = q[2];
  asm volatile("v_sub_u32 %0, %0, %4\n\t"
               "v_cmp_ne_u32 vcc, 7, %0\n\t"
               "s_and_b32 %2, vcc_hi, 0xffff\n\t"
               "s_cbranch_scc0 2f\n"
               "1:\n\t"
               "s_cmp_eq_u32 %3, 0\n\t"
               "s_cbranch_scc1 2f\n\t"
               "buffer_load_dword %0, %5, %6, %7 offen offset:8 sc1\n\t"
               "s_sub_u32 %3, %3, 1\n\t"
               "s_waitcnt vmcnt(0)\n\t"
               "v_sub_u32 %0, %0, %4\n\t"
               "v_cmp_ne_u32 vcc, 7, %0\n\t"
               "s_and_b32 %2, vcc_hi, 0xffff\n\t"
               "s_cbranch_scc1 1b\n\t"
               "buffer_load_dwordx4 %1, %5, %6, %7 offen sc1\n\t"
               "s_waitcnt vmcnt(0)\n"
               "2:"
               : "+v"(f), "+v"(q), "=&s"(s), "+s"(cx.budget)
               : "v"(cx.tk_kv), "v"(cx.fvz), "s"(cx.rs), "s"(so)
               : "vcc", "scc", "memory");
}
#define FORCE_BAD 0
DEVI bool bad(Cx& cx, unsigned sum) { return __builtin_amdgcn_readlane(wsum(sum), 63) + (FORCE_BAD ? cx.w & 1 : 0) != 0; }
DEVI bool again(Cx& cx, unsigned sum) {
  const unsigned r = __builtin_amdgcn_readlane(wsum(sum), 63);
  return (r != 0) & (cx.budget >= 64);
}
DEVI void spend(Cx& cx, int id) {
  asm volatile("" ::: "memory"); cx.budget = cx.budget >= 64 ? cx.budget - 64 : 0; }
#define D_O 8
#define D_X1 6
#define D_X2 2
#define CROT 0
#define THB 8
#define PRE 1
#define TOUCH 1
template <int N> DEVI void nap() { if (N > 0) { SB(); __builtin_amdgcn_s_sleep(N); SB(); } }
DEVI unsigned usel(unsigned long long m, unsigned t, unsigned f) { unsigned r; asm("v_cndmask_b32_e64 %0, %1, %2, %3" : "=v"(r) : "v"(f), "v"(t), "s"(m)); return r; }
DEVI void mail_wait(Cx& cx, unsigned a, int mvo, int so, unsigned v) {
  int s;
  asm volatile("v_cmp_ne_u32 vcc, %3, %0\n\t"
               "s_and_b32 %1, vcc_lo, 0xffff\n\t"
               "s_cbranch_scc0 2f\n"
               "1:\n\t"
               "s_cmp_eq_u32 %2, 0\n\t"
               "s_cbranch_scc1 2f\n\t"
               "buffer_load_dword %0, %4, %5, %6 offen sc1\n\t"
               "s_sub_u32 %2, %2, 1\n\t"
               "s_waitcnt vmcnt(0)\n\t"
               "v_cmp_ne_u32 vcc, %3, %0\n\t"
               "s_and_b32 %1, vcc_lo, 0xffff\n\t"
               "s_cbranch_scc1 1b\n"
               "2:"
               : "+v"(a), "=&s"(s), "+s"(cx.budget)
               : "s"(v), "v"(mvo), "s"(cx.rs), "s"(so)
               : "vcc", "scc", "memory");
}
DEVI void kv_write_v(Cx& cx, const f32x16& accv, float rstd) {
  float vn[16];
#pragma unroll
  for (int r = 0; r < 16; r++) vn[r] = accv[r] * rstd;
  u32x4 v0, v1; pack16(vn, v0, v1);
  char* p = cx.C + 2 * cx.hk * INBOX + C_V + (32 * cx.w + cx.tok) * 16;
  *(u32x4*)p = v0; *(u32x4*)(p + INBOX + 128) = v1;
}
template <int VMW>
DEVI void kv_tail(Cx& cx, const u32x4 (&phk)[4], int kvb) {
  char* p = cx.C + 2 * cx.hk * INBOX + (32 * cx.w + cx.tok) * 16;
  *(u32x4*)p = phk[0]; *(u32x4*)(p + 2176) = phk[1]; *(u32x4*)(p + INBOX) = phk[2]; *(u32x4*)(p + INBOX + 2176) = phk[3];
  const int G = cx.lane >> 4, d = cx.lane & 15, q4 = d >> 2, p4 = cx.lane & 3;
  const int ro = (4 * G + q4) * 16 + (p4 & 1) * 8;
  const char* pa = cx.C + cx.w * INBOX + (p4 >> 1) * 2176 + ro;
  const char* pb = cx.C + ((cx.w & 2) + (p4 >> 1)) * INBOX + C_V + (p4 >> 1) * 128 + ro;
  if (VMW >= 0) vm_wait<(VMW >= 0 ? VMW : 0)>();
  lds_barrier();
  u32x4 a[4], b[4];
#pragma unroll
  for (int t = 0; t < 4; t++) { a[t] = tr_readc<256>(pa + t * 512); b[t] = tr_readc<256>(pb + t * 512); }
  const u32x4 ones = {0x3f803f80u, 0x3f803f80u, 0x3f803f80u, 0x3f803f80u};
  f32x4 kv = {}, z = {};
#pragma unroll
  for (int t = 0; t < 4; t++) { kv = MFMA16(a[t], b[t], kv); z = MFMA16(a[t], ones, z); }
  const u32x2 k2 = {pk2(kv[0], kv[1]), pk2(kv[2], kv[3])}, z2 = {pk2(z[0], z[1]), pk2(z[2], z[3])};
  const int e = cx.w * 16 + cx.g;
  stc2(cx.rs, (G >> 1) * 256 + d * 16 + (G & 1) * 8, kvb + e * 512, k2);
  const unsigned cs = -7u - wsum(k2[0] + k2[1] + usel(0x1000900010001000ull, z2[0] + z2[1], 0u));
  const u32x2 zl = {usel(1ull << 63, cx.N + 7, z2[0]), usel(1ull << 63, cs, z2[1])};
  stc2(cx.rs, d >= 12 ? (d - 12) * 32 + G * 8 : OOB, kvb + KV_F + e * FS, zl);
}
struct KvIn { u32x4 pa[8], pz; };
struct Misc { u32x4 xa, xb, ba, bb, cs[4], msg; int pw, ra[2]; unsigned jv; u16 b1v[4]; };
DEVI u32x4 kv_poll(Cx& cx, int kvb) { return ldc(cx.rs, cx.fvz, kvb + KV_F + cx.w * 16 * FS); }
DEVI void kv_load(Cx& cx, KvIn& k, int kvb) {
#pragma unroll
  for (int m = 0; m < 8; m++) k.pa[m] = ldck(cx.rs, cx.vo, kvb + cx.w * 8192, m);
}

#define WOFO(k) ((k) < 8 ? 2 * (k) + 1 : 2 * ((k) - 8))
#define W2KEEP 45
template <int B, class K> DEVI void wload(Cx& cx, K, u32x4 (&w2f)[56], Misc& mi) {
  constexpr int k = B + K::v;
  if (k < 0) return;
  if (k < 4) mi.cs[k & 3] = ldck(cx.rs, cx.vo, CS1 + cx.c * 16384 + cx.w * 4096, k & 3);
  else if (k < 16) dma<((k - 4) & 15)>(cx.rs, cx.B + cx.w * 12288, cx.vo, WT_1 + cx.c * W1_C + 65536 + cx.w * 12288);
  else if (k < 32) dma<(k & 15)>(cx.rs, cx.A + cx.w * 16384, cx.vo, WT_1 + cx.c * W1_C + cx.w * 16384);
  else if (k < W2KEEP) w2f[k] = ldck(cx.rs, cx.vo, WT_2 + (cx.cb * 4 + cx.w) * 57344, k);
}
template <int K> DEVI void rawld(Cx& cx, rsrc_t r, int vo, int so, int bs) {
  __builtin_amdgcn_raw_ptr_buffer_load_lds(r, (ldsp)(cx.A + cx.w * 16384 + (K >> 2) * 4096), 16, vo, so + (K >> 2) * bs + (K & 3) * 7168, (K & 3) * 1024, 0);
}
template <int K> DEVI void rawlda(Cx& cx, rsrc_t r, int vo, int so, int bs) {
  if (!(K & 3)) asm volatile("s_mov_b32 m0, %0" :: "s"((unsigned)(size_t)cx.A + cx.w * 16384 + (K >> 2) * 4096) : "memory", "m0");
  asm volatile("s_nop 0\n\tbuffer_load_dwordx4 %0, %1, %2 offen offset:%3 lds" :: "v"(vo), "s"(r), "s"(so + (K >> 2) * bs + (K & 3) * 7168), "n"((K & 3) * 1024) : "memory");
}
DEVI u32x4 rawrd(Cx& cx, const Misc& mi, int blk, int e) { return tr_readc<512>(cx.C + mi.ra[e & 1] + blk * 4096 + (e >> 1) * 1024); }
#define RK 4
#define RA 12
#define QJ 1
#define QR 28
#define QM 2
#define QS 15
#define QCS 12
#define SND 26
#define MBI 8
DEVI f32x4 sq4(const u32x4& f, int h, f32x4 c) {
  const u32x2 t = {f[2 * h], f[2 * h + 1]};
  return __builtin_amdgcn_mfma_f32_4x4x4bf16_1k(__builtin_bit_cast(s16x4, t), __builtin_bit_cast(s16x4, t), c, 0, 0, 0);
}
#define KQP0 31
#define KQW0 12

#define S1SCH \
  120, 121, 122,  123, 124, 125,  134, 135,  60,  130, 131,  30,  132, 133,  61,  144, 145,  62,  154, 155,  63,  140, 141,  64, \
  142, 143,  65,  164, 165,   0,  174, 175,   0,  150, 151,   0,  152, 153,   0,  116, 126,   0,  136, 146,   0,  160, 161,   0, \
  162, 163,   0,  156, 166,   0,  176,   0,   0,  170, 171,   0,  172, 173,  40,   31,  32,   0,    0,   0,   0,   10,  11,   0, \
   12,  13,   0,    0,   0,   0,    0,   0,   0,    0,   0,  50,    0,   0,   0,    0,   0,   0,    0,   0,   0,    0,   0,   0
#define S1PRE 106, 0, 0, 0
constexpr int S1P = 19;
constexpr int s1_op[S1P + 96] = {100, 101, 102, 103, 104, 105, 2, 3, 110, 111, 112, 113, 114, 115, 4, S1PRE, S1SCH};
constexpr int s1_key(int o) { return o >= 100 ? o / 10 * 10 + (o % 10 >= 4) + (o % 10 == 6) : o >= 10 && o < 20 ? 10 : (o >= 20 && o < 40) || o >= 50 ? 0 : o; }
constexpr bool s1_m0(int i) {
  for (int k = i - 1; k >= 0; k--) if (s1_key(s1_op[k])) return s1_key(s1_op[k]) != s1_key(s1_op[i]);
  return true;
}
constexpr int s1_pos(int o) { for (int k = 0; k < S1P + 96; k++) if (s1_op[k] == o) return k; return -1; }
constexpr int s1_step(int o) { return (s1_pos(o) - S1P) / 3; }
constexpr int s1_cnt(int n, int lo, int hi, int e = 0) {
  int last = e ? s1_pos(e) : -1, r = 0;
  for (int o = lo; o <= hi; o++) if (s1_pos(o) > last) last = s1_pos(o);
  for (int k = last + 1; k < S1P + 3 * n; k++) r += s1_op[k] != 0;
  return r;
}
constexpr int s1_last(int lo, int hi, int d) {
  int last = -1, r = 0;
  for (int o = lo; o <= hi; o += d) if (s1_pos(o) > last) last = s1_pos(o);
  for (int k = last + 1; k < S1P + 96; k++) r += s1_op[k] != 0;
  return r;
}
template <int OFF, bool M> DEVI void gldm(unsigned voff, const void* sbase, unsigned lds_base) {
  if (M) gldso<OFF>(voff, sbase, lds_base);
  else asm volatile("global_load_lds_dwordx4 %0, %1 offset:%2" :: "v"(voff), "s"(sbase), "n"(OFF) : "memory");
}

DEVI void stage1a(Cx& cx, const u16* __restrict__ x, const u16* __restrict__ wqkv, const u16* __restrict__ wphi,
                  const u16* __restrict__ wo, const u16* __restrict__ w1, const u16* __restrict__ b1, const u16* __restrict__ w2,
                  const u16* __restrict__ b2, u32x4 (&phq)[4], unsigned (&csw)[3], u32x4 (&aphi)[2], KvIn& kin, u32x4 (&w2f)[56],
                  u32x4 (&wof)[16], Misc& mi) {
  const int lane = cx.lane, w = cx.w, tok = cx.tok, hk = cx.hk, g = cx.g, c = cx.c;
  const int rot = (c + 2 * g) & 7;
  const rsrc_t rs = cx.rs;
  const unsigned lA = (unsigned)(size_t)cx.A, lB = (unsigned)(size_t)cx.B, lC = (unsigned)(size_t)cx.C;
  char* xs = cx.C + w * INBOX;
  char* stg = cx.B + 32768 + w * 4096;
  const unsigned lstg = lB + 32768 + w * 4096;
  unsigned vx[4];
#pragma unroll
  for (int i = 0; i < 4; i++) vx[i] = (8 * i + (lane >> 3)) * 1024 + (((lane & 7) ^ ((4 * i + (lane >> 4)) & 7)) * 16) - i * 1024;
  const char* xb = (const char*)(x + (size_t)(128 * g + 32 * w) * 512);
  const unsigned xl = lC + w * INBOX;
  const unsigned wvo = (lane >> 2) * 3072 + (lane & 3) * 16;
  const char* wb = (const char*)(wqkv + (size_t)(16 * w) * 1536 + 32 * c);
  auto cload = [&](auto kk) LAMBDA {
    constexpr int j = s1_op[kk.v] / 10 - 10, i = s1_op[kk.v] % 10; constexpr bool M = s1_m0(kk.v);
    const int cj = (j + rot) & 7;
    if (i < 4) gldm<(i & 3) * 1024, M>(vx[i & 3], xb + cj * 128, xl + (j % 3) * 4096);
    else if (i < 6) gldm<(i & 1) * 1024, M>(wvo + 1024, wb + cj * (64 * 3072), lA + j * 8192 + w * 2048);
    else gldm<0, M>(wvo, wb + cj * (64 * 3072), lB + j * 4096 + w * 1024);
  };
  sfor<6>([&](auto kk) LAMBDA { cload(kk); });
  const unsigned tvo = lane < 16 ? TOKR + g * 128 + lane * 4 : TOKC + cx.cb * 128 + ((lane - 16) & 31) * 4;
  const unsigned mvo = MBX + (g * 16 + c) * 512 + (lane & 31) * 16;
  gldsc1(tvo, cx.ws, lstg);
  gldsc4(mvo, cx.ws, lstg + 1024);
  sfor<6>([&](auto kk) LAMBDA { cload(ic<8 + kk.v>()); });
  const int ch = (lane >> 4) & 1, q4 = (lane & 15) >> 2, p4 = lane & 3;
  const int cperm = (16 * (p4 & 1) + 4 * (2 * ch + (p4 >> 1))) * 2;
  int r_tr, r_a, r_b, r_c, r_t3, r_c3, r_dst, r_d1, r_cs = OOB; unsigned r_d3;
  const char *src, *src2, *src3; unsigned v0, v1, v2;
  gldso<0>(lane * 16, wphi + c * 512, lstg + 2048);
  {
    const int cb = cx.cb, hf = g & 1, cc = 8 * cx.th + (g >> 1), kq = cc >> 2;
    const char* s2 = (const char*)(w2 + (size_t)(112 * cc) * 512 + 64 * cb);
    const int d2 = WT_2 + ((cb * 4 + kq) * 28 + (cc & 3)) * 2048;
    if (w < 2) {
      const int n = 2 * g + w;
      src = src2 = (const char*)(w1 + (size_t)(32 * g + 8 * w) * 1792 + 112 * c);
      auto vo = [&](int t) LAMBDA { const int i = (t * 4682) >> 16; return (i + (i & 8)) * 3584 + (t - 14 * i) * 16; };
      v0 = vo(lane); v1 = vo(lane + 64); v2 = vo(lane + 128);
      src3 = hk ? s2 + (96 + 8 * w + (tok >> 2)) * 1024 + hf * 64 + p4 * 16 : src + vo(lane + 192);
      r_tr = (8 * hk + q4) * 224 + cperm; r_a = 64; r_b = 128; r_c = 896;
      r_t3 = hk ? 3584 + q4 * 64 + cperm : (8 * ch + q4) * 224 + 192 + (8 * (p4 & 1) + 4 * (p4 >> 1)) * 2; r_c3 = hk ? 256 : 896;
      r_dst = WT_1 + c * W1_C + n * 2048; r_d1 = 65536 - n * 512;
      r_d3 = hk ? d2 + 24 * 2048 + (hf ^ (kq & 1)) * 1024 + (32 * w + tok) * 16 : r_dst + r_d1 + 1024 + lane * 16;
      r_cs = CS1 + c * 16384 + n * 512;
    } else {
      v0 = (lane >> 3) * 1024 + (lane & 7) * 16; r_c = r_c3 = 512;
      if (w == 2) {
        src = src2 = s2 + hf * 65536; v1 = v0 + 8192; v2 = v0 + 16384; src3 = src + v0 + 24576;
        r_tr = (16 * hk + q4) * 128 + cperm; r_b = 1024;
        r_dst = d2 + hf * 32768; r_d1 = 8192;
      } else {
        src = s2 + (32 + 8 * hf) * 1024; src2 = (const char*)(wo + (size_t)(32 * cc + 8 * hf) * 512 + 64 * cb);
        v1 = v0 + 16384; v2 = v0; src3 = src2 + v1;
        r_tr = (8 * hk + q4) * 128 + cperm; r_b = 2048;
        r_dst = d2 + (8 + 4 * hf) * 2048; r_d1 = WT_O + ((cb * 4 + kq) * 8 + ((2 * cc) & 7) + hf) * 2048 - r_dst;
      }
      r_tr += (kq & 1) * 64; r_a = (kq & 1) ? -64 : 64;
      r_t3 = r_tr + r_a + r_b; r_d3 = r_dst + r_d1 + 1024 + lane * 16;
    }
    r_dst = RFL(r_dst); r_d1 = RFL(r_d1); r_cs = RFL(r_cs); src2 -= 2048; src3 -= 3072; v1 -= 1024;
  }
  const int xro = tok * 128 + (((2 * hk) ^ ((tok >> 1) & 7) ^ (4 * (w >> 1) + (w & 1))) * 16);
  const char* wk = cx.A + hk * 2048 + q4 * 64 + cperm;
  const char* wq = cx.B + hk * 1024 + q4 * 64 + cperm;
  const char* wks[4];
#pragma unroll
  for (int s = 0; s < 4; s++) { const int t = s ^ w; wks[s] = wk + (t >> 1) * 4096 + (t & 1) * 512; }
  const unsigned on1 = (ch == ((lane >> 2) & 1)) ? 0x3f803f80u : 0u;
  const u32x4 sel = {on1, on1, on1, on1};
  const int mdst = w != 0 || lane >= 48 ? OOB
                 : lane < 16 ? MBX + (g * 16 + lane) * 512 + c * 8
                             : MBX + (((lane - 16) & 15) * 16 + ((c & ~THB) | ((lane - 16) >> 4) * THB)) * 512 + (16 + g + 16 * cx.th) * 8;
  unsigned N = 0, sent = 0;
  u32x4 xf[33], ka[3], va[3], rf[4];
  f32x16 acck = {}, accv = {};
  f32x4 csk = {}, csv = {}, s1v = {}, q0 = {}, q1 = {}, cj[4];
  f32x2 cso[3] = {};
  auto rdx = [&](int n) LAMBDA { xf[n] = *(const u32x4*)(xs + ((n >> 2) % 3) * 4096 + (xro ^ ((4 * ((n >> 1) & 1) + (n & 1)) * 16))); };
  auto rdk = [&](int n) LAMBDA { ka[n % 3] = tr_readc<256>(wks[n & 3] + (n >> 2) * 8192); };
  auto rdv = [&](int n) LAMBDA { va[n % 3] = tr_readc<256>(wks[n & 3] + 1024 + (n >> 2) * 8192); };
  unsigned tk0, tk1;
  const int kvb = KVA + c * KV_C;
  int tch;
  {
    const int t = lane >> 3;
    tch = (t < 5 ? G_FO + t * FHL + cx.fo : t == 5 ? G_FX1 + cx.fxo : t == 6 ? G_FX2 + cx.fxo : kvb + KV_L + KV_F + (w * 16 + g) * FS - cx.gb) + (lane & 7) * 16 + cx.gb;
    asm volatile("" : "+v"(tch));
  }
  const int col = 64 * cx.cb + 32 * (w & 1) + 16 * hk;
  const int pi = 16 * ((tok >> 2) & 1) + (tok & 3) + 4 * (tok >> 3), ph = 8 * ((tok >> 2) & 1) + (tok & 3) + 4 * ((tok >> 3) & 1);
  const u16* bp = b1 + 112 * c;
  auto issue = [&](auto kk) LAMBDA {
    constexpr int o = s1_op[kk.v]; constexpr bool M = s1_m0(kk.v);
    if (o >= 100) cload(kk);
    if (o == 10) gldm<0, M>(v0, src, xl + 8192);
    if (o == 11) gldm<1024, M>(v1, src, xl + 8192);
    if (o == 12) gldm<2048, M>(v2, src2, xl + 8192);
    if (o == 13) {
      if (M) asm volatile("s_mov_b32 m0, %0\n\ts_nop 0" :: "s"(xl + 8192) : "memory", "m0");
      asm volatile("global_load_lds_dwordx4 %0, off offset:3072" :: "v"(src3) : "memory");
    }
    if (o == 30) {
      N = __builtin_amdgcn_readlane(tk0, c) + 64;
      while (BALLOT(tk0 - N < 64u || tk1 - N < 64u) != 0) N += 64;
      stc1(rs, w == 0 && (lane == c || lane == 16 + 16 * cx.th + g) ? (int)tvo : OOB, 0, N);
      *(unsigned*)(stg + 768) = N;
    }
    if (o == 31 || o == 32) {
      const u32x4 v = {cx.N, cx.N, cx.N, cx.N};
      if (o == 31) stc(rs, tch, 0, v); else stc(rs, lane < 48 ? tch : lane < 56 && w == 0 ? MB2 - G_L + (g * 16 + c) * FS + (lane & 7) * 16 : OOB, G_L, v);
    }
    if (o == 40) gldsc1(tvo, cx.ws, lstg + 256);
    if (o >= 60 && o < 64) mi.b1v[o & 3] = bp[o == 63 ? 96 + ph : 32 * (o - 60) + pi];
    if (o == 64) mi.ba = *(const u32x4*)(b2 + col);
    if (o == 65) mi.bb = *(const u32x4*)(b2 + col + 8);
    if (o >= 50 && o < 60) {
      const u32x2 m = {tk0, N};
      stc2(rs, o == 50 || tk0 != sent ? mdst : OOB, 0, m);
      sent = tk0;
    }
    SB();
  };
  static_assert(s1_step(50) > SND && SND % 4 == 2 && s1_step(30) < 5 && s1_step(30) > 2, "");
  static_assert(s1_step(10) > 22 && s1_step(11) > 22 && s1_step(12) > 22 && s1_step(13) > 22, "");
  sfor<4>([&](auto kk) LAMBDA { issue(ic<15 + kk.v>()); });
  vm_wait<s1_cnt(0, 100, 105)>(); bar(); STAMP(1); FST(1, 0);
  rdx(0); rdk(0); rdv(0); rdx(1); rdk(1); rdv(1);
  SB();
  float s2 = 0.f;
  sfor<32>([&](auto nn) LAMBDA {
    constexpr int n = nn, j = n >> 2, s = n & 3;
    if (s == 2 && j < 7) {
      constexpr int e = n == SND ? 40 : j == 1 ? 4 : 0;
      constexpr int cnt = s1_cnt(n, 110 + 10 * j, 115 + 10 * j, e);
      static_assert(n != SND || s1_step(40) < n, "");
      FST(1, 2 * j + 1); vm_wait<cnt>();
      if (j == 6) lds_barrier_n<5>(); else bar();
      FST(1, 2 * j + 2);
    }
    constexpr bool cs = s == 0 || j == 7;
    acck = MFMA(ka[n % 3], xf[n], acck); SB();
    if (n == 25) { const f32x2 v = {csk[0], csv[0]}; *(f32x2*)(stg + 3072 + lane * 8) = v; SB(); }
    issue(ic<S1P + 3 * n>());
    if (n + 2 < 32) rdx(n + 2);
    if (n == 27) {
#pragma unroll
      for (int t = 0; t < 3; t++) cso[t] = *(const f32x2*)(cx.B + 32768 + (w ^ (t + 1)) * 4096 + 3072 + lane * 8);
    }
    if (n == 29) { cso[0] += cso[1] + cso[2]; asm volatile("" : "+v"(cso[0])); }
    if (n == 2) { tk0 = *(const unsigned*)(stg + lane * 4); tk1 = *(const unsigned*)(stg + 1024 + lane * 8); }
    if (n == SND) tk0 = *(const unsigned*)(stg + 256 + lane * 4);
    if (n == 7) tk1 = *(const unsigned*)(cx.B + 32768 + 768);
    if (n == 9) cx.N = RFL(tk1);
    SB();
    if (cs) { csk = MFMA16(sel, ka[n % 3], csk); SB(); }
    if (n + 2 < 32) rdk(n + 2);
    SB();
    accv = MFMA(va[n % 3], xf[n], accv); SB();
    issue(ic<S1P + 3 * n + 1>());
    SB();
    if (cs) { csv = MFMA16(sel, va[n % 3], csv); SB(); }
    if (n + 2 < 32) rdv(n + 2);
    SB();
    if (n == 0) { q0 = sq4(xf[0], 0, q0); q1 = sq4(xf[0], 1, q1); s1v = MFMA16(sel, xf[0], s1v); SB(); }
    if (n < 31) { q0 = sq4(xf[n + 1], 0, q0); q1 = sq4(xf[n + 1], 1, q1); SB(); }
    if (n == 10) {
      const char* ap = stg + 2048 + hk * 512 + q4 * 32 + (2 * ch + (p4 >> 1)) * 8;
      aphi[0] = tr_readc<128>(ap); aphi[1] = tr_readc<128>(ap + 256);
    }
    if (n == 24) {
      const bool on = ((lane >> 2) & 1) == hk;
#pragma unroll
      for (int t = 0; t < 4; t++) { aphi[0][t] = on ? aphi[0][t] : 0u; aphi[1][t] = on ? aphi[1][t] : 0u; }
      asm volatile("" : "+v"(aphi[0]), "+v"(aphi[1]));
    }
    if (n == 31) {
      q0 += q1;
      const float u0 = lsel(0xCCCCCCCCCCCCCCCCull, q0[2], q0[0]), u1 = lsel(0xCCCCCCCCCCCCCCCCull, q0[3], q0[1]);
      s2 = xhalf_sum(lsel(0xAAAAAAAAAAAAAAAAull, u1, u0));
    }
    SB();
    if (n < 31) { s1v = MFMA16(sel, xf[n + 1], s1v); SB(); }
    issue(ic<S1P + 3 * n + 2>());
    if (n < 31) asm volatile("" : "+a"(acck), "+a"(accv), "+a"(csk), "+a"(csv), "+a"(s1v), "+a"(q0), "+a"(q1));
    SB();
  });
  STAMP(2); FST(1, 15); FST(11, 0);
  const float mean = s1v[0] * (1.f / 512.f);
  xf[32] = aug_tok(mean, 0.f, hk);
  csw[1] = cs_pack(csk[0] + cso[0][0]);
  acck = MFMA(aug_w(csw[1], 0u), xf[32], acck);
  csw[2] = cs_pack(csv[0] + cso[0][1]);
  accv = MFMA(aug_w(csw[2], 0u), xf[32], accv);
  const float rstd = __builtin_amdgcn_rsqf(fmaxf(s2 * (1.f / 512.f) - mean * mean, 0.f) + 1e-5f);
  const float sc = rstd * LOG2E;
  f32x16 p = {};
  { u32x4 b0, b1; pack16(acck, b0, b1); p = MFMA(aphi[0], b0, p); p = MFMA(aphi[1], b1, p); }
  SB();
  const int js = ((g >> 1) - rot) & 7;
  u32x4 wf0, wf1;
  { const char* wsrc = wk + (w & 1) * 1024 + js * 8192 + (g & 1) * 4096; wf0 = tr_readc<256>(wsrc); wf1 = tr_readc<256>(wsrc + 512); }
  const rsrc_t ro = __builtin_amdgcn_make_buffer_rsrc((void*)wo, 0, 512 * 1024, 0x00020000), r2 = __builtin_amdgcn_make_buffer_rsrc((void*)w2, 0, 1792 * 1024, 0x00020000);
  int rv = (lane >> 3) * 1024 + ((lane & 7) ^ (4 * ch)) * 16;
  asm volatile("" : "+v"(rv));
  lds_barrier();
  STAMP(3); FST(11, 1);
  {
    Softmax sm; u32x4 phk[4];
    sfor<29>([&](auto ii) LAMBDA {
      constexpr int i = ii;
      sm.step(i, p, sc, phk);
      if (i == 2) kv_write_v(cx, accv, rstd);
      if (i >= RK && i < RK + 16) rawlda<((i - RK) & 15)>(cx, ro, rv, w * 131072 + cx.cb * 128, 32768);
      SB();
    });
    static_assert(s1_last(106, 176, 10) >= s1_last(170, 175, 1) && s1_last(31, 32, 1) >= s1_last(10, 13, 1), "");
    STAMP(4); FST(11, 2);
    kv_tail<-1>(cx, phk, kvb);
  }
  STAMP(5); FST(11, 3);
  {
    f32x16 accq = {}; f32x4 csq = {};
    u32x4 qa[3];
    int wqs[4];
#pragma unroll
    for (int s = 0; s < 4; s++) {
      const int t = s ^ w;
      wqs[s] = hk * 1024 + q4 * 64 + cperm + (t >> 1) * 2048 + (t & 1) * 512;
      asm volatile("" : "+v"(wqs[s]));
    }
    auto rdq = [&](int n) LAMBDA { qa[n % 3] = tr_readc<256>(cx.B + wqs[n & 3] + (n >> 2) * 4096); };
    rdq(0); rdq(1); rdq(2);
    u32x2 m;
    auto shake = [&]() LAMBDA {
      FST(11, 7);
      RAW(1, __builtin_popcountll(BALLOT(m[0] != cx.N && lane < 16))); RAW(2, __builtin_popcountll(BALLOT(m[0] != cx.N && lane >= 16 && lane < 48)));
      RAW(3, __builtin_popcountll(BALLOT(m[0] == cx.N && m[1] != sent && lane < 48))); RAW(4, 65536 - cx.budget);
      if (UNLIKELY(BALLOT((m[0] != cx.N || (w == 0 && m[1] != sent)) && lane < 48) != 0)) {
        while (true) {
          const bool valid = m[0] == cx.N;
          if (BALLOT(valid && m[1] != sent && lane < 48) != 0) {
            const u32x2 r = {m[1], cx.N};
            stc2(rs, valid && m[1] != sent ? mdst : OOB, 0, r);
            sent = valid ? m[1] : sent;
          }
          if (BALLOT(!valid && lane < 48) == 0) break;
          if (cx.budget == 0) break;
          cx.budget--;
          asm volatile("" ::: "memory");
          m = ldc2(rs, lane * 8, MBX + (g * 16 + c) * 512);
        }
        cx.budget = RFL(cx.budget);
      }
      FST(11, 9); RAW(5, 65536 - cx.budget);
      const int e = lane >> 1, t = lane < 48 ? (int)m[1] : 0;
      mi.jv = mi.pw != OOB ? m[1] + 2 : 0u;
      cx.tk_kv = __builtin_amdgcn_ds_bpermute(((lane >> 4) == 2 ? 16 * cx.th + lane - 16 : 63) * 4, t);
      cx.tk_oh = __builtin_amdgcn_ds_bpermute((lane < 8 ? 4 * w + e : 63) * 4, t);
      cx.tk_x = __builtin_amdgcn_ds_bpermute((lane < 16 ? (THB == 8 ? 8 * (w >> 1) + e : 8 * (e >> 2) + 4 * (w >> 1) + (e & 3)) : 63) * 4, t);
    };
    static_assert(QJ > 0 && QM > QJ && QM < QJ + 3 && QM < QS && QS <= KQP0 && QR > QCS && QR <= KQP0 && QCS > 9 && QJ < QCS && RA > 11 && RA + 12 <= QR && QJ + 4 < RA && QS < RA + 4 && KQP0 < 32 + KQW0, "");
    const int wd = WT_QKV + c * WQ_C;
    const char* own = cx.B + w * 1024 + hk * 512 + q4 * 64 + cperm;
    u32x4 ca[2]; float co[3];
    ca[0] = tr_readc<256>(own); ca[1] = tr_readc<256>(own + 4096);
    SB();
    sfor<32>([&](auto nn) LAMBDA {
      constexpr int n = nn;
      accq = MFMA(qa[n % 3], xf[n], accq); SB();
      if (n == 0) {
        const int l = lane - 16;
        mi.pw = (unsigned)l < 32u ? ((l & 15) * 16 + ((c & ~THB) | (l >> 4) * THB)) * FS : OOB;
        asm volatile("" : "+v"(mi.pw));
      }
      if (n == 1 && w == 2) { const char* wsrc = wq + js * 4096 + (g & 1) * 2048; wf0 = tr_readc<256>(wsrc); wf1 = tr_readc<256>(wsrc + 512); }
      const char* jb = xs + 8192; const char* st = jb + r_tr;
      const f32x4 z4 = {};
      if (n == QJ) {
        FST(11, 4);
        vm_wait<s1_last(10, 13, 1) + 18>();
        FST(11, 5);
        rf[0] = tr_read2(st, st + r_c); rf[1] = tr_read2(st + r_a, st + r_a + r_c);
      }
      if (n == QJ + 1) { rf[2] = tr_read2(st + r_b, st + r_b + r_c); rf[3] = tr_read2(jb + r_t3, jb + r_t3 + r_c3); }
      if (n == QJ + 3) { stc(rs, cx.vo, r_dst, rf[0]); stc(rs, cx.vo + 1024, r_dst, rf[1]); }
      if (n == QJ + 4) { stc(rs, cx.vo, r_dst + r_d1, rf[2]); stc(rs, r_d3, 0, rf[3]); }
      if (n == 8) { cj[0] = MFMA16(sel, rf[0], z4); cj[2] = MFMA16(sel, rf[2], z4); }
      if (n == 9) {
        const unsigned o2 = hk ? 0u : 0x3f803f80u;
        const u32x4 sl = {o2, o2, o2, o2};
        cj[1] = MFMA16(sel, rf[1], z4); cj[3] = MFMA16(sl, rf[3], z4);
      }
      if (n == 10) { const u32x2 cv = {__float_as_uint(hk ? cj[2][0] : cj[0][0]), __float_as_uint(hk ? cj[3][0] : cj[1][0])}; stc2(rs, lane * 8, r_cs, cv); }
      if (n == 11) {
        const int so = w < 2 ? wd + g * 4096 + w * 1024 : w == 2 ? wd + 65536 + g * 2048 : OOB;
        stc(rs, cx.vo, so, wf0); stc(rs, cx.vo, so + (w < 2 ? 2048 : 1024), wf1);
      }
      if (n == QR) { FST(11, 6); vm_wait<18>(); FST(11, 8); stc1(rs, lane ? OOB : 0, MB2 + (g * 16 + c) * FS + w * 4, cx.N + 2); }
      if (n == 3) {
        const int o = (int)(lA - lC) + w * 16384 + (16 * hk + q4) * 128 + cperm, h = ((w ^ (q4 >> 1)) & 1) * 64;
        mi.ra[0] = o + h; mi.ra[1] = o + 64 - h;
        asm volatile("" : "+v"(mi.ra[0]), "+v"(mi.ra[1]));
      }
      if (n == RA) { FST(11, 25); vm_wait<10>(); FST(11, 26); }
      if (n >= RA && n < RA + 8) {
        constexpr int b = ((n - RA) >> 1) & 3, e = 2 * ((n - RA) & 1);
        wof[4 * b + e] = rawrd(cx, mi, b, e); wof[4 * b + e + 1] = rawrd(cx, mi, b, e + 1);
      }
      if (n >= RA + 4 && n < RA + 12) {
        rawlda<((2 * (n - RA - 4)) & 15)>(cx, r2, rv, w * 458752 + cx.cb * 128, 114688);
        rawlda<((2 * (n - RA - 4) + 1) & 15)>(cx, r2, rv, w * 458752 + cx.cb * 128, 114688);
      }
      if (n == QM) m = ldc2(rs, lane * 8, MBX + (g * 16 + c) * 512);
      if (n == QS) shake();
      if (n == KQP0) kin.pz = kv_poll(cx, kvb);
      if (n == 9) *(float*)(stg + 3072 + lane * 4) = csq[0];
      if (n == QCS) {
        lds_barrier_n<2>();
#pragma unroll
        for (int t = 0; t < 3; t++) co[t] = *(const float*)(cx.B + 32768 + (w ^ (t + 1)) * 4096 + 3072 + lane * 4);
      }
      if (n == QCS + 4) { csw[0] = cs_pack((csq[0] + co[0]) + (co[1] + co[2])); asm volatile("" : "+v"(csw[0])); }
      SB();
      if (n < 8) { csq = MFMA16(sel, ca[n & 1], csq); SB(); }
      if (n + 2 < 8) ca[n & 1] = tr_readc<256>(own + (n + 2) * 4096);
      if (n + 3 < 32) rdq(n + 3);
      asm volatile("" : "+a"(accq));
      SB();
    });
    FST(11, 10);
    accq = MFMA(aug_w(csw[0], 0u), xf[32], accq);
    u32x4 q0, q1; pack16(accq, q0, q1);
    f32x16 pq = {}; pq = MFMA(aphi[0], q0, pq); pq = MFMA(aphi[1], q1, pq);
    Softmax sq;
    const u16* xr = x + (size_t)(128 * g + 64 * cx.th + 32 * (w >> 1) + tok) * 512 + col;
    SB();
    sfor<29>([&](auto ii) LAMBDA {
      constexpr int i = ii;
      sq.step(i, pq, sc, phq);
      if (i + 32 == KQP0) kin.pz = kv_poll(cx, kvb);
      if (i == 0) FST(11, 11);
      if (i == KQW0) {
        SB(); FST(11, 12); kv_wait(cx, kin.pz, kvb + KV_F + w * 16 * FS); STAMP(6); FST(11, 13); kv_load(cx, kin, kvb);
        mi.xa = *(const u32x4*)xr; mi.xb = *(const u32x4*)(xr + 8);
      }
      if (i > KQW0 && i <= KQW0 + 4) {
        constexpr int b = (i - KQW0 - 1) & 3;
#pragma unroll
        for (int e = 0; e < 4; e++) w2f[8 * (e >> 1) + 2 * b + (e & 1)] = rawrd(cx, mi, b, e);
      }
      if (i == KQW0 + 7) asm volatile("s_waitcnt lgkmcnt(0)" ::: "memory");
      if (i >= KQW0 + 7 && i < KQW0 + 15) {
        rawld<((2 * (i - KQW0 - 7)) & 15)>(cx, r2, rv, w * 458752 + cx.cb * 128 + 32768, 114688);
        rawld<((2 * (i - KQW0 - 7) + 1) & 15)>(cx, r2, rv, w * 458752 + cx.cb * 128 + 32768, 114688);
      }
      SB();
    });
  }
}
DEVI void red_send(Cx& cx, const f32x16& a, int j) {
  char* dst = cx.C + (cx.w ^ j) * INBOX + (j - 1) * 4096 + cx.vo;
#pragma unroll
  for (int m = 0; m < 4; m++) { f32x4 v = {a[4 * m], a[4 * m + 1], a[4 * m + 2], a[4 * m + 3]}; *(f32x4*)(dst + m * 1024) = v; }
}
DEVI void red_send1(Cx& cx, const f32x16& a, int j, int m) {
  const f32x4 v = {a[4 * m], a[4 * m + 1], a[4 * m + 2], a[4 * m + 3]};
  *(f32x4*)(cx.C + (cx.w ^ j) * INBOX + (j - 1) * 4096 + cx.vo + m * 1024) = v;
}
struct Red {
  f32x4 r1[4], r2[4], r3[4]; unsigned chk; int cp;
  DEVI void rd32(Cx& cx, int m) {
    const char* p = cx.C + cx.w * INBOX + m * 1024 + cx.vo;
    r2[m] = *(const f32x4*)(p + 4096); r3[m] = *(const f32x4*)(p + 8192);
  }
  DEVI void put(Cx& cx, unsigned tot) {
    *(unsigned*)(cx.C + cx.w * INBOX + 4096 + cx.lane * 4) = tot + (FORCE_BAD ? cx.w & 1 : 0);
    cp = (cx.lane & 3) * INBOX + 4096 + 252;
    asm volatile("" : "+v"(cp));
  }
  DEVI void rd1(Cx& cx) {
    chk = *(const unsigned*)(cx.C + cp);
    SB();
#pragma unroll
    for (int m = 0; m < 4; m++) r1[m] = *(const f32x4*)(cx.C + cx.w * INBOX + m * 1024 + cx.vo);
  }
  DEVI void add32(int m) {
#pragma unroll
    for (int e = 0; e < 4; e++) asm volatile("v_add_f32 %0, %0, %1" : "+v"(r2[m][e]) : "v"(r3[m][e]));
  }
  DEVI bool fin(const f32x16& own, float (&xr)[16]) {
#pragma unroll
    for (int m = 0; m < 4; m++) { const f32x4 o = {own[4 * m], own[4 * m + 1], own[4 * m + 2], own[4 * m + 3]}; r2[m] += o; }
    asm volatile("" : "+v"(r2[0]), "+v"(r2[1]), "+v"(r2[2]), "+v"(r2[3]));
#pragma unroll
    for (int r = 0; r < 16; r++) xr[r] = r2[r >> 2][r & 3] + r1[r >> 2][r & 3];
    return BALLOT(chk != 0) != 0;
  }
};
DEVI void red_recv(Cx& cx, const f32x16& own, float (&xr)[16]) {
  f32x4 v[12];
#pragma unroll
  for (int k = 0; k < 12; k++) v[k] = *(const f32x4*)(cx.C + cx.w * INBOX + ((k % 3) * 4 + k / 3) * 1024 + cx.vo);
#pragma unroll
  for (int m = 0; m < 4; m++)
#pragma unroll
    for (int e = 0; e < 4; e++) xr[4 * m + e] = (own[4 * m + e] + v[3 * m][e]) + (v[3 * m + 1][e] + v[3 * m + 2][e]);
}
DEVI void unpack16(const u32x4& a, const u32x4& b, float (&x)[16]) {
#pragma unroll
  for (int t = 0; t < 4; t++) { x[2 * t] = bflo(a[t]); x[2 * t + 1] = bfhi(a[t]); x[8 + 2 * t] = bflo(b[t]); x[9 + 2 * t] = bfhi(b[t]); }
}
#define EFX 3
#define EFO 0
DEVI void publish_x(Cx& cx, float (&xr)[16], int xb, int sb, int fb) {
  u32x4 f0, f1; pack16(xr, f0, f1);
  stc(cx.rs, cx.vo, xb + cx.xo, f0);
  stc(cx.rs, cx.vo + 1024, xb + cx.xo, f1);
  if (EFX == 0) put_flag(cx, fb + cx.fxo, 7);
  SB();
  f32x2 s1 = {xr[0], xr[1]}, s2 = s1 * s1;
#pragma unroll
  for (int r = 2; r < 16; r += 2) { const f32x2 v = {xr[r], xr[r + 1]}; s1 += v; s2 = __builtin_elementwise_fma(v, v, s2); }
  auto t = __builtin_amdgcn_permlane32_swap(__float_as_uint(s1[0] + s1[1]), __float_as_uint(s2[0] + s2[1]), false, false);
  const unsigned st = __float_as_uint(__uint_as_float(t[0]) + __uint_as_float(t[1]));
  stc1(cx.rs, (cx.lane & 15) * 16 + (cx.lane >> 4 & 1) * 8 + cx.hk * 4, sb + cx.so, st);
  if (EFX == 3) put_flag(cx, fb + cx.fxo, 7);
  SB();
  put_final(cx, fb + CKX + cx.fxo, sum4(sum4(st, f0), f1));
  unpack16(f0, f1, xr);
}
DEVI void park(Cx& cx, const float (&xr)[16]) {
  char* p = cx.C + cx.w * INBOX + C_PARK + cx.vo;
#pragma unroll
  for (int m = 0; m < 4; m++) { f32x4 v = {xr[4 * m], xr[4 * m + 1], xr[4 * m + 2], xr[4 * m + 3]}; *(f32x4*)(p + m * 1024) = v; }
}
DEVI f32x16 unpark(Cx& cx) {
  const char* p = cx.C + cx.w * INBOX + C_PARK + cx.vo;
  f32x16 r;
#pragma unroll
  for (int m = 0; m < 4; m++) { f32x4 v = *(const f32x4*)(p + m * 1024); r[4 * m] = v[0]; r[4 * m + 1] = v[1]; r[4 * m + 2] = v[2]; r[4 * m + 3] = v[3]; }
  return r;
}
DEVI f32x16 tovec(const float (&x)[16]) {
  f32x16 r;
#pragma unroll
  for (int e = 0; e < 16; e++) r[e] = x[e];
  return r;
}

#define RDW 2
#define XS 0
#define XA 14
#define XE 1
template <class AUG, class WIN>
DEVI void xpass(Cx& cx, int xb, int sb, int fb, u32x4 (&xf)[33], f32x16& a0, f32x16& a1, int sti, AUG aug, WIN win) {
  const rsrc_t rs = cx.rs;
  xb += cx.w * 32768; sb += cx.w * 4096; fb += cx.w * 16 * FS;
  if (sti == 15) FST(7, 0);
  if (sti == 25) FST(8, 0);
  if (sti == 15) nap<D_X2>(); else nap<D_X1>();
  poll_wait<16>(cx, ldc1(rs, cx.fv16, fb), fb, cx.tk_x, 7);
  STAMP(sti); if (sti != 15) FST(sti == 11 ? 4 : 5, 0); else FST(7, 1);
  if (sti == 25) FST(8, 1);
  u32x4 st[4], ra[3], rb[3], w0, w1; unsigned ck;
  auto ld = [&](int q) LAMBDA {
    if (q >= XS && q < XS + 4) st[q - XS] = ldck(rs, cx.vo, sb, q - XS);
    else if (q < 36) xf[q < XS ? q : q - 4] = ldck(rs, cx.vo, xb, q < XS ? q : q - 4);
    if (q == 30) ck = ldc1(rs, cx.fv16, fb + CKX + 4);
  };
  f32x4 sv; float t0, t1;
  auto stats = [&](int k) LAMBDA {
    if (k == 0) sv = __builtin_bit_cast(f32x4, st[0]) + __builtin_bit_cast(f32x4, st[1]);
    if (k == 1) sv += __builtin_bit_cast(f32x4, st[2]) + __builtin_bit_cast(f32x4, st[3]);
    if (k < 2) asm volatile("" : "+v"(sv));
    if (k == 2) {
      auto p = __builtin_amdgcn_permlane32_swap(__float_as_uint(sv[0]), __float_as_uint(sv[1]), false, false);
      auto q = __builtin_amdgcn_permlane32_swap(__float_as_uint(sv[2]), __float_as_uint(sv[3]), false, false);
      t0 = __uint_as_float(p[0]) + __uint_as_float(p[1]); t1 = __uint_as_float(q[0]) + __uint_as_float(q[1]);
    }
    if (k == 3) {
      auto r = __builtin_amdgcn_permlane16_swap(__float_as_uint(t0), __float_as_uint(t1), false, false);
      t0 = (__uint_as_float(r[0]) + __uint_as_float(r[1])) * (1.f / 512.f);
    }
    if (k == 4) {
      auto u = __builtin_amdgcn_permlane32_swap(__float_as_uint(t0), __float_as_uint(t0), false, false);
      t0 = __uint_as_float(u[0]);
      t1 = fmaxf(__uint_as_float(u[1]) - t0 * t0, 0.f);
    }
    if (k >= 2 && k < 5) asm volatile("" : "+v"(t0), "+v"(t1));
    if (k >= 5) aug(k - 5, t0, t1, w0, w1);
  };
  const char* pa = cx.A + cx.vo;
#pragma unroll
  for (int q = 0; q < 10; q++) { ld(q); SB(); }
  if (sti == 15) FST(7, 2);
  if (sti == 25) FST(8, 2);
  bar();
  if (sti == 15) FST(7, 3);
  if (sti == 25) FST(8, 3);
#pragma unroll
  for (int n = 0; n < 3; n++) { ra[n] = *(const u32x4*)(pa + n * 2048); rb[n] = *(const u32x4*)(pa + n * 2048 + 1024); }
  SB();
  unsigned sum = -cx.tk_x;
  const f32x16 zero = {};
#pragma unroll
  for (int q = 10; q < XA; q++) { ld(q); SB(); }
  sfor<32>([&](auto nn) LAMBDA {
    constexpr int n = nn, k = n - XS - 4;
    if (n % 8 == 0 && sti != 15) FST(sti == 11 ? 4 : 5, 1 + n / 8);
    a0 = MFMA(ra[n % 3], xf[n], n ? a0 : zero); SB();
    if (n % 4 == 0 && sti == 15) FST(7, 4 + n / 4);
    if (n % 4 == 0 && sti == 25) FST(8, 4 + n / 4);
    if (n + 3 < 32) ra[n % 3] = *(const u32x4*)(pa + (n + 3) * 2048);
    if (n + XA < 36) ld(n + XA);
    if (n >= 22) win(ic<(n < 22 ? 0 : n - 22)>());
    if (k >= -4 && k < 0) sum = sum4(sum, st[k & 3]);
    if (k >= 0 && k < 9) stats(k);
    if (XE && n == 31) sum = sum4(sum, xf[n]);
    SB();
    if (XE && n == 31) { a0 = MFMA(w0, xf[32], a0); SB(); wsum_step<0>(sum); wsum_step<1>(sum); SB(); }
    a1 = MFMA(rb[n % 3], xf[n], n ? a1 : zero); SB();
    if (n + 3 < 32) rb[n % 3] = *(const u32x4*)(pa + (n + 3) * 2048 + 1024);
    if (!XE || n < 31) sum = sum4(sum, xf[n]);
    if (n == 29) sum += ck;
    if (XE && n == 31) { wsum_step<2>(sum); wsum_step<3>(sum); SB(); a1 = MFMA(w1, xf[32], a1); SB(); wsum_step<4>(sum); wsum_step<5>(sum); }
    SB();
    if (!XE && k == 9) { a0 = MFMA(w0, xf[32], a0); a1 = MFMA(w1, xf[32], a1); SB(); }
  });
  if (sti != 15) FST(sti == 11 ? 4 : 5, 5); else FST(7, 12);
  if (sti == 25) FST(8, 12);
  asm volatile("" : "+a"(a0), "+a"(a1));
  if (UNLIKELY(XE ? __builtin_amdgcn_readlane(sum, 63) + (FORCE_BAD ? cx.w & 1 : 0) != 0 : bad(cx, sum))) {
    do {
      spend(cx, sti == 11 ? 2 : sti == 15 ? 4 : 7);
#pragma unroll
      for (int q = 0; q < 36; q++) ld(q);
      sum = ck - cx.tk_x;
#pragma unroll
      for (int q = 0; q < 32; q++) sum = sum4(sum, xf[q]);
#pragma unroll
      for (int q = 0; q < 4; q++) sum = sum4(sum, st[q]);
    } while (again(cx, sum));
#pragma unroll
    for (int k = 0; k < 9; k++) stats(k);
    a0 = MFMA(w0, xf[32], zero); a1 = MFMA(w1, xf[32], zero);
#pragma unroll
    for (int n = 0; n < 32; n++) {
      a0 = MFMA(*(const u32x4*)(pa + n * 2048), xf[n], a0); a1 = MFMA(*(const u32x4*)(pa + n * 2048 + 1024), xf[n], a1);
      SB();
    }
  }
}

template <bool P> DEVI f32x2 mul2(f32x2 a, f32x2 b) {
  f32x2 r;
  if (P) asm volatile("v_pk_mul_f32 %0, %1, %2" : "=v"(r) : "v"(a), "v"(b));
  else {
    asm volatile("v_mul_f32 %0, %1, %2" : "=v"(r[0]) : "v"(a[0]), "v"(b[0]));
    asm volatile("v_mul_f32 %0, %1, %2" : "=v"(r[1]) : "v"(a[1]), "v"(b[1]));
  }
  return r;
}
template <bool P> DEVI f32x2 fma2(f32x2 a, f32x2 c) {
  f32x2 r;
  if (P) asm volatile("v_pk_fma_f32 %0, %1, %2, %2 op_sel:[0,1,0] op_sel_hi:[1,1,0]" : "=v"(r) : "v"(a), "v"(c));
  else {
    asm volatile("v_fma_f32 %0, %1, %2, %3" : "=v"(r[0]) : "v"(a[0]), "v"(c[1]), "v"(c[0]));
    asm volatile("v_fma_f32 %0, %1, %2, %3" : "=v"(r[1]) : "v"(a[1]), "v"(c[1]), "v"(c[0]));
  }
  return r;
}
struct Gelu {
  f32x2 cc, ia;
  f32x2 a[4], t, d, e, r, u;
  u32x4 f; unsigned sum;
  template <int K0, int K1, int K, int H, class MID>
  DEVI void step(Cx& cx, const f32x16& lo, const f32x16& hi, int hb, MID mid) {
    constexpr bool P = H == 2, p0 = K >= K0 && K < K1, p1 = K - 1 >= K0 && K - 1 < K1, p2 = K - 2 >= K0 && K - 2 < K1;
    constexpr int v = K - 2;
    f32x2 s, y;
    auto rc = [&]() LAMBDA { if (p2) { r[0] = __builtin_amdgcn_rcpf(d[0]); r[1] = __builtin_amdgcn_rcpf(d[1]); SB(); } };
    auto ex = [&]() LAMBDA { if (p1) { e[0] = __builtin_amdgcn_exp2f(t[0]); e[1] = __builtin_amdgcn_exp2f(t[1]); SB(); } };
    auto rd = [&]() LAMBDA {
      if (p0) {
        const f32x16& h = (K & 8) ? hi : lo;
        float x0, x1;
        asm volatile("v_accvgpr_read_b32 %0, %2\n\tv_accvgpr_read_b32 %1, %3" : "=v"(x0), "=v"(x1) : "a"(h[(2 * K) & 15]), "a"(h[(2 * K + 1) & 15]));
        a[K & 3][0] = x0; a[K & 3][1] = x1;
        SB();
      }
    };
    auto fin = [&]() LAMBDA { if (p2 && (v & 3) == 3) { stc(cx.rs, cx.vo, hb + (v >> 2) * 8192, f); sum = sum4(sum, f); } };
    if (H != 1) rd();
    if (H != 0) rc();
    if (H != 1) ex();
    mid(); SB();
    if (H != 0 && p0 && p2) {
      float y0, y1;
      asm volatile("v_mul_f32 %2, %6, %8\n\t"
                   "v_mul_f32 %3, %7, %9\n\t"
                   "v_pk_mul_f32 %0, %5, %5\n\t"
                   "v_cvt_pk_bf16_f32 %4, %2, %3\n\t"
                   "v_pk_fma_f32 %0, %0, %11, %11 op_sel:[0,1,0] op_sel_hi:[1,1,0]\n\t"
                   "v_pk_fma_f32 %1, %10, %12, %12\n\t"
                   "v_pk_mul_f32 %0, %5, %0"
                   : "=&v"(t), "=&v"(d), "=&v"(y0), "=&v"(y1), "=&v"(f[v & 3])
                   : "v"(a[K & 3]), "v"(a[v & 3][0]), "v"(a[v & 3][1]), "v"(r[0]), "v"(r[1]), "v"(e), "v"(cc), "v"(ia));
    } else if (H != 0) {
      if (p0) s = mul2<0>(a[K & 3], a[K & 3]);
      if (p2) y = mul2<0>(a[v & 3], r);
      if (p0) u = fma2<0>(s, cc);
      if (p1) d = fma2<0>(e, ia);
      if (p0) t = mul2<0>(a[K & 3], u);
      if (p2) f[v & 3] = pk2(y[0], y[1]);
    }
    if (H != 0) fin();
  }
};

#define GB2 34
#define GP1 26
#define GW1 40
#define GP3 9
#define GP4 37
#define GP2 42
#define GW3 26
#define H3F 1
#define GBR0 78
#define GBR1 90
#define WL_P 10
#define WL_D 2
#define WL_H 9
#define WL_N ((24 + WL_D) / WL_D)
#define NW2_H (WL_P + WL_N + WL_H)
#define D_O0 2
#define NB1H 6
#define NQK 8
#define KQP1 31
#define KQW1 12

constexpr int h_iss(int i) { return i < 32 ? i : i < 40 ? 33 + 2 * (i - 32) : i < 48 ? 32 + 2 * (i - 40) : i < 52 ? 49 + 2 * (i - 48) : 48 + 2 * (i - 52); }
constexpr int s4_f(int G) {
  return G < 64 ? G >> 1 : G < 80 ? 33 + 2 * ((G - 64) >> 1) : G < 84 ? 32 + 2 * (G - 80) : G < 92 ? 49 + 2 * ((G - 84) >> 1) : G < 100 ? 40 + 2 * (G - 92) : 32 + 2 * (G - 100);
}
constexpr int s4_j(int G) { return G < 64 ? (G & 2) + 1 - (G & 1) : G < 80 || (G >= 84 && G < 92) ? 3 - (G & 1) : G < 100 ? 1 : 0; }

#define BL 1
#define BUDL(i) do { if (L == BL) BUD(53 + (i)); } while (0)
template <int L>
DEVI void layer(Cx& cx, const u16* __restrict__ x, const u16* __restrict__ b1, const u16* __restrict__ b2, u16* __restrict__ out,
                u32x4 (&phq)[4], const unsigned (&csw)[3], const u32x4 (&aphi)[2], KvIn& kin, u32x4 (&w2f)[56], u32x4 (&wof)[16],
                Misc& mi, unsigned (&csp)[4], unsigned (&b1p)[2]) {
  const int lane = cx.lane, w = cx.w, tok = cx.tok, hk = cx.hk, g = cx.g, c = cx.c;
  const rsrc_t rs = cx.rs;
  const int gl = cx.gb + L * G_L;
  const int kvb = KVA + L * KV_L + c * KV_C;
  const int w2b = WT_2 + (cx.cb * 4 + w) * 57344;
  const f32x16 zero = {};
  constexpr int SO = 14 * L;
  u32x4 xa, xb2;
  const int col = 64 * cx.cb + 32 * (w & 1) + 16 * hk;
  const size_t xrow = (size_t)(128 * g + 64 * cx.th + 32 * (w >> 1) + tok) * 512 + col;
  BUDL(-1);
  if (L == 1) {
    u32x4 xf[33]; f32x16 acck, accv; float rstd;
    xpass(cx, cx.gb + G_X2, cx.gb + G_ST2, cx.gb + G_FX2, xf, acck, accv, 15,
          [&](int k, float mean, float var, u32x4& w0, u32x4& w1) LAMBDA {
            if (k == 0) { rstd = __builtin_amdgcn_rsqf(var + 1e-5f); asm volatile("" : "+v"(rstd)); }
            if (k == 1) { xf[32] = aug_tok(mean, 0.f, hk); asm volatile("" : "+v"(xf[32])); }
            if (k == 2) { w0 = aug_w(csw[1], 0u); w1 = aug_w(csw[2], 0u); asm volatile("" : "+v"(w0), "+v"(w1)); }
          },
          [&](auto ii) LAMBDA { if (ii.v < 8) dma<(ii.v & 7)>(rs, cx.B + w * 8192, cx.vo, WT_QKV + c * WQ_C + 65536 + w * 8192); });
    STAMP(16); FST(6, 0); FST(7, 13); BUDL(0);
    const float sc = rstd * LOG2E;
    f32x16 p;
    { u32x4 b0, b1; pack16(acck, b0, b1); p = MFMA(aphi[0], b0, zero); p = MFMA(aphi[1], b1, p); }
    FST(6, 1);
    kv_write_v(cx, accv, rstd);
    SB();
    {
      Softmax sm; u32x4 phk[4];
      sfor<29>([&](auto ii) LAMBDA {
        constexpr int i = ii;
        if (i % 4 == 0) FST(6, 2 + i / 4);
        sm.step(i, p, sc, phk);
        SB();
      });
      STAMP(17); FST(6, 10);
      kv_tail<0>(cx, phk, kvb);
    }
    STAMP(18); FST(6, 11);
    f32x16 accq = MFMA(aug_w(csw[0], 0u), xf[32], zero);
    u32x4 qa[3];
    const char* pq = cx.B + cx.vo;
#pragma unroll
    for (int n = 0; n < 3; n++) qa[n] = *(const u32x4*)(pq + n * 1024);
    char* dA = cx.A + w * 16384;
    const int sA = WT_1 + c * W1_C + w * 16384;
    SB();
    sfor<32>([&](auto nn) LAMBDA {
      constexpr int n = nn;
      accq = MFMA(qa[n % 3], xf[n], accq); SB();
      if (n + 3 < 32) qa[n % 3] = *(const u32x4*)(pq + (n + 3) * 1024);
      if (n >= 1 && n < 17) wof[n - 1] = ldck(rs, cx.vo, WT_O + (cx.cb * 4 + w) * 16384, n - 1);
      if (n >= 17 && (n & 1)) dma<(n >= 17 ? (n - 17) / 2 : 0)>(rs, dA, cx.vo, sA);
      if (n == KQP1) kin.pz = kv_poll(cx, kvb);
      if (n % 8 == 7) FST(6, 12 + n / 8);
      SB();
    });
    u32x4 b0, b1; pack16(accq, b0, b1);
    f32x16 pq2 = MFMA(aphi[0], b0, zero); pq2 = MFMA(aphi[1], b1, pq2);
    Softmax sq;
    sfor<29>([&](auto ii) LAMBDA {
      constexpr int i = ii;
      sq.step(i, pq2, sc, phq);
      if (i + 32 == KQP1) kin.pz = kv_poll(cx, kvb);
      if (i == KQW1) { SB(); kv_wait(cx, kin.pz, kvb + KV_F + w * 16 * FS); STAMP(19); kv_load(cx, kin, kvb); }
      if (i >= 19 && i < 27) dma<(i >= 19 && i < 27 ? i - 11 : 0)>(rs, dA, cx.vo, sA);
      if (i == 27) mi.ba = *(const u32x4*)(b2 + col);
      if (i == 28) mi.bb = *(const u32x4*)(b2 + col + 8);
      SB();
    });
  }
  asm volatile("" : "+v"(phq[0]), "+v"(phq[1]), "+v"(phq[2]), "+v"(phq[3]));
  SB();
  STAMP(7 + SO); BUDL(1); if (L == 0) FST(11, 14);
  {
    const int G = lane >> 4, d = lane & 15;
    u32x4 idn;
#pragma unroll
    for (int t = 0; t < 4; t++) { const int k = 8 * (G & 1) + 2 * t; idn[t] = k == d ? 0x3f80u : k + 1 == d ? 0x3f800000u : 0u; }
    f32x4 kv; unsigned sum;
    auto red = [&]() LAMBDA {
      f32x4 k0 = {}, k1 = {}; sum = -cx.tk_kv;
#pragma unroll
      for (int m = 0; m < 8; m += 2) {
        k0 = MFMA16(idn, kin.pa[m], k0); k1 = MFMA16(idn, kin.pa[m + 1], k1);
        sum = sum4(sum4(sum, kin.pa[m]), kin.pa[m + 1]);
      }
      sum = sum4(sum, kin.pz); kv = k0 + k1;
    };
    red();
    asm volatile("" : "+v"(kv));
    if (UNLIKELY(bad(cx, sum))) {
      do { spend(cx, 5 * L); kin.pz = kv_poll(cx, kvb); kv_load(cx, kin, kvb); red(); } while (again(cx, sum));
    }
    char* img = cx.C + w * INBOX + C_IMG;
    { const u32x2 v = {pk2(kv[0], kv[1]), pk2(kv[2], kv[3])}; *(u32x2*)(img + d * 32 + G * 8) = v; }
    { const u32x4 z4 = {}; *(u32x4*)(img + 512 + cx.vo) = hk ? z4 : kin.pz; }
    lds_barrier();
    const int hp = (tok >> 2) & 1, dd = (tok & 3) + 4 * (tok >> 3);
    const char* ap = cx.C + (hp == hk ? 2 * hp * INBOX + C_IMG + dd * 32 : C_ZERO);
    const int zo = hp == hk ? 512 : 0;
    u32x4 a[4], az[4];
#pragma unroll
    for (int s4 = 0; s4 < 4; s4++) az[s4] = *(const u32x4*)(ap + zo + (s4 >> 1) * INBOX + (s4 & 1) * 16);
#pragma unroll
    for (int s4 = 0; s4 < 4; s4++) a[s4] = *(const u32x4*)(ap + (s4 >> 1) * INBOX + (s4 & 1) * 16);
    if (L == 0) stc1(rs, w == 0 && (lane >> 4) == 2 ? (lane - 32) * 8192 : OOB, MBX + c * 512 + (48 + g) * 8, cx.tk_kv + 1);
    if (L == 0) mi.msg = ldc(rs, mi.pw, MB2);
    SB();
    f32x16 num = {}, den = {};
#pragma unroll
    for (int s4 = 0; s4 < 4; s4++) den = MFMA(az[s4], phq[s4], den);
#pragma unroll
    for (int s4 = 0; s4 < 4; s4++) num = MFMA(a[s4], phq[s4], num);
    float ds[4];
#pragma unroll
    for (int r = 0; r < 4; r++) ds[r] = (den[r] + den[4 + r]) + (den[8 + r] + den[12 + r]);
    const float inv = __builtin_amdgcn_rcpf(((ds[0] + ds[1]) + (ds[2] + ds[3])) + 1e-6f);
    float ov[16];
#pragma unroll
    for (int r = 0; r < 16; r++) ov[r] = num[r] * inv;
    u32x4 f0, f1; pack16(ov, f0, f1);
    stc(rs, cx.vo, gl + G_O + cx.oo, f0);
    stc(rs, cx.vo + 1024, gl + G_O + cx.oo, f1);
    if (EFO == 0) put_flag(cx, gl + G_FO + cx.fo, 7);
    put_final(cx, gl + G_FO + CKO + cx.fo, sum4(sum4(0u, f0), f1));
    STAMP(8 + SO); if (L == 0) FST(11, 15);
  }
  if (L == 0) {
    SB();
    auto miss = [&]() LAMBDA { return BALLOT(((mi.msg[0] ^ mi.jv) | (mi.msg[1] ^ mi.jv) | (mi.msg[2] ^ mi.jv) | (mi.msg[3] ^ mi.jv)) != 0); };
    RAW(6, __builtin_popcountll(miss())); RAW(7, 65536 - cx.budget);
    if (UNLIKELY(miss() != 0)) {
      do {
        if (cx.budget == 0) break;
        cx.budget--;
        asm volatile("" ::: "memory");
        mi.msg = ldc(rs, mi.pw, MB2);
      } while (miss() != 0);
      cx.budget = RFL(cx.budget);
    }
    FST(11, 16); RAW(8, 65536 - cx.budget);
    static_assert(WL_P >= 4 && WL_P <= 16 && NW2_H >= 32, "");
    sfor<WL_P>([&](auto kk) LAMBDA { wload<0>(cx, kk, w2f, mi); SB(); });
#pragma unroll
    for (int k = 0; k < 16; k++) w2f[16 + 8 * (k >> 1 & 1) + 2 * (k >> 2) + (k & 1)] = rawrd(cx, mi, k >> 2, k & 3);
  }
  float xr[16];
  {
    const int ob = gl + G_O + (cx.th * 4 + w) * 16384, fb = gl + G_FO + (cx.th * 4 + w) * 8 * FS;
    f32x16 acc[4];
    if (L == 0) {
      unpack16(mi.xa, mi.xb, xr); acc[0] = tovec(xr);
      b1p[0] = mi.b1v[0] | ((unsigned)mi.b1v[1] << 16); b1p[1] = mi.b1v[2] | ((unsigned)mi.b1v[3] << 16);
    } else acc[0] = unpark(cx);
    SB();
    char* dB = cx.B + w * 12288;
    const int sB = WT_1 + c * W1_C + 65536 + w * 12288;
    if (L == 1) sfor<NB1H>([&](auto ii) LAMBDA { dma<ii.v>(rs, dB, cx.vo, sB); });
    if (L == 0) FST(11, 17);
    nap<(L ? D_O : D_O0)>();
    poll_wait<8>(cx, ldc1(rs, cx.fv8, fb), fb, cx.tk_oh, 7);
    STAMP(9 + SO); if (L == 1) FST(9, 0); if (L == 0) FST(11, 18);
    BUDL(2);
    u32x4 of[16];
#pragma unroll
    for (int k = 0; k < 10; k++) { of[k ^ 8] = ldck(rs, cx.vo, ob, k ^ 8); SB(); }
    if (L == 0) {
      const f32x4 v = (__builtin_bit_cast(f32x4, mi.cs[0]) + __builtin_bit_cast(f32x4, mi.cs[1])) + (__builtin_bit_cast(f32x4, mi.cs[2]) + __builtin_bit_cast(f32x4, mi.cs[3]));
      *(f32x4*)(cx.C + w * INBOX + 3072 + cx.vo) = v;
    }
    lds_barrier();
    f32x2 cq[8];
    unsigned sum = -cx.tk_oh, ck;
    Red rd;
    sfor<32>([&](auto nn) LAMBDA {
      constexpr int n = nn, j = n < 12 ? 3 - (n % 3 == 2) : 3 - n / 8, s = n < 12 ? (n % 3 == 2 ? n / 3 : n - n / 3) : n & 7;
      constexpr int i = n >= 13 && n % 3 != 0 ? n - 13 - (n - 13) / 3 : -1;
      if (L == 1 && n % 4 == 0) FST(9, 1 + n / 4);
      if (L == 0 && n % 8 == 0) FST(11, 19 + n / 8);
      acc[j] = MFMA(wof[2 * s + (j & 1)], of[8 * (j >> 1) + s], s == 0 && j ? zero : acc[j]); SB();
      if (n < 6) of[(n + 10) ^ 8] = ldck(rs, cx.vo, ob, (n + 10) ^ 8);
      if (n == 6) ck = ldc1(rs, cx.fv8, fb + CKO + 4);
      if (L == 1 && n >= 7 && n < 19 - NB1H) dma<(n >= 7 && n < 19 - NB1H ? n - 7 + NB1H : 0)>(rs, dB, cx.vo, sB);
      if (L == 0 && n >= 7 && (n - 7) % WL_D == 0) wload<WL_P>(cx, ic<(n - 7) / WL_D>(), w2f, mi);
      if (L == 0 && n == 1) {
#pragma unroll
        for (int k = 0; k < 8; k++) cq[k] = *(const f32x2*)(cx.C + (hk * 2 + (k >> 1 & 1)) * INBOX + 3072 + (k >> 2) * 256 + (k & 1) * 512 + tok * 8);
      }
      if (L == 0 && (n == 4 || n == 5)) {
        constexpr int k = n & 1;
        const f32x2 v = (cq[4 * k] + cq[4 * k + 1]) + (cq[4 * k + 2] + cq[4 * k + 3]);
        csp[2 * k] = cs_pack(xhalf_sum(v[0])); csp[2 * k + 1] = cs_pack(xhalf_sum(v[1]));
        asm volatile("" : "+v"(csp[2 * k]), "+v"(csp[2 * k + 1]));
      }
      if (j & 1) sum = sum4(sum, of[8 * (j >> 1) + s]);
      if (i >= 0 && i < 12) red_send1(cx, acc[3 - (i / 4 & 3)], 3 - (i / 4 & 3), i & 3);
      if (n == 24) sum += ck;
      if (n >= 24 && n < 27) { wsum_step<(n >= 24 && n < 27 ? 2 * (n - 24) : 0)>(sum); SB(); wsum_step<(n >= 24 && n < 27 ? 2 * (n - 24) + 1 : 0)>(sum); }
      if (n == 27) { lds_barrier_n<2>(); rd.rd32(cx, 0); rd.rd32(cx, 1); rd.rd32(cx, 2); rd.rd32(cx, 3); rd.put(cx, sum); }
      if (n >= 30) { rd.add32(2 * (n & 1)); rd.add32(2 * (n & 1) + 1); }
      SB();
    });
    if (L == 1) FST(9, 9); else FST(11, 23);
    lds_barrier();
    if (L == 1) FST(9, 10);
    rd.rd1(cx);
    SB();
    if (UNLIKELY(rd.fin(acc[0], xr))) {
      lds_barrier();
      do {
        spend(cx, 1 + 5 * L);
        sum = ldc1(rs, cx.fv8, fb + CKO + 4) - cx.tk_oh;
#pragma unroll
        for (int k = 0; k < 16; k++) { of[k] = ldck(rs, cx.vo, ob, k); sum = sum4(sum, of[k]); }
      } while (again(cx, sum));
      if (L == 0) { xa = *(const u32x4*)(x + xrow); xb2 = *(const u32x4*)(x + xrow + 8); }
      else { xa = ldc(rs, cx.vo, cx.gb + G_X2 + cx.xo); xb2 = ldc(rs, cx.vo + 1024, cx.gb + G_X2 + cx.xo); }
      unpack16(xa, xb2, xr); acc[0] = tovec(xr); acc[1] = zero; acc[2] = zero; acc[3] = zero;
#pragma unroll
      for (int n = 0; n < 32; n++) acc[n >> 3] = MFMA(wof[2 * (n & 7) + ((n >> 3) & 1)], of[8 * (n >> 4) + (n & 7)], acc[n >> 3]);
      red_send(cx, acc[1], 1); red_send(cx, acc[2], 2); red_send(cx, acc[3], 3);
      lds_barrier();
      red_recv(cx, acc[0], xr);
    }
    if (L == 1) FST(9, 11);
    publish_x(cx, xr, gl + G_X1, gl + G_ST1, gl + G_FX1);
    if (L == 1) FST(9, 12); else FST(11, 24);
    STAMP(10 + SO);
    if (L == 0) sfor<WL_H>([&](auto kk) LAMBDA { wload<WL_P + WL_N>(cx, kk, w2f, mi); });
    float bv[16]; unpack16(mi.ba, mi.bb, bv);
#pragma unroll
    for (int r = 0; r < 16; r++) xr[r] += bv[r];
    park(cx, xr);
  }
  {
    u32x4 xf[33]; f32x16 h0, h1, h2, h3;
    Gelu ge;
    const char* pb = cx.B + cx.vo;
    const char* ph = cx.B + 1024 + (hk * 16 + (lane & 15)) * 16;
    u32x4 ra[4];
    auto rdw = [&](int G) LAMBDA { ra[G & 3] = *(const u32x4*)((G < 32 ? pb : ph) + (G & 31) * 1536); };
    xpass(cx, gl + G_X1, gl + G_ST1, gl + G_FX1, xf, h0, h1, 11 + SO,
          [&](int k, float mean, float var, u32x4& w0, u32x4& w1) LAMBDA {
            if (k == 0) {
              const float r1 = __builtin_amdgcn_rsqf(var + 1e-5f);
              ge.cc[0] = r1; ge.cc[1] = var * r1 * r1 + 1e-5f;
              asm volatile("" : "+v"(ge.cc));
            }
            if (k == 1) {
              const float A = ge.cc[0] * __builtin_amdgcn_rsqf(ge.cc[1]);
              const float iA = __builtin_amdgcn_rcpf(A);
              ge.ia[0] = iA; ge.ia[1] = iA;
              const float k0 = -2.f * LOG2E * 0.7978845608028654f, k1 = k0 * 0.044715f;
              ge.cc[0] = k0 * A; ge.cc[1] = k1 * A * A * A;
              asm volatile("" : "+v"(ge.cc), "+v"(ge.ia));
            }
            if (k == 2) { xf[32] = aug_tok(mean, ge.ia[0], hk); asm volatile("" : "+v"(xf[32])); }
            if (k == 3) { w0 = aug_w(csp[0], b1p[0]); w1 = aug_w(csp[1], b1p[0] >> 16); asm volatile("" : "+v"(w0), "+v"(w1)); }
          },
          [&](auto ii) LAMBDA {
            constexpr int k = NW2_H + ii.v - 8;
            if (ii.v < 8) dma<(ii.v & 7)>(rs, cx.C + w * INBOX, cx.vo, w2b + 48 * 1024);
            else if (L == 0 && k < W2KEEP) w2f[k] = ldck(rs, cx.vo, w2b, k);
            if (ii.v >= 10 - RDW) rdw(ii.v - 10 + RDW);
          });
    STAMP(12 + SO); if (L == 1) FST(8, 13);
    BUDL(3);
    const int hs = gl + G_H + cx.ho, hfo = gl + G_FH + cx.fo;
    const int hb = gl + G_H + (cx.th * 4 + w) * 57344, fb = gl + G_FH + (cx.th * 4 + w) * 8 * FS;
    ge.sum = 0;
    asm volatile("" : "+a"(h0), "+a"(h1));
    h2 = MFMA(aug_w(csp[2], b1p[1]), xf[32], zero); h3 = MFMA(aug_w(csp[3], b1p[1] >> 16), xf[32], zero);
    u32x4 hf[56];
    auto ldh = [&](int k) LAMBDA { hf[h_iss(k)] = ldck(rs, cx.vo, hb, h_iss(k)); };
#pragma unroll
    for (int n = RDW; n < 4; n++) rdw(n);
    SB();
    unsigned pl = 0, sum = -cx.tk_oh, ck;
    f32x16 acc[4];
    sfor<64>([&](auto gg) LAMBDA {
      constexpr int G = gg, n = G & 31;
      if (G % 8 == 0) FST(4 + L, 6 + G / 8);
      if (G < 32) h2 = MFMA(ra[G & 3], xf[n], h2); else h3 = MFMA(ra[G & 3], xf[n], h3);
      SB();
      auto mid = [&]() LAMBDA { if (G + 4 < 64) rdw(G + 4); };
      if (G < 10) ge.step<0, 16, G, 2>(cx, h0, h1, hs, mid);
      else if (G < 26) ge.step<0, 16, 10 + (G - 10) / 2, G & 1>(cx, h0, h1, hs, mid);
      else if (G >= GB2 && G < GB2 + 20) ge.step<16, 24, 16 + (G - GB2) / 2, (G - GB2) & 1>(cx, h2, h2, hs, mid);
      else mid();
      if (L == 0 && NW2_H + 2 + G < W2KEEP) w2f[NW2_H + 2 + G] = ldck(rs, cx.vo, w2b, NW2_H + 2 + G);
      if (G == 9) put_flag(cx, hfo + FHL, 1);
      if (G == 25) put_flag(cx, hfo + 2 * FHL, 3);
      if (G == GB2 + 19) put_flag(cx, hfo + 3 * FHL, 5);
      if (G == GP1) pl = ldc1(rs, cx.fv8, fb + FHL);
      if (G == GW1) { SB(); poll_wait<8>(cx, pl, fb + FHL, cx.tk_oh, 1); STAMP(13 + SO); BUDL(4); }
      if (G >= GW1 && G < GW1 + 16) ldh(G - GW1);
      if (G == GP2) pl = ldc1(rs, cx.fv8, fb + 2 * FHL);
      if (G == 56) { SB(); poll_wait<8>(cx, pl, fb + 2 * FHL, cx.tk_oh, 3); BUDL(5); }
      if (G >= 56 && !(G & 1)) ldh(G / 2 - 12);
      if (G == 60) acc[0] = unpark(cx);
      SB();
    });
    FST(4 + L, 14);
    STAMP(14 + SO);
    unsigned msg = 0;
    char* dA = cx.A + w * 16384;
    const int sA = WT_QKV + c * WQ_C + w * 16384;
    Red rd;
    sfor<112>([&](auto gg) LAMBDA {
      constexpr int G = gg, f = s4_f(G), q = f >> 1, j = s4_j(G);
      constexpr int i = G >= 92 && G % 3 != 1 ? G - 92 - (G - 92) / 3 : -1;
      if (G % 8 == 0) FST(2 + L, G / 8);
      acc[j] = MFMA(w2f[2 * q + (j & 1)], hf[f], q == 0 && j ? zero : acc[j]); SB();
      if (H3F) {
        if (G >= 2 && G < 8) ge.step<24, 28, 22 + G, 2>(cx, h3, h3, hs, []() LAMBDA {});
        if (G == 7) put_flag(cx, hfo, 7);
      } else {
        if (G >= 2 && G < 14) ge.step<24, 28, 24 + (G - 2) / 2, G & 1>(cx, h3, h3, hs, []() LAMBDA {});
        if (G == 13) put_flag(cx, hfo, 7);
      }
      if (G == 15) put_final(cx, hfo + CKH, ge.sum);
      if (G == GW3) { SB(); poll_wait<8>(cx, pl, fb + 3 * FHL, cx.tk_oh, 5); BUDL(6); }
      if (G == GW3 + 32) { SB(); poll_wait<8>(cx, pl, fb, cx.tk_oh, 7); BUDL(7); }
      if (G < 8) ldh(20 + G);
      else if (G < 16) { if (!(G & 1)) ldh(24 + G / 2); }
      else if (G >= GW3 && G < GW3 + 48 && !((G - GW3) & 1)) ldh(32 + (G - GW3) / 2);
      if (G == GP3) pl = ldc1(rs, cx.fv8, fb + 3 * FHL);
      if (G == GP4) pl = ldc1(rs, cx.fv8, fb);
      if (G == GW3 + 45) ck = ldc1(rs, cx.fv8, fb + CKH + 4);
      if (G >= 16 && G < 24) w2f[G + 32] = *(const u32x4*)(cx.C + w * INBOX + (G - 16) * 1024 + cx.vo);
      if (G == (L ? GBR1 : GBR0)) lds_barrier();
      if (!(G & 1) && G >= 16 && (G - 16) / 2 + W2KEEP < 48) w2f[(G - 16) / 2 + W2KEEP] = ldck(rs, cx.vo, w2b, (G - 16) / 2 + W2KEEP);
      if (L == 0) {
        if (G == 51) msg = ldc1(rs, (lane & 15) * 8, MBX + (g * 16 + c) * 512 + 384);
        if (G == 79) { SB(); mail_wait(cx, msg, (lane & 15) * 8, MBX + (g * 16 + c) * 512 + 384, cx.N + 1); }
        if (G >= 80 && !(G & 1) && (G - 80) / 2 < NQK) dma<(G >= 80 ? (G - 80) / 2 : 0)>(rs, dA, cx.vo, sA);
      }
      if (j & 1) sum = sum4(sum, hf[f]);
      if (i >= 0 && i < 12) red_send1(cx, acc[3 - (i / 4 & 3)], 3 - (i / 4 & 3), i & 3);
      if (G == 100) sum += ck;
      if (G >= 100 && G < 106) wsum_step<(G >= 100 && G < 106 ? G - 100 : 0)>(sum);
      if (G == 106) { lds_barrier_n<2>(); rd.rd32(cx, 0); rd.rd32(cx, 1); rd.rd32(cx, 2); rd.rd32(cx, 3); rd.put(cx, sum); }
      if (G >= 109) rd.add32(G & 3);
      if (G == 111) rd.add32(0);
      SB();
    });
    lds_barrier();
    rd.rd1(cx);
    SB();
    FST(2 + L, 14);
    if (UNLIKELY(rd.fin(acc[0], xr))) {
      lds_barrier();
      do {
        spend(cx, 3 + 5 * L);
        xa = ldc(rs, cx.vo, gl + G_X1 + cx.xo); xb2 = ldc(rs, cx.vo + 1024, gl + G_X1 + cx.xo);
        float bv[16]; unpack16(*(const u32x4*)(b2 + col), *(const u32x4*)(b2 + col + 8), bv);
        unpack16(xa, xb2, xr);
#pragma unroll
        for (int r = 0; r < 16; r++) acc[0][r] = xr[r] + bv[r];
        acc[1] = zero; acc[2] = zero; acc[3] = zero; sum = ldc1(rs, cx.fv8, fb + CKH + 4) - cx.tk_oh;
#pragma unroll
        for (int k = 0; k < 56; k++) {
          const u32x4 f = ldck(rs, cx.vo, hb, k);
          sum = sum4(sum, f);
          acc[2 * (k & 1)] = MFMA(w2f[k & ~1], f, acc[2 * (k & 1)]); acc[2 * (k & 1) + 1] = MFMA(w2f[k | 1], f, acc[2 * (k & 1) + 1]);
          if ((k & 7) == 7) SB();
        }
      } while (again(cx, sum));
      red_send(cx, acc[1], 1); red_send(cx, acc[2], 2); red_send(cx, acc[3], 3);
      lds_barrier();
      red_recv(cx, acc[0], xr);
    }
    FST(2 + L, 15);
    if (L == 0) {
      publish_x(cx, xr, cx.gb + G_X2, cx.gb + G_ST2, cx.gb + G_FX2);
      STAMP(20);
      sfor<16 - NQK>([&](auto ii) LAMBDA { dma<NQK + ii.v>(rs, dA, cx.vo, sA); });
      park(cx, xr);
    } else {
      u32x4 f0, f1; pack16(xr, f0, f1);
      *(u32x4*)(out + xrow) = f0; *(u32x4*)(out + xrow + 8) = f1;
      STAMP(29);
    }
  }
}

__global__ __launch_bounds__(256, 1) void fused(const u16* __restrict__ x, const u16* __restrict__ wqkv, const u16* __restrict__ wphi,
                                                const u16* __restrict__ wo, const u16* __restrict__ w1, const u16* __restrict__ b1,
                                                const u16* __restrict__ w2, const u16* __restrict__ b2, char* ws, u16* __restrict__ out) {
  __shared__ __attribute__((aligned(16))) char ldsA[65536];
  __shared__ __attribute__((aligned(16))) char ldsB[49152];
  __shared__ __attribute__((aligned(16))) char ldsC[49152];
  Cx cx;
  STAMP(0); CSTAMP(30);
  {
    unsigned long long pc; unsigned len;
    asm volatile("s_getpc_b64 %0\n.Lcb0:\n\ts_mov_b32 %1, .Lce0-.Lcb0-16" : "=s"(pc), "=s"(len));
    unsigned o = ((blockIdx.x >> 3) * 256 + threadIdx.x) * 16;
    gldso<0>(o < len ? o : 0u, (const void*)pc, (unsigned)(size_t)ldsC + 48128u);
  }
  cx.ws = ws; cx.A = ldsA; cx.B = ldsB; cx.C = ldsC;
  cx.lane = threadIdx.x & 63; cx.w = RFL(threadIdx.x >> 6);
  cx.tok = cx.lane & 31; cx.hk = cx.lane >> 5; cx.vo = cx.lane * 16; cx.budget = SPIN_MAX;
  { const int b = blockIdx.x, xx = b & 7, j = b >> 3; cx.g = 4 * (xx >> 1) + (j >> 3); cx.c = 8 * (xx & 1) + ((j + CROT) & 7); }
  cx.cb = THB == 8 ? cx.c & 7 : 4 * (cx.c >> 3) + (cx.c & 3); cx.th = THB == 8 ? cx.c >> 3 : (cx.c >> 2) & 1;
  cx.rs = __builtin_amdgcn_make_buffer_rsrc(ws, 0, WS_USED, 0x00020000);
  cx.gb = GRP + cx.g * G_S;
  cx.fv16 = cx.lane < 16 ? cx.lane * FS + 120 : OOB; cx.fv8 = cx.lane < 8 ? cx.lane * FS + 120 : OOB; cx.f63 = cx.lane >= 48 ? (cx.lane - 48) * 8 : OOB;
  cx.fvz = cx.lane < 32 ? (cx.lane >> 1) * FS + (cx.lane & 1) * 16 : cx.lane < 48 ? (cx.lane - 32) * FS + 112 : OOB;
  {
    const int w = cx.w, kq = cx.c >> 2, tx = (w & 1) ^ (kq >> 1), ln = (w >> 1) * 4 + kq, tb = 2 * cx.th + (w >> 1), blk = 2 * cx.cb + (w & 1);
    cx.oo = (ln * 16 + tx * 8 + (cx.c & 3) * 2) * 1024;
    cx.ho = (ln * 56 + (cx.c & 3) * 2 + tx) * 1024;
    cx.fo = (ln * 8 + (cx.c & 3) * 2 + (w & 1)) * FS;
    cx.xo = (tb * 32 + 2 * blk) * 1024;
    cx.so = tb * 4096 + blk * 256;
    cx.fxo = (tb * 16 + blk) * FS;
  }
  u32x4 phq[4], aphi[2], w2f[56], wof[16]; unsigned csw[3], csp[4], b1p[2]; KvIn kin; Misc mi;
  stage1a(cx, x, wqkv, wphi, wo, w1, b1, w2, b2, phq, csw, aphi, kin, w2f, wof, mi);
  layer<0>(cx, x, b1, b2, out, phq, csw, aphi, kin, w2f, wof, mi, csp, b1p);
  asm volatile("" : "+v"(cx.lane), "+v"(cx.tok), "+v"(cx.hk), "+v"(cx.vo));
  asm volatile("" : "+v"(csp[0]), "+v"(csp[1]), "+v"(csp[2]), "+v"(csp[3]), "+v"(b1p[0]), "+v"(b1p[1]), "+v"(csw[0]), "+v"(csw[1]), "+v"(csw[2]));
#pragma unroll
  for (int k = 0; k < W2KEEP; k++) asm volatile("" : "+a"(w2f[k]));
  layer<1>(cx, x, b1, b2, out, phq, csw, aphi, kin, w2f, wof, mi, csp, b1p);
  asm volatile(".Lce0:");
}

extern "C" void kernel_launch(void* const* d_in, const int* in_sizes, int n_in, void* d_out, int out_size, void* d_ws, size_t ws_size,
                              hipStream_t stream) {
  fused<<<256, 256, 0, stream>>>((const u16*)d_in[0], (const u16*)d_in[1], (const u16*)d_in[2], (const u16*)d_in[3], (const u16*)d_in[5],
                                 (const u16*)d_in[6], (const u16*)d_in[7], (const u16*)d_in[8], (char*)d_ws, (u16*)d_out);
}
```

```cpp
#include <hip/hip_runtime.h>
#include <stdint.h>

typedef unsigned short u16;
typedef __attribute__((ext_vector_type(8))) __bf16 bf16x8;
typedef __attribute__((ext_vector_type(2))) __bf16 bf16x2;
typedef __attribute__((ext_vector_type(16))) float f32x16;
typedef __attribute__((ext_vector_type(4))) float f32x4;
typedef __attribute__((ext_vector_type(2))) float f32x2;
typedef __attribute__((ext_vector_type(4))) unsigned u32x4;
typedef __attribute__((ext_vector_type(2))) unsigned u32x2;
typedef __attribute__((ext_vector_type(4))) short s16x4;
typedef __amdgpu_buffer_rsrc_t rsrc_t;
typedef __attribute__((address_space(3))) void* ldsp;

#define DEVI __device__ __forceinline__
#define LAMBDA __attribute__((always_inline))
#define MFMA(a, b, c) __builtin_amdgcn_mfma_f32_32x32x16_bf16(__builtin_bit_cast(bf16x8, a), __builtin_bit_cast(bf16x8, b), c, 0, 0, 0)
#define MFMA16(a, b, c) __builtin_amdgcn_mfma_f32_16x16x32_bf16(__builtin_bit_cast(bf16x8, a), __builtin_bit_cast(bf16x8, b), c, 0, 0, 0)
#define SB() __builtin_amdgcn_sched_barrier(0)
#define RFL(v) __builtin_amdgcn_readfirstlane(v)
#define UNLIKELY(x) __builtin_expect(!!(x), 0)
#define BALLOT(p) __builtin_amdgcn_ballot_w64(p)

constexpr int WT_QKV = 0x0, WQ_C = 98304;
constexpr int WT_O   = 0x180000;
constexpr int WT_1   = 0x200000, W1_C = 114688;
constexpr int CS1    = 0x3C0000;
constexpr int WT_2   = 0x400000;
constexpr int MBX    = 0x5C0000;
constexpr int TOKR   = 0x5E0000;
constexpr int TOKC   = 0x5E1000;
constexpr int KVA    = 0x600000, KV_C = 0xC000, KV_L = 0xC0000, KV_F = 0x8000;
constexpr int MB2    = 0x780000;
constexpr int GRP    = 0x800000, G_S = 0x200000, G_L = 0xC6000;
constexpr int G_O = 0, G_X1 = 0x20000, G_H = 0x40000, G_ST1 = 0xB0000, G_FO = 0xB4000, G_FH = 0xB6000, G_FX1 = 0xBE000;
constexpr int FS = 128, FHL = 0x2000, CKO = 0xC000, CKX = 0x4000, CKH = 0xE000;
constexpr int G_X2 = 0x18C000, G_ST2 = 0x1AC000, G_FX2 = 0x1B0000;
constexpr int WS_USED = 0x2800000;
constexpr int OOB = 0x40000000;
#define STAMP_OFF 0x2800000
#define STAMP_N 64

constexpr int INBOX = 12288;
constexpr int C_V = 4352, C_IMG = 6656, C_ZERO = 7680, C_PARK = 8192;
constexpr float LOG2E = 1.4426950408889634f;
constexpr int SPIN_MAX = 1 << 16;

DEVI unsigned pk2(float a, float b) { f32x2 v = {a, b}; return __builtin_bit_cast(unsigned, __builtin_convertvector(v, bf16x2)); }
DEVI float bflo(unsigned u) { return __uint_as_float(u << 16); }
DEVI float bfhi(unsigned u) { return __uint_as_float(u & 0xffff0000u); }
DEVI void lds_barrier() { asm volatile("s_waitcnt lgkmcnt(0)\n\ts_barrier" ::: "memory"); }
template <int N> DEVI void lds_barrier_n() { asm volatile("s_waitcnt lgkmcnt(%0)\n\ts_barrier" :: "n"(N) : "memory"); }
DEVI void bar() { asm volatile("s_barrier" ::: "memory"); }
template <int N> DEVI void vm_wait() { asm volatile("s_waitcnt vmcnt(%0)" :: "n"(N) : "memory"); }
template <int OFF> DEVI void gldso(unsigned voff, const void* sbase, unsigned lds_base) {
  asm volatile("s_mov_b32 m0, %2\n\ts_nop 0\n\tglobal_load_lds_dwordx4 %0, %1 offset:%3" :: "v"(voff), "s"(sbase), "s"(lds_base), "n"(OFF) : "memory", "m0");
}
DEVI void gldsv(const void* p, unsigned lds_base) {
  asm volatile("s_mov_b32 m0, %1\n\ts_nop 0\n\tglobal_load_lds_dwordx4 %0, off" :: "v"(p), "s"(lds_base) : "memory", "m0");
}
DEVI void gldsc4(unsigned voff, const void* sbase, unsigned lds_base) {
  asm volatile("s_mov_b32 m0, %2\n\ts_nop 0\n\tglobal_load_lds_dwordx4 %0, %1 sc1" :: "v"(voff), "s"(sbase), "s"(lds_base) : "memory", "m0");
}
DEVI void gldsc1(unsigned voff, const void* sbase, unsigned lds_base) {
  asm volatile("s_mov_b32 m0, %2\n\ts_nop 0\n\tglobal_load_lds_dword %0, %1 sc1" :: "v"(voff), "s"(sbase), "s"(lds_base) : "memory", "m0");
}
DEVI float xhalf_sum(float v) {
  auto t = __builtin_amdgcn_permlane32_swap(__float_as_uint(v), __float_as_uint(v), false, false);
  return __uint_as_float(t[0]) + __uint_as_float(t[1]);
}
DEVI u32x4 ldc(rsrc_t rs, int vo, int so) { return __builtin_amdgcn_raw_buffer_load_b128(rs, vo, so, 16); }
DEVI u32x2 ldc2(rsrc_t rs, int vo, int so) { return __builtin_amdgcn_raw_buffer_load_b64(rs, vo, so, 16); }
DEVI unsigned ldc1(rsrc_t rs, int vo, int so) { return __builtin_amdgcn_raw_buffer_load_b32(rs, vo, so, 16); }
DEVI void stc(rsrc_t rs, int vo, int so, u32x4 v) { __builtin_amdgcn_raw_buffer_store_b128(v, rs, vo, so, 16); }
DEVI void stc2(rsrc_t rs, int vo, int so, u32x2 v) { __builtin_amdgcn_raw_buffer_store_b64(v, rs, vo, so, 16); }
DEVI void stc1(rsrc_t rs, int vo, int so, unsigned v) { __builtin_amdgcn_raw_buffer_store_b32(v, rs, vo, so, 16); }
DEVI u32x4 ldck(rsrc_t rs, int vo, int so, int kb) { return ldc(rs, vo + (kb & 3) * 1024, so + (kb >> 2) * 4096); }
template <int KB> DEVI void dma(rsrc_t rs, char* dst, int vo, int so) {
  __builtin_amdgcn_raw_ptr_buffer_load_lds(rs, (ldsp)(dst + (KB >> 2) * 4096), 16, vo, so + (KB >> 2) * 4096, (KB & 3) * 1024, 16);
}
DEVI float lsel(unsigned long long m, float t, float f) { float r; asm("v_cndmask_b32_e64 %0, %1, %2, %3" : "=v"(r) : "v"(f), "v"(t), "s"(m)); return r; }
DEVI float fmax3(float a, float b, float c) { return fmaxf(fmaxf(a, b), c); }
DEVI float fmin3(float a, float b, float c) { return fminf(fminf(a, b), c); }
DEVI unsigned sum4(unsigned s, const u32x4& f) { s = (s + f[0] + f[1]) + f[2] + f[3]; asm volatile("" : "+v"(s)); return s; }
DEVI unsigned wsum(unsigned v) {
  v += __builtin_amdgcn_update_dpp(0u, v, 0x111, 0xf, 0xf, false);
  v += __builtin_amdgcn_update_dpp(0u, v, 0x112, 0xf, 0xf, false);
  v += __builtin_amdgcn_update_dpp(0u, v, 0x114, 0xf, 0xf, false);
  v += __builtin_amdgcn_update_dpp(0u, v, 0x118, 0xf, 0xf, false);
  v += __builtin_amdgcn_update_dpp(0u, v, 0x142, 0xa, 0xf, false);
  v += __builtin_amdgcn_update_dpp(0u, v, 0x143, 0xc, 0xf, false);
  return v;
}

template <int I> DEVI void wsum_step(unsigned& v) {
  constexpr int ctl[6] = {0x111, 0x112, 0x114, 0x118, 0x142, 0x143}, rm[6] = {0xf, 0xf, 0xf, 0xf, 0xa, 0xc};
  v += __builtin_amdgcn_update_dpp(0u, v, ctl[I], rm[I], 0xf, false);
}

#define FINE 0
#define FST(sel, i)
#define RAW(i, v)
#define STAMP(i)
#define CSTAMP(i)
#define COUNT(i)
#define BUD(i)

template <class T>
DEVI void pack16(const T& v, u32x4& a, u32x4& b) {
#pragma unroll
  for (int i = 0; i < 4; i++) { a[i] = pk2(v[2 * i], v[2 * i + 1]); b[i] = pk2(v[8 + 2 * i], v[9 + 2 * i]); }
}
template <int I> struct ic { static constexpr int v = I; constexpr operator int() const { return I; } };
template <int... I> struct iseq {};
template <int N, int... I> struct mkseq : mkseq<N - 1, N - 1, I...> {};
template <int... I> struct mkseq<0, I...> { typedef iseq<I...> type; };
template <class F, int... I> DEVI void sfor_impl(F& f, iseq<I...>) { (f(ic<I>()), ...); }
template <int N, class F> DEVI void sfor(F f) { sfor_impl(f, typename mkseq<N>::type()); }

struct Softmax {
  float mx, mn; f32x2 mm, sv, ss, a[2], e[16];
  DEVI f32x2 arg(float v) { const f32x2 t = {v, v}; return __builtin_elementwise_fma(t, sv, mm); }
  DEVI void step(int i, const f32x16& p, float sc, u32x4 (&ph)[4]) {
    if (i == 0) { mx = fmax3(p[0], p[1], p[2]); mn = fmin3(p[0], p[1], p[2]); mx = fmax3(mx, p[3], p[4]); mn = fmin3(mn, p[3], p[4]); sv[0] = sc; sv[1] = -sc; }
    else if (i < 3) { int b = 5 + 4 * (i - 1); mx = fmax3(mx, p[b], p[b + 1]); mn = fmin3(mn, p[b], p[b + 1]); mx = fmax3(mx, p[b + 2], p[b + 3]); mn = fmin3(mn, p[b + 2], p[b + 3]); }
    else if (i == 3) { mx = fmax3(mx, p[13], p[14]); mn = fmin3(mn, p[13], p[14]); mm[0] = fmaxf(mx, p[15]) * -sc; mm[1] = fminf(mn, p[15]) * sc; a[0] = arg(p[0]); }
    else if (i < 20) {
      int r = i - 4;
      if (r < 15) { a[(r + 1) & 1] = arg(p[r + 1]); SB(); }
      e[r][0] = __builtin_amdgcn_exp2f(a[r & 1][0]); e[r][1] = __builtin_amdgcn_exp2f(a[r & 1][1]);
      SB();
      if (r == 2) ss = e[0] + e[1]; else if (r > 2) ss += e[r - 1];
    } else if (i == 20) { ss += e[15]; ss[0] = __builtin_amdgcn_rcpf(ss[0]); ss[1] = __builtin_amdgcn_rcpf(ss[1]); }
    else if (i < 29) {
      int t = i - 21;
      const f32x2 u = e[2 * t] * ss, v = e[2 * t + 1] * ss;
      ph[t >> 2][t & 3] = pk2(u[0], v[0]);
      ph[2 + (t >> 2)][t & 3] = pk2(u[1], v[1]);
    }
  }
};

template <int C> DEVI u32x4 tr_readc(const char* p) {
  typedef __attribute__((address_space(3))) s16x4* lp;
  s16x4 a = __builtin_amdgcn_ds_read_tr16_b64_v4i16((lp)(p));
  s16x4 b = __builtin_amdgcn_ds_read_tr16_b64_v4i16((lp)(p + C));
  u32x2 ua = __builtin_bit_cast(u32x2, a), ub = __builtin_bit_cast(u32x2, b);
  u32x4 r = {ua[0], ua[1], ub[0], ub[1]};
  return r;
}
DEVI u32x4 tr_read2(const char* p, const char* p2) {
  typedef __attribute__((address_space(3))) s16x4* lp;
  s16x4 a = __builtin_amdgcn_ds_read_tr16_b64_v4i16((lp)(p));
  s16x4 b = __builtin_amdgcn_ds_read_tr16_b64_v4i16((lp)(p2));
  u32x2 ua = __builtin_bit_cast(u32x2, a), ub = __builtin_bit_cast(u32x2, b);
  u32x4 r = {ua[0], ua[1], ub[0], ub[1]};
  return r;
}

DEVI u32x4 aug_tok(float mean, float ia, int hk) {
  unsigned mh = pk2(-mean, ia);
  unsigned ml = pk2(-mean - bflo(mh), ia - bfhi(mh));
  u32x4 t;
  t[0] = __builtin_amdgcn_perm(ml, mh, 0x05040100u);
  t[1] = mh;
  t[2] = ml >> 16;
  t[3] = 0;
  if (hk) { t[0] = 0; t[1] = 0; t[2] = 0; }
  return t;
}
DEVI unsigned cs_pack(float cs) { unsigned h = pk2(cs, 0.f); return pk2(cs, cs - bflo(h)); }
DEVI u32x4 aug_w(unsigned csp, unsigned b) {
  u32x4 t;
  t[0] = __builtin_amdgcn_perm(csp, csp, 0x01000100u);
  t[1] = __builtin_amdgcn_perm(b, csp, 0x05040302u);
  t[2] = b & 0xffffu;
  t[3] = 0;
  return t;
}

struct Cx {
  rsrc_t rs; char* ws; char *A, *B, *C;
  int lane, w, tok, hk, g, c, cb, th, vo, gb;
  int fv16, fv8, fvz, f63, oo, ho, fo, xo, so, fxo;
  unsigned N, tk_kv, tk_oh, tk_x, ka[2], po; const char* pc;
  int budget;
};

DEVI void put_flag(Cx& cx, int so, int i) { u32x2 v = {cx.N + i, cx.N + i}; stc2(cx.rs, cx.f63, so, v); }
DEVI void put_final(Cx& cx, int so, unsigned sum) { u32x2 v = {cx.N + 7, cx.N - wsum(sum)}; stc2(cx.rs, cx.f63, so, v); }
template <int NP> DEVI void poll_wait(Cx& cx, unsigned a, int so, unsigned tk, int i) {
  unsigned t; int s;
  asm volatile("v_sub_u32 %0, %3, %4\n\t"
               "v_cmp_ne_u32 vcc, %9, %0\n\t"
               "s_and_b32 %1, vcc_lo, %8\n\t"
               "s_cbranch_scc0 2f\n"
               "1:\n\t"
               "s_cmp_eq_u32 %2, 0\n\t"
               "s_cbranch_scc1 2f\n\t"
               "buffer_load_dword %0, %5, %6, %7 offen sc1\n\t"
               "s_sub_u32 %2, %2, 1\n\t"
               "s_waitcnt vmcnt(0)\n\t"
               "v_sub_u32 %0, %0, %4\n\t"
               "v_cmp_ne_u32 vcc, %9, %0\n\t"
               "s_and_b32 %1, vcc_lo, %8\n\t"
               "s_cbranch_scc1 1b\n"
               "2:"
               : "=&v"(t), "=&s"(s), "+s"(cx.budget)
               : "v"(a), "v"(tk), "v"(NP == 16 ? cx.fv16 : cx.fv8), "s"(cx.rs), "s"(so), "n"((1 << NP) - 1), "n"(i)
               : "vcc", "scc", "memory");
}
DEVI void kv_wait(Cx& cx, u32x4& q, int so) {
  int s; unsigned f = q[2];
  asm volatile("v_sub_u32 %0, %0, %4\n\t"
               "v_cmp_ne_u32 vcc, 7, %0\n\t"
               "s_and_b32 %2, vcc_hi, 0xffff\n\t"
               "s_cbranch_scc0 2f\n"
               "1:\n\t"
               "s_cmp_eq_u32 %3, 0\n\t"
               "s_cbranch_scc1 2f\n\t"
               "buffer_load_dword %0, %5, %6, %7 offen offset:8 sc1\n\t"
               "s_sub_u32 %3, %3, 1\n\t"
               "s_waitcnt vmcnt(0)\n\t"
               "v_sub_u32 %0, %0, %4\n\t"
               "v_cmp_ne_u32 vcc, 7, %0\n\t"
               "s_and_b32 %2, vcc_hi, 0xffff\n\t"
               "s_cbranch_scc1 1b\n\t"
               "buffer_load_dwordx4 %1, %5, %6, %7 offen sc1\n\t"
               "s_waitcnt vmcnt(0)\n"
               "2:"
               : "+v"(f), "+v"(q), "=&s"(s), "+s"(cx.budget)
               : "v"(cx.tk_kv), "v"(cx.fvz), "s"(cx.rs), "s"(so)
               : "vcc", "scc", "memory");
}
#define FORCE_BAD 0
DEVI bool bad(Cx& cx, unsigned sum) { return __builtin_amdgcn_readlane(wsum(sum), 63) + (FORCE_BAD ? cx.w & 1 : 0) != 0; }
DEVI bool again(Cx& cx, unsigned sum) {
  const unsigned r = __builtin_amdgcn_readlane(wsum(sum), 63);
  return (r != 0) & (cx.budget >= 64);
}
DEVI void spend(Cx& cx, int id) {
  asm volatile("" ::: "memory"); cx.budget = cx.budget >= 64 ? cx.budget - 64 : 0; }
#define D_O 8
#define D_X1 6
#define D_X2 2
#define CROT 0
#define THB 8
#define PRE 32
#define TOUCH 1
template <int N> DEVI void nap() { if (N > 0) { SB(); __builtin_amdgcn_s_sleep(N); SB(); } }
DEVI unsigned usel(unsigned long long m, unsigned t, unsigned f) { unsigned r; asm("v_cndmask_b32_e64 %0, %1, %2, %3" : "=v"(r) : "v"(f), "v"(t), "s"(m)); return r; }
DEVI void mail_wait(Cx& cx, unsigned a, int mvo, int so, unsigned v) {
  int s;
  asm volatile("v_cmp_ne_u32 vcc, %3, %0\n\t"
               "s_and_b32 %1, vcc_lo, 0xffff\n\t"
               "s_cbranch_scc0 2f\n"
               "1:\n\t"
               "s_cmp_eq_u32 %2, 0\n\t"
               "s_cbranch_scc1 2f\n\t"
               "buffer_load_dword %0, %4, %5, %6 offen sc1\n\t"
               "s_sub_u32 %2, %2, 1\n\t"
               "s_waitcnt vmcnt(0)\n\t"
               "v_cmp_ne_u32 vcc, %3, %0\n\t"
               "s_and_b32 %1, vcc_lo, 0xffff\n\t"
               "s_cbranch_scc1 1b\n"
               "2:"
               : "+v"(a), "=&s"(s), "+s"(cx.budget)
               : "s"(v), "v"(mvo), "s"(cx.rs), "s"(so)
               : "vcc", "scc", "memory");
}
DEVI void kv_write_v(Cx& cx, const f32x16& accv, float rstd) {
  float vn[16];
#pragma unroll
  for (int r = 0; r < 16; r++) vn[r] = accv[r] * rstd;
  u32x4 v0, v1; pack16(vn, v0, v1);
  char* p = cx.C + 2 * cx.hk * INBOX + C_V + (32 * cx.w + cx.tok) * 16;
  *(u32x4*)p = v0; *(u32x4*)(p + INBOX + 128) = v1;
}
template <int VMW>
DEVI void kv_tail(Cx& cx, const u32x4 (&phk)[4], int kvb) {
  char* p = cx.C + 2 * cx.hk * INBOX + (32 * cx.w + cx.tok) * 16;
  *(u32x4*)p = phk[0]; *(u32x4*)(p + 2176) = phk[1]; *(u32x4*)(p + INBOX) = phk[2]; *(u32x4*)(p + INBOX + 2176) = phk[3];
  const int G = cx.lane >> 4, d = cx.lane & 15, q4 = d >> 2, p4 = cx.lane & 3;
  const int ro = (4 * G + q4) * 16 + (p4 & 1) * 8;
  const char* pa = cx.C + cx.w * INBOX + (p4 >> 1) * 2176 + ro;
  const char* pb = cx.C + ((cx.w & 2) + (p4 >> 1)) * INBOX + C_V + (p4 >> 1) * 128 + ro;
  if (VMW >= 0) vm_wait<(VMW >= 0 ? VMW : 0)>();
  lds_barrier();
  u32x4 a[4], b[4];
#pragma unroll
  for (int t = 0; t < 4; t++) { a[t] = tr_readc<256>(pa + t * 512); b[t] = tr_readc<256>(pb + t * 512); }
  const u32x4 ones = {0x3f803f80u, 0x3f803f80u, 0x3f803f80u, 0x3f803f80u};
  f32x4 kv = {}, z = {};
#pragma unroll
  for (int t = 0; t < 4; t++) { kv = MFMA16(a[t], b[t], kv); z = MFMA16(a[t], ones, z); }
  const u32x2 k2 = {pk2(kv[0], kv[1]), pk2(kv[2], kv[3])}, z2 = {pk2(z[0], z[1]), pk2(z[2], z[3])};
  const int e = cx.w * 16 + cx.g;
  stc2(cx.rs, (G >> 1) * 256 + d * 16 + (G & 1) * 8, kvb + e * 512, k2);
  const unsigned cs = -7u - wsum(k2[0] + k2[1] + usel(0x1000900010001000ull, z2[0] + z2[1], 0u));
  const u32x2 zl = {usel(1ull << 63, cx.N + 7, z2[0]), usel(1ull << 63, cs, z2[1])};
  stc2(cx.rs, d >= 12 ? (d - 12) * 32 + G * 8 : OOB, kvb + KV_F + e * FS, zl);
}
struct KvIn { u32x4 pa[8], pz; };
struct Misc { u32x4 xa, xb, ba, bb, cs[4], msg; int pw, ra[2]; unsigned jv; u16 b1v[4]; };
DEVI u32x4 kv_poll(Cx& cx, int kvb) { return ldc(cx.rs, cx.fvz, kvb + KV_F + cx.w * 16 * FS); }
DEVI void kv_load(Cx& cx, KvIn& k, int kvb) {
#pragma unroll
  for (int m = 0; m < 8; m++) k.pa[m] = ldck(cx.rs, cx.vo, kvb + cx.w * 8192, m);
}

#define WOFO(k) ((k) < 8 ? 2 * (k) + 1 : 2 * ((k) - 8))
#define W2KEEP 45
template <int B, class K> DEVI void wload(Cx& cx, K, u32x4 (&w2f)[56], Misc& mi) {
  constexpr int k = B + K::v;
  if (k < 0) return;
  if (k < 4) mi.cs[k & 3] = ldck(cx.rs, cx.vo, CS1 + cx.c * 16384 + cx.w * 4096, k & 3);
  else if (k < 16) dma<((k - 4) & 15)>(cx.rs, cx.B + cx.w * 12288, cx.vo, WT_1 + cx.c * W1_C + 65536 + cx.w * 12288);
  else if (k < 32) dma<(k & 15)>(cx.rs, cx.A + cx.w * 16384, cx.vo, WT_1 + cx.c * W1_C + cx.w * 16384);
  else if (k < W2KEEP) w2f[k] = ldck(cx.rs, cx.vo, WT_2 + (cx.cb * 4 + cx.w) * 57344, k);
}
template <int K> DEVI void rawld(Cx& cx, rsrc_t r, int vo, int so, int bs) {
  __builtin_amdgcn_raw_ptr_buffer_load_lds(r, (ldsp)(cx.A + cx.w * 16384 + (K >> 2) * 4096), 16, vo, so + (K >> 2) * bs + (K & 3) * 7168, (K & 3) * 1024, 0);
}
template <int K> DEVI void rawlda(Cx& cx, rsrc_t r, int vo, int so, int bs) {
  if (!(K & 3)) asm volatile("s_mov_b32 m0, %0" :: "s"((unsigned)(size_t)cx.A + cx.w * 16384 + (K >> 2) * 4096) : "memory", "m0");
  asm volatile("s_nop 0\n\tbuffer_load_dwordx4 %0, %1, %2 offen offset:%3 lds" :: "v"(vo), "s"(r), "s"(so + (K >> 2) * bs + (K & 3) * 7168), "n"((K & 3) * 1024) : "memory");
}
DEVI u32x4 rawrd(Cx& cx, const Misc& mi, int blk, int e) { return tr_readc<512>(cx.C + mi.ra[e & 1] + blk * 4096 + (e >> 1) * 1024); }
#define JS 13
#define ROTM c + 2 * g
#define XT 0
#define R2B 1
#define RKD 1
#define RK 0
#define RA 12
#define QJ 1
#define QR 28
#define QM 2
#define QS 15
#define QCS 12
#define SND 26
#define MBI 8
DEVI f32x4 sq4(const u32x4& f, int h, f32x4 c) {
  const u32x2 t = {f[2 * h], f[2 * h + 1]};
  return __builtin_amdgcn_mfma_f32_4x4x4bf16_1k(__builtin_bit_cast(s16x4, t), __builtin_bit_cast(s16x4, t), c, 0, 0, 0);
}
#define KQP0 31
#define KQW0 12

#define S1SCH \
  120, 121, 122,  123, 124, 125,  134, 135,  60,  130, 131,   0,  132, 133,  61,  144, 145,  62,  154, 155,  63,  140, 141,  64, \
  142, 143,  65,  164, 165,   5,  174, 175,   0,  150, 151,   0,  152, 153,   0,  116, 126,   0,  136, 146,   0,  160, 161,   0, \
  162, 163,   0,  156, 166,   0,  176,   0,   0,  170, 171,   0,  172, 173,  40,   31,  32,   0,    0,   0,   0,   10,  11,   0, \
   12,  13,   0,    0,   0,   0,    0,   0,   0,    0,   0,  50,   70,   0,   0,   71,   0,   0,   72,   0,   0,   73,   0,   0
#define S1PRE 2, 3, 100, 101, 102, 103, 104, 105, 110, 111, 30, 112, 113, 114, 115, 4, 106
constexpr int S1P = 17;
constexpr int s1_op[S1P + 96] = {S1PRE, S1SCH};
constexpr int s1_key(int o) { return o >= 100 ? o / 10 * 10 + (o % 10 >= 4) + (o % 10 == 6) : o >= 10 && o < 20 ? 10 : o >= 70 && o < 80 ? 70 + (o >= 74) : (o >= 20 && o < 40) || o >= 50 ? 0 : o; }
constexpr bool s1_m0(int i) {
  for (int k = i - 1; k >= 0; k--) if (s1_key(s1_op[k])) return s1_key(s1_op[k]) != s1_key(s1_op[i]);
  return true;
}
constexpr int s1_pos(int o) { for (int k = 0; k < S1P + 96; k++) if (s1_op[k] == o) return k; return -1; }
constexpr int R0A = s1_pos(74) >= 0 ? 8 : s1_pos(70) >= 0 ? 4 : 0;
constexpr int slot(int j) { return (((j & 3) << 1) | (j >> 2)) * 8192; }
constexpr int s1_step(int o) { return (s1_pos(o) - S1P) / 3; }
constexpr int s1_cnt(int n, int lo, int hi, int e = 0) {
  int last = e ? s1_pos(e) : -1, r = 0;
  for (int o = lo; o <= hi; o++) if (s1_pos(o) > last) last = s1_pos(o);
  for (int k = last + 1; k < S1P + 3 * n; k++) r += s1_op[k] != 0;
  return r;
}
constexpr int s1_last(int lo, int hi, int d) {
  int last = -1, r = 0;
  for (int o = lo; o <= hi; o += d) if (s1_pos(o) > last) last = s1_pos(o);
  for (int k = last + 1; k < S1P + 96; k++) r += s1_op[k] != 0;
  return r;
}
template <int OFF, bool M> DEVI void gldm(unsigned voff, const void* sbase, unsigned lds_base) {
  if (M) gldso<OFF>(voff, sbase, lds_base);
  else asm volatile("global_load_lds_dwordx4 %0, %1 offset:%2" :: "v"(voff), "s"(sbase), "n"(OFF) : "memory");
}

DEVI void stage1a(Cx& cx, const u16* __restrict__ x, const u16* __restrict__ wqkv, const u16* __restrict__ wphi,
                  const u16* __restrict__ wo, const u16* __restrict__ w1, const u16* __restrict__ b1, const u16* __restrict__ w2,
                  const u16* __restrict__ b2, u32x4 (&phq)[4], unsigned (&csw)[3], u32x4 (&aphi)[2], KvIn& kin, u32x4 (&w2f)[56],
                  u32x4 (&wof)[16], Misc& mi) {
  const int lane = cx.lane, w = cx.w, tok = cx.tok, hk = cx.hk, g = cx.g, c = cx.c;
  const int rot = (ROTM) & 7;
  const rsrc_t rs = cx.rs;
  const unsigned lA = (unsigned)(size_t)cx.A, lB = (unsigned)(size_t)cx.B, lC = (unsigned)(size_t)cx.C;
  char* xs = cx.C + w * INBOX;
  char* stg = cx.B + 32768 + w * 4096;
  const unsigned lstg = lB + 32768 + w * 4096;
  unsigned vx[4];
#pragma unroll
  for (int i = 0; i < 4; i++) vx[i] = (8 * i + (lane >> 3)) * 1024 + (((lane & 7) ^ ((4 * i + (lane >> 4)) & 7)) * 16) - i * 1024;
  const char* xb = (const char*)(x + (size_t)(128 * g + 32 * w) * 512);
  const unsigned xl = lC + w * INBOX;
  const unsigned wvo = (lane >> 2) * 3072 + (lane & 3) * 16;
  const char* wb = (const char*)(wqkv + (size_t)(16 * w) * 1536 + 32 * c);
  auto cload = [&](auto kk) LAMBDA {
    constexpr int j = s1_op[kk.v] / 10 - 10, i = s1_op[kk.v] % 10; constexpr bool M = s1_m0(kk.v);
    const int cj = (j + rot) & 7;
    if (i < 4) gldm<(i & 3) * 1024, M>(vx[i & 3], xb + cj * 128, xl + (j % 3) * 4096);
    else if (i < 6) gldm<(i & 1) * 1024, M>(wvo + 1024, wb + cj * (64 * 3072), lA + slot(j) + w * 2048);
    else gldm<0, M>(wvo, wb + cj * (64 * 3072), lB + j * 4096 + w * 1024);
  };
  const unsigned tvo = lane < 16 ? TOKR + g * 128 + lane * 4 : TOKC + cx.cb * 128 + ((lane - 16) & 31) * 4;
  const unsigned mvo = MBX + (g * 16 + c) * 512 + (lane & 31) * 16;
  asm volatile("" :: "s"(xb), "s"(wb), "s"(cx.ws), "s"(cx.ka[0]), "s"(cx.ka[1]));
  FST(12, 2);
  sfor<s1_pos(30)>([&](auto kk) LAMBDA {
    constexpr int o = s1_op[kk.v];
    if (o == 2) gldsc1(tvo, cx.ws, lstg); else if (o == 3) gldsc4(mvo, cx.ws, lstg + 1024); else cload(kk);
  });
  SB(); FST(12, 3);
  cx.gb = GRP + cx.g * G_S;
  cx.fv16 = cx.lane < 16 ? cx.lane * FS + 120 : OOB; cx.fv8 = cx.lane < 8 ? cx.lane * FS + 120 : OOB; cx.f63 = cx.lane >= 48 ? (cx.lane - 48) * 8 : OOB;
  cx.fvz = cx.lane < 32 ? (cx.lane >> 1) * FS + (cx.lane & 1) * 16 : cx.lane < 48 ? (cx.lane - 32) * FS + 112 : OOB;
  {
    const int kq = cx.c >> 2, tx = (w & 1) ^ (kq >> 1), ln = (w >> 1) * 4 + kq, tb = 2 * cx.th + (w >> 1), blk = 2 * cx.cb + (w & 1);
    cx.oo = (ln * 16 + tx * 8 + (cx.c & 3) * 2) * 1024;
    cx.ho = (ln * 56 + (cx.c & 3) * 2 + tx) * 1024;
    cx.fo = (ln * 8 + (cx.c & 3) * 2 + (w & 1)) * FS;
    cx.xo = (tb * 32 + 2 * blk) * 1024;
    cx.so = tb * 4096 + blk * 256;
    cx.fxo = (tb * 16 + blk) * FS;
  }
  const int ch = (lane >> 4) & 1, q4 = (lane & 15) >> 2, p4 = lane & 3;
  const int cperm = (16 * (p4 & 1) + 4 * (2 * ch + (p4 >> 1))) * 2;
  int r_tr, r_a, r_b, r_c, r_t3, r_c3, r_dst, r_d1, r_cs = OOB; unsigned r_d3;
  const char *src, *src2, *src3; unsigned v0, v1, v2;
  {
    const int cb = cx.cb, hf = g & 1, cc = 8 * cx.th + (g >> 1), kq = cc >> 2;
    const char* s2 = (const char*)(w2 + (size_t)(112 * cc) * 512 + 64 * cb);
    const int d2 = WT_2 + ((cb * 4 + kq) * 28 + (cc & 3)) * 2048;
    if (w < 2) {
      const int n = 2 * g + w;
      src = src2 = (const char*)(w1 + (size_t)(32 * g + 8 * w) * 1792 + 112 * c);
      auto vo = [&](int t) LAMBDA { const int i = (t * 4682) >> 16; return (i + (i & 8)) * 3584 + (t - 14 * i) * 16; };
      v0 = vo(lane); v1 = vo(lane + 64); v2 = vo(lane + 128);
      src3 = hk ? s2 + (96 + 8 * w + (tok >> 2)) * 1024 + hf * 64 + p4 * 16 : src + vo(lane + 192);
      r_tr = (8 * hk + q4) * 224 + cperm; r_a = 64; r_b = 128; r_c = 896;
      r_t3 = hk ? 3584 + q4 * 64 + cperm : (8 * ch + q4) * 224 + 192 + (8 * (p4 & 1) + 4 * (p4 >> 1)) * 2; r_c3 = hk ? 256 : 896;
      r_dst = WT_1 + c * W1_C + n * 2048; r_d1 = 65536 - n * 512;
      r_d3 = hk ? d2 + 24 * 2048 + (hf ^ (kq & 1)) * 1024 + (32 * w + tok) * 16 : r_dst + r_d1 + 1024 + lane * 16;
      r_cs = CS1 + c * 16384 + n * 512;
    } else {
      v0 = (lane >> 3) * 1024 + (lane & 7) * 16; r_c = r_c3 = 512;
      if (w == 2) {
        src = src2 = s2 + hf * 65536; v1 = v0 + 8192; v2 = v0 + 16384; src3 = src + v0 + 24576;
        r_tr = (16 * hk + q4) * 128 + cperm; r_b = 1024;
        r_dst = d2 + hf * 32768; r_d1 = 8192;
      } else {
        src = s2 + (32 + 8 * hf) * 1024; src2 = (const char*)(wo + (size_t)(32 * cc + 8 * hf) * 512 + 64 * cb);
        v1 = v0 + 16384; v2 = v0; src3 = src2 + v1;
        r_tr = (8 * hk + q4) * 128 + cperm; r_b = 2048;
        r_dst = d2 + (8 + 4 * hf) * 2048; r_d1 = WT_O + ((cb * 4 + kq) * 8 + ((2 * cc) & 7) + hf) * 2048 - r_dst;
      }
      r_tr += (kq & 1) * 64; r_a = (kq & 1) ? -64 : 64;
      r_t3 = r_tr + r_a + r_b; r_d3 = r_dst + r_d1 + 1024 + lane * 16;
    }
    r_dst = RFL(r_dst); r_d1 = RFL(r_d1); r_cs = RFL(r_cs); src2 -= 2048; src3 -= 3072; v1 -= 1024;
    asm volatile("" : "+v"(v0), "+v"(v1), "+v"(v2), "+v"(src3), "+v"(r_tr), "+v"(r_t3), "+v"(r_d3), "+s"(src), "+s"(src2), "+s"(r_dst), "+s"(r_d1), "+s"(r_cs));
  }
  FST(12, 4);
  const int xro = tok * 128 + (((2 * hk) ^ ((tok >> 1) & 7) ^ (4 * (w >> 1) + (w & 1))) * 16);
  const char* wk = cx.A + hk * 2048 + q4 * 64 + cperm;
  const char* wq = cx.B + hk * 1024 + q4 * 64 + cperm;
  const char* wks[4];
#pragma unroll
  for (int s = 0; s < 4; s++) { const int t = s ^ w; wks[s] = wk + (t >> 1) * 4096 + (t & 1) * 512; }
  const unsigned on1 = (ch == ((lane >> 2) & 1)) ? 0x3f803f80u : 0u;
  const u32x4 sel = {on1, on1, on1, on1};
  int mdst = w != 0 || lane >= 48 ? OOB
                 : lane < 16 ? MBX + (g * 16 + lane) * 512 + c * 8
                             : MBX + (((lane - 16) & 15) * 16 + ((c & ~THB) | ((lane - 16) >> 4) * THB)) * 512 + (16 + g + 16 * cx.th) * 8;
  unsigned N = 0, sent = 0;
  u32x4 xf[33], ka[3], va[3], rf[4];
  f32x16 acck = {}, accv = {};
  f32x4 csk = {}, csv = {}, s1v = {}, q0 = {}, q1 = {}, cj[4];
  f32x2 cso[3] = {};
  auto rdx = [&](int n) LAMBDA { if ((XT & 1) && n > 1) { xf[n] = xf[n & 1]; return; } xf[n] = *(const u32x4*)(xs + ((n >> 2) % 3) * 4096 + (xro ^ ((4 * ((n >> 1) & 1) + (n & 1)) * 16))); };
  auto rdk = [&](int n) LAMBDA { ka[n % 3] = tr_readc<256>(wks[n & 3] + slot(n >> 2)); };
  auto rdv = [&](int n) LAMBDA { if (XT & 2) { va[n % 3] = ka[n % 3]; return; } va[n % 3] = tr_readc<256>(wks[n & 3] + 1024 + slot(n >> 2)); };
  unsigned tk0, tk1;
  const int kvb = KVA + c * KV_C;
  int tch;
  {
    const int t = lane >> 3;
    tch = (t < 5 ? G_FO + t * FHL + cx.fo : t == 5 ? G_FX1 + cx.fxo : t == 6 ? G_FX2 + cx.fxo : kvb + KV_L + KV_F + (w * 16 + g) * FS - cx.gb) + (lane & 7) * 16 + cx.gb;
  }
  int tc2 = lane < 48 ? tch : lane < 56 && w == 0 ? MB2 - G_L + (g * 16 + c) * FS + (lane & 7) * 16 : OOB;
  int tso = w == 0 && (lane == c || lane == 16 + 16 * cx.th + g) ? (int)tvo : OOB;
  const int col = 64 * cx.cb + 32 * (w & 1) + 16 * hk;
  const int pi = 16 * ((tok >> 2) & 1) + (tok & 3) + 4 * (tok >> 3), ph = 8 * ((tok >> 2) & 1) + (tok & 3) + 4 * ((tok >> 3) & 1);
  const char* bp = (const char*)(b1 + 112 * c);
  unsigned opi = 2 * pi, oph = 2 * ph + 192, ocol = 2 * col;
  asm volatile("" : "+v"(tch), "+v"(tc2), "+v"(tso), "+v"(mdst), "+v"(opi), "+v"(oph), "+v"(ocol));
  const int js = ((g >> 1) - rot) & 7;
  u32x4 wf0, wf1;
  auto rdwf = [&]() LAMBDA { const char* wsrc = wk + (w & 1) * 1024 + slot(js) + (g & 1) * 4096; wf0 = tr_readc<256>(wsrc); wf1 = tr_readc<256>(wsrc + 512); };
  const rsrc_t ro = __builtin_amdgcn_make_buffer_rsrc((void*)wo, 0, 512 * 1024, 0x00020000), r2 = __builtin_amdgcn_make_buffer_rsrc((void*)w2, 0, 1792 * 1024, 0x00020000);
  int rv = (lane >> 3) * 1024 + ((lane & 7) ^ (4 * ch)) * 16;
  asm volatile("" : "+v"(rv));
  auto issue = [&](auto kk) LAMBDA {
    constexpr int o = s1_op[kk.v]; constexpr bool M = s1_m0(kk.v);
    if (o >= 100) cload(kk);
    if (o == 10) gldm<0, M>(v0, src, xl + 8192);
    if (o == 11) gldm<1024, M>(v1, src, xl + 8192);
    if (o == 12) gldm<2048, M>(v2, src2, xl + 8192);
    if (o == 13) {
      if (M) asm volatile("s_mov_b32 m0, %0\n\ts_nop 0" :: "s"(xl + 8192) : "memory", "m0");
      asm volatile("global_load_lds_dwordx4 %0, off offset:3072" :: "v"(src3) : "memory");
    }
    if (o == 30) {
      N = __builtin_amdgcn_readlane(tk0, c) + 64;
      while (BALLOT(tk0 - N < 64u || tk1 - N < 64u) != 0) N += 64;
      stc1(rs, tso, 0, N);
      *(unsigned*)(stg + 768) = N;
    }
    if (o == 31 || o == 32) {
      const u32x4 v = {cx.N, cx.N, cx.N, cx.N};
      if (o == 31) stc(rs, tch, 0, v); else stc(rs, tc2, G_L, v);
    }
    if (o == 4) gldso<0>(lane * 16, wphi + c * 512, lstg + 2048);
    if (o == 5) gldso<0>(cx.po, cx.pc, lstg + 1024);
    if (o >= 70 && o < 80) { static_assert(o < 70 || o >= 80 || M == !((o - 70) & 3), ""); rawlda<((o - 70) & 15)>(cx, ro, rv, w * 131072 + cx.cb * 128, 32768); }
    if (o == 40) gldsc1(tvo, cx.ws, lstg + 256);
    if (o >= 60 && o < 64) mi.b1v[o & 3] = *(const u16*)(bp + (o == 63 ? oph : opi) + (o == 63 ? 0 : 64 * (o - 60)));
    if (o == 64) mi.ba = *(const u32x4*)((const char*)b2 + ocol);
    if (o == 65) mi.bb = *(const u32x4*)((const char*)b2 + ocol + 16);
    if (o >= 50 && o < 60) {
      const u32x2 m = {tk0, N};
      stc2(rs, o == 50 || tk0 != sent ? mdst : OOB, 0, m);
      sent = tk0;
    }
    SB();
  };
  static_assert(s1_step(50) > SND && SND % 4 == 2 && s1_pos(30) < S1P && s1_pos(2) < s1_pos(3) && s1_pos(3) < s1_pos(30), "");
  static_assert(!R0A || s1_step(70) > 22, "");
  static_assert(s1_step(10) > 22 && s1_step(11) > 22 && s1_step(12) > 22 && s1_step(13) > 22, "");
  SB(); FST(12, 5);
  vm_wait<s1_pos(30) - s1_pos(3) - 1>();
  tk0 = *(const unsigned*)(stg + lane * 4); tk1 = *(const unsigned*)(stg + 1024 + lane * 8);
  sfor<S1P - s1_pos(30)>([&](auto kk) LAMBDA { issue(ic<s1_pos(30) + kk.v>()); });
  FST(12, 6); vm_wait<s1_cnt(0, 100, 105)>(); FST(12, 7); bar(); STAMP(1); FST(1, 0); FST(12, 8);
  rdx(0); rdk(0); rdv(0); rdx(1); rdk(1); rdv(1);
  SB();
  float s2 = 0.f;
  sfor<32>([&](auto nn) LAMBDA {
    constexpr int n = nn, j = n >> 2, s = n & 3;
    if (n < 31) FST(13, n + 1);
    if (s == 2 && j < 7) {
      constexpr int e = n == SND ? 40 : j == 1 ? 4 : 0;
      constexpr int cnt = s1_cnt(n, 110 + 10 * j, 115 + 10 * j, e);
      static_assert(n != SND || s1_step(40) < n, "");
      FST(1, 2 * j + 1); FST(12, 9 + 2 * j); vm_wait<cnt>();
      if (j == 6) lds_barrier_n<5>(); else bar();
      FST(1, 2 * j + 2); FST(12, 10 + 2 * j);
    }
    constexpr bool cs = s == 0 || j == 7;
    acck = MFMA(ka[n % 3], xf[n], acck); SB();
    if (n == 25) { const f32x2 v = {csk[0], csv[0]}; *(f32x2*)(stg + 3072 + lane * 8) = v; SB(); }
    issue(ic<S1P + 3 * n>());
    if (n + 2 < 32) rdx(n + 2);
    if (n == 27) {
#pragma unroll
      for (int t = 0; t < 3; t++) cso[t] = *(const f32x2*)(cx.B + 32768 + (w ^ (t + 1)) * 4096 + 3072 + lane * 8);
    }
    if (n == 29) { cso[0] += cso[1] + cso[2]; asm volatile("" : "+v"(cso[0])); }
    if (n == 17 && R0A) rdwf();
    if (n == SND) tk0 = *(const unsigned*)(stg + 256 + lane * 4);
    if (n == 7) tk1 = *(const unsigned*)(cx.B + 32768 + 768);
    if (n == 9) cx.N = RFL(tk1);
    SB();
    if (cs) { csk = MFMA16(sel, ka[n % 3], csk); SB(); }
    if (n + 2 < 32) rdk(n + 2);
    SB();
    accv = MFMA(va[n % 3], xf[n], accv); SB();
    issue(ic<S1P + 3 * n + 1>());
    SB();
    if (cs) { csv = MFMA16(sel, va[n % 3], csv); SB(); }
    if (n + 2 < 32) rdv(n + 2);
    SB();
    if (n == 0) { q0 = sq4(xf[0], 0, q0); q1 = sq4(xf[0], 1, q1); s1v = MFMA16(sel, xf[0], s1v); SB(); }
    if (n < 31 && !(XT & 4)) { q0 = sq4(xf[n + 1], 0, q0); q1 = sq4(xf[n + 1], 1, q1); SB(); }
    if (n == 10) {
      const char* ap = stg + 2048 + hk * 512 + q4 * 32 + (2 * ch + (p4 >> 1)) * 8;
      aphi[0] = tr_readc<128>(ap); aphi[1] = tr_readc<128>(ap + 256);
    }
    if (n == 24) {
      const bool on = ((lane >> 2) & 1) == hk;
#pragma unroll
      for (int t = 0; t < 4; t++) { aphi[0][t] = on ? aphi[0][t] : 0u; aphi[1][t] = on ? aphi[1][t] : 0u; }
      asm volatile("" : "+v"(aphi[0]), "+v"(aphi[1]));
    }
    if (n == 31) {
      q0 += q1;
      const float u0 = lsel(0xCCCCCCCCCCCCCCCCull, q0[2], q0[0]), u1 = lsel(0xCCCCCCCCCCCCCCCCull, q0[3], q0[1]);
      s2 = xhalf_sum(lsel(0xAAAAAAAAAAAAAAAAull, u1, u0));
    }
    SB();
    if (n < 31 && !(XT & 4)) { s1v = MFMA16(sel, xf[n + 1], s1v); SB(); }
    issue(ic<S1P + 3 * n + 2>());
    if (n < 31) asm volatile("" : "+a"(acck), "+a"(accv), "+a"(csk), "+a"(csv), "+a"(s1v), "+a"(q0), "+a"(q1));
    SB();
  });
  STAMP(2); FST(1, 15); FST(11, 0);
  const float mean = s1v[0] * (1.f / 512.f);
  xf[32] = aug_tok(mean, 0.f, hk);
  csw[1] = cs_pack(csk[0] + cso[0][0]);
  acck = MFMA(aug_w(csw[1], 0u), xf[32], acck);
  csw[2] = cs_pack(csv[0] + cso[0][1]);
  accv = MFMA(aug_w(csw[2], 0u), xf[32], accv);
  const float rstd = __builtin_amdgcn_rsqf(fmaxf(s2 * (1.f / 512.f) - mean * mean, 0.f) + 1e-5f);
  const float sc = rstd * LOG2E;
  f32x16 p = {};
  { u32x4 b0, b1; pack16(acck, b0, b1); p = MFMA(aphi[0], b0, p); p = MFMA(aphi[1], b1, p); }
  SB();
  if (!R0A || js >= 4) rdwf();
  lds_barrier();
  STAMP(3); FST(11, 1);
  {
    Softmax sm; u32x4 phk[4];
    sfor<29>([&](auto ii) LAMBDA {
      constexpr int i = ii;
      sm.step(i, p, sc, phk);
      if (i == 2) kv_write_v(cx, accv, rstd);
      static_assert(RK + RKD * (15 - R0A) < 29, "");
      if (i >= RK && (i - RK) % RKD == 0 && (i - RK) / RKD < 16 - R0A) rawlda<(((i - RK) / RKD + R0A) & 15)>(cx, ro, rv, w * 131072 + cx.cb * 128, 32768);
      SB();
    });
    static_assert(s1_last(31, 32, 1) >= s1_last(10, 13, 1), "");
    STAMP(4); FST(11, 2);
    kv_tail<(s1_last(106, 176, 10) >= s1_last(170, 175, 1) ? -1 : s1_last(106, 176, 10) + 16 - R0A)>(cx, phk, kvb);
  }
  STAMP(5); FST(11, 3);
  {
    f32x16 accq = {}; f32x4 csq = {};
    u32x4 qa[3];
    int wqs[4];
#pragma unroll
    for (int s = 0; s < 4; s++) {
      const int t = s ^ w;
      wqs[s] = hk * 1024 + q4 * 64 + cperm + (t >> 1) * 2048 + (t & 1) * 512;
      asm volatile("" : "+v"(wqs[s]));
    }
    auto rdq = [&](int n) LAMBDA { qa[n % 3] = tr_readc<256>(cx.B + wqs[n & 3] + (n >> 2) * 4096); };
    rdq(0); rdq(1); rdq(2);
    u32x2 m;
    auto shake = [&]() LAMBDA {
      FST(11, 7);
      RAW(1, __builtin_popcountll(BALLOT(m[0] != cx.N && lane < 16))); RAW(2, __builtin_popcountll(BALLOT(m[0] != cx.N && lane >= 16 && lane < 48)));
      RAW(3, __builtin_popcountll(BALLOT(m[0] == cx.N && m[1] != sent && lane < 48))); RAW(4, 65536 - cx.budget);
      if (UNLIKELY(BALLOT((m[0] != cx.N || (w == 0 && m[1] != sent)) && lane < 48) != 0)) {
        while (true) {
          const bool valid = m[0] == cx.N;
          if (BALLOT(valid && m[1] != sent && lane < 48) != 0) {
            const u32x2 r = {m[1], cx.N};
            stc2(rs, valid && m[1] != sent ? mdst : OOB, 0, r);
            sent = valid ? m[1] : sent;
          }
          if (BALLOT(!valid && lane < 48) == 0) break;
          if (cx.budget == 0) break;
          cx.budget--;
          asm volatile("" ::: "memory");
          m = ldc2(rs, lane * 8, MBX + (g * 16 + c) * 512);
        }
        cx.budget = RFL(cx.budget);
        __builtin_amdgcn_s_waitcnt(0x0F70);
      }
      FST(11, 9); RAW(5, 65536 - cx.budget);
      const int e = lane >> 1, t = lane < 48 ? (int)m[1] : 0;
      mi.jv = mi.pw != OOB ? m[1] + 2 : 0u;
      cx.tk_kv = __builtin_amdgcn_ds_bpermute(((lane >> 4) == 2 ? 16 * cx.th + lane - 16 : 63) * 4, t);
      cx.tk_oh = __builtin_amdgcn_ds_bpermute((lane < 8 ? 4 * w + e : 63) * 4, t);
      cx.tk_x = __builtin_amdgcn_ds_bpermute((lane < 16 ? (THB == 8 ? 8 * (w >> 1) + e : 8 * (e >> 2) + 4 * (w >> 1) + (e & 3)) : 63) * 4, t);
    };
    static_assert(QJ > 0 && QM > QJ && QM < QJ + 3 && QM < QS && QS <= KQP0 && QR > QCS && QR <= KQP0 && QCS > 9 && QJ < QCS && RA > 11 && RA + 12 <= QR && QJ + 4 < RA && QS < RA + 4 && KQP0 < 32 + KQW0, "");
    const int wd = WT_QKV + c * WQ_C;
    const char* own = cx.B + w * 1024 + hk * 512 + q4 * 64 + cperm;
    u32x4 ca[2]; float co[3];
    ca[0] = tr_readc<256>(own); ca[1] = tr_readc<256>(own + 4096);
    SB();
    sfor<32>([&](auto nn) LAMBDA {
      constexpr int n = nn;
      accq = MFMA(qa[n % 3], xf[n], accq); SB();
      if (n == 0) {
        const int l = lane - 16;
        mi.pw = (unsigned)l < 32u ? ((l & 15) * 16 + ((c & ~THB) | (l >> 4) * THB)) * FS : OOB;
        asm volatile("" : "+v"(mi.pw));
      }
      if (n == 1 && w == 2) { const char* wsrc = wq + js * 4096 + (g & 1) * 2048; wf0 = tr_readc<256>(wsrc); wf1 = tr_readc<256>(wsrc + 512); }
      const char* jb = xs + 8192; const char* st = jb + r_tr;
      const f32x4 z4 = {};
      if (n == QJ) {
        FST(11, 4);
        vm_wait<s1_last(10, 13, 1) + 18 - R0A>();
        FST(11, 5);
        rf[0] = tr_read2(st, st + r_c); rf[1] = tr_read2(st + r_a, st + r_a + r_c);
      }
      if (n == QJ + 1) { rf[2] = tr_read2(st + r_b, st + r_b + r_c); rf[3] = tr_read2(jb + r_t3, jb + r_t3 + r_c3); }
      if (n == QJ + 3) { stc(rs, cx.vo, r_dst, rf[0]); stc(rs, cx.vo + 1024, r_dst, rf[1]); }
      if (n == QJ + 4) { stc(rs, cx.vo, r_dst + r_d1, rf[2]); stc(rs, r_d3, 0, rf[3]); }
      if (n == 8) { cj[0] = MFMA16(sel, rf[0], z4); cj[2] = MFMA16(sel, rf[2], z4); }
      if (n == 9) {
        const unsigned o2 = hk ? 0u : 0x3f803f80u;
        const u32x4 sl = {o2, o2, o2, o2};
        cj[1] = MFMA16(sel, rf[1], z4); cj[3] = MFMA16(sl, rf[3], z4);
      }
      if (n == 10) { const u32x2 cv = {__float_as_uint(hk ? cj[2][0] : cj[0][0]), __float_as_uint(hk ? cj[3][0] : cj[1][0])}; stc2(rs, lane * 8, r_cs, cv); }
      if (n == 11) {
        const int so = w < 2 ? wd + g * 4096 + w * 1024 : w == 2 ? wd + 65536 + g * 2048 : OOB;
        stc(rs, cx.vo, so, wf0); stc(rs, cx.vo, so + (w < 2 ? 2048 : 1024), wf1);
      }
      if (n == QR) { FST(11, 6); vm_wait<18>(); FST(11, 8); stc1(rs, lane ? OOB : 0, MB2 + (g * 16 + c) * FS + w * 4, cx.N + 2); }
      if (n == 3) {
        const int o = (int)(lA - lC) + w * 16384 + (16 * hk + q4) * 128 + cperm, h = ((w ^ (q4 >> 1)) & 1) * 64;
        mi.ra[0] = o + h; mi.ra[1] = o + 64 - h;
        asm volatile("" : "+v"(mi.ra[0]), "+v"(mi.ra[1]));
      }
      if (n == RA) { FST(11, 25); vm_wait<10>(); FST(11, 26); }
      if (n >= RA && n < RA + 8) {
        constexpr int b = ((n - RA) >> 1) & 3, e = 2 * ((n - RA) & 1);
        wof[4 * b + e] = rawrd(cx, mi, b, e); wof[4 * b + e + 1] = rawrd(cx, mi, b, e + 1);
      }
      if (n >= RA + 4 && n < RA + 12) {
        rawlda<((2 * (n - RA - 4)) & 15)>(cx, r2, rv, w * 458752 + cx.cb * 128, 114688);
        rawlda<((2 * (n - RA - 4) + 1) & 15)>(cx, r2, rv, w * 458752 + cx.cb * 128, 114688);
      }
      if (n == QM) m = ldc2(rs, lane * 8, MBX + (g * 16 + c) * 512);
      if (n == QS) shake();
      if (n == KQP0) kin.pz = kv_poll(cx, kvb);
      if (n == 9) *(float*)(stg + 3072 + lane * 4) = csq[0];
      if (n == QCS) {
        lds_barrier_n<2>();
#pragma unroll
        for (int t = 0; t < 3; t++) co[t] = *(const float*)(cx.B + 32768 + (w ^ (t + 1)) * 4096 + 3072 + lane * 4);
      }
      if (n == QCS + 4) { csw[0] = cs_pack((csq[0] + co[0]) + (co[1] + co[2])); asm volatile("" : "+v"(csw[0])); }
      SB();
      if (n < 8) { csq = MFMA16(sel, ca[n & 1], csq); SB(); }
      if (n + 2 < 8) ca[n & 1] = tr_readc<256>(own + (n + 2) * 4096);
      if (n + 3 < 32) rdq(n + 3);
      asm volatile("" : "+a"(accq));
      SB();
    });
    FST(11, 10);
    accq = MFMA(aug_w(csw[0], 0u), xf[32], accq);
    u32x4 q0, q1; pack16(accq, q0, q1);
    f32x16 pq = {}; pq = MFMA(aphi[0], q0, pq); pq = MFMA(aphi[1], q1, pq);
    Softmax sq;
    const u16* xr = x + (size_t)(128 * g + 64 * cx.th + 32 * (w >> 1) + tok) * 512 + col;
    SB();
    sfor<29>([&](auto ii) LAMBDA {
      constexpr int i = ii;
      sq.step(i, pq, sc, phq);
      if (i + 32 == KQP0) kin.pz = kv_poll(cx, kvb);
      if (i == 0) FST(11, 11);
      if (i == KQW0) {
        SB(); FST(11, 12); kv_wait(cx, kin.pz, kvb + KV_F + w * 16 * FS); STAMP(6); FST(11, 13); kv_load(cx, kin, kvb);
        mi.xa = *(const u32x4*)xr; mi.xb = *(const u32x4*)(xr + 8);
      }
      if (i > KQW0 && i <= KQW0 + 4) {
        constexpr int b = (i - KQW0 - 1) & 3;
#pragma unroll
        for (int e = 0; e < 4; e++) w2f[8 * (e >> 1) + 2 * b + (e & 1)] = rawrd(cx, mi, b, e);
      }
      if (i == KQW0 + 7) asm volatile("s_waitcnt lgkmcnt(0)" ::: "memory");
      if (i >= KQW0 + 7 && i < KQW0 + 7 + 2 * R2B) {
        rawld<((2 * (i - KQW0 - 7)) & 15)>(cx, r2, rv, w * 458752 + cx.cb * 128 + 32768, 114688);
        rawld<((2 * (i - KQW0 - 7) + 1) & 15)>(cx, r2, rv, w * 458752 + cx.cb * 128 + 32768, 114688);
      }
      SB();
    });
  }
}
DEVI void red_send(Cx& cx, const f32x16& a, int j) {
  char* dst = cx.C + (cx.w ^ j) * INBOX + (j - 1) * 4096 + cx.vo;
#pragma unroll
  for (int m = 0; m < 4; m++) { f32x4 v = {a[4 * m], a[4 * m + 1], a[4 * m + 2], a[4 * m + 3]}; *(f32x4*)(dst + m * 1024) = v; }
}
DEVI void red_send1(Cx& cx, const f32x16& a, int j, int m) {
  const f32x4 v = {a[4 * m], a[4 * m + 1], a[4 * m + 2], a[4 * m + 3]};
  *(f32x4*)(cx.C + (cx.w ^ j) * INBOX + (j - 1) * 4096 + cx.vo + m * 1024) = v;
}
struct Red {
  f32x4 r1[4], r2[4], r3[4]; unsigned chk; int cp;
  DEVI void rd32(Cx& cx, int m) {
    const char* p = cx.C + cx.w * INBOX + m * 1024 + cx.vo;
    r2[m] = *(const f32x4*)(p + 4096); r3[m] = *(const f32x4*)(p + 8192);
  }
  DEVI void put(Cx& cx, unsigned tot) {
    *(unsigned*)(cx.C + cx.w * INBOX + 4096 + cx.lane * 4) = tot + (FORCE_BAD ? cx.w & 1 : 0);
    cp = (cx.lane & 3) * INBOX + 4096 + 252;
    asm volatile("" : "+v"(cp));
  }
  DEVI void rd1(Cx& cx) {
    chk = *(const unsigned*)(cx.C + cp);
    SB();
#pragma unroll
    for (int m = 0; m < 4; m++) r1[m] = *(const f32x4*)(cx.C + cx.w * INBOX + m * 1024 + cx.vo);
  }
  DEVI void add32(int m) {
#pragma unroll
    for (int e = 0; e < 4; e++) asm volatile("v_add_f32 %0, %0, %1" : "+v"(r2[m][e]) : "v"(r3[m][e]));
  }
  DEVI bool fin(const f32x16& own, float (&xr)[16]) {
#pragma unroll
    for (int m = 0; m < 4; m++) { const f32x4 o = {own[4 * m], own[4 * m + 1], own[4 * m + 2], own[4 * m + 3]}; r2[m] += o; }
    asm volatile("" : "+v"(r2[0]), "+v"(r2[1]), "+v"(r2[2]), "+v"(r2[3]));
#pragma unroll
    for (int r = 0; r < 16; r++) xr[r] = r2[r >> 2][r & 3] + r1[r >> 2][r & 3];
    return BALLOT(chk != 0) != 0;
  }
};
DEVI void red_recv(Cx& cx, const f32x16& own, float (&xr)[16]) {
  f32x4 v[12];
#pragma unroll
  for (int k = 0; k < 12; k++) v[k] = *(const f32x4*)(cx.C + cx.w * INBOX + ((k % 3) * 4 + k / 3) * 1024 + cx.vo);
#pragma unroll
  for (int m = 0; m < 4; m++)
#pragma unroll
    for (int e = 0; e < 4; e++) xr[4 * m + e] = (own[4 * m + e] + v[3 * m][e]) + (v[3 * m + 1][e] + v[3 * m + 2][e]);
}
DEVI void unpack16(const u32x4& a, const u32x4& b, float (&x)[16]) {
#pragma unroll
  for (int t = 0; t < 4; t++) { x[2 * t] = bflo(a[t]); x[2 * t + 1] = bfhi(a[t]); x[8 + 2 * t] = bflo(b[t]); x[9 + 2 * t] = bfhi(b[t]); }
}
#define EFX 3
#define EFO 0
DEVI void publish_x(Cx& cx, float (&xr)[16], int xb, int sb, int fb) {
  u32x4 f0, f1; pack16(xr, f0, f1);
  stc(cx.rs, cx.vo, xb + cx.xo, f0);
  stc(cx.rs, cx.vo + 1024, xb + cx.xo, f1);
  if (EFX == 0) put_flag(cx, fb + cx.fxo, 7);
  SB();
  f32x2 s1 = {xr[0], xr[1]}, s2 = s1 * s1;
#pragma unroll
  for (int r = 2; r < 16; r += 2) { const f32x2 v = {xr[r], xr[r + 1]}; s1 += v; s2 = __builtin_elementwise_fma(v, v, s2); }
  auto t = __builtin_amdgcn_permlane32_swap(__float_as_uint(s1[0] + s1[1]), __float_as_uint(s2[0] + s2[1]), false, false);
  const unsigned st = __float_as_uint(__uint_as_float(t[0]) + __uint_as_float(t[1]));
  stc1(cx.rs, (cx.lane & 15) * 16 + (cx.lane >> 4 & 1) * 8 + cx.hk * 4, sb + cx.so, st);
  if (EFX == 3) put_flag(cx, fb + cx.fxo, 7);
  SB();
  put_final(cx, fb + CKX + cx.fxo, sum4(sum4(st, f0), f1));
  unpack16(f0, f1, xr);
}
DEVI void park(Cx& cx, const float (&xr)[16]) {
  char* p = cx.C + cx.w * INBOX + C_PARK + cx.vo;
#pragma unroll
  for (int m = 0; m < 4; m++) { f32x4 v = {xr[4 * m], xr[4 * m + 1], xr[4 * m + 2], xr[4 * m + 3]}; *(f32x4*)(p + m * 1024) = v; }
}
DEVI f32x16 unpark(Cx& cx) {
  const char* p = cx.C + cx.w * INBOX + C_PARK + cx.vo;
  f32x16 r;
#pragma unroll
  for (int m = 0; m < 4; m++) { f32x4 v = *(const f32x4*)(p + m * 1024); r[4 * m] = v[0]; r[4 * m + 1] = v[1]; r[4 * m + 2] = v[2]; r[4 * m + 3] = v[3]; }
  return r;
}
DEVI f32x16 tovec(const float (&x)[16]) {
  f32x16 r;
#pragma unroll
  for (int e = 0; e < 16; e++) r[e] = x[e];
  return r;
}

#define RDW 2
#define XS 0
#define XA 14
#define XE 1
template <class AUG, class WIN>
DEVI void xpass(Cx& cx, int xb, int sb, int fb, u32x4 (&xf)[33], f32x16& a0, f32x16& a1, int sti, AUG aug, WIN win) {
  const rsrc_t rs = cx.rs;
  xb += cx.w * 32768; sb += cx.w * 4096; fb += cx.w * 16 * FS;
  if (sti == 15) FST(7, 0);
  if (sti == 25) FST(8, 0);
  if (sti == 15) nap<D_X2>(); else nap<D_X1>();
  poll_wait<16>(cx, ldc1(rs, cx.fv16, fb), fb, cx.tk_x, 7);
  STAMP(sti); if (sti != 15) FST(sti == 11 ? 4 : 5, 0); else FST(7, 1);
  if (sti == 25) FST(8, 1);
  u32x4 st[4], ra[3], rb[3], w0, w1; unsigned ck;
  auto ld = [&](int q) LAMBDA {
    if (q >= XS && q < XS + 4) st[q - XS] = ldck(rs, cx.vo, sb, q - XS);
    else if (q < 36) xf[q < XS ? q : q - 4] = ldck(rs, cx.vo, xb, q < XS ? q : q - 4);
    if (q == 30) ck = ldc1(rs, cx.fv16, fb + CKX + 4);
  };
  f32x4 sv; float t0, t1;
  auto stats = [&](int k) LAMBDA {
    if (k == 0) sv = __builtin_bit_cast(f32x4, st[0]) + __builtin_bit_cast(f32x4, st[1]);
    if (k == 1) sv += __builtin_bit_cast(f32x4, st[2]) + __builtin_bit_cast(f32x4, st[3]);
    if (k < 2) asm volatile("" : "+v"(sv));
    if (k == 2) {
      auto p = __builtin_amdgcn_permlane32_swap(__float_as_uint(sv[0]), __float_as_uint(sv[1]), false, false);
      auto q = __builtin_amdgcn_permlane32_swap(__float_as_uint(sv[2]), __float_as_uint(sv[3]), false, false);
      t0 = __uint_as_float(p[0]) + __uint_as_float(p[1]); t1 = __uint_as_float(q[0]) + __uint_as_float(q[1]);
    }
    if (k == 3) {
      auto r = __builtin_amdgcn_permlane16_swap(__float_as_uint(t0), __float_as_uint(t1), false, false);
      t0 = (__uint_as_float(r[0]) + __uint_as_float(r[1])) * (1.f / 512.f);
    }
    if (k == 4) {
      auto u = __builtin_amdgcn_permlane32_swap(__float_as_uint(t0), __float_as_uint(t0), false, false);
      t0 = __uint_as_float(u[0]);
      t1 = fmaxf(__uint_as_float(u[1]) - t0 * t0, 0.f);
    }
    if (k >= 2 && k < 5) asm volatile("" : "+v"(t0), "+v"(t1));
    if (k >= 5) aug(k - 5, t0, t1, w0, w1);
  };
  const char* pa = cx.A + cx.vo;
#pragma unroll
  for (int q = 0; q < 10; q++) { ld(q); SB(); }
  if (sti == 15) FST(7, 2);
  if (sti == 25) FST(8, 2);
  bar();
  if (sti == 15) FST(7, 3);
  if (sti == 25) FST(8, 3);
#pragma unroll
  for (int n = 0; n < 3; n++) { ra[n] = *(const u32x4*)(pa + n * 2048); rb[n] = *(const u32x4*)(pa + n * 2048 + 1024); }
  SB();
  unsigned sum = -cx.tk_x;
  const f32x16 zero = {};
#pragma unroll
  for (int q = 10; q < XA; q++) { ld(q); SB(); }
  sfor<32>([&](auto nn) LAMBDA {
    constexpr int n = nn, k = n - XS - 4;
    if (n % 8 == 0 && sti != 15) FST(sti == 11 ? 4 : 5, 1 + n / 8);
    a0 = MFMA(ra[n % 3], xf[n], n ? a0 : zero); SB();
    if (n % 4 == 0 && sti == 15) FST(7, 4 + n / 4);
    if (n % 4 == 0 && sti == 25) FST(8, 4 + n / 4);
    if (n + 3 < 32) ra[n % 3] = *(const u32x4*)(pa + (n + 3) * 2048);
    if (n + XA < 36) ld(n + XA);
    if (n >= 22) win(ic<(n < 22 ? 0 : n - 22)>());
    if (k >= -4 && k < 0) sum = sum4(sum, st[k & 3]);
    if (k >= 0 && k < 9) stats(k);
    if (XE && n == 31) sum = sum4(sum, xf[n]);
    SB();
    if (XE && n == 31) { a0 = MFMA(w0, xf[32], a0); SB(); wsum_step<0>(sum); wsum_step<1>(sum); SB(); }
    a1 = MFMA(rb[n % 3], xf[n], n ? a1 : zero); SB();
    if (n + 3 < 32) rb[n % 3] = *(const u32x4*)(pa + (n + 3) * 2048 + 1024);
    if (!XE || n < 31) sum = sum4(sum, xf[n]);
    if (n == 29) sum += ck;
    if (XE && n == 31) { wsum_step<2>(sum); wsum_step<3>(sum); SB(); a1 = MFMA(w1, xf[32], a1); SB(); wsum_step<4>(sum); wsum_step<5>(sum); }
    SB();
    if (!XE && k == 9) { a0 = MFMA(w0, xf[32], a0); a1 = MFMA(w1, xf[32], a1); SB(); }
  });
  if (sti != 15) FST(sti == 11 ? 4 : 5, 5); else FST(7, 12);
  if (sti == 25) FST(8, 12);
  asm volatile("" : "+a"(a0), "+a"(a1));
  if (UNLIKELY(XE ? __builtin_amdgcn_readlane(sum, 63) + (FORCE_BAD ? cx.w & 1 : 0) != 0 : bad(cx, sum))) {
    do {
      spend(cx, sti == 11 ? 2 : sti == 15 ? 4 : 7);
#pragma unroll
      for (int q = 0; q < 36; q++) ld(q);
      sum = ck - cx.tk_x;
#pragma unroll
      for (int q = 0; q < 32; q++) sum = sum4(sum, xf[q]);
#pragma unroll
      for (int q = 0; q < 4; q++) sum = sum4(sum, st[q]);
    } while (again(cx, sum));
#pragma unroll
    for (int k = 0; k < 9; k++) stats(k);
    a0 = MFMA(w0, xf[32], zero); a1 = MFMA(w1, xf[32], zero);
#pragma unroll
    for (int n = 0; n < 32; n++) {
      a0 = MFMA(*(const u32x4*)(pa + n * 2048), xf[n], a0); a1 = MFMA(*(const u32x4*)(pa + n * 2048 + 1024), xf[n], a1);
      SB();
    }
  }
}

template <bool P> DEVI f32x2 mul2(f32x2 a, f32x2 b) {
  f32x2 r;
  if (P) asm volatile("v_pk_mul_f32 %0, %1, %2" : "=v"(r) : "v"(a), "v"(b));
  else {
    asm volatile("v_mul_f32 %0, %1, %2" : "=v"(r[0]) : "v"(a[0]), "v"(b[0]));
    asm volatile("v_mul_f32 %0, %1, %2" : "=v"(r[1]) : "v"(a[1]), "v"(b[1]));
  }
  return r;
}
template <bool P> DEVI f32x2 fma2(f32x2 a, f32x2 c) {
  f32x2 r;
  if (P) asm volatile("v_pk_fma_f32 %0, %1, %2, %2 op_sel:[0,1,0] op_sel_hi:[1,1,0]" : "=v"(r) : "v"(a), "v"(c));
  else {
    asm volatile("v_fma_f32 %0, %1, %2, %3" : "=v"(r[0]) : "v"(a[0]), "v"(c[1]), "v"(c[0]));
    asm volatile("v_fma_f32 %0, %1, %2, %3" : "=v"(r[1]) : "v"(a[1]), "v"(c[1]), "v"(c[0]));
  }
  return r;
}
struct Gelu {
  f32x2 cc, ia;
  f32x2 a[4], t, d, e, r, u;
  u32x4 f; unsigned sum;
  template <int K0, int K1, int K, int H, class MID>
  DEVI void step(Cx& cx, const f32x16& lo, const f32x16& hi, int hb, MID mid) {
    constexpr bool P = H == 2, p0 = K >= K0 && K < K1, p1 = K - 1 >= K0 && K - 1 < K1, p2 = K - 2 >= K0 && K - 2 < K1;
    constexpr int v = K - 2;
    f32x2 s, y;
    auto rc = [&]() LAMBDA { if (p2) { r[0] = __builtin_amdgcn_rcpf(d[0]); r[1] = __builtin_amdgcn_rcpf(d[1]); SB(); } };
    auto ex = [&]() LAMBDA { if (p1) { e[0] = __builtin_amdgcn_exp2f(t[0]); e[1] = __builtin_amdgcn_exp2f(t[1]); SB(); } };
    auto rd = [&]() LAMBDA {
      if (p0) {
        const f32x16& h = (K & 8) ? hi : lo;
        float x0, x1;
        asm volatile("v_accvgpr_read_b32 %0, %2\n\tv_accvgpr_read_b32 %1, %3" : "=v"(x0), "=v"(x1) : "a"(h[(2 * K) & 15]), "a"(h[(2 * K + 1) & 15]));
        a[K & 3][0] = x0; a[K & 3][1] = x1;
        SB();
      }
    };
    auto fin = [&]() LAMBDA { if (p2 && (v & 3) == 3) { stc(cx.rs, cx.vo, hb + (v >> 2) * 8192, f); sum = sum4(sum, f); } };
    if (H != 1) rd();
    if (H != 0) rc();
    if (H != 1) ex();
    mid(); SB();
    if (H != 0 && p0 && p2) {
      float y0, y1;
      asm volatile("v_mul_f32 %2, %6, %8\n\t"
                   "v_mul_f32 %3, %7, %9\n\t"
                   "v_pk_mul_f32 %0, %5, %5\n\t"
                   "v_cvt_pk_bf16_f32 %4, %2, %3\n\t"
                   "v_pk_fma_f32 %0, %0, %11, %11 op_sel:[0,1,0] op_sel_hi:[1,1,0]\n\t"
                   "v_pk_fma_f32 %1, %10, %12, %12\n\t"
                   "v_pk_mul_f32 %0, %5, %0"
                   : "=&v"(t), "=&v"(d), "=&v"(y0), "=&v"(y1), "=&v"(f[v & 3])
                   : "v"(a[K & 3]), "v"(a[v & 3][0]), "v"(a[v & 3][1]), "v"(r[0]), "v"(r[1]), "v"(e), "v"(cc), "v"(ia));
    } else if (H != 0) {
      if (p0) s = mul2<0>(a[K & 3], a[K & 3]);
      if (p2) y = mul2<0>(a[v & 3], r);
      if (p0) u = fma2<0>(s, cc);
      if (p1) d = fma2<0>(e, ia);
      if (p0) t = mul2<0>(a[K & 3], u);
      if (p2) f[v & 3] = pk2(y[0], y[1]);
    }
    if (H != 0) fin();
  }
};

#define GB2 34
#define GP1 26
#define GW1 40
#define GP3 9
#define GP4 37
#define GP2 42
#define GW3 26
#define H3F 1
#define GBR0 78
#define GBR1 90
#define WL_P 14
#define WL_D 2
#define WL_H 13
#define WL_N ((24 + WL_D) / WL_D)
#define NW2_H (WL_P + WL_N + WL_H)
#define D_O0 2
#define NB1H 6
#define NQK 8
#define KQP1 31
#define KQW1 12

constexpr int h_iss(int i) { return i < 32 ? i : i < 40 ? 33 + 2 * (i - 32) : i < 48 ? 32 + 2 * (i - 40) : i < 52 ? 49 + 2 * (i - 48) : 48 + 2 * (i - 52); }
constexpr int s4_f(int G) {
  return G < 64 ? G >> 1 : G < 80 ? 33 + 2 * ((G - 64) >> 1) : G < 84 ? 32 + 2 * (G - 80) : G < 92 ? 49 + 2 * ((G - 84) >> 1) : G < 100 ? 40 + 2 * (G - 92) : 32 + 2 * (G - 100);
}
constexpr int s4_j(int G) { return G < 64 ? (G & 2) + 1 - (G & 1) : G < 80 || (G >= 84 && G < 92) ? 3 - (G & 1) : G < 100 ? 1 : 0; }

#define BL 1
#define BUDL(i) do { if (L == BL) BUD(53 + (i)); } while (0)
template <int L>
DEVI void layer(Cx& cx, const u16* __restrict__ x, const u16* __restrict__ b1, const u16* __restrict__ b2, u16* __restrict__ out,
                u32x4 (&phq)[4], const unsigned (&csw)[3], const u32x4 (&aphi)[2], KvIn& kin, u32x4 (&w2f)[56], u32x4 (&wof)[16],
                Misc& mi, unsigned (&csp)[4], unsigned (&b1p)[2]) {
  const int lane = cx.lane, w = cx.w, tok = cx.tok, hk = cx.hk, g = cx.g, c = cx.c;
  const rsrc_t rs = cx.rs;
  const int gl = cx.gb + L * G_L;
  const int kvb = KVA + L * KV_L + c * KV_C;
  const int w2b = WT_2 + (cx.cb * 4 + w) * 57344;
  const f32x16 zero = {};
  constexpr int SO = 14 * L;
  u32x4 xa, xb2;
  const int col = 64 * cx.cb + 32 * (w & 1) + 16 * hk;
  const size_t xrow = (size_t)(128 * g + 64 * cx.th + 32 * (w >> 1) + tok) * 512 + col;
  BUDL(-1);
  if (L == 1) {
    u32x4 xf[33]; f32x16 acck, accv; float rstd;
    xpass(cx, cx.gb + G_X2, cx.gb + G_ST2, cx.gb + G_FX2, xf, acck, accv, 15,
          [&](int k, float mean, float var, u32x4& w0, u32x4& w1) LAMBDA {
            if (k == 0) { rstd = __builtin_amdgcn_rsqf(var + 1e-5f); asm volatile("" : "+v"(rstd)); }
            if (k == 1) { xf[32] = aug_tok(mean, 0.f, hk); asm volatile("" : "+v"(xf[32])); }
            if (k == 2) { w0 = aug_w(csw[1], 0u); w1 = aug_w(csw[2], 0u); asm volatile("" : "+v"(w0), "+v"(w1)); }
          },
          [&](auto ii) LAMBDA { if (ii.v < 8) dma<(ii.v & 7)>(rs, cx.B + w * 8192, cx.vo, WT_QKV + c * WQ_C + 65536 + w * 8192); });
    STAMP(16); FST(6, 0); FST(7, 13); BUDL(0);
    const float sc = rstd * LOG2E;
    f32x16 p;
    { u32x4 b0, b1; pack16(acck, b0, b1); p = MFMA(aphi[0], b0, zero); p = MFMA(aphi[1], b1, p); }
    FST(6, 1);
    kv_write_v(cx, accv, rstd);
    SB();
    {
      Softmax sm; u32x4 phk[4];
      sfor<29>([&](auto ii) LAMBDA {
        constexpr int i = ii;
        if (i % 4 == 0) FST(6, 2 + i / 4);
        sm.step(i, p, sc, phk);
        SB();
      });
      STAMP(17); FST(6, 10);
      kv_tail<0>(cx, phk, kvb);
    }
    STAMP(18); FST(6, 11);
    f32x16 accq = MFMA(aug_w(csw[0], 0u), xf[32], zero);
    u32x4 qa[3];
    const char* pq = cx.B + cx.vo;
#pragma unroll
    for (int n = 0; n < 3; n++) qa[n] = *(const u32x4*)(pq + n * 1024);
    char* dA = cx.A + w * 16384;
    const int sA = WT_1 + c * W1_C + w * 16384;
    SB();
    sfor<32>([&](auto nn) LAMBDA {
      constexpr int n = nn;
      accq = MFMA(qa[n % 3], xf[n], accq); SB();
      if (n + 3 < 32) qa[n % 3] = *(const u32x4*)(pq + (n + 3) * 1024);
      if (n >= 1 && n < 17) wof[n - 1] = ldck(rs, cx.vo, WT_O + (cx.cb * 4 + w) * 16384, n - 1);
      if (n >= 17 && (n & 1)) dma<(n >= 17 ? (n - 17) / 2 : 0)>(rs, dA, cx.vo, sA);
      if (n == KQP1) kin.pz = kv_poll(cx, kvb);
      if (n % 8 == 7) FST(6, 12 + n / 8);
      SB();
    });
    u32x4 b0, b1; pack16(accq, b0, b1);
    f32x16 pq2 = MFMA(aphi[0], b0, zero); pq2 = MFMA(aphi[1], b1, pq2);
    Softmax sq;
    sfor<29>([&](auto ii) LAMBDA {
      constexpr int i = ii;
      sq.step(i, pq2, sc, phq);
      if (i + 32 == KQP1) kin.pz = kv_poll(cx, kvb);
      if (i == KQW1) { SB(); kv_wait(cx, kin.pz, kvb + KV_F + w * 16 * FS); STAMP(19); kv_load(cx, kin, kvb); }
      if (i >= 19 && i < 27) dma<(i >= 19 && i < 27 ? i - 11 : 0)>(rs, dA, cx.vo, sA);
      if (i == 27) mi.ba = *(const u32x4*)(b2 + col);
      if (i == 28) mi.bb = *(const u32x4*)(b2 + col + 8);
      SB();
    });
  }
  asm volatile("" : "+v"(phq[0]), "+v"(phq[1]), "+v"(phq[2]), "+v"(phq[3]));
  SB();
  STAMP(7 + SO); BUDL(1); if (L == 0) FST(11, 14);
  {
    const int G = lane >> 4, d = lane & 15;
    u32x4 idn;
#pragma unroll
    for (int t = 0; t < 4; t++) { const int k = 8 * (G & 1) + 2 * t; idn[t] = k == d ? 0x3f80u : k + 1 == d ? 0x3f800000u : 0u; }
    f32x4 kv; unsigned sum;
    auto red = [&]() LAMBDA {
      f32x4 k0 = {}, k1 = {}; sum = -cx.tk_kv;
#pragma unroll
      for (int m = 0; m < 8; m += 2) {
        k0 = MFMA16(idn, kin.pa[m], k0); k1 = MFMA16(idn, kin.pa[m + 1], k1);
        sum = sum4(sum4(sum, kin.pa[m]), kin.pa[m + 1]);
      }
      sum = sum4(sum, kin.pz); kv = k0 + k1;
    };
    red();
    asm volatile("" : "+v"(kv));
    if (UNLIKELY(bad(cx, sum))) {
      do { spend(cx, 5 * L); kin.pz = kv_poll(cx, kvb); kv_load(cx, kin, kvb); red(); } while (again(cx, sum));
    }
    char* img = cx.C + w * INBOX + C_IMG;
    { const u32x2 v = {pk2(kv[0], kv[1]), pk2(kv[2], kv[3])}; *(u32x2*)(img + d * 32 + G * 8) = v; }
    { const u32x4 z4 = {}; *(u32x4*)(img + 512 + cx.vo) = hk ? z4 : kin.pz; }
    lds_barrier();
    const int hp = (tok >> 2) & 1, dd = (tok & 3) + 4 * (tok >> 3);
    const char* ap = cx.C + (hp == hk ? 2 * hp * INBOX + C_IMG + dd * 32 : C_ZERO);
    const int zo = hp == hk ? 512 : 0;
    u32x4 a[4], az[4];
#pragma unroll
    for (int s4 = 0; s4 < 4; s4++) az[s4] = *(const u32x4*)(ap + zo + (s4 >> 1) * INBOX + (s4 & 1) * 16);
#pragma unroll
    for (int s4 = 0; s4 < 4; s4++) a[s4] = *(const u32x4*)(ap + (s4 >> 1) * INBOX + (s4 & 1) * 16);
    if (L == 0) stc1(rs, w == 0 && (lane >> 4) == 2 ? (lane - 32) * 8192 : OOB, MBX + c * 512 + (48 + g) * 8, cx.tk_kv + 1);
    if (L == 0) mi.msg = ldc(rs, mi.pw, MB2);
    SB();
    f32x16 num = {}, den = {};
#pragma unroll
    for (int s4 = 0; s4 < 4; s4++) den = MFMA(az[s4], phq[s4], den);
#pragma unroll
    for (int s4 = 0; s4 < 4; s4++) num = MFMA(a[s4], phq[s4], num);
    float ds[4];
#pragma unroll
    for (int r = 0; r < 4; r++) ds[r] = (den[r] + den[4 + r]) + (den[8 + r] + den[12 + r]);
    const float inv = __builtin_amdgcn_rcpf(((ds[0] + ds[1]) + (ds[2] + ds[3])) + 1e-6f);
    float ov[16];
#pragma unroll
    for (int r = 0; r < 16; r++) ov[r] = num[r] * inv;
    u32x4 f0, f1; pack16(ov, f0, f1);
    stc(rs, cx.vo, gl + G_O + cx.oo, f0);
    stc(rs, cx.vo + 1024, gl + G_O + cx.oo, f1);
    if (EFO == 0) put_flag(cx, gl + G_FO + cx.fo, 7);
    put_final(cx, gl + G_FO + CKO + cx.fo, sum4(sum4(0u, f0), f1));
    STAMP(8 + SO); if (L == 0) FST(11, 15);
  }
  if (L == 0) {
    SB();
    auto miss = [&]() LAMBDA { return BALLOT(((mi.msg[0] ^ mi.jv) | (mi.msg[1] ^ mi.jv) | (mi.msg[2] ^ mi.jv) | (mi.msg[3] ^ mi.jv)) != 0); };
    RAW(6, __builtin_popcountll(miss())); RAW(7, 65536 - cx.budget);
    if (UNLIKELY(miss() != 0)) {
      do {
        if (cx.budget == 0) break;
        cx.budget--;
        asm volatile("" ::: "memory");
        mi.msg = ldc(rs, mi.pw, MB2);
      } while (miss() != 0);
      cx.budget = RFL(cx.budget);
    }
    FST(11, 16); RAW(8, 65536 - cx.budget);
    static_assert(WL_P >= 4 && WL_P <= 16 && NW2_H >= 32, "");
    sfor<WL_P>([&](auto kk) LAMBDA { wload<0>(cx, kk, w2f, mi); SB(); });
#pragma unroll
    for (int k = 0; k < 4 * R2B; k++) w2f[16 + 8 * (k >> 1 & 1) + 2 * (k >> 2) + (k & 1)] = rawrd(cx, mi, k >> 2, k & 3);
  }
  float xr[16];
  {
    const int ob = gl + G_O + (cx.th * 4 + w) * 16384, fb = gl + G_FO + (cx.th * 4 + w) * 8 * FS;
    f32x16 acc[4];
    if (L == 0) {
      unpack16(mi.xa, mi.xb, xr); acc[0] = tovec(xr);
      b1p[0] = mi.b1v[0] | ((unsigned)mi.b1v[1] << 16); b1p[1] = mi.b1v[2] | ((unsigned)mi.b1v[3] << 16);
    } else acc[0] = unpark(cx);
    SB();
    char* dB = cx.B + w * 12288;
    const int sB = WT_1 + c * W1_C + 65536 + w * 12288;
    if (L == 1) sfor<NB1H>([&](auto ii) LAMBDA { dma<ii.v>(rs, dB, cx.vo, sB); });
    if (L == 0) FST(11, 17);
    nap<(L ? D_O : D_O0)>();
    poll_wait<8>(cx, ldc1(rs, cx.fv8, fb), fb, cx.tk_oh, 7);
    STAMP(9 + SO); if (L == 1) FST(9, 0); if (L == 0) FST(11, 18);
    BUDL(2);
    u32x4 of[16];
#pragma unroll
    for (int k = 0; k < 10; k++) { of[k ^ 8] = ldck(rs, cx.vo, ob, k ^ 8); SB(); }
    if (L == 0) {
      const f32x4 v = (__builtin_bit_cast(f32x4, mi.cs[0]) + __builtin_bit_cast(f32x4, mi.cs[1])) + (__builtin_bit_cast(f32x4, mi.cs[2]) + __builtin_bit_cast(f32x4, mi.cs[3]));
      *(f32x4*)(cx.C + w * INBOX + 3072 + cx.vo) = v;
    }
    lds_barrier();
    f32x2 cq[8];
    unsigned sum = -cx.tk_oh, ck;
    Red rd;
    sfor<32>([&](auto nn) LAMBDA {
      constexpr int n = nn, j = n < 12 ? 3 - (n % 3 == 2) : 3 - n / 8, s = n < 12 ? (n % 3 == 2 ? n / 3 : n - n / 3) : n & 7;
      constexpr int i = n >= 13 && n % 3 != 0 ? n - 13 - (n - 13) / 3 : -1;
      if (L == 1 && n % 4 == 0) FST(9, 1 + n / 4);
      if (L == 0 && n % 8 == 0) FST(11, 19 + n / 8);
      acc[j] = MFMA(wof[2 * s + (j & 1)], of[8 * (j >> 1) + s], s == 0 && j ? zero : acc[j]); SB();
      if (n < 6) of[(n + 10) ^ 8] = ldck(rs, cx.vo, ob, (n + 10) ^ 8);
      if (n == 6) ck = ldc1(rs, cx.fv8, fb + CKO + 4);
      if (L == 1 && n >= 7 && n < 19 - NB1H) dma<(n >= 7 && n < 19 - NB1H ? n - 7 + NB1H : 0)>(rs, dB, cx.vo, sB);
      if (L == 0 && n >= 7 && (n - 7) % WL_D == 0) wload<WL_P>(cx, ic<(n - 7) / WL_D>(), w2f, mi);
      if (L == 0 && n == 1) {
#pragma unroll
        for (int k = 0; k < 8; k++) cq[k] = *(const f32x2*)(cx.C + (hk * 2 + (k >> 1 & 1)) * INBOX + 3072 + (k >> 2) * 256 + (k & 1) * 512 + tok * 8);
      }
      if (L == 0 && (n == 4 || n == 5)) {
        constexpr int k = n & 1;
        const f32x2 v = (cq[4 * k] + cq[4 * k + 1]) + (cq[4 * k + 2] + cq[4 * k + 3]);
        csp[2 * k] = cs_pack(xhalf_sum(v[0])); csp[2 * k + 1] = cs_pack(xhalf_sum(v[1]));
        asm volatile("" : "+v"(csp[2 * k]), "+v"(csp[2 * k + 1]));
      }
      if (j & 1) sum = sum4(sum, of[8 * (j >> 1) + s]);
      if (i >= 0 && i < 12) red_send1(cx, acc[3 - (i / 4 & 3)], 3 - (i / 4 & 3), i & 3);
      if (n == 24) sum += ck;
      if (n >= 24 && n < 27) { wsum_step<(n >= 24 && n < 27 ? 2 * (n - 24) : 0)>(sum); SB(); wsum_step<(n >= 24 && n < 27 ? 2 * (n - 24) + 1 : 0)>(sum); }
      if (n == 27) { lds_barrier_n<2>(); rd.rd32(cx, 0); rd.rd32(cx, 1); rd.rd32(cx, 2); rd.rd32(cx, 3); rd.put(cx, sum); }
      if (n >= 30) { rd.add32(2 * (n & 1)); rd.add32(2 * (n & 1) + 1); }
      SB();
    });
    if (L == 1) FST(9, 9); else FST(11, 23);
    lds_barrier();
    if (L == 1) FST(9, 10);
    rd.rd1(cx);
    SB();
    if (UNLIKELY(rd.fin(acc[0], xr))) {
      lds_barrier();
      do {
        spend(cx, 1 + 5 * L);
        sum = ldc1(rs, cx.fv8, fb + CKO + 4) - cx.tk_oh;
#pragma unroll
        for (int k = 0; k < 16; k++) { of[k] = ldck(rs, cx.vo, ob, k); sum = sum4(sum, of[k]); }
      } while (again(cx, sum));
      if (L == 0) { xa = *(const u32x4*)(x + xrow); xb2 = *(const u32x4*)(x + xrow + 8); }
      else { xa = ldc(rs, cx.vo, cx.gb + G_X2 + cx.xo); xb2 = ldc(rs, cx.vo + 1024, cx.gb + G_X2 + cx.xo); }
      unpack16(xa, xb2, xr); acc[0] = tovec(xr); acc[1] = zero; acc[2] = zero; acc[3] = zero;
#pragma unroll
      for (int n = 0; n < 32; n++) acc[n >> 3] = MFMA(wof[2 * (n & 7) + ((n >> 3) & 1)], of[8 * (n >> 4) + (n & 7)], acc[n >> 3]);
      red_send(cx, acc[1], 1); red_send(cx, acc[2], 2); red_send(cx, acc[3], 3);
      lds_barrier();
      red_recv(cx, acc[0], xr);
    }
    if (L == 1) FST(9, 11);
    publish_x(cx, xr, gl + G_X1, gl + G_ST1, gl + G_FX1);
    if (L == 1) FST(9, 12); else FST(11, 24);
    STAMP(10 + SO);
    if (L == 0) sfor<WL_H>([&](auto kk) LAMBDA { wload<WL_P + WL_N>(cx, kk, w2f, mi); });
    float bv[16]; unpack16(mi.ba, mi.bb, bv);
#pragma unroll
    for (int r = 0; r < 16; r++) xr[r] += bv[r];
    park(cx, xr);
  }
  {
    u32x4 xf[33]; f32x16 h0, h1, h2, h3;
    Gelu ge;
    const char* pb = cx.B + cx.vo;
    const char* ph = cx.B + 1024 + (hk * 16 + (lane & 15)) * 16;
    u32x4 ra[4];
    auto rdw = [&](int G) LAMBDA { ra[G & 3] = *(const u32x4*)((G < 32 ? pb : ph) + (G & 31) * 1536); };
    xpass(cx, gl + G_X1, gl + G_ST1, gl + G_FX1, xf, h0, h1, 11 + SO,
          [&](int k, float mean, float var, u32x4& w0, u32x4& w1) LAMBDA {
            if (k == 0) {
              const float r1 = __builtin_amdgcn_rsqf(var + 1e-5f);
              ge.cc[0] = r1; ge.cc[1] = var * r1 * r1 + 1e-5f;
              asm volatile("" : "+v"(ge.cc));
            }
            if (k == 1) {
              const float A = ge.cc[0] * __builtin_amdgcn_rsqf(ge.cc[1]);
              const float iA = __builtin_amdgcn_rcpf(A);
              ge.ia[0] = iA; ge.ia[1] = iA;
              const float k0 = -2.f * LOG2E * 0.7978845608028654f, k1 = k0 * 0.044715f;
              ge.cc[0] = k0 * A; ge.cc[1] = k1 * A * A * A;
              asm volatile("" : "+v"(ge.cc), "+v"(ge.ia));
            }
            if (k == 2) { xf[32] = aug_tok(mean, ge.ia[0], hk); asm volatile("" : "+v"(xf[32])); }
            if (k == 3) { w0 = aug_w(csp[0], b1p[0]); w1 = aug_w(csp[1], b1p[0] >> 16); asm volatile("" : "+v"(w0), "+v"(w1)); }
          },
          [&](auto ii) LAMBDA {
            constexpr int k = NW2_H + ii.v - 8;
            if (ii.v < 8) dma<(ii.v & 7)>(rs, cx.C + w * INBOX, cx.vo, w2b + 48 * 1024);
            else if (L == 0 && k < W2KEEP) w2f[k] = ldck(rs, cx.vo, w2b, k);
            if (ii.v >= 10 - RDW) rdw(ii.v - 10 + RDW);
          });
    STAMP(12 + SO); if (L == 1) FST(8, 13);
    BUDL(3);
    const int hs = gl + G_H + cx.ho, hfo = gl + G_FH + cx.fo;
    const int hb = gl + G_H + (cx.th * 4 + w) * 57344, fb = gl + G_FH + (cx.th * 4 + w) * 8 * FS;
    ge.sum = 0;
    asm volatile("" : "+a"(h0), "+a"(h1));
    h2 = MFMA(aug_w(csp[2], b1p[1]), xf[32], zero); h3 = MFMA(aug_w(csp[3], b1p[1] >> 16), xf[32], zero);
    u32x4 hf[56];
    auto ldh = [&](int k) LAMBDA { hf[h_iss(k)] = ldck(rs, cx.vo, hb, h_iss(k)); };
#pragma unroll
    for (int n = RDW; n < 4; n++) rdw(n);
    SB();
    unsigned pl = 0, sum = -cx.tk_oh, ck;
    f32x16 acc[4];
    sfor<64>([&](auto gg) LAMBDA {
      constexpr int G = gg, n = G & 31;
      if (G % 8 == 0) FST(4 + L, 6 + G / 8);
      if (G < 32) h2 = MFMA(ra[G & 3], xf[n], h2); else h3 = MFMA(ra[G & 3], xf[n], h3);
      SB();
      auto mid = [&]() LAMBDA { if (G + 4 < 64) rdw(G + 4); };
      if (G < 10) ge.step<0, 16, G, 2>(cx, h0, h1, hs, mid);
      else if (G < 26) ge.step<0, 16, 10 + (G - 10) / 2, G & 1>(cx, h0, h1, hs, mid);
      else if (G >= GB2 && G < GB2 + 20) ge.step<16, 24, 16 + (G - GB2) / 2, (G - GB2) & 1>(cx, h2, h2, hs, mid);
      else mid();
      if (L == 0) {
        constexpr int k = G + 4 * R2B, f = k < 16 ? 16 + 8 * (k >> 1 & 1) + 2 * (k >> 2) + (k & 1) : NW2_H + k - 14;
        if (f < W2KEEP) w2f[f < W2KEEP ? f : 0] = ldck(rs, cx.vo, w2b, f);
      }
      if (G == 9) put_flag(cx, hfo + FHL, 1);
      if (G == 25) put_flag(cx, hfo + 2 * FHL, 3);
      if (G == GB2 + 19) put_flag(cx, hfo + 3 * FHL, 5);
      if (G == GP1) pl = ldc1(rs, cx.fv8, fb + FHL);
      if (G == GW1) { SB(); poll_wait<8>(cx, pl, fb + FHL, cx.tk_oh, 1); STAMP(13 + SO); BUDL(4); }
      if (G >= GW1 && G < GW1 + 16) ldh(G - GW1);
      if (G == GP2) pl = ldc1(rs, cx.fv8, fb + 2 * FHL);
      if (G == 56) { SB(); poll_wait<8>(cx, pl, fb + 2 * FHL, cx.tk_oh, 3); BUDL(5); }
      if (G >= 56 && !(G & 1)) ldh(G / 2 - 12);
      if (G == 60) acc[0] = unpark(cx);
      SB();
    });
    FST(4 + L, 14);
    STAMP(14 + SO);
    unsigned msg = 0;
    char* dA = cx.A + w * 16384;
    const int sA = WT_QKV + c * WQ_C + w * 16384;
    Red rd;
    sfor<112>([&](auto gg) LAMBDA {
      constexpr int G = gg, f = s4_f(G), q = f >> 1, j = s4_j(G);
      constexpr int i = G >= 92 && G % 3 != 1 ? G - 92 - (G - 92) / 3 : -1;
      if (G % 8 == 0) FST(2 + L, G / 8);
      acc[j] = MFMA(w2f[2 * q + (j & 1)], hf[f], q == 0 && j ? zero : acc[j]); SB();
      if (H3F) {
        if (G >= 2 && G < 8) ge.step<24, 28, 22 + G, 2>(cx, h3, h3, hs, []() LAMBDA {});
        if (G == 7) put_flag(cx, hfo, 7);
      } else {
        if (G >= 2 && G < 14) ge.step<24, 28, 24 + (G - 2) / 2, G & 1>(cx, h3, h3, hs, []() LAMBDA {});
        if (G == 13) put_flag(cx, hfo, 7);
      }
      if (G == 15) put_final(cx, hfo + CKH, ge.sum);
      if (G == GW3) { SB(); poll_wait<8>(cx, pl, fb + 3 * FHL, cx.tk_oh, 5); BUDL(6); }
      if (G == GW3 + 32) { SB(); poll_wait<8>(cx, pl, fb, cx.tk_oh, 7); BUDL(7); }
      if (G < 8) ldh(20 + G);
      else if (G < 16) { if (!(G & 1)) ldh(24 + G / 2); }
      else if (G >= GW3 && G < GW3 + 48 && !((G - GW3) & 1)) ldh(32 + (G - GW3) / 2);
      if (G == GP3) pl = ldc1(rs, cx.fv8, fb + 3 * FHL);
      if (G == GP4) pl = ldc1(rs, cx.fv8, fb);
      if (G == GW3 + 45) ck = ldc1(rs, cx.fv8, fb + CKH + 4);
      if (G >= 16 && G < 24) w2f[G + 32] = *(const u32x4*)(cx.C + w * INBOX + (G - 16) * 1024 + cx.vo);
      if (G == (L ? GBR1 : GBR0)) lds_barrier();
      if (!(G & 1) && G >= 16 && (G - 16) / 2 + W2KEEP < 48) w2f[(G - 16) / 2 + W2KEEP] = ldck(rs, cx.vo, w2b, (G - 16) / 2 + W2KEEP);
      if (L == 0) {
        if (G == 51) msg = ldc1(rs, (lane & 15) * 8, MBX + (g * 16 + c) * 512 + 384);
        if (G == 79) { SB(); mail_wait(cx, msg, (lane & 15) * 8, MBX + (g * 16 + c) * 512 + 384, cx.N + 1); }
        if (G >= 80 && !(G & 1) && (G - 80) / 2 < NQK) dma<(G >= 80 ? (G - 80) / 2 : 0)>(rs, dA, cx.vo, sA);
      }
      if (j & 1) sum = sum4(sum, hf[f]);
      if (i >= 0 && i < 12) red_send1(cx, acc[3 - (i / 4 & 3)], 3 - (i / 4 & 3), i & 3);
      if (G == 100) sum += ck;
      if (G >= 100 && G < 106) wsum_step<(G >= 100 && G < 106 ? G - 100 : 0)>(sum);
      if (G == 106) { lds_barrier_n<2>(); rd.rd32(cx, 0); rd.rd32(cx, 1); rd.rd32(cx, 2); rd.rd32(cx, 3); rd.put(cx, sum); }
      if (G >= 109) rd.add32(G & 3);
      if (G == 111) rd.add32(0);
      SB();
    });
    lds_barrier();
    rd.rd1(cx);
    SB();
    FST(2 + L, 14);
    if (UNLIKELY(rd.fin(acc[0], xr))) {
      lds_barrier();
      do {
        spend(cx, 3 + 5 * L);
        xa = ldc(rs, cx.vo, gl + G_X1 + cx.xo); xb2 = ldc(rs, cx.vo + 1024, gl + G_X1 + cx.xo);
        float bv[16]; unpack16(*(const u32x4*)(b2 + col), *(const u32x4*)(b2 + col + 8), bv);
        unpack16(xa, xb2, xr);
#pragma unroll
        for (int r = 0; r < 16; r++) acc[0][r] = xr[r] + bv[r];
        acc[1] = zero; acc[2] = zero; acc[3] = zero; sum = ldc1(rs, cx.fv8, fb + CKH + 4) - cx.tk_oh;
#pragma unroll
        for (int k = 0; k < 56; k++) {
          const u32x4 f = ldck(rs, cx.vo, hb, k);
          sum = sum4(sum, f);
          acc[2 * (k & 1)] = MFMA(w2f[k & ~1], f, acc[2 * (k & 1)]); acc[2 * (k & 1) + 1] = MFMA(w2f[k | 1], f, acc[2 * (k & 1) + 1]);
          if ((k & 7) == 7) SB();
        }
      } while (again(cx, sum));
      red_send(cx, acc[1], 1); red_send(cx, acc[2], 2); red_send(cx, acc[3], 3);
      lds_barrier();
      red_recv(cx, acc[0], xr);
    }
    FST(2 + L, 15);
    if (L == 0) {
      publish_x(cx, xr, cx.gb + G_X2, cx.gb + G_ST2, cx.gb + G_FX2);
      STAMP(20);
      sfor<16 - NQK>([&](auto ii) LAMBDA { dma<NQK + ii.v>(rs, dA, cx.vo, sA); });
      park(cx, xr);
    } else {
      u32x4 f0, f1; pack16(xr, f0, f1);
      *(u32x4*)(out + xrow) = f0; *(u32x4*)(out + xrow + 8) = f1;
      STAMP(29);
    }
  }
}

__global__ __launch_bounds__(256, 1) void fused(const u16* __restrict__ x, const u16* __restrict__ wqkv, char* ws, const u16* __restrict__ wphi,
                                                const u16* __restrict__ b1, const u16* __restrict__ b2, const u16* __restrict__ w1,
                                                const u16* __restrict__ w2, const u16* __restrict__ wo, u16* __restrict__ out) {
  __shared__ __attribute__((aligned(16))) char ldsA[65536];
  __shared__ __attribute__((aligned(16))) char ldsB[49152];
  __shared__ __attribute__((aligned(16))) char ldsC[49152];
  Cx cx;
  STAMP(0); CSTAMP(30); FST(12, 0); FST(13, 0);
  asm volatile("s_load_dword %0, %2, 0x0\n\ts_load_dword %1, %2, 0x40" : "=&s"(cx.ka[0]), "=&s"(cx.ka[1]) : "s"(__builtin_amdgcn_kernarg_segment_ptr()));
  {
    unsigned long long pc; unsigned len;
    asm volatile("s_getpc_b64 %0\n.Lcb0:\n\ts_mov_b32 %1, .Lce0-.Lcb0-16" : "=s"(pc), "=s"(len));
    unsigned o = ((blockIdx.x >> 3) * 256 + threadIdx.x) * 16;
    if (PRE == 1 || o < PRE * 1024u) gldso<0>(o < len ? o : 0u, (const void*)pc, (unsigned)(size_t)ldsC + 48128u);
    cx.pc = (const char*)pc; cx.po = o < len && o >= PRE * 1024u ? o : 0u;
  }
  FST(12, 1);
  cx.ws = ws; cx.A = ldsA; cx.B = ldsB; cx.C = ldsC;
  cx.lane = threadIdx.x & 63; cx.w = RFL(threadIdx.x >> 6);
  cx.tok = cx.lane & 31; cx.hk = cx.lane >> 5; cx.vo = cx.lane * 16; cx.budget = SPIN_MAX;
  { const int b = blockIdx.x, xx = b & 7, j = b >> 3; cx.g = 4 * (xx >> 1) + (j >> 3); cx.c = 8 * (xx & 1) + ((j + CROT) & 7); }
  cx.cb = THB == 8 ? cx.c & 7 : 4 * (cx.c >> 3) + (cx.c & 3); cx.th = THB == 8 ? cx.c >> 3 : (cx.c >> 2) & 1;
  cx.rs = __builtin_amdgcn_make_buffer_rsrc(ws, 0, WS_USED, 0x00020000);
  u32x4 phq[4], aphi[2], w2f[56], wof[16]; unsigned csw[3], csp[4], b1p[2]; KvIn kin; Misc mi;
  stage1a(cx, x, wqkv, wphi, wo, w1, b1, w2, b2, phq, csw, aphi, kin, w2f, wof, mi);
  layer<0>(cx, x, b1, b2, out, phq, csw, aphi, kin, w2f, wof, mi, csp, b1p);
  asm volatile("" : "+v"(cx.lane), "+v"(cx.tok), "+v"(cx.hk), "+v"(cx.vo));
  asm volatile("" : "+v"(csp[0]), "+v"(csp[1]), "+v"(csp[2]), "+v"(csp[3]), "+v"(b1p[0]), "+v"(b1p[1]), "+v"(csw[0]), "+v"(csw[1]), "+v"(csw[2]));
#pragma unroll
  for (int k = 0; k < W2KEEP; k++) asm volatile("" : "+a"(w2f[k]));
  layer<1>(cx, x, b1, b2, out, phq, csw, aphi, kin, w2f, wof, mi, csp, b1p);
  asm volatile(".Lce0:");
}

extern "C" void kernel_launch(void* const* d_in, const int* in_sizes, int n_in, void* d_out, int out_size, void* d_ws, size_t ws_size,
                              hipStream_t stream) {
  fused<<<256, 256, 0, stream>>>((const u16*)d_in[0], (const u16*)d_in[1], (char*)d_ws, (const u16*)d_in[2], (const u16*)d_in[6],
                                 (const u16*)d_in[8], (const u16*)d_in[5], (const u16*)d_in[7], (const u16*)d_in[3], (u16*)d_out);
}
```

```cpp
#include <hip/hip_runtime.h>
#include <stdint.h>

typedef unsigned short u16;
typedef __attribute__((ext_vector_type(8))) __bf16 bf16x8;
typedef __attribute__((ext_vector_type(2))) __bf16 bf16x2;
typedef __attribute__((ext_vector_type(16))) float f32x16;
typedef __attribute__((ext_vector_type(4))) float f32x4;
typedef __attribute__((ext_vector_type(2))) float f32x2;
typedef __attribute__((ext_vector_type(4))) unsigned u32x4;
typedef __attribute__((ext_vector_type(2))) unsigned u32x2;
typedef __attribute__((ext_vector_type(4))) short s16x4;
typedef __amdgpu_buffer_rsrc_t rsrc_t;
typedef __attribute__((address_space(3))) void* ldsp;

#define DEVI __device__ __forceinline__
#define LAMBDA __attribute__((always_inline))
#define MFMA(a, b, c) __builtin_amdgcn_mfma_f32_32x32x16_bf16(__builtin_bit_cast(bf16x8, a), __builtin_bit_cast(bf16x8, b), c, 0, 0, 0)
#define MFMA16(a, b, c) __builtin_amdgcn_mfma_f32_16x16x32_bf16(__builtin_bit_cast(bf16x8, a), __builtin_bit_cast(bf16x8, b), c, 0, 0, 0)
#define SB() __builtin_amdgcn_sched_barrier(0)
#define RFL(v) __builtin_amdgcn_readfirstlane(v)
#define UNLIKELY(x) __builtin_expect(!!(x), 0)
#define BALLOT(p) __builtin_amdgcn_ballot_w64(p)

constexpr int WT_QKV = 0x0, WQ_C = 98304;
constexpr int WT_O   = 0x180000;
constexpr int WT_1   = 0x200000, W1_C = 114688;
constexpr int CS1    = 0x3C0000;
constexpr int WT_2   = 0x400000;
constexpr int MBX    = 0x5C0000;
constexpr int TOKR   = 0x5E0000;
constexpr int TOKC   = 0x5E1000;
constexpr int KVA    = 0x600000, KV_C = 0xC000, KV_L = 0xC0000, KV_F = 0x8000;
constexpr int MB2    = 0x780000;
constexpr int GRP    = 0x800000, G_S = 0x200000, G_L = 0xC6000;
constexpr int G_O = 0, G_X1 = 0x20000, G_H = 0x40000, G_ST1 = 0xB0000, G_FO = 0xB4000, G_FH = 0xB6000, G_FX1 = 0xBE000;
constexpr int FS = 128, FHL = 0x2000, CKO = 0xC000, CKX = 0x4000, CKH = 0xE000;
constexpr int G_X2 = 0x18C000, G_ST2 = 0x1AC000, G_FX2 = 0x1B0000;
constexpr int WS_USED = 0x2800000;
constexpr int OOB = 0x40000000;
#define STAMP_OFF 0x2800000
#define STAMP_N 64

constexpr int INBOX = 12288;
constexpr int C_V = 4352, C_IMG = 6656, C_ZERO = 7680, C_PARK = 8192;
constexpr float LOG2E = 1.4426950408889634f;
constexpr int SPIN_MAX = 1 << 16;

DEVI unsigned pk2(float a, float b) { f32x2 v = {a, b}; return __builtin_bit_cast(unsigned, __builtin_convertvector(v, bf16x2)); }
DEVI float bflo(unsigned u) { return __uint_as_float(u << 16); }
DEVI float bfhi(unsigned u) { return __uint_as_float(u & 0xffff0000u); }
DEVI void lds_barrier() { asm volatile("s_waitcnt lgkmcnt(0)\n\ts_barrier" ::: "memory"); }
template <int N> DEVI void lds_barrier_n() { asm volatile("s_waitcnt lgkmcnt(%0)\n\ts_barrier" :: "n"(N) : "memory"); }
DEVI void bar() { asm volatile("s_barrier" ::: "memory"); }
template <int N> DEVI void vm_wait() { asm volatile("s_waitcnt vmcnt(%0)" :: "n"(N) : "memory"); }
template <int OFF> DEVI void gldso(unsigned voff, const void* sbase, unsigned lds_base) {
  asm volatile("s_mov_b32 m0, %2\n\ts_nop 0\n\tglobal_load_lds_dwordx4 %0, %1 offset:%3" :: "v"(voff), "s"(sbase), "s"(lds_base), "n"(OFF) : "memory", "m0");
}
DEVI void gldsv(const void* p, unsigned lds_base) {
  asm volatile("s_mov_b32 m0, %1\n\ts_nop 0\n\tglobal_load_lds_dwordx4 %0, off" :: "v"(p), "s"(lds_base) : "memory", "m0");
}
DEVI void gldsc4(unsigned voff, const void* sbase, unsigned lds_base) {
  asm volatile("s_mov_b32 m0, %2\n\ts_nop 0\n\tglobal_load_lds_dwordx4 %0, %1 sc1" :: "v"(voff), "s"(sbase), "s"(lds_base) : "memory", "m0");
}
DEVI void gldsc1(unsigned voff, const void* sbase, unsigned lds_base) {
  asm volatile("s_mov_b32 m0, %2\n\ts_nop 0\n\tglobal_load_lds_dword %0, %1 sc1" :: "v"(voff), "s"(sbase), "s"(lds_base) : "memory", "m0");
}
DEVI float xhalf_sum(float v) {
  auto t = __builtin_amdgcn_permlane32_swap(__float_as_uint(v), __float_as_uint(v), false, false);
  return __uint_as_float(t[0]) + __uint_as_float(t[1]);
}
DEVI u32x4 ldc(rsrc_t rs, int vo, int so) { return __builtin_amdgcn_raw_buffer_load_b128(rs, vo, so, 16); }
DEVI u32x2 ldc2(rsrc_t rs, int vo, int so) { return __builtin_amdgcn_raw_buffer_load_b64(rs, vo, so, 16); }
DEVI unsigned ldc1(rsrc_t rs, int vo, int so) { return __builtin_amdgcn_raw_buffer_load_b32(rs, vo, so, 16); }
DEVI void stc(rsrc_t rs, int vo, int so, u32x4 v) { __builtin_amdgcn_raw_buffer_store_b128(v, rs, vo, so, 16); }
DEVI void stc2(rsrc_t rs, int vo, int so, u32x2 v) { __builtin_amdgcn_raw_buffer_store_b64(v, rs, vo, so, 16); }
DEVI void stc1(rsrc_t rs, int vo, int so, unsigned v) { __builtin_amdgcn_raw_buffer_store_b32(v, rs, vo, so, 16); }
DEVI u32x4 ldck(rsrc_t rs, int vo, int so, int kb) { return ldc(rs, vo + (kb & 3) * 1024, so + (kb >> 2) * 4096); }
template <int KB> DEVI void dma(rsrc_t rs, char* dst, int vo, int so) {
  __builtin_amdgcn_raw_ptr_buffer_load_lds(rs, (ldsp)(dst + (KB >> 2) * 4096), 16, vo, so + (KB >> 2) * 4096, (KB & 3) * 1024, 16);
}
DEVI float lsel(unsigned long long m, float t, float f) { float r; asm("v_cndmask_b32_e64 %0, %1, %2, %3" : "=v"(r) : "v"(f), "v"(t), "s"(m)); return r; }
DEVI float fmax3(float a, float b, float c) { return fmaxf(fmaxf(a, b), c); }
DEVI float fmin3(float a, float b, float c) { return fminf(fminf(a, b), c); }
DEVI unsigned sum4(unsigned s, const u32x4& f) { s = (s + f[0] + f[1]) + f[2] + f[3]; asm volatile("" : "+v"(s)); return s; }
DEVI unsigned wsum(unsigned v) {
  v += __builtin_amdgcn_update_dpp(0u, v, 0x111, 0xf, 0xf, false);
  v += __builtin_amdgcn_update_dpp(0u, v, 0x112, 0xf, 0xf, false);
  v += __builtin_amdgcn_update_dpp(0u, v, 0x114, 0xf, 0xf, false);
  v += __builtin_amdgcn_update_dpp(0u, v, 0x118, 0xf, 0xf, false);
  v += __builtin_amdgcn_update_dpp(0u, v, 0x142, 0xa, 0xf, false);
  v += __builtin_amdgcn_update_dpp(0u, v, 0x143, 0xc, 0xf, false);
  return v;
}

template <int I> DEVI void wsum_step(unsigned& v) {
  constexpr int ctl[6] = {0x111, 0x112, 0x114, 0x118, 0x142, 0x143}, rm[6] = {0xf, 0xf, 0xf, 0xf, 0xa, 0xc};
  v += __builtin_amdgcn_update_dpp(0u, v, ctl[I], rm[I], 0xf, false);
}

#define FINE 0
#define FST(sel, i)
#define RAW(i, v)
#define STAMP(i)
#define CSTAMP(i)
#define COUNT(i)
#define BUD(i)

template <class T>
DEVI void pack16(const T& v, u32x4& a, u32x4& b) {
#pragma unroll
  for (int i = 0; i < 4; i++) { a[i] = pk2(v[2 * i], v[2 * i + 1]); b[i] = pk2(v[8 + 2 * i], v[9 + 2 * i]); }
}
template <int I> struct ic { static constexpr int v = I; constexpr operator int() const { return I; } };
template <int... I> struct iseq {};
template <int N, int... I> struct mkseq : mkseq<N - 1, N - 1, I...> {};
template <int... I> struct mkseq<0, I...> { typedef iseq<I...> type; };
template <class F, int... I> DEVI void sfor_impl(F& f, iseq<I...>) { (f(ic<I>()), ...); }
template <int N, class F> DEVI void sfor(F f) { sfor_impl(f, typename mkseq<N>::type()); }

struct Softmax {
  float mx, mn; f32x2 mm, sv, ss, a[2], e[16];
  DEVI f32x2 arg(float v) { const f32x2 t = {v, v}; return __builtin_elementwise_fma(t, sv, mm); }
  DEVI void step(int i, const f32x16& p, float sc, u32x4 (&ph)[4]) {
    if (i == 0) { mx = fmax3(p[0], p[1], p[2]); mn = fmin3(p[0], p[1], p[2]); mx = fmax3(mx, p[3], p[4]); mn = fmin3(mn, p[3], p[4]); sv[0] = sc; sv[1] = -sc; }
    else if (i < 3) { int b = 5 + 4 * (i - 1); mx = fmax3(mx, p[b], p[b + 1]); mn = fmin3(mn, p[b], p[b + 1]); mx = fmax3(mx, p[b + 2], p[b + 3]); mn = fmin3(mn, p[b + 2], p[b + 3]); }
    else if (i == 3) { mx = fmax3(mx, p[13], p[14]); mn = fmin3(mn, p[13], p[14]); mm[0] = fmaxf(mx, p[15]) * -sc; mm[1] = fminf(mn, p[15]) * sc; a[0] = arg(p[0]); }
    else if (i < 20) {
      int r = i - 4;
      if (r < 15) { a[(r + 1) & 1] = arg(p[r + 1]); SB(); }
      e[r][0] = __builtin_amdgcn_exp2f(a[r & 1][0]); e[r][1] = __builtin_amdgcn_exp2f(a[r & 1][1]);
      SB();
      if (r == 2) ss = e[0] + e[1]; else if (r > 2) ss += e[r - 1];
    } else if (i == 20) { ss += e[15]; ss[0] = __builtin_amdgcn_rcpf(ss[0]); ss[1] = __builtin_amdgcn_rcpf(ss[1]); }
    else if (i < 29) {
      int t = i - 21;
      const f32x2 u = e[2 * t] * ss, v = e[2 * t + 1] * ss;
      ph[t >> 2][t & 3] = pk2(u[0], v[0]);
      ph[2 + (t >> 2)][t & 3] = pk2(u[1], v[1]);
    }
  }
};

template <int C> DEVI u32x4 tr_readc(const char* p) {
  typedef __attribute__((address_space(3))) s16x4* lp;
  s16x4 a = __builtin_amdgcn_ds_read_tr16_b64_v4i16((lp)(p));
  s16x4 b = __builtin_amdgcn_ds_read_tr16_b64_v4i16((lp)(p + C));
  u32x2 ua = __builtin_bit_cast(u32x2, a), ub = __builtin_bit_cast(u32x2, b);
  u32x4 r = {ua[0], ua[1], ub[0], ub[1]};
  return r;
}
DEVI u32x4 tr_read2(const char* p, const char* p2) {
  typedef __attribute__((address_space(3))) s16x4* lp;
  s16x4 a = __builtin_amdgcn_ds_read_tr16_b64_v4i16((lp)(p));
  s16x4 b = __builtin_amdgcn_ds_read_tr16_b64_v4i16((lp)(p2));
  u32x2 ua = __builtin_bit_cast(u32x2, a), ub = __builtin_bit_cast(u32x2, b);
  u32x4 r = {ua[0], ua[1], ub[0], ub[1]};
  return r;
}

DEVI u32x4 aug_tok(float mean, float ia, int hk) {
  unsigned mh = pk2(-mean, ia);
  unsigned ml = pk2(-mean - bflo(mh), ia - bfhi(mh));
  u32x4 t;
  t[0] = __builtin_amdgcn_perm(ml, mh, 0x05040100u);
  t[1] = mh;
  t[2] = ml >> 16;
  t[3] = 0;
  if (hk) { t[0] = 0; t[1] = 0; t[2] = 0; }
  return t;
}
DEVI unsigned cs_pack(float cs) { unsigned h = pk2(cs, 0.f); return pk2(cs, cs - bflo(h)); }
DEVI u32x4 aug_w(unsigned csp, unsigned b) {
  u32x4 t;
  t[0] = __builtin_amdgcn_perm(csp, csp, 0x01000100u);
  t[1] = __builtin_amdgcn_perm(b, csp, 0x05040302u);
  t[2] = b & 0xffffu;
  t[3] = 0;
  return t;
}

struct Cx {
  rsrc_t rs; char* ws; char *A, *B, *C;
  int lane, w, tok, hk, g, c, cb, th, vo, gb;
  int fv16, fv8, fvz, f63, oo, ho, fo, xo, so, fxo;
  unsigned N, tk_kv, tk_oh, tk_x, ka[2], po; const char* pc;
  int budget;
};

DEVI void put_flag(Cx& cx, int so, int i) { u32x2 v = {cx.N + i, cx.N + i}; stc2(cx.rs, cx.f63, so, v); }
DEVI void put_final(Cx& cx, int so, unsigned sum) { u32x2 v = {cx.N + 7, cx.N - wsum(sum)}; stc2(cx.rs, cx.f63, so, v); }
template <int NP> DEVI void poll_wait(Cx& cx, unsigned a, int so, unsigned tk, int i) {
  unsigned t; int s;
  asm volatile("v_sub_u32 %0, %3, %4\n\t"
               "v_cmp_ne_u32 vcc, %9, %0\n\t"
               "s_and_b32 %1, vcc_lo, %8\n\t"
               "s_cbranch_scc0 2f\n"
               "1:\n\t"
               "s_cmp_eq_u32 %2, 0\n\t"
               "s_cbranch_scc1 2f\n\t"
               "buffer_load_dword %0, %5, %6, %7 offen sc1\n\t"
               "s_sub_u32 %2, %2, 1\n\t"
               "s_waitcnt vmcnt(0)\n\t"
               "v_sub_u32 %0, %0, %4\n\t"
               "v_cmp_ne_u32 vcc, %9, %0\n\t"
               "s_and_b32 %1, vcc_lo, %8\n\t"
               "s_cbranch_scc1 1b\n"
               "2:"
               : "=&v"(t), "=&s"(s), "+s"(cx.budget)
               : "v"(a), "v"(tk), "v"(NP == 16 ? cx.fv16 : cx.fv8), "s"(cx.rs), "s"(so), "n"((1 << NP) - 1), "n"(i)
               : "vcc", "scc", "memory");
}
DEVI void kv_wait(Cx& cx, u32x4& q, int so) {
  int s; unsigned f = q[2];
  asm volatile("v_sub_u32 %0, %0, %4\n\t"
               "v_cmp_ne_u32 vcc, 7, %0\n\t"
               "s_and_b32 %2, vcc_hi, 0xffff\n\t"
               "s_cbranch_scc0 2f\n"
               "1:\n\t"
               "s_cmp_eq_u32 %3, 0\n\t"
               "s_cbranch_scc1 2f\n\t"
               "buffer_load_dword %0, %5, %6, %7 offen offset:8 sc1\n\t"
               "s_sub_u32 %3, %3, 1\n\t"
               "s_waitcnt vmcnt(0)\n\t"
               "v_sub_u32 %0, %0, %4\n\t"
               "v_cmp_ne_u32 vcc, 7, %0\n\t"
               "s_and_b32 %2, vcc_hi, 0xffff\n\t"
               "s_cbranch_scc1 1b\n\t"
               "buffer_load_dwordx4 %1, %5, %6, %7 offen sc1\n\t"
               "s_waitcnt vmcnt(0)\n"
               "2:"
               : "+v"(f), "+v"(q), "=&s"(s), "+s"(cx.budget)
               : "v"(cx.tk_kv), "v"(cx.fvz), "s"(cx.rs), "s"(so)
               : "vcc", "scc", "memory");
}
#define FORCE_BAD 0
DEVI bool bad(Cx& cx, unsigned sum) { return __builtin_amdgcn_readlane(wsum(sum), 63) + (FORCE_BAD ? cx.w & 1 : 0) != 0; }
DEVI bool again(Cx& cx, unsigned sum) {
  const unsigned r = __builtin_amdgcn_readlane(wsum(sum), 63);
  return (r != 0) & (cx.budget >= 64);
}
DEVI void spend(Cx& cx, int id) {
  asm volatile("" ::: "memory"); cx.budget = cx.budget >= 64 ? cx.budget - 64 : 0; }
#define D_O 8
#define D_X1 6
#define D_X2 2
#define CROT 0
#define THB 8
#define PRE 32
#define TOUCH 1
template <int N> DEVI void nap() { if (N > 0) { SB(); __builtin_amdgcn_s_sleep(N); SB(); } }
DEVI unsigned usel(unsigned long long m, unsigned t, unsigned f) { unsigned r; asm("v_cndmask_b32_e64 %0, %1, %2, %3" : "=v"(r) : "v"(f), "v"(t), "s"(m)); return r; }
DEVI void mail_wait(Cx& cx, unsigned a, int mvo, int so, unsigned v) {
  int s;
  asm volatile("v_cmp_ne_u32 vcc, %3, %0\n\t"
               "s_and_b32 %1, vcc_lo, 0xffff\n\t"
               "s_cbranch_scc0 2f\n"
               "1:\n\t"
               "s_cmp_eq_u32 %2, 0\n\t"
               "s_cbranch_scc1 2f\n\t"
               "buffer_load_dword %0, %4, %5, %6 offen sc1\n\t"
               "s_sub_u32 %2, %2, 1\n\t"
               "s_waitcnt vmcnt(0)\n\t"
               "v_cmp_ne_u32 vcc, %3, %0\n\t"
               "s_and_b32 %1, vcc_lo, 0xffff\n\t"
               "s_cbranch_scc1 1b\n"
               "2:"
               : "+v"(a), "=&s"(s), "+s"(cx.budget)
               : "s"(v), "v"(mvo), "s"(cx.rs), "s"(so)
               : "vcc", "scc", "memory");
}
DEVI void kv_write_v(Cx& cx, const f32x16& accv, float rstd) {
  float vn[16];
#pragma unroll
  for (int r = 0; r < 16; r++) vn[r] = accv[r] * rstd;
  u32x4 v0, v1; pack16(vn, v0, v1);
  char* p = cx.C + 2 * cx.hk * INBOX + C_V + (32 * cx.w + cx.tok) * 16;
  *(u32x4*)p = v0; *(u32x4*)(p + INBOX + 128) = v1;
}
template <int VMW>
DEVI void kv_tail(Cx& cx, const u32x4 (&phk)[4], int kvb) {
  char* p = cx.C + 2 * cx.hk * INBOX + (32 * cx.w + cx.tok) * 16;
  *(u32x4*)p = phk[0]; *(u32x4*)(p + 2176) = phk[1]; *(u32x4*)(p + INBOX) = phk[2]; *(u32x4*)(p + INBOX + 2176) = phk[3];
  const int G = cx.lane >> 4, d = cx.lane & 15, q4 = d >> 2, p4 = cx.lane & 3;
  const int ro = (4 * G + q4) * 16 + (p4 & 1) * 8;
  const char* pa = cx.C + cx.w * INBOX + (p4 >> 1) * 2176 + ro;
  const char* pb = cx.C + ((cx.w & 2) + (p4 >> 1)) * INBOX + C_V + (p4 >> 1) * 128 + ro;
  if (VMW >= 0) vm_wait<(VMW >= 0 ? VMW : 0)>();
  lds_barrier();
  u32x4 a[4], b[4];
#pragma unroll
  for (int t = 0; t < 4; t++) { a[t] = tr_readc<256>(pa + t * 512); b[t] = tr_readc<256>(pb + t * 512); }
  const u32x4 ones = {0x3f803f80u, 0x3f803f80u, 0x3f803f80u, 0x3f803f80u};
  f32x4 kv = {}, z = {};
#pragma unroll
  for (int t = 0; t < 4; t++) { kv = MFMA16(a[t], b[t], kv); z = MFMA16(a[t], ones, z); }
  const u32x2 k2 = {pk2(kv[0], kv[1]), pk2(kv[2], kv[3])}, z2 = {pk2(z[0], z[1]), pk2(z[2], z[3])};
  const int e = cx.w * 16 + cx.g;
  stc2(cx.rs, (G >> 1) * 256 + d * 16 + (G & 1) * 8, kvb + e * 512, k2);
  const unsigned cs = -7u - wsum(k2[0] + k2[1] + usel(0x1000900010001000ull, z2[0] + z2[1], 0u));
  const u32x2 zl = {usel(1ull << 63, cx.N + 7, z2[0]), usel(1ull << 63, cs, z2[1])};
  stc2(cx.rs, d >= 12 ? (d - 12) * 32 + G * 8 : OOB, kvb + KV_F + e * FS, zl);
}
struct KvIn { u32x4 pa[8], pz; };
struct Misc { u32x4 xa, xb, ba, bb, cs[4], msg; int pw, ra[2]; unsigned jv; u16 b1v[4]; };
DEVI u32x4 kv_poll(Cx& cx, int kvb) { return ldc(cx.rs, cx.fvz, kvb + KV_F + cx.w * 16 * FS); }
DEVI void kv_load(Cx& cx, KvIn& k, int kvb) {
#pragma unroll
  for (int m = 0; m < 8; m++) k.pa[m] = ldck(cx.rs, cx.vo, kvb + cx.w * 8192, m);
}

#define WOFO(k) ((k) < 8 ? 2 * (k) + 1 : 2 * ((k) - 8))
#define W2KEEP 45
template <int B, class K> DEVI void wload(Cx& cx, K, u32x4 (&w2f)[56], Misc& mi) {
  constexpr int k = B + K::v;
  if (k < 0) return;
  if (k < 4) mi.cs[k & 3] = ldck(cx.rs, cx.vo, CS1 + cx.c * 16384 + cx.w * 4096, k & 3);
  else if (k < 16) dma<((k - 4) & 15)>(cx.rs, cx.B + cx.w * 12288, cx.vo, WT_1 + cx.c * W1_C + 65536 + cx.w * 12288);
  else if (k < 32) dma<(k & 15)>(cx.rs, cx.A + cx.w * 16384, cx.vo, WT_1 + cx.c * W1_C + cx.w * 16384);
  else if (k < W2KEEP) w2f[k] = ldck(cx.rs, cx.vo, WT_2 + (cx.cb * 4 + cx.w) * 57344, k);
}
template <int K> DEVI void rawld(Cx& cx, rsrc_t r, int vo, int so, int bs) {
  __builtin_amdgcn_raw_ptr_buffer_load_lds(r, (ldsp)(cx.A + cx.w * 16384 + (K >> 2) * 4096), 16, vo, so + (K >> 2) * bs + (K & 3) * 7168, (K & 3) * 1024, 0);
}
template <int K> DEVI void rawlda(Cx& cx, rsrc_t r, int vo, int so, int bs) {
  if (!(K & 3)) asm volatile("s_mov_b32 m0, %0" :: "s"((unsigned)(size_t)cx.A + cx.w * 16384 + (K >> 2) * 4096) : "memory", "m0");
  asm volatile("s_nop 0\n\tbuffer_load_dwordx4 %0, %1, %2 offen offset:%3 lds" :: "v"(vo), "s"(r), "s"(so + (K >> 2) * bs + (K & 3) * 7168), "n"((K & 3) * 1024) : "memory");
}
DEVI u32x4 rawrd(Cx& cx, const Misc& mi, int blk, int e) { return tr_readc<512>(cx.C + mi.ra[e & 1] + blk * 4096 + (e >> 1) * 1024); }
#define JS 13
#define ROTM c + 2 * g
#define XT 0
#define R2B 1
#define RKD 1
#define RK 0
#define RA 12
#define QJ 1
#define QR 28
#define QM 2
#define QS 15
#define QCS 12
#define SND 26
#define MBI 8
DEVI f32x4 sq4(const u32x4& f, int h, f32x4 c) {
  const u32x2 t = {f[2 * h], f[2 * h + 1]};
  return __builtin_amdgcn_mfma_f32_4x4x4bf16_1k(__builtin_bit_cast(s16x4, t), __builtin_bit_cast(s16x4, t), c, 0, 0, 0);
}
#define KQP0 31
#define KQW0 12

#define S1SCH \
  120, 121, 122,  123, 124, 125,  134, 135,  60,  130, 131,   0,  132, 133,  61,  144, 145,  62,  154, 155,  63,  140, 141,  64, \
  142, 143,  65,  164, 165,   5,  174, 175,   0,  150, 151,   0,  152, 153,   0,  116, 126,   0,  136, 146,   0,  160, 161,   0, \
  162, 163,   0,  156, 166,   0,  176,   0,   0,  170, 171,   0,  172, 173,  40,   31,  32,   0,    0,   0,   0,   10,  11,   0, \
   12,  13,   0,    0,   0,   0,    0,   0,   0,    0,   0,  50,   70,   0,   0,   71,   0,   0,   72,   0,   0,   73,   0,   0
#define S1PRE 2, 3, 100, 101, 102, 103, 104, 105, 110, 111, 30, 112, 113, 114, 115, 4, 106
constexpr int S1P = 17;
constexpr int s1_op[S1P + 96] = {S1PRE, S1SCH};
constexpr int s1_key(int o) { return o >= 100 ? o / 10 * 10 + (o % 10 >= 4) + (o % 10 == 6) : o >= 10 && o < 20 ? 10 : o >= 70 && o < 80 ? 70 + (o >= 74) : (o >= 20 && o < 40) || o >= 50 ? 0 : o; }
constexpr bool s1_m0(int i) {
  for (int k = i - 1; k >= 0; k--) if (s1_key(s1_op[k])) return s1_key(s1_op[k]) != s1_key(s1_op[i]);
  return true;
}
constexpr int s1_pos(int o) { for (int k = 0; k < S1P + 96; k++) if (s1_op[k] == o) return k; return -1; }
constexpr int R0A = s1_pos(74) >= 0 ? 8 : s1_pos(70) >= 0 ? 4 : 0;
constexpr int slot(int j) { return (((j & 3) << 1) | (j >> 2)) * 8192; }
constexpr int s1_step(int o) { return (s1_pos(o) - S1P) / 3; }
constexpr int s1_cnt(int n, int lo, int hi, int e = 0) {
  int last = e ? s1_pos(e) : -1, r = 0;
  for (int o = lo; o <= hi; o++) if (s1_pos(o) > last) last = s1_pos(o);
  for (int k = last + 1; k < S1P + 3 * n; k++) r += s1_op[k] != 0;
  return r;
}
constexpr int s1_last(int lo, int hi, int d) {
  int last = -1, r = 0;
  for (int o = lo; o <= hi; o += d) if (s1_pos(o) > last) last = s1_pos(o);
  for (int k = last + 1; k < S1P + 96; k++) r += s1_op[k] != 0;
  return r;
}
template <int OFF, bool M> DEVI void gldm(unsigned voff, const void* sbase, unsigned lds_base) {
  if (M) gldso<OFF>(voff, sbase, lds_base);
  else asm volatile("global_load_lds_dwordx4 %0, %1 offset:%2" :: "v"(voff), "s"(sbase), "n"(OFF) : "memory");
}

DEVI void stage1a(Cx& cx, const u16* __restrict__ x, const u16* __restrict__ wqkv, const u16* __restrict__ wphi,
                  const u16* __restrict__ wo, const u16* __restrict__ w1, const u16* __restrict__ b1, const u16* __restrict__ w2,
                  const u16* __restrict__ b2, u32x4 (&phq)[4], unsigned (&csw)[3], u32x4 (&aphi)[2], KvIn& kin, u32x4 (&w2f)[56],
                  u32x4 (&wof)[16], Misc& mi) {
  const int lane = cx.lane, w = cx.w, tok = cx.tok, hk = cx.hk, g = cx.g, c = cx.c;
  const int rot = (ROTM) & 7;
  const rsrc_t rs = cx.rs;
  const unsigned lA = (unsigned)(size_t)cx.A, lB = (unsigned)(size_t)cx.B, lC = (unsigned)(size_t)cx.C;
  char* xs = cx.C + w * INBOX;
  char* stg = cx.B + 32768 + w * 4096;
  const unsigned lstg = lB + 32768 + w * 4096;
  unsigned vx[4];
#pragma unroll
  for (int i = 0; i < 4; i++) vx[i] = (8 * i + (lane >> 3)) * 1024 + (((lane & 7) ^ ((4 * i + (lane >> 4)) & 7)) * 16) - i * 1024;
  const char* xb = (const char*)(x + (size_t)(128 * g + 32 * w) * 512);
  const unsigned xl = lC + w * INBOX;
  const unsigned wvo = (lane >> 2) * 3072 + (lane & 3) * 16;
  const char* wb = (const char*)(wqkv + (size_t)(16 * w) * 1536 + 32 * c);
  auto cload = [&](auto kk) LAMBDA {
    constexpr int j = s1_op[kk.v] / 10 - 10, i = s1_op[kk.v] % 10; constexpr bool M = s1_m0(kk.v);
    const int cj = (j + rot) & 7;
    if (i < 4) gldm<(i & 3) * 1024, M>(vx[i & 3], xb + cj * 128, xl + (j % 3) * 4096);
    else if (i < 6) gldm<(i & 1) * 1024, M>(wvo + 1024, wb + cj * (64 * 3072), lA + slot(j) + w * 2048);
    else gldm<0, M>(wvo, wb + cj * (64 * 3072), lB + j * 4096 + w * 1024);
  };
  const unsigned tvo = lane < 16 ? TOKR + g * 128 + lane * 4 : TOKC + cx.cb * 128 + ((lane - 16) & 31) * 4;
  const unsigned mvo = MBX + (g * 16 + c) * 512 + (lane & 31) * 16;
  asm volatile("" :: "s"(xb), "s"(wb), "s"(cx.ws), "s"(cx.ka[0]), "s"(cx.ka[1]));
  FST(12, 2);
  sfor<s1_pos(30)>([&](auto kk) LAMBDA {
    constexpr int o = s1_op[kk.v];
    if (o == 2) gldsc1(tvo, cx.ws, lstg); else if (o == 3) gldsc4(mvo, cx.ws, lstg + 1024); else cload(kk);
  });
  SB(); FST(12, 3);
  cx.gb = GRP + cx.g * G_S;
  cx.fv16 = cx.lane < 16 ? cx.lane * FS + 120 : OOB; cx.fv8 = cx.lane < 8 ? cx.lane * FS + 120 : OOB; cx.f63 = cx.lane >= 48 ? (cx.lane - 48) * 8 : OOB;
  cx.fvz = cx.lane < 32 ? (cx.lane >> 1) * FS + (cx.lane & 1) * 16 : cx.lane < 48 ? (cx.lane - 32) * FS + 112 : OOB;
  {
    const int kq = cx.c >> 2, tx = (w & 1) ^ (kq >> 1), ln = (w >> 1) * 4 + kq, tb = 2 * cx.th + (w >> 1), blk = 2 * cx.cb + (w & 1);
    cx.oo = (ln * 16 + tx * 8 + (cx.c & 3) * 2) * 1024;
    cx.ho = (ln * 56 + (cx.c & 3) * 2 + tx) * 1024;
    cx.fo = (ln * 8 + (cx.c & 3) * 2 + (w & 1)) * FS;
    cx.xo = (tb * 32 + 2 * blk) * 1024;
    cx.so = tb * 4096 + blk * 256;
    cx.fxo = (tb * 16 + blk) * FS;
  }
  const int ch = (lane >> 4) & 1, q4 = (lane & 15) >> 2, p4 = lane & 3;
  const int cperm = (16 * (p4 & 1) + 4 * (2 * ch + (p4 >> 1))) * 2;
  int r_tr, r_a, r_b, r_c, r_t3, r_c3, r_dst, r_d1, r_cs = OOB; unsigned r_d3;
  const char *src, *src2, *src3; unsigned v0, v1, v2;
  {
    const int cb = cx.cb, hf = g & 1, cc = 8 * cx.th + (g >> 1), kq = cc >> 2;
    const char* s2 = (const char*)(w2 + (size_t)(112 * cc) * 512 + 64 * cb);
    const int d2 = WT_2 + ((cb * 4 + kq) * 28 + (cc & 3)) * 2048;
    if (w < 2) {
      const int n = 2 * g + w;
      src = src2 = (const char*)(w1 + (size_t)(32 * g + 8 * w) * 1792 + 112 * c);
      auto vo = [&](int t) LAMBDA { const int i = (t * 4682) >> 16; return (i + (i & 8)) * 3584 + (t - 14 * i) * 16; };
      v0 = vo(lane); v1 = vo(lane + 64); v2 = vo(lane + 128);
      src3 = hk ? s2 + (96 + 8 * w + (tok >> 2)) * 1024 + hf * 64 + p4 * 16 : src + vo(lane + 192);
      r_tr = (8 * hk + q4) * 224 + cperm; r_a = 64; r_b = 128; r_c = 896;
      r_t3 = hk ? 3584 + q4 * 64 + cperm : (8 * ch + q4) * 224 + 192 + (8 * (p4 & 1) + 4 * (p4 >> 1)) * 2; r_c3 = hk ? 256 : 896;
      r_dst = WT_1 + c * W1_C + n * 2048; r_d1 = 65536 - n * 512;
      r_d3 = hk ? d2 + 24 * 2048 + (hf ^ (kq & 1)) * 1024 + (32 * w + tok) * 16 : r_dst + r_d1 + 1024 + lane * 16;
      r_cs = CS1 + c * 16384 + n * 512;
    } else {
      v0 = (lane >> 3) * 1024 + (lane & 7) * 16; r_c = r_c3 = 512;
      if (w == 2) {
        src = src2 = s2 + hf * 65536; v1 = v0 + 8192; v2 = v0 + 16384; src3 = src + v0 + 24576;
        r_tr = (16 * hk + q4) * 128 + cperm; r_b = 1024;
        r_dst = d2 + hf * 32768; r_d1 = 8192;
      } else {
        src = s2 + (32 + 8 * hf) * 1024; src2 = (const char*)(wo + (size_t)(32 * cc + 8 * hf) * 512 + 64 * cb);
        v1 = v0 + 16384; v2 = v0; src3 = src2 + v1;
        r_tr = (8 * hk + q4) * 128 + cperm; r_b = 2048;
        r_dst = d2 + (8 + 4 * hf) * 2048; r_d1 = WT_O + ((cb * 4 + kq) * 8 + ((2 * cc) & 7) + hf) * 2048 - r_dst;
      }
      r_tr += (kq & 1) * 64; r_a = (kq & 1) ? -64 : 64;
      r_t3 = r_tr + r_a + r_b; r_d3 = r_dst + r_d1 + 1024 + lane * 16;
    }
    r_dst = RFL(r_dst); r_d1 = RFL(r_d1); r_cs = RFL(r_cs); src2 -= 2048; src3 -= 3072; v1 -= 1024;
    asm volatile("" : "+v"(v0), "+v"(v1), "+v"(v2), "+v"(src3), "+v"(r_tr), "+v"(r_t3), "+v"(r_d3), "+s"(src), "+s"(src2), "+s"(r_dst), "+s"(r_d1), "+s"(r_cs));
  }
  FST(12, 4);
  const int xro = tok * 128 + (((2 * hk) ^ ((tok >> 1) & 7) ^ (4 * (w >> 1) + (w & 1))) * 16);
  const char* wk = cx.A + hk * 2048 + q4 * 64 + cperm;
  const char* wq = cx.B + hk * 1024 + q4 * 64 + cperm;
  const char* wks[4];
#pragma unroll
  for (int s = 0; s < 4; s++) { const int t = s ^ w; wks[s] = wk + (t >> 1) * 4096 + (t & 1) * 512; }
  const unsigned on1 = (ch == ((lane >> 2) & 1)) ? 0x3f803f80u : 0u;
  const u32x4 sel = {on1, on1, on1, on1};
  int mdst = w != 0 || lane >= 48 ? OOB
                 : lane < 16 ? MBX + (g * 16 + lane) * 512 + c * 8
                             : MBX + (((lane - 16) & 15) * 16 + ((c & ~THB) | ((lane - 16) >> 4) * THB)) * 512 + (16 + g + 16 * cx.th) * 8;
  unsigned N = 0, sent = 0;
  u32x4 xf[33], ka[3], va[3], rf[4];
  f32x16 acck = {}, accv = {};
  f32x4 csk = {}, csv = {}, s1v = {}, q0 = {}, q1 = {}, cj[4];
  f32x2 cso[3] = {};
  auto rdx = [&](int n) LAMBDA { if ((XT & 1) && n > 1) { xf[n] = xf[n & 1]; return; } xf[n] = *(const u32x4*)(xs + ((n >> 2) % 3) * 4096 + (xro ^ ((4 * ((n >> 1) & 1) + (n & 1)) * 16))); };
  auto rdk = [&](int n) LAMBDA { ka[n % 3] = tr_readc<256>(wks[n & 3] + slot(n >> 2)); };
  auto rdv = [&](int n) LAMBDA { if (XT & 2) { va[n % 3] = ka[n % 3]; return; } va[n % 3] = tr_readc<256>(wks[n & 3] + 1024 + slot(n >> 2)); };
  unsigned tk0, tk1;
  const int kvb = KVA + c * KV_C;
  int tch;
  {
    const int t = lane >> 3;
    tch = (t < 5 ? G_FO + t * FHL + cx.fo : t == 5 ? G_FX1 + cx.fxo : t == 6 ? G_FX2 + cx.fxo : kvb + KV_L + KV_F + (w * 16 + g) * FS - cx.gb) + (lane & 7) * 16 + cx.gb;
  }
  int tc2 = lane < 48 ? tch : lane < 56 && w == 0 ? MB2 - G_L + (g * 16 + c) * FS + (lane & 7) * 16 : OOB;
  int tso = w == 0 && (lane == c || lane == 16 + 16 * cx.th + g) ? (int)tvo : OOB;
  const int col = 64 * cx.cb + 32 * (w & 1) + 16 * hk;
  const int pi = 16 * ((tok >> 2) & 1) + (tok & 3) + 4 * (tok >> 3), ph = 8 * ((tok >> 2) & 1) + (tok & 3) + 4 * ((tok >> 3) & 1);
  const char* bp = (const char*)(b1 + 112 * c);
  unsigned opi = 2 * pi, oph = 2 * ph + 192, ocol = 2 * col;
  asm volatile("" : "+v"(tch), "+v"(tc2), "+v"(tso), "+v"(mdst), "+v"(opi), "+v"(oph), "+v"(ocol));
  const int js = ((g >> 1) - rot) & 7;
  u32x4 wf0, wf1;
  auto rdwf = [&]() LAMBDA { const char* wsrc = wk + (w & 1) * 1024 + slot(js) + (g & 1) * 4096; wf0 = tr_readc<256>(wsrc); wf1 = tr_readc<256>(wsrc + 512); };
  const rsrc_t ro = __builtin_amdgcn_make_buffer_rsrc((void*)wo, 0, 512 * 1024, 0x00020000), r2 = __builtin_amdgcn_make_buffer_rsrc((void*)w2, 0, 1792 * 1024, 0x00020000);
  int rv = (lane >> 3) * 1024 + ((lane & 7) ^ (4 * ch)) * 16;
  asm volatile("" : "+v"(rv));
  auto issue = [&](auto kk) LAMBDA {
    constexpr int o = s1_op[kk.v]; constexpr bool M = s1_m0(kk.v);
    if (o >= 100) cload(kk);
    if (o == 10) gldm<0, M>(v0, src, xl + 8192);
    if (o == 11) gldm<1024, M>(v1, src, xl + 8192);
    if (o == 12) gldm<2048, M>(v2, src2, xl + 8192);
    if (o == 13) {
      if (M) asm volatile("s_mov_b32 m0, %0\n\ts_nop 0" :: "s"(xl + 8192) : "memory", "m0");
      asm volatile("global_load_lds_dwordx4 %0, off offset:3072" :: "v"(src3) : "memory");
    }
    if (o == 30) {
      N = __builtin_amdgcn_readlane(tk0, c) + 64;
      while (BALLOT(tk0 - N < 64u || tk1 - N < 64u) != 0) N += 64;
      stc1(rs, tso, 0, N);
      *(unsigned*)(stg + 768) = N;
    }
    if (o == 31 || o == 32) {
      const u32x4 v = {cx.N, cx.N, cx.N, cx.N};
      if (o == 31) stc(rs, tch, 0, v); else stc(rs, tc2, G_L, v);
    }
    if (o == 4) gldso<0>(lane * 16, wphi + c * 512, lstg + 2048);
    if (o == 5) gldso<0>(cx.po, cx.pc, lstg + 1024);
    if (o >= 70 && o < 80) { static_assert(o < 70 || o >= 80 || M == !((o - 70) & 3), ""); rawlda<((o - 70) & 15)>(cx, ro, rv, w * 131072 + cx.cb * 128, 32768); }
    if (o == 40) gldsc1(tvo, cx.ws, lstg + 256);
    if (o >= 60 && o < 64) mi.b1v[o & 3] = *(const u16*)(bp + (o == 63 ? oph : opi) + (o == 63 ? 0 : 64 * (o - 60)));
    if (o == 64) mi.ba = *(const u32x4*)((const char*)b2 + ocol);
    if (o == 65) mi.bb = *(const u32x4*)((const char*)b2 + ocol + 16);
    if (o >= 50 && o < 60) {
      const u32x2 m = {tk0, N};
      stc2(rs, o == 50 || tk0 != sent ? mdst : OOB, 0, m);
      sent = tk0;
    }
    SB();
  };
  static_assert(s1_step(50) > SND && SND % 4 == 2 && s1_pos(30) < S1P && s1_pos(2) < s1_pos(3) && s1_pos(3) < s1_pos(30), "");
  static_assert(!R0A || s1_step(70) > 22, "");
  static_assert(s1_step(10) > 22 && s1_step(11) > 22 && s1_step(12) > 22 && s1_step(13) > 22, "");
  SB(); FST(12, 5);
  vm_wait<s1_pos(30) - s1_pos(3) - 1>();
  tk0 = *(const unsigned*)(stg + lane * 4); tk1 = *(const unsigned*)(stg + 1024 + lane * 8);
  sfor<S1P - s1_pos(30)>([&](auto kk) LAMBDA { issue(ic<s1_pos(30) + kk.v>()); });
  FST(12, 6); vm_wait<s1_cnt(0, 100, 105)>(); FST(12, 7); bar(); STAMP(1); FST(1, 0); FST(12, 8);
  rdx(0); rdk(0); rdv(0); rdx(1); rdk(1); rdv(1);
  SB();
  float s2 = 0.f;
  sfor<32>([&](auto nn) LAMBDA {
    constexpr int n = nn, j = n >> 2, s = n & 3;
    if (n < 31) FST(13, n + 1);
    if (s == 2 && j < 7) {
      constexpr int e = n == SND ? 40 : j == 1 ? 4 : 0;
      constexpr int cnt = s1_cnt(n, 110 + 10 * j, 115 + 10 * j, e);
      static_assert(n != SND || s1_step(40) < n, "");
      FST(1, 2 * j + 1); FST(12, 9 + 2 * j); vm_wait<cnt>();
      if (j == 6) lds_barrier_n<5>(); else bar();
      FST(1, 2 * j + 2); FST(12, 10 + 2 * j);
    }
    constexpr bool cs = s == 0 || j == 7;
    acck = MFMA(ka[n % 3], xf[n], acck); SB();
    if (n == 25) { const f32x2 v = {csk[0], csv[0]}; *(f32x2*)(stg + 3072 + lane * 8) = v; SB(); }
    issue(ic<S1P + 3 * n>());
    if (n + 2 < 32) rdx(n + 2);
    if (n == 27) {
#pragma unroll
      for (int t = 0; t < 3; t++) cso[t] = *(const f32x2*)(cx.B + 32768 + (w ^ (t + 1)) * 4096 + 3072 + lane * 8);
    }
    if (n == 29) { cso[0] += cso[1] + cso[2]; asm volatile("" : "+v"(cso[0])); }
    if (n == 17 && R0A) rdwf();
    if (n == SND) tk0 = *(const unsigned*)(stg + 256 + lane * 4);
    if (n == 7) tk1 = *(const unsigned*)(cx.B + 32768 + 768);
    if (n == 9) cx.N = RFL(tk1);
    SB();
    if (cs) { csk = MFMA16(sel, ka[n % 3], csk); SB(); }
    if (n + 2 < 32) rdk(n + 2);
    SB();
    accv = MFMA(va[n % 3], xf[n], accv); SB();
    issue(ic<S1P + 3 * n + 1>());
    SB();
    if (cs) { csv = MFMA16(sel, va[n % 3], csv); SB(); }
    if (n + 2 < 32) rdv(n + 2);
    SB();
    if (n == 0) { q0 = sq4(xf[0], 0, q0); q1 = sq4(xf[0], 1, q1); s1v = MFMA16(sel, xf[0], s1v); SB(); }
    if (n < 31 && !(XT & 4)) { q0 = sq4(xf[n + 1], 0, q0); q1 = sq4(xf[n + 1], 1, q1); SB(); }
    if (n == 10) {
      const char* ap = stg + 2048 + hk * 512 + q4 * 32 + (2 * ch + (p4 >> 1)) * 8;
      aphi[0] = tr_readc<128>(ap); aphi[1] = tr_readc<128>(ap + 256);
    }
    if (n == 24) {
      const bool on = ((lane >> 2) & 1) == hk;
#pragma unroll
      for (int t = 0; t < 4; t++) { aphi[0][t] = on ? aphi[0][t] : 0u; aphi[1][t] = on ? aphi[1][t] : 0u; }
      asm volatile("" : "+v"(aphi[0]), "+v"(aphi[1]));
    }
    if (n == 31) {
      q0 += q1;
      const float u0 = lsel(0xCCCCCCCCCCCCCCCCull, q0[2], q0[0]), u1 = lsel(0xCCCCCCCCCCCCCCCCull, q0[3], q0[1]);
      s2 = xhalf_sum(lsel(0xAAAAAAAAAAAAAAAAull, u1, u0));
    }
    SB();
    if (n < 31 && !(XT & 4)) { s1v = MFMA16(sel, xf[n + 1], s1v); SB(); }
    issue(ic<S1P + 3 * n + 2>());
    if (n < 31) asm volatile("" : "+a"(acck), "+a"(accv), "+a"(csk), "+a"(csv), "+a"(s1v), "+a"(q0), "+a"(q1));
    SB();
  });
  STAMP(2); FST(1, 15); FST(11, 0);
  const float mean = s1v[0] * (1.f / 512.f);
  xf[32] = aug_tok(mean, 0.f, hk);
  csw[1] = cs_pack(csk[0] + cso[0][0]);
  acck = MFMA(aug_w(csw[1], 0u), xf[32], acck);
  csw[2] = cs_pack(csv[0] + cso[0][1]);
  accv = MFMA(aug_w(csw[2], 0u), xf[32], accv);
  const float rstd = __builtin_amdgcn_rsqf(fmaxf(s2 * (1.f / 512.f) - mean * mean, 0.f) + 1e-5f);
  const float sc = rstd * LOG2E;
  f32x16 p = {};
  { u32x4 b0, b1; pack16(acck, b0, b1); p = MFMA(aphi[0], b0, p); p = MFMA(aphi[1], b1, p); }
  SB();
  if (!R0A || js >= 4) rdwf();
  lds_barrier();
  STAMP(3); FST(11, 1);
  {
    Softmax sm; u32x4 phk[4];
    sfor<29>([&](auto ii) LAMBDA {
      constexpr int i = ii;
      sm.step(i, p, sc, phk);
      if (i == 2) kv_write_v(cx, accv, rstd);
      static_assert(RK + RKD * (15 - R0A) < 29, "");
      if (i >= RK && (i - RK) % RKD == 0 && (i - RK) / RKD < 16 - R0A) rawlda<(((i - RK) / RKD + R0A) & 15)>(cx, ro, rv, w * 131072 + cx.cb * 128, 32768);
      SB();
    });
    static_assert(s1_last(31, 32, 1) >= s1_last(10, 13, 1), "");
    STAMP(4); FST(11, 2);
    kv_tail<(s1_last(106, 176, 10) >= s1_last(170, 175, 1) ? -1 : s1_last(106, 176, 10) + 16 - R0A)>(cx, phk, kvb);
  }
  STAMP(5); FST(11, 3);
  {
    f32x16 accq = {}; f32x4 csq = {};
    u32x4 qa[3];
    int wqs[4];
#pragma unroll
    for (int s = 0; s < 4; s++) {
      const int t = s ^ w;
      wqs[s] = hk * 1024 + q4 * 64 + cperm + (t >> 1) * 2048 + (t & 1) * 512;
      asm volatile("" : "+v"(wqs[s]));
    }
    auto rdq = [&](int n) LAMBDA { qa[n % 3] = tr_readc<256>(cx.B + wqs[n & 3] + (n >> 2) * 4096); };
    rdq(0); rdq(1); rdq(2);
    u32x2 m;
    auto shake = [&]() LAMBDA {
      FST(11, 7);
      RAW(1, __builtin_popcountll(BALLOT(m[0] != cx.N && lane < 16))); RAW(2, __builtin_popcountll(BALLOT(m[0] != cx.N && lane >= 16 && lane < 48)));
      RAW(3, __builtin_popcountll(BALLOT(m[0] == cx.N && m[1] != sent && lane < 48))); RAW(4, 65536 - cx.budget);
      if (UNLIKELY(BALLOT((m[0] != cx.N || (w == 0 && m[1] != sent)) && lane < 48) != 0)) {
        while (true) {
          const bool valid = m[0] == cx.N;
          if (BALLOT(valid && m[1] != sent && lane < 48) != 0) {
            const u32x2 r = {m[1], cx.N};
            stc2(rs, valid && m[1] != sent ? mdst : OOB, 0, r);
            sent = valid ? m[1] : sent;
          }
          if (BALLOT(!valid && lane < 48) == 0) break;
          if (cx.budget == 0) break;
          cx.budget--;
          asm volatile("" ::: "memory");
          m = ldc2(rs, lane * 8, MBX + (g * 16 + c) * 512);
        }
        cx.budget = RFL(cx.budget);
        __builtin_amdgcn_s_waitcnt(0x0F70);
      }
      FST(11, 9); RAW(5, 65536 - cx.budget);
      const int e = lane >> 1, t = lane < 48 ? (int)m[1] : 0;
      mi.jv = mi.pw != OOB ? m[1] + 2 : 0u;
      cx.tk_kv = __builtin_amdgcn_ds_bpermute(((lane >> 4) == 2 ? 16 * cx.th + lane - 16 : 63) * 4, t);
      cx.tk_oh = __builtin_amdgcn_ds_bpermute((lane < 8 ? 4 * w + e : 63) * 4, t);
      cx.tk_x = __builtin_amdgcn_ds_bpermute((lane < 16 ? (THB == 8 ? 8 * (w >> 1) + e : 8 * (e >> 2) + 4 * (w >> 1) + (e & 3)) : 63) * 4, t);
    };
    static_assert(QJ > 0 && QM > QJ && QM < QJ + 3 && QM < QS && QS <= KQP0 && QR > QCS && QR <= KQP0 && QCS > 9 && QJ < QCS && RA > 11 && RA + 12 <= QR && QJ + 4 < RA && QS < RA + 4 && KQP0 < 32 + KQW0, "");
    const int wd = WT_QKV + c * WQ_C;
    const char* own = cx.B + w * 1024 + hk * 512 + q4 * 64 + cperm;
    u32x4 ca[2]; float co[3];
    ca[0] = tr_readc<256>(own); ca[1] = tr_readc<256>(own + 4096);
    SB();
    sfor<32>([&](auto nn) LAMBDA {
      constexpr int n = nn;
      accq = MFMA(qa[n % 3], xf[n], accq); SB();
      if (n == 0) {
        const int l = lane - 16;
        mi.pw = (unsigned)l < 32u ? ((l & 15) * 16 + ((c & ~THB) | (l >> 4) * THB)) * FS : OOB;
        asm volatile("" : "+v"(mi.pw));
      }
      if (n == 1 && w == 2) { const char* wsrc = wq + js * 4096 + (g & 1) * 2048; wf0 = tr_readc<256>(wsrc); wf1 = tr_readc<256>(wsrc + 512); }
      const char* jb = xs + 8192; const char* st = jb + r_tr;
      const f32x4 z4 = {};
      if (n == QJ) {
        FST(11, 4);
        vm_wait<s1_last(10, 13, 1) + 18 - R0A>();
        FST(11, 5);
        rf[0] = tr_read2(st, st + r_c); rf[1] = tr_read2(st + r_a, st + r_a + r_c);
      }
      if (n == QJ + 1) { rf[2] = tr_read2(st + r_b, st + r_b + r_c); rf[3] = tr_read2(jb + r_t3, jb + r_t3 + r_c3); }
      if (n == QJ + 3) { stc(rs, cx.vo, r_dst, rf[0]); stc(rs, cx.vo + 1024, r_dst, rf[1]); }
      if (n == QJ + 4) { stc(rs, cx.vo, r_dst + r_d1, rf[2]); stc(rs, r_d3, 0, rf[3]); }
      if (n == 8) { cj[0] = MFMA16(sel, rf[0], z4); cj[2] = MFMA16(sel, rf[2], z4); }
      if (n == 9) {
        const unsigned o2 = hk ? 0u : 0x3f803f80u;
        const u32x4 sl = {o2, o2, o2, o2};
        cj[1] = MFMA16(sel, rf[1], z4); cj[3] = MFMA16(sl, rf[3], z4);
      }
      if (n == 10) { const u32x2 cv = {__float_as_uint(hk ? cj[2][0] : cj[0][0]), __float_as_uint(hk ? cj[3][0] : cj[1][0])}; stc2(rs, lane * 8, r_cs, cv); }
      if (n == 11) {
        const int so = w < 2 ? wd + g * 4096 + w * 1024 : w == 2 ? wd + 65536 + g * 2048 : OOB;
        stc(rs, cx.vo, so, wf0); stc(rs, cx.vo, so + (w < 2 ? 2048 : 1024), wf1);
      }
      if (n == QR) { FST(11, 6); vm_wait<18>(); FST(11, 8); stc1(rs, lane ? OOB : 0, MB2 + (g * 16 + c) * FS + w * 4, cx.N + 2); }
      if (n == 3) {
        const int o = (int)(lA - lC) + w * 16384 + (16 * hk + q4) * 128 + cperm, h = ((w ^ (q4 >> 1)) & 1) * 64;
        mi.ra[0] = o + h; mi.ra[1] = o + 64 - h;
        asm volatile("" : "+v"(mi.ra[0]), "+v"(mi.ra[1]));
      }
      if (n == RA) { FST(11, 25); vm_wait<10>(); FST(11, 26); }
      if (n >= RA && n < RA + 8) {
        constexpr int b = ((n - RA) >> 1) & 3, e = 2 * ((n - RA) & 1);
        wof[4 * b + e] = rawrd(cx, mi, b, e); wof[4 * b + e + 1] = rawrd(cx, mi, b, e + 1);
      }
      if (n >= RA + 4 && n < RA + 12) {
        rawlda<((2 * (n - RA - 4)) & 15)>(cx, r2, rv, w * 458752 + cx.cb * 128, 114688);
        rawlda<((2 * (n - RA - 4) + 1) & 15)>(cx, r2, rv, w * 458752 + cx.cb * 128, 114688);
      }
      if (n == QM) m = ldc2(rs, lane * 8, MBX + (g * 16 + c) * 512);
      if (n == QS) shake();
      if (n == KQP0) kin.pz = kv_poll(cx, kvb);
      if (n == 9) *(float*)(stg + 3072 + lane * 4) = csq[0];
      if (n == QCS) {
        lds_barrier_n<2>();
#pragma unroll
        for (int t = 0; t < 3; t++) co[t] = *(const float*)(cx.B + 32768 + (w ^ (t + 1)) * 4096 + 3072 + lane * 4);
      }
      if (n == QCS + 4) { csw[0] = cs_pack((csq[0] + co[0]) + (co[1] + co[2])); asm volatile("" : "+v"(csw[0])); }
      SB();
      if (n < 8) { csq = MFMA16(sel, ca[n & 1], csq); SB(); }
      if (n + 2 < 8) ca[n & 1] = tr_readc<256>(own + (n + 2) * 4096);
      if (n + 3 < 32) rdq(n + 3);
      asm volatile("" : "+a"(accq));
      SB();
    });
    FST(11, 10);
    accq = MFMA(aug_w(csw[0], 0u), xf[32], accq);
    u32x4 q0, q1; pack16(accq, q0, q1);
    f32x16 pq = {}; pq = MFMA(aphi[0], q0, pq); pq = MFMA(aphi[1], q1, pq);
    Softmax sq;
    const u16* xr = x + (size_t)(128 * g + 64 * cx.th + 32 * (w >> 1) + tok) * 512 + col;
    SB();
    sfor<29>([&](auto ii) LAMBDA {
      constexpr int i = ii;
      sq.step(i, pq, sc, phq);
      if (i + 32 == KQP0) kin.pz = kv_poll(cx, kvb);
      if (i == 0) FST(11, 11);
      if (i == KQW0) {
        SB(); FST(11, 12); kv_wait(cx, kin.pz, kvb + KV_F + w * 16 * FS); STAMP(6); FST(11, 13); kv_load(cx, kin, kvb);
        mi.xa = *(const u32x4*)xr; mi.xb = *(const u32x4*)(xr + 8);
      }
      if (i > KQW0 && i <= KQW0 + 4) {
        constexpr int b = (i - KQW0 - 1) & 3;
#pragma unroll
        for (int e = 0; e < 4; e++) w2f[8 * (e >> 1) + 2 * b + (e & 1)] = rawrd(cx, mi, b, e);
      }
      if (i == KQW0 + 7) asm volatile("s_waitcnt lgkmcnt(0)" ::: "memory");
      if (i >= KQW0 + 7 && i < KQW0 + 7 + 2 * R2B) {
        rawld<((2 * (i - KQW0 - 7)) & 15)>(cx, r2, rv, w * 458752 + cx.cb * 128 + 32768, 114688);
        rawld<((2 * (i - KQW0 - 7) + 1) & 15)>(cx, r2, rv, w * 458752 + cx.cb * 128 + 32768, 114688);
      }
      SB();
    });
  }
}
DEVI void red_send(Cx& cx, const f32x16& a, int j) {
  char* dst = cx.C + (cx.w ^ j) * INBOX + (j - 1) * 4096 + cx.vo;
#pragma unroll
  for (int m = 0; m < 4; m++) { f32x4 v = {a[4 * m], a[4 * m + 1], a[4 * m + 2], a[4 * m + 3]}; *(f32x4*)(dst + m * 1024) = v; }
}
DEVI void red_send1(Cx& cx, const f32x16& a, int j, int m) {
  const f32x4 v = {a[4 * m], a[4 * m + 1], a[4 * m + 2], a[4 * m + 3]};
  *(f32x4*)(cx.C + (cx.w ^ j) * INBOX + (j - 1) * 4096 + cx.vo + m * 1024) = v;
}
struct Red {
  f32x4 r1[4], r2[4], r3[4]; unsigned chk; int cp;
  DEVI void rd32(Cx& cx, int m) {
    const char* p = cx.C + cx.w * INBOX + m * 1024 + cx.vo;
    r2[m] = *(const f32x4*)(p + 4096); r3[m] = *(const f32x4*)(p + 8192);
  }
  DEVI void put(Cx& cx, unsigned tot) {
    *(unsigned*)(cx.C + cx.w * INBOX + 4096 + cx.lane * 4) = tot + (FORCE_BAD ? cx.w & 1 : 0);
    cp = (cx.lane & 3) * INBOX + 4096 + 252;
    asm volatile("" : "+v"(cp));
  }
  DEVI void rd1(Cx& cx) {
    chk = *(const unsigned*)(cx.C + cp);
    SB();
#pragma unroll
    for (int m = 0; m < 4; m++) r1[m] = *(const f32x4*)(cx.C + cx.w * INBOX + m * 1024 + cx.vo);
  }
  DEVI void add32(int m) {
#pragma unroll
    for (int e = 0; e < 4; e++) asm volatile("v_add_f32 %0, %0, %1" : "+v"(r2[m][e]) : "v"(r3[m][e]));
  }
  DEVI bool fin(const f32x16& own, float (&xr)[16]) {
#pragma unroll
    for (int m = 0; m < 4; m++) { const f32x4 o = {own[4 * m], own[4 * m + 1], own[4 * m + 2], own[4 * m + 3]}; r2[m] += o; }
    asm volatile("" : "+v"(r2[0]), "+v"(r2[1]), "+v"(r2[2]), "+v"(r2[3]));
#pragma unroll
    for (int r = 0; r < 16; r++) xr[r] = r2[r >> 2][r & 3] + r1[r >> 2][r & 3];
    return BALLOT(chk != 0) != 0;
  }
};
DEVI void red_recv(Cx& cx, const f32x16& own, float (&xr)[16]) {
  f32x4 v[12];
#pragma unroll
  for (int k = 0; k < 12; k++) v[k] = *(const f32x4*)(cx.C + cx.w * INBOX + ((k % 3) * 4 + k / 3) * 1024 + cx.vo);
#pragma unroll
  for (int m = 0; m < 4; m++)
#pragma unroll
    for (int e = 0; e < 4; e++) xr[4 * m + e] = (own[4 * m + e] + v[3 * m][e]) + (v[3 * m + 1][e] + v[3 * m + 2][e]);
}
DEVI void unpack16(const u32x4& a, const u32x4& b, float (&x)[16]) {
#pragma unroll
  for (int t = 0; t < 4; t++) { x[2 * t] = bflo(a[t]); x[2 * t + 1] = bfhi(a[t]); x[8 + 2 * t] = bflo(b[t]); x[9 + 2 * t] = bfhi(b[t]); }
}
#define OST "sc0 sc1 nt"
#define EFX 3
#define EFO 0
DEVI void publish_x(Cx& cx, float (&xr)[16], int xb, int sb, int fb) {
  u32x4 f0, f1; pack16(xr, f0, f1);
  stc(cx.rs, cx.vo, xb + cx.xo, f0);
  stc(cx.rs, cx.vo + 1024, xb + cx.xo, f1);
  if (EFX == 0) put_flag(cx, fb + cx.fxo, 7);
  SB();
  f32x2 s1 = {xr[0], xr[1]}, s2 = s1 * s1;
#pragma unroll
  for (int r = 2; r < 16; r += 2) { const f32x2 v = {xr[r], xr[r + 1]}; s1 += v; s2 = __builtin_elementwise_fma(v, v, s2); }
  auto t = __builtin_amdgcn_permlane32_swap(__float_as_uint(s1[0] + s1[1]), __float_as_uint(s2[0] + s2[1]), false, false);
  const unsigned st = __float_as_uint(__uint_as_float(t[0]) + __uint_as_float(t[1]));
  stc1(cx.rs, (cx.lane & 15) * 16 + (cx.lane >> 4 & 1) * 8 + cx.hk * 4, sb + cx.so, st);
  if (EFX == 3) put_flag(cx, fb + cx.fxo, 7);
  SB();
  put_final(cx, fb + CKX + cx.fxo, sum4(sum4(st, f0), f1));
  unpack16(f0, f1, xr);
}
DEVI void park(Cx& cx, const float (&xr)[16]) {
  char* p = cx.C + cx.w * INBOX + C_PARK + cx.vo;
#pragma unroll
  for (int m = 0; m < 4; m++) { f32x4 v = {xr[4 * m], xr[4 * m + 1], xr[4 * m + 2], xr[4 * m + 3]}; *(f32x4*)(p + m * 1024) = v; }
}
DEVI f32x16 unpark(Cx& cx) {
  const char* p = cx.C + cx.w * INBOX + C_PARK + cx.vo;
  f32x16 r;
#pragma unroll
  for (int m = 0; m < 4; m++) { f32x4 v = *(const f32x4*)(p + m * 1024); r[4 * m] = v[0]; r[4 * m + 1] = v[1]; r[4 * m + 2] = v[2]; r[4 * m + 3] = v[3]; }
  return r;
}
DEVI f32x16 tovec(const float (&x)[16]) {
  f32x16 r;
#pragma unroll
  for (int e = 0; e < 16; e++) r[e] = x[e];
  return r;
}

#define RDW 2
#define XS 0
#define XA 14
#define XE 1
template <class AUG, class WIN>
DEVI void xpass(Cx& cx, int xb, int sb, int fb, u32x4 (&xf)[33], f32x16& a0, f32x16& a1, int sti, AUG aug, WIN win) {
  const rsrc_t rs = cx.rs;
  xb += cx.w * 32768; sb += cx.w * 4096; fb += cx.w * 16 * FS;
  if (sti == 15) FST(7, 0);
  if (sti == 25) FST(8, 0);
  if (sti == 15) nap<D_X2>(); else nap<D_X1>();
  poll_wait<16>(cx, ldc1(rs, cx.fv16, fb), fb, cx.tk_x, 7);
  STAMP(sti); if (sti != 15) FST(sti == 11 ? 4 : 5, 0); else FST(7, 1);
  if (sti == 25) FST(8, 1);
  u32x4 st[4], ra[3], rb[3], w0, w1; unsigned ck;
  auto ld = [&](int q) LAMBDA {
    if (q >= XS && q < XS + 4) st[q - XS] = ldck(rs, cx.vo, sb, q - XS);
    else if (q < 36) xf[q < XS ? q : q - 4] = ldck(rs, cx.vo, xb, q < XS ? q : q - 4);
    if (q == 30) ck = ldc1(rs, cx.fv16, fb + CKX + 4);
  };
  f32x4 sv; float t0, t1;
  auto stats = [&](int k) LAMBDA {
    if (k == 0) sv = __builtin_bit_cast(f32x4, st[0]) + __builtin_bit_cast(f32x4, st[1]);
    if (k == 1) sv += __builtin_bit_cast(f32x4, st[2]) + __builtin_bit_cast(f32x4, st[3]);
    if (k < 2) asm volatile("" : "+v"(sv));
    if (k == 2) {
      auto p = __builtin_amdgcn_permlane32_swap(__float_as_uint(sv[0]), __float_as_uint(sv[1]), false, false);
      auto q = __builtin_amdgcn_permlane32_swap(__float_as_uint(sv[2]), __float_as_uint(sv[3]), false, false);
      t0 = __uint_as_float(p[0]) + __uint_as_float(p[1]); t1 = __uint_as_float(q[0]) + __uint_as_float(q[1]);
    }
    if (k == 3) {
      auto r = __builtin_amdgcn_permlane16_swap(__float_as_uint(t0), __float_as_uint(t1), false, false);
      t0 = (__uint_as_float(r[0]) + __uint_as_float(r[1])) * (1.f / 512.f);
    }
    if (k == 4) {
      auto u = __builtin_amdgcn_permlane32_swap(__float_as_uint(t0), __float_as_uint(t0), false, false);
      t0 = __uint_as_float(u[0]);
      t1 = fmaxf(__uint_as_float(u[1]) - t0 * t0, 0.f);
    }
    if (k >= 2 && k < 5) asm volatile("" : "+v"(t0), "+v"(t1));
    if (k >= 5) aug(k - 5, t0, t1, w0, w1);
  };
  const char* pa = cx.A + cx.vo;
#pragma unroll
  for (int q = 0; q < 10; q++) { ld(q); SB(); }
  if (sti == 15) FST(7, 2);
  if (sti == 25) FST(8, 2);
  bar();
  if (sti == 15) FST(7, 3);
  if (sti == 25) FST(8, 3);
#pragma unroll
  for (int n = 0; n < 3; n++) { ra[n] = *(const u32x4*)(pa + n * 2048); rb[n] = *(const u32x4*)(pa + n * 2048 + 1024); }
  SB();
  unsigned sum = -cx.tk_x;
  const f32x16 zero = {};
#pragma unroll
  for (int q = 10; q < XA; q++) { ld(q); SB(); }
  sfor<32>([&](auto nn) LAMBDA {
    constexpr int n = nn, k = n - XS - 4;
    if (n % 8 == 0 && sti != 15) FST(sti == 11 ? 4 : 5, 1 + n / 8);
    a0 = MFMA(ra[n % 3], xf[n], n ? a0 : zero); SB();
    if (n % 4 == 0 && sti == 15) FST(7, 4 + n / 4);
    if (n % 4 == 0 && sti == 25) FST(8, 4 + n / 4);
    if (n + 3 < 32) ra[n % 3] = *(const u32x4*)(pa + (n + 3) * 2048);
    if (n + XA < 36) ld(n + XA);
    if (n >= 22) win(ic<(n < 22 ? 0 : n - 22)>());
    if (k >= -4 && k < 0) sum = sum4(sum, st[k & 3]);
    if (k >= 0 && k < 9) stats(k);
    if (XE && n == 31) sum = sum4(sum, xf[n]);
    SB();
    if (XE && n == 31) { a0 = MFMA(w0, xf[32], a0); SB(); wsum_step<0>(sum); wsum_step<1>(sum); SB(); }
    a1 = MFMA(rb[n % 3], xf[n], n ? a1 : zero); SB();
    if (n + 3 < 32) rb[n % 3] = *(const u32x4*)(pa + (n + 3) * 2048 + 1024);
    if (!XE || n < 31) sum = sum4(sum, xf[n]);
    if (n == 29) sum += ck;
    if (XE && n == 31) { wsum_step<2>(sum); wsum_step<3>(sum); SB(); a1 = MFMA(w1, xf[32], a1); SB(); wsum_step<4>(sum); wsum_step<5>(sum); }
    SB();
    if (!XE && k == 9) { a0 = MFMA(w0, xf[32], a0); a1 = MFMA(w1, xf[32], a1); SB(); }
  });
  if (sti != 15) FST(sti == 11 ? 4 : 5, 5); else FST(7, 12);
  if (sti == 25) FST(8, 12);
  asm volatile("" : "+a"(a0), "+a"(a1));
  if (UNLIKELY(XE ? __builtin_amdgcn_readlane(sum, 63) + (FORCE_BAD ? cx.w & 1 : 0) != 0 : bad(cx, sum))) {
    do {
      spend(cx, sti == 11 ? 2 : sti == 15 ? 4 : 7);
#pragma unroll
      for (int q = 0; q < 36; q++) ld(q);
      sum = ck - cx.tk_x;
#pragma unroll
      for (int q = 0; q < 32; q++) sum = sum4(sum, xf[q]);
#pragma unroll
      for (int q = 0; q < 4; q++) sum = sum4(sum, st[q]);
    } while (again(cx, sum));
#pragma unroll
    for (int k = 0; k < 9; k++) stats(k);
    a0 = MFMA(w0, xf[32], zero); a1 = MFMA(w1, xf[32], zero);
#pragma unroll
    for (int n = 0; n < 32; n++) {
      a0 = MFMA(*(const u32x4*)(pa + n * 2048), xf[n], a0); a1 = MFMA(*(const u32x4*)(pa + n * 2048 + 1024), xf[n], a1);
      SB();
    }
  }
}

template <bool P> DEVI f32x2 mul2(f32x2 a, f32x2 b) {
  f32x2 r;
  if (P) asm volatile("v_pk_mul_f32 %0, %1, %2" : "=v"(r) : "v"(a), "v"(b));
  else {
    asm volatile("v_mul_f32 %0, %1, %2" : "=v"(r[0]) : "v"(a[0]), "v"(b[0]));
    asm volatile("v_mul_f32 %0, %1, %2" : "=v"(r[1]) : "v"(a[1]), "v"(b[1]));
  }
  return r;
}
template <bool P> DEVI f32x2 fma2(f32x2 a, f32x2 c) {
  f32x2 r;
  if (P) asm volatile("v_pk_fma_f32 %0, %1, %2, %2 op_sel:[0,1,0] op_sel_hi:[1,1,0]" : "=v"(r) : "v"(a), "v"(c));
  else {
    asm volatile("v_fma_f32 %0, %1, %2, %3" : "=v"(r[0]) : "v"(a[0]), "v"(c[1]), "v"(c[0]));
    asm volatile("v_fma_f32 %0, %1, %2, %3" : "=v"(r[1]) : "v"(a[1]), "v"(c[1]), "v"(c[0]));
  }
  return r;
}
struct Gelu {
  f32x2 cc, ia;
  f32x2 a[4], t, d, e, r, u;
  u32x4 f; unsigned sum;
  template <int K0, int K1, int K, int H, class MID>
  DEVI void step(Cx& cx, const f32x16& lo, const f32x16& hi, int hb, MID mid) {
    constexpr bool P = H == 2, p0 = K >= K0 && K < K1, p1 = K - 1 >= K0 && K - 1 < K1, p2 = K - 2 >= K0 && K - 2 < K1;
    constexpr int v = K - 2;
    f32x2 s, y;
    auto rc = [&]() LAMBDA { if (p2) { r[0] = __builtin_amdgcn_rcpf(d[0]); r[1] = __builtin_amdgcn_rcpf(d[1]); SB(); } };
    auto ex = [&]() LAMBDA { if (p1) { e[0] = __builtin_amdgcn_exp2f(t[0]); e[1] = __builtin_amdgcn_exp2f(t[1]); SB(); } };
    auto rd = [&]() LAMBDA {
      if (p0) {
        const f32x16& h = (K & 8) ? hi : lo;
        float x0, x1;
        asm volatile("v_accvgpr_read_b32 %0, %2\n\tv_accvgpr_read_b32 %1, %3" : "=v"(x0), "=v"(x1) : "a"(h[(2 * K) & 15]), "a"(h[(2 * K + 1) & 15]));
        a[K & 3][0] = x0; a[K & 3][1] = x1;
        SB();
      }
    };
    auto fin = [&]() LAMBDA { if (p2 && (v & 3) == 3) { stc(cx.rs, cx.vo, hb + (v >> 2) * 8192, f); sum = sum4(sum, f); } };
    if (H != 1) rd();
    if (H != 0) rc();
    if (H != 1) ex();
    mid(); SB();
    if (H != 0 && p0 && p2) {
      float y0, y1;
      asm volatile("v_mul_f32 %2, %6, %8\n\t"
                   "v_mul_f32 %3, %7, %9\n\t"
                   "v_pk_mul_f32 %0, %5, %5\n\t"
                   "v_cvt_pk_bf16_f32 %4, %2, %3\n\t"
                   "v_pk_fma_f32 %0, %0, %11, %11 op_sel:[0,1,0] op_sel_hi:[1,1,0]\n\t"
                   "v_pk_fma_f32 %1, %10, %12, %12\n\t"
                   "v_pk_mul_f32 %0, %5, %0"
                   : "=&v"(t), "=&v"(d), "=&v"(y0), "=&v"(y1), "=&v"(f[v & 3])
                   : "v"(a[K & 3]), "v"(a[v & 3][0]), "v"(a[v & 3][1]), "v"(r[0]), "v"(r[1]), "v"(e), "v"(cc), "v"(ia));
    } else if (H != 0) {
      if (p0) s = mul2<0>(a[K & 3], a[K & 3]);
      if (p2) y = mul2<0>(a[v & 3], r);
      if (p0) u = fma2<0>(s, cc);
      if (p1) d = fma2<0>(e, ia);
      if (p0) t = mul2<0>(a[K & 3], u);
      if (p2) f[v & 3] = pk2(y[0], y[1]);
    }
    if (H != 0) fin();
  }
};

#define GB2 34
#define GP1 26
#define GW1 40
#define GP3 9
#define GP4 37
#define GP2 42
#define GW3 26
#define H3F 1
#define GBR0 78
#define GBR1 90
#define WL_P 14
#define WL_D 2
#define WL_H 13
#define WL_N ((24 + WL_D) / WL_D)
#define NW2_H (WL_P + WL_N + WL_H)
#define D_O0 2
#define NB1H 6
#define NQK 8
#define KQP1 31
#define KQW1 12

constexpr int h_iss(int i) { return i < 32 ? i : i < 40 ? 33 + 2 * (i - 32) : i < 48 ? 32 + 2 * (i - 40) : i < 52 ? 49 + 2 * (i - 48) : 48 + 2 * (i - 52); }
constexpr int s4_f(int G) {
  return G < 64 ? G >> 1 : G < 80 ? 33 + 2 * ((G - 64) >> 1) : G < 84 ? 32 + 2 * (G - 80) : G < 92 ? 49 + 2 * ((G - 84) >> 1) : G < 100 ? 40 + 2 * (G - 92) : 32 + 2 * (G - 100);
}
constexpr int s4_j(int G) { return G < 64 ? (G & 2) + 1 - (G & 1) : G < 80 || (G >= 84 && G < 92) ? 3 - (G & 1) : G < 100 ? 1 : 0; }

#define BL 1
#define BUDL(i) do { if (L == BL) BUD(53 + (i)); } while (0)
template <int L>
DEVI void layer(Cx& cx, const u16* __restrict__ x, const u16* __restrict__ b1, const u16* __restrict__ b2, u16* __restrict__ out,
                u32x4 (&phq)[4], const unsigned (&csw)[3], const u32x4 (&aphi)[2], KvIn& kin, u32x4 (&w2f)[56], u32x4 (&wof)[16],
                Misc& mi, unsigned (&csp)[4], unsigned (&b1p)[2]) {
  const int lane = cx.lane, w = cx.w, tok = cx.tok, hk = cx.hk, g = cx.g, c = cx.c;
  const rsrc_t rs = cx.rs;
  const int gl = cx.gb + L * G_L;
  const int kvb = KVA + L * KV_L + c * KV_C;
  const int w2b = WT_2 + (cx.cb * 4 + w) * 57344;
  const f32x16 zero = {};
  constexpr int SO = 14 * L;
  u32x4 xa, xb2;
  const int col = 64 * cx.cb + 32 * (w & 1) + 16 * hk;
  const size_t xrow = (size_t)(128 * g + 64 * cx.th + 32 * (w >> 1) + tok) * 512 + col;
  BUDL(-1);
  if (L == 1) {
    u32x4 xf[33]; f32x16 acck, accv; float rstd;
    xpass(cx, cx.gb + G_X2, cx.gb + G_ST2, cx.gb + G_FX2, xf, acck, accv, 15,
          [&](int k, float mean, float var, u32x4& w0, u32x4& w1) LAMBDA {
            if (k == 0) { rstd = __builtin_amdgcn_rsqf(var + 1e-5f); asm volatile("" : "+v"(rstd)); }
            if (k == 1) { xf[32] = aug_tok(mean, 0.f, hk); asm volatile("" : "+v"(xf[32])); }
            if (k == 2) { w0 = aug_w(csw[1], 0u); w1 = aug_w(csw[2], 0u); asm volatile("" : "+v"(w0), "+v"(w1)); }
          },
          [&](auto ii) LAMBDA { if (ii.v < 8) dma<(ii.v & 7)>(rs, cx.B + w * 8192, cx.vo, WT_QKV + c * WQ_C + 65536 + w * 8192); });
    STAMP(16); FST(6, 0); FST(7, 13); BUDL(0);
    const float sc = rstd * LOG2E;
    f32x16 p;
    { u32x4 b0, b1; pack16(acck, b0, b1); p = MFMA(aphi[0], b0, zero); p = MFMA(aphi[1], b1, p); }
    FST(6, 1);
    kv_write_v(cx, accv, rstd);
    SB();
    {
      Softmax sm; u32x4 phk[4];
      sfor<29>([&](auto ii) LAMBDA {
        constexpr int i = ii;
        if (i % 4 == 0) FST(6, 2 + i / 4);
        sm.step(i, p, sc, phk);
        SB();
      });
      STAMP(17); FST(6, 10);
      kv_tail<0>(cx, phk, kvb);
    }
    STAMP(18); FST(6, 11);
    f32x16 accq = MFMA(aug_w(csw[0], 0u), xf[32], zero);
    u32x4 qa[3];
    const char* pq = cx.B + cx.vo;
#pragma unroll
    for (int n = 0; n < 3; n++) qa[n] = *(const u32x4*)(pq + n * 1024);
    char* dA = cx.A + w * 16384;
    const int sA = WT_1 + c * W1_C + w * 16384;
    SB();
    sfor<32>([&](auto nn) LAMBDA {
      constexpr int n = nn;
      accq = MFMA(qa[n % 3], xf[n], accq); SB();
      if (n + 3 < 32) qa[n % 3] = *(const u32x4*)(pq + (n + 3) * 1024);
      if (n >= 1 && n < 17) wof[n - 1] = ldck(rs, cx.vo, WT_O + (cx.cb * 4 + w) * 16384, n - 1);
      if (n >= 17 && (n & 1)) dma<(n >= 17 ? (n - 17) / 2 : 0)>(rs, dA, cx.vo, sA);
      if (n == KQP1) kin.pz = kv_poll(cx, kvb);
      if (n % 8 == 7) FST(6, 12 + n / 8);
      SB();
    });
    u32x4 b0, b1; pack16(accq, b0, b1);
    f32x16 pq2 = MFMA(aphi[0], b0, zero); pq2 = MFMA(aphi[1], b1, pq2);
    Softmax sq;
    sfor<29>([&](auto ii) LAMBDA {
      constexpr int i = ii;
      sq.step(i, pq2, sc, phq);
      if (i + 32 == KQP1) kin.pz = kv_poll(cx, kvb);
      if (i == KQW1) { SB(); kv_wait(cx, kin.pz, kvb + KV_F + w * 16 * FS); STAMP(19); kv_load(cx, kin, kvb); }
      if (i >= 19 && i < 27) dma<(i >= 19 && i < 27 ? i - 11 : 0)>(rs, dA, cx.vo, sA);
      if (i == 27) mi.ba = *(const u32x4*)(b2 + col);
      if (i == 28) mi.bb = *(const u32x4*)(b2 + col + 8);
      SB();
    });
  }
  asm volatile("" : "+v"(phq[0]), "+v"(phq[1]), "+v"(phq[2]), "+v"(phq[3]));
  SB();
  STAMP(7 + SO); BUDL(1); if (L == 0) FST(11, 14);
  {
    const int G = lane >> 4, d = lane & 15;
    u32x4 idn;
#pragma unroll
    for (int t = 0; t < 4; t++) { const int k = 8 * (G & 1) + 2 * t; idn[t] = k == d ? 0x3f80u : k + 1 == d ? 0x3f800000u : 0u; }
    f32x4 kv; unsigned sum;
    auto red = [&]() LAMBDA {
      f32x4 k0 = {}, k1 = {}; sum = -cx.tk_kv;
#pragma unroll
      for (int m = 0; m < 8; m += 2) {
        k0 = MFMA16(idn, kin.pa[m], k0); k1 = MFMA16(idn, kin.pa[m + 1], k1);
        sum = sum4(sum4(sum, kin.pa[m]), kin.pa[m + 1]);
      }
      sum = sum4(sum, kin.pz); kv = k0 + k1;
    };
    red();
    asm volatile("" : "+v"(kv));
    if (UNLIKELY(bad(cx, sum))) {
      do { spend(cx, 5 * L); kin.pz = kv_poll(cx, kvb); kv_load(cx, kin, kvb); red(); } while (again(cx, sum));
    }
    char* img = cx.C + w * INBOX + C_IMG;
    { const u32x2 v = {pk2(kv[0], kv[1]), pk2(kv[2], kv[3])}; *(u32x2*)(img + d * 32 + G * 8) = v; }
    { const u32x4 z4 = {}; *(u32x4*)(img + 512 + cx.vo) = hk ? z4 : kin.pz; }
    lds_barrier();
    const int hp = (tok >> 2) & 1, dd = (tok & 3) + 4 * (tok >> 3);
    const char* ap = cx.C + (hp == hk ? 2 * hp * INBOX + C_IMG + dd * 32 : C_ZERO);
    const int zo = hp == hk ? 512 : 0;
    u32x4 a[4], az[4];
#pragma unroll
    for (int s4 = 0; s4 < 4; s4++) az[s4] = *(const u32x4*)(ap + zo + (s4 >> 1) * INBOX + (s4 & 1) * 16);
#pragma unroll
    for (int s4 = 0; s4 < 4; s4++) a[s4] = *(const u32x4*)(ap + (s4 >> 1) * INBOX + (s4 & 1) * 16);
    if (L == 0) stc1(rs, w == 0 && (lane >> 4) == 2 ? (lane - 32) * 8192 : OOB, MBX + c * 512 + (48 + g) * 8, cx.tk_kv + 1);
    if (L == 0) mi.msg = ldc(rs, mi.pw, MB2);
    SB();
    f32x16 num = {}, den = {};
#pragma unroll
    for (int s4 = 0; s4 < 4; s4++) den = MFMA(az[s4], phq[s4], den);
#pragma unroll
    for (int s4 = 0; s4 < 4; s4++) num = MFMA(a[s4], phq[s4], num);
    float ds[4];
#pragma unroll
    for (int r = 0; r < 4; r++) ds[r] = (den[r] + den[4 + r]) + (den[8 + r] + den[12 + r]);
    const float inv = __builtin_amdgcn_rcpf(((ds[0] + ds[1]) + (ds[2] + ds[3])) + 1e-6f);
    float ov[16];
#pragma unroll
    for (int r = 0; r < 16; r++) ov[r] = num[r] * inv;
    u32x4 f0, f1; pack16(ov, f0, f1);
    stc(rs, cx.vo, gl + G_O + cx.oo, f0);
    stc(rs, cx.vo + 1024, gl + G_O + cx.oo, f1);
    if (EFO == 0) put_flag(cx, gl + G_FO + cx.fo, 7);
    put_final(cx, gl + G_FO + CKO + cx.fo, sum4(sum4(0u, f0), f1));
    STAMP(8 + SO); if (L == 0) FST(11, 15);
  }
  if (L == 0) {
    SB();
    auto miss = [&]() LAMBDA { return BALLOT(((mi.msg[0] ^ mi.jv) | (mi.msg[1] ^ mi.jv) | (mi.msg[2] ^ mi.jv) | (mi.msg[3] ^ mi.jv)) != 0); };
    RAW(6, __builtin_popcountll(miss())); RAW(7, 65536 - cx.budget);
    if (UNLIKELY(miss() != 0)) {
      do {
        if (cx.budget == 0) break;
        cx.budget--;
        asm volatile("" ::: "memory");
        mi.msg = ldc(rs, mi.pw, MB2);
      } while (miss() != 0);
      cx.budget = RFL(cx.budget);
    }
    FST(11, 16); RAW(8, 65536 - cx.budget);
    static_assert(WL_P >= 4 && WL_P <= 16 && NW2_H >= 32, "");
    sfor<WL_P>([&](auto kk) LAMBDA { wload<0>(cx, kk, w2f, mi); SB(); });
#pragma unroll
    for (int k = 0; k < 4 * R2B; k++) w2f[16 + 8 * (k >> 1 & 1) + 2 * (k >> 2) + (k & 1)] = rawrd(cx, mi, k >> 2, k & 3);
  }
  float xr[16];
  {
    const int ob = gl + G_O + (cx.th * 4 + w) * 16384, fb = gl + G_FO + (cx.th * 4 + w) * 8 * FS;
    f32x16 acc[4];
    if (L == 0) {
      unpack16(mi.xa, mi.xb, xr); acc[0] = tovec(xr);
      b1p[0] = mi.b1v[0] | ((unsigned)mi.b1v[1] << 16); b1p[1] = mi.b1v[2] | ((unsigned)mi.b1v[3] << 16);
    } else acc[0] = unpark(cx);
    SB();
    char* dB = cx.B + w * 12288;
    const int sB = WT_1 + c * W1_C + 65536 + w * 12288;
    if (L == 1) sfor<NB1H>([&](auto ii) LAMBDA { dma<ii.v>(rs, dB, cx.vo, sB); });
    if (L == 0) FST(11, 17);
    nap<(L ? D_O : D_O0)>();
    poll_wait<8>(cx, ldc1(rs, cx.fv8, fb), fb, cx.tk_oh, 7);
    STAMP(9 + SO); if (L == 1) FST(9, 0); if (L == 0) FST(11, 18);
    BUDL(2);
    u32x4 of[16];
#pragma unroll
    for (int k = 0; k < 10; k++) { of[k ^ 8] = ldck(rs, cx.vo, ob, k ^ 8); SB(); }
    if (L == 0) {
      const f32x4 v = (__builtin_bit_cast(f32x4, mi.cs[0]) + __builtin_bit_cast(f32x4, mi.cs[1])) + (__builtin_bit_cast(f32x4, mi.cs[2]) + __builtin_bit_cast(f32x4, mi.cs[3]));
      *(f32x4*)(cx.C + w * INBOX + 3072 + cx.vo) = v;
    }
    lds_barrier();
    f32x2 cq[8];
    unsigned sum = -cx.tk_oh, ck;
    Red rd;
    sfor<32>([&](auto nn) LAMBDA {
      constexpr int n = nn, j = n < 12 ? 3 - (n % 3 == 2) : 3 - n / 8, s = n < 12 ? (n % 3 == 2 ? n / 3 : n - n / 3) : n & 7;
      constexpr int i = n >= 13 && n % 3 != 0 ? n - 13 - (n - 13) / 3 : -1;
      if (L == 1 && n % 4 == 0) FST(9, 1 + n / 4);
      if (L == 0 && n % 8 == 0) FST(11, 19 + n / 8);
      acc[j] = MFMA(wof[2 * s + (j & 1)], of[8 * (j >> 1) + s], s == 0 && j ? zero : acc[j]); SB();
      if (n < 6) of[(n + 10) ^ 8] = ldck(rs, cx.vo, ob, (n + 10) ^ 8);
      if (n == 6) ck = ldc1(rs, cx.fv8, fb + CKO + 4);
      if (L == 1 && n >= 7 && n < 19 - NB1H) dma<(n >= 7 && n < 19 - NB1H ? n - 7 + NB1H : 0)>(rs, dB, cx.vo, sB);
      if (L == 0 && n >= 7 && (n - 7) % WL_D == 0) wload<WL_P>(cx, ic<(n - 7) / WL_D>(), w2f, mi);
      if (L == 0 && n == 1) {
#pragma unroll
        for (int k = 0; k < 8; k++) cq[k] = *(const f32x2*)(cx.C + (hk * 2 + (k >> 1 & 1)) * INBOX + 3072 + (k >> 2) * 256 + (k & 1) * 512 + tok * 8);
      }
      if (L == 0 && (n == 4 || n == 5)) {
        constexpr int k = n & 1;
        const f32x2 v = (cq[4 * k] + cq[4 * k + 1]) + (cq[4 * k + 2] + cq[4 * k + 3]);
        csp[2 * k] = cs_pack(xhalf_sum(v[0])); csp[2 * k + 1] = cs_pack(xhalf_sum(v[1]));
        asm volatile("" : "+v"(csp[2 * k]), "+v"(csp[2 * k + 1]));
      }
      if (j & 1) sum = sum4(sum, of[8 * (j >> 1) + s]);
      if (i >= 0 && i < 12) red_send1(cx, acc[3 - (i / 4 & 3)], 3 - (i / 4 & 3), i & 3);
      if (n == 24) sum += ck;
      if (n >= 24 && n < 27) { wsum_step<(n >= 24 && n < 27 ? 2 * (n - 24) : 0)>(sum); SB(); wsum_step<(n >= 24 && n < 27 ? 2 * (n - 24) + 1 : 0)>(sum); }
      if (n == 27) { lds_barrier_n<2>(); rd.rd32(cx, 0); rd.rd32(cx, 1); rd.rd32(cx, 2); rd.rd32(cx, 3); rd.put(cx, sum); }
      if (n >= 30) { rd.add32(2 * (n & 1)); rd.add32(2 * (n & 1) + 1); }
      SB();
    });
    if (L == 1) FST(9, 9); else FST(11, 23);
    lds_barrier();
    if (L == 1) FST(9, 10);
    rd.rd1(cx);
    SB();
    if (UNLIKELY(rd.fin(acc[0], xr))) {
      lds_barrier();
      do {
        spend(cx, 1 + 5 * L);
        sum = ldc1(rs, cx.fv8, fb + CKO + 4) - cx.tk_oh;
#pragma unroll
        for (int k = 0; k < 16; k++) { of[k] = ldck(rs, cx.vo, ob, k); sum = sum4(sum, of[k]); }
      } while (again(cx, sum));
      if (L == 0) { xa = *(const u32x4*)(x + xrow); xb2 = *(const u32x4*)(x + xrow + 8); }
      else { xa = ldc(rs, cx.vo, cx.gb + G_X2 + cx.xo); xb2 = ldc(rs, cx.vo + 1024, cx.gb + G_X2 + cx.xo); }
      unpack16(xa, xb2, xr); acc[0] = tovec(xr); acc[1] = zero; acc[2] = zero; acc[3] = zero;
#pragma unroll
      for (int n = 0; n < 32; n++) acc[n >> 3] = MFMA(wof[2 * (n & 7) + ((n >> 3) & 1)], of[8 * (n >> 4) + (n & 7)], acc[n >> 3]);
      red_send(cx, acc[1], 1); red_send(cx, acc[2], 2); red_send(cx, acc[3], 3);
      lds_barrier();
      red_recv(cx, acc[0], xr);
    }
    if (L == 1) FST(9, 11);
    publish_x(cx, xr, gl + G_X1, gl + G_ST1, gl + G_FX1);
    if (L == 1) FST(9, 12); else FST(11, 24);
    STAMP(10 + SO);
    if (L == 0) sfor<WL_H>([&](auto kk) LAMBDA { wload<WL_P + WL_N>(cx, kk, w2f, mi); });
    float bv[16]; unpack16(mi.ba, mi.bb, bv);
#pragma unroll
    for (int r = 0; r < 16; r++) xr[r] += bv[r];
    park(cx, xr);
  }
  {
    u32x4 xf[33]; f32x16 h0, h1, h2, h3;
    Gelu ge;
    const char* pb = cx.B + cx.vo;
    const char* ph = cx.B + 1024 + (hk * 16 + (lane & 15)) * 16;
    u32x4 ra[4];
    auto rdw = [&](int G) LAMBDA { ra[G & 3] = *(const u32x4*)((G < 32 ? pb : ph) + (G & 31) * 1536); };
    xpass(cx, gl + G_X1, gl + G_ST1, gl + G_FX1, xf, h0, h1, 11 + SO,
          [&](int k, float mean, float var, u32x4& w0, u32x4& w1) LAMBDA {
            if (k == 0) {
              const float r1 = __builtin_amdgcn_rsqf(var + 1e-5f);
              ge.cc[0] = r1; ge.cc[1] = var * r1 * r1 + 1e-5f;
              asm volatile("" : "+v"(ge.cc));
            }
            if (k == 1) {
              const float A = ge.cc[0] * __builtin_amdgcn_rsqf(ge.cc[1]);
              const float iA = __builtin_amdgcn_rcpf(A);
              ge.ia[0] = iA; ge.ia[1] = iA;
              const float k0 = -2.f * LOG2E * 0.7978845608028654f, k1 = k0 * 0.044715f;
              ge.cc[0] = k0 * A; ge.cc[1] = k1 * A * A * A;
              asm volatile("" : "+v"(ge.cc), "+v"(ge.ia));
            }
            if (k == 2) { xf[32] = aug_tok(mean, ge.ia[0], hk); asm volatile("" : "+v"(xf[32])); }
            if (k == 3) { w0 = aug_w(csp[0], b1p[0]); w1 = aug_w(csp[1], b1p[0] >> 16); asm volatile("" : "+v"(w0), "+v"(w1)); }
          },
          [&](auto ii) LAMBDA {
            constexpr int k = NW2_H + ii.v - 8;
            if (ii.v < 8) dma<(ii.v & 7)>(rs, cx.C + w * INBOX, cx.vo, w2b + 48 * 1024);
            else if (L == 0 && k < W2KEEP) w2f[k] = ldck(rs, cx.vo, w2b, k);
            if (ii.v >= 10 - RDW) rdw(ii.v - 10 + RDW);
          });
    STAMP(12 + SO); if (L == 1) FST(8, 13);
    BUDL(3);
    const int hs = gl + G_H + cx.ho, hfo = gl + G_FH + cx.fo;
    const int hb = gl + G_H + (cx.th * 4 + w) * 57344, fb = gl + G_FH + (cx.th * 4 + w) * 8 * FS;
    ge.sum = 0;
    asm volatile("" : "+a"(h0), "+a"(h1));
    h2 = MFMA(aug_w(csp[2], b1p[1]), xf[32], zero); h3 = MFMA(aug_w(csp[3], b1p[1] >> 16), xf[32], zero);
    u32x4 hf[56];
    auto ldh = [&](int k) LAMBDA { hf[h_iss(k)] = ldck(rs, cx.vo, hb, h_iss(k)); };
#pragma unroll
    for (int n = RDW; n < 4; n++) rdw(n);
    SB();
    unsigned pl = 0, sum = -cx.tk_oh, ck;
    f32x16 acc[4];
    sfor<64>([&](auto gg) LAMBDA {
      constexpr int G = gg, n = G & 31;
      if (G % 8 == 0) FST(4 + L, 6 + G / 8);
      if (G < 32) h2 = MFMA(ra[G & 3], xf[n], h2); else h3 = MFMA(ra[G & 3], xf[n], h3);
      SB();
      auto mid = [&]() LAMBDA { if (G + 4 < 64) rdw(G + 4); };
      if (G < 10) ge.step<0, 16, G, 2>(cx, h0, h1, hs, mid);
      else if (G < 26) ge.step<0, 16, 10 + (G - 10) / 2, G & 1>(cx, h0, h1, hs, mid);
      else if (G >= GB2 && G < GB2 + 20) ge.step<16, 24, 16 + (G - GB2) / 2, (G - GB2) & 1>(cx, h2, h2, hs, mid);
      else mid();
      if (L == 0) {
        constexpr int k = G + 4 * R2B, f = k < 16 ? 16 + 8 * (k >> 1 & 1) + 2 * (k >> 2) + (k & 1) : NW2_H + k - 14;
        if (f < W2KEEP) w2f[f < W2KEEP ? f : 0] = ldck(rs, cx.vo, w2b, f);
      }
      if (G == 9) put_flag(cx, hfo + FHL, 1);
      if (G == 25) put_flag(cx, hfo + 2 * FHL, 3);
      if (G == GB2 + 19) put_flag(cx, hfo + 3 * FHL, 5);
      if (G == GP1) pl = ldc1(rs, cx.fv8, fb + FHL);
      if (G == GW1) { SB(); poll_wait<8>(cx, pl, fb + FHL, cx.tk_oh, 1); STAMP(13 + SO); BUDL(4); }
      if (G >= GW1 && G < GW1 + 16) ldh(G - GW1);
      if (G == GP2) pl = ldc1(rs, cx.fv8, fb + 2 * FHL);
      if (G == 56) { SB(); poll_wait<8>(cx, pl, fb + 2 * FHL, cx.tk_oh, 3); BUDL(5); }
      if (G >= 56 && !(G & 1)) ldh(G / 2 - 12);
      if (G == 60) acc[0] = unpark(cx);
      SB();
    });
    FST(4 + L, 14);
    STAMP(14 + SO);
    unsigned msg = 0;
    char* dA = cx.A + w * 16384;
    const int sA = WT_QKV + c * WQ_C + w * 16384;
    Red rd;
    sfor<112>([&](auto gg) LAMBDA {
      constexpr int G = gg, f = s4_f(G), q = f >> 1, j = s4_j(G);
      constexpr int i = G >= 92 && G % 3 != 1 ? G - 92 - (G - 92) / 3 : -1;
      if (G % 8 == 0) FST(2 + L, G / 8);
      acc[j] = MFMA(w2f[2 * q + (j & 1)], hf[f], q == 0 && j ? zero : acc[j]); SB();
      if (H3F) {
        if (G >= 2 && G < 8) ge.step<24, 28, 22 + G, 2>(cx, h3, h3, hs, []() LAMBDA {});
        if (G == 7) put_flag(cx, hfo, 7);
      } else {
        if (G >= 2 && G < 14) ge.step<24, 28, 24 + (G - 2) / 2, G & 1>(cx, h3, h3, hs, []() LAMBDA {});
        if (G == 13) put_flag(cx, hfo, 7);
      }
      if (G == 15) put_final(cx, hfo + CKH, ge.sum);
      if (G == GW3) { SB(); poll_wait<8>(cx, pl, fb + 3 * FHL, cx.tk_oh, 5); BUDL(6); }
      if (G == GW3 + 32) { SB(); poll_wait<8>(cx, pl, fb, cx.tk_oh, 7); BUDL(7); }
      if (G < 8) ldh(20 + G);
      else if (G < 16) { if (!(G & 1)) ldh(24 + G / 2); }
      else if (G >= GW3 && G < GW3 + 48 && !((G - GW3) & 1)) ldh(32 + (G - GW3) / 2);
      if (G == GP3) pl = ldc1(rs, cx.fv8, fb + 3 * FHL);
      if (G == GP4) pl = ldc1(rs, cx.fv8, fb);
      if (G == GW3 + 45) ck = ldc1(rs, cx.fv8, fb + CKH + 4);
      if (G >= 16 && G < 24) w2f[G + 32] = *(const u32x4*)(cx.C + w * INBOX + (G - 16) * 1024 + cx.vo);
      if (G == (L ? GBR1 : GBR0)) lds_barrier();
      if (!(G & 1) && G >= 16 && (G - 16) / 2 + W2KEEP < 48) w2f[(G - 16) / 2 + W2KEEP] = ldck(rs, cx.vo, w2b, (G - 16) / 2 + W2KEEP);
      if (L == 0) {
        if (G == 51) msg = ldc1(rs, (lane & 15) * 8, MBX + (g * 16 + c) * 512 + 384);
        if (G == 79) { SB(); mail_wait(cx, msg, (lane & 15) * 8, MBX + (g * 16 + c) * 512 + 384, cx.N + 1); }
        if (G >= 80 && !(G & 1) && (G - 80) / 2 < NQK) dma<(G >= 80 ? (G - 80) / 2 : 0)>(rs, dA, cx.vo, sA);
      }
      if (j & 1) sum = sum4(sum, hf[f]);
      if (i >= 0 && i < 12) red_send1(cx, acc[3 - (i / 4 & 3)], 3 - (i / 4 & 3), i & 3);
      if (G == 100) sum += ck;
      if (G >= 100 && G < 106) wsum_step<(G >= 100 && G < 106 ? G - 100 : 0)>(sum);
      if (G == 106) { lds_barrier_n<2>(); rd.rd32(cx, 0); rd.rd32(cx, 1); rd.rd32(cx, 2); rd.rd32(cx, 3); rd.put(cx, sum); }
      if (G >= 109) rd.add32(G & 3);
      if (G == 111) rd.add32(0);
      SB();
    });
    lds_barrier();
    rd.rd1(cx);
    SB();
    FST(2 + L, 14);
    if (UNLIKELY(rd.fin(acc[0], xr))) {
      lds_barrier();
      do {
        spend(cx, 3 + 5 * L);
        xa = ldc(rs, cx.vo, gl + G_X1 + cx.xo); xb2 = ldc(rs, cx.vo + 1024, gl + G_X1 + cx.xo);
        float bv[16]; unpack16(*(const u32x4*)(b2 + col), *(const u32x4*)(b2 + col + 8), bv);
        unpack16(xa, xb2, xr);
#pragma unroll
        for (int r = 0; r < 16; r++) acc[0][r] = xr[r] + bv[r];
        acc[1] = zero; acc[2] = zero; acc[3] = zero; sum = ldc1(rs, cx.fv8, fb + CKH + 4) - cx.tk_oh;
#pragma unroll
        for (int k = 0; k < 56; k++) {
          const u32x4 f = ldck(rs, cx.vo, hb, k);
          sum = sum4(sum, f);
          acc[2 * (k & 1)] = MFMA(w2f[k & ~1], f, acc[2 * (k & 1)]); acc[2 * (k & 1) + 1] = MFMA(w2f[k | 1], f, acc[2 * (k & 1) + 1]);
          if ((k & 7) == 7) SB();
        }
      } while (again(cx, sum));
      red_send(cx, acc[1], 1); red_send(cx, acc[2], 2); red_send(cx, acc[3], 3);
      lds_barrier();
      red_recv(cx, acc[0], xr);
    }
    FST(2 + L, 15);
    if (L == 0) {
      publish_x(cx, xr, cx.gb + G_X2, cx.gb + G_ST2, cx.gb + G_FX2);
      STAMP(20);
      sfor<16 - NQK>([&](auto ii) LAMBDA { dma<NQK + ii.v>(rs, dA, cx.vo, sA); });
      park(cx, xr);
    } else {
      u32x4 f0, f1; pack16(xr, f0, f1);
      int ln = lane; asm volatile("" : "+v"(ln));
      char* tb = cx.C + w * INBOX + 8192;
      const int wa = (ln & 31) * 64 + ((((ln >> 5) * 2) ^ ((ln >> 1) & 3)) << 4);
      *(u32x4*)(tb + wa) = f0; *(u32x4*)(tb + (wa ^ 16)) = f1;
      asm volatile("" ::: "memory");
      f0 = *(const u32x4*)(tb + ln * 16); f1 = *(const u32x4*)(tb + ln * 16 + 1024);
      u16* o = out + (size_t)(128 * g + 64 * cx.th + 32 * (w >> 1)) * 512 + 64 * cx.cb + 32 * (w & 1);
      const unsigned oo = (ln >> 2) * 512 + (((ln ^ (ln >> 3)) & 3) << 3);
      asm volatile("global_store_dwordx4 %0, %1, %2 " OST :: "v"(oo * 2), "v"(f0), "s"(o) : "memory");
      asm volatile("global_store_dwordx4 %0, %1, %2 " OST :: "v"(oo * 2), "v"(f1), "s"(o + 16 * 512) : "memory");
      STAMP(29);
    }
  }
}

__global__ __launch_bounds__(256, 1) void fused(const u16* __restrict__ x, const u16* __restrict__ wqkv, char* ws, const u16* __restrict__ wphi,
                                                const u16* __restrict__ b1, const u16* __restrict__ b2, const u16* __restrict__ w1,
                                                const u16* __restrict__ w2, const u16* __restrict__ wo, u16* __restrict__ out) {
  __shared__ __attribute__((aligned(16))) char ldsA[65536];
  __shared__ __attribute__((aligned(16))) char ldsB[49152];
  __shared__ __attribute__((aligned(16))) char ldsC[49152];
  Cx cx;
  STAMP(0); CSTAMP(30); FST(12, 0); FST(13, 0);
  asm volatile("s_load_dword %0, %2, 0x0\n\ts_load_dword %1, %2, 0x40" : "=&s"(cx.ka[0]), "=&s"(cx.ka[1]) : "s"(__builtin_amdgcn_kernarg_segment_ptr()));
  {
    unsigned long long pc; unsigned len;
    asm volatile("s_getpc_b64 %0\n.Lcb0:\n\ts_mov_b32 %1, .Lce0-.Lcb0-16" : "=s"(pc), "=s"(len));
    unsigned o = ((blockIdx.x >> 3) * 256 + threadIdx.x) * 16;
    if (PRE == 1 || o < PRE * 1024u) gldso<0>(o < len ? o : 0u, (const void*)pc, (unsigned)(size_t)ldsC + 48128u);
    cx.pc = (const char*)pc; cx.po = o < len && o >= PRE * 1024u ? o : 0u;
  }
  FST(12, 1);
  cx.ws = ws; cx.A = ldsA; cx.B = ldsB; cx.C = ldsC;
  cx.lane = threadIdx.x & 63; cx.w = RFL(threadIdx.x >> 6);
  cx.tok = cx.lane & 31; cx.hk = cx.lane >> 5; cx.vo = cx.lane * 16; cx.budget = SPIN_MAX;
  { const int b = blockIdx.x, xx = b & 7, j = b >> 3; cx.g = 4 * (xx >> 1) + (j >> 3); cx.c = 8 * (xx & 1) + ((j + CROT) & 7); }
  cx.cb = THB == 8 ? cx.c & 7 : 4 * (cx.c >> 3) + (cx.c & 3); cx.th = THB == 8 ? cx.c >> 3 : (cx.c >> 2) & 1;
  cx.rs = __builtin_amdgcn_make_buffer_rsrc(ws, 0, WS_USED, 0x00020000);
  u32x4 phq[4], aphi[2], w2f[56], wof[16]; unsigned csw[3], csp[4], b1p[2]; KvIn kin; Misc mi;
  stage1a(cx, x, wqkv, wphi, wo, w1, b1, w2, b2, phq, csw, aphi, kin, w2f, wof, mi);
  layer<0>(cx, x, b1, b2, out, phq, csw, aphi, kin, w2f, wof, mi, csp, b1p);
  asm volatile("" : "+v"(cx.lane), "+v"(cx.tok), "+v"(cx.hk), "+v"(cx.vo));
  asm volatile("" : "+v"(csp[0]), "+v"(csp[1]), "+v"(csp[2]), "+v"(csp[3]), "+v"(b1p[0]), "+v"(b1p[1]), "+v"(csw[0]), "+v"(csw[1]), "+v"(csw[2]));
#pragma unroll
  for (int k = 0; k < W2KEEP; k++) asm volatile("" : "+a"(w2f[k]));
  layer<1>(cx, x, b1, b2, out, phq, csw, aphi, kin, w2f, wof, mi, csp, b1p);
  asm volatile(".Lce0:");
}

extern "C" void kernel_launch(void* const* d_in, const int* in_sizes, int n_in, void* d_out, int out_size, void* d_ws, size_t ws_size,
                              hipStream_t stream) {
  fused<<<256, 256, 0, stream>>>((const u16*)d_in[0], (const u16*)d_in[1], (char*)d_ws, (const u16*)d_in[2], (const u16*)d_in[6],
                                 (const u16*)d_in[8], (const u16*)d_in[5], (const u16*)d_in[7], (const u16*)d_in[3], (u16*)d_out);
}
```
